```python
import torch

def _softmax(x, axis=-1):
    return torch.softmax(x, dim=axis)
def _gelu(x):
    return torch.nn.functional.gelu(x, approximate="tanh")
def _silu(x):
    return torch.nn.functional.silu(x)
def _sigmoid(x):
    return torch.sigmoid(x)
def _layernorm(x, axis=-1, eps=1e-5):
    m = x.mean(dim=axis, keepdim=True)
    v = x.var(dim=axis, keepdim=True, unbiased=False)
    return (x - m) / torch.sqrt(v + eps)
def _scatter_add(tgt, idx, src, axis=0):
    return tgt.clone().index_add_(axis, idx, src)
def _segment_sum(src, seg, n):
    out = torch.zeros((n,) + src.shape[1:], dtype=src.dtype)
    return out.index_add_(0, seg, src)
def _pad(x, pw):
    flat = tuple(v for pair in reversed(pw) for v in pair)
    return torch.nn.functional.pad(x, flat)

kernel_name = 'gated_mix__ssm_conv_s5__moe_arch_mod__prompt'

def setup_inputs():

    _w = (lambda a: (lambda t: t.to(torch.bfloat16) if t.is_floating_point() else t)(torch.as_tensor(a)))
    def _A_setup():
        B, S, D = 1, 2048, 1024
        P = D
        Lambda_re = -0.5 * (lambda _x: torch.exp(torch.as_tensor(_x)))((lambda *s: torch.randn(*s, dtype=torch.bfloat16) * 0.8)(P))
        Lambda_im = (lambda *s: torch.randn(*s, dtype=torch.bfloat16) * 0.8)(P) * 1.0
        B_re = (lambda *s: torch.randn(*s, dtype=torch.bfloat16) * 0.8)(P, D) * (D ** -0.5)
        B_im = (lambda *s: torch.randn(*s, dtype=torch.bfloat16) * 0.8)(P, D) * (D ** -0.5)
        C_re = (lambda *s: torch.randn(*s, dtype=torch.bfloat16) * 0.8)(D, P) * (P ** -0.5)
        C_im = (lambda *s: torch.randn(*s, dtype=torch.bfloat16) * 0.8)(D, P) * (P ** -0.5)
        log_dt = (lambda *s: torch.randn(*s, dtype=torch.bfloat16) * 0.8)(P) * 0.3 - 2.0
        Dskip = (lambda *s: torch.randn(*s, dtype=torch.bfloat16) * 0.8)(D)
        return {
            "x": _w((lambda *s: torch.randn(*s, dtype=torch.bfloat16) * 0.05)(B, S, D)),
            "Lambda_re": _w(Lambda_re),
            "Lambda_im": _w(Lambda_im),
            "B_re": _w(B_re),
            "B_im": _w(B_im),
            "C_re": _w(C_re),
            "C_im": _w(C_im),
            "log_dt": _w(log_dt),
            "Dskip": _w(Dskip),
        }
    _A_in = _A_setup()

    def _B_setup():
        B, S, D, F, C = 1, 2048, 1024, 3584, 8
        return {
            "x": _w((lambda *s: torch.randn(*s, dtype=torch.bfloat16) * 0.05)(B, S, D)),
            "w_r": _w((lambda *s: torch.randn(*s, dtype=torch.bfloat16) * 1.6)(D)),
            "w_gate": _w((lambda *s: torch.randn(*s, dtype=torch.bfloat16) * 0.1)(D, F)),
            "w_up": _w((lambda *s: torch.randn(*s, dtype=torch.bfloat16) * 0.1)(D, F)),
            "w_down": _w((lambda *s: torch.randn(*s, dtype=torch.bfloat16) * 0.1)(F, D)),
        }
    _B_in = _B_setup()

    return {
        "A_x": _A_in["x"],
        "A_Lambda_re": _A_in["Lambda_re"],
        "A_Lambda_im": _A_in["Lambda_im"],
        "A_B_re": _A_in["B_re"],
        "A_B_im": _A_in["B_im"],
        "A_C_re": _A_in["C_re"],
        "A_C_im": _A_in["C_im"],
        "A_log_dt": _A_in["log_dt"],
        "A_Dskip": _A_in["Dskip"],
        "B_x": _B_in["x"],
        "B_w_r": _B_in["w_r"],
        "B_w_gate": _B_in["w_gate"],
        "B_w_up": _B_in["w_up"],
        "B_w_down": _B_in["w_down"],
        "gate_w": _w((lambda *s: torch.randn(*s, dtype=torch.bfloat16) * 0.05)(1024) * 16.0),
    }

def reference(A_x, A_Lambda_re, A_Lambda_im, A_B_re, A_B_im, A_C_re, A_C_im, A_log_dt, A_Dskip, B_x, B_w_r, B_w_gate, B_w_up, B_w_down, gate_w):
    def _A_ref(x, Lambda_re, Lambda_im, B_re, B_im, C_re, C_im, log_dt, Dskip):
        Bsz, L, H = x.shape
        Lam = Lambda_re + 1j * Lambda_im
        Bmat = B_re + 1j * B_im
        Cmat = C_re + 1j * C_im
        dt = torch.exp(log_dt)
        Lam_bar = torch.exp(dt * Lam)
        B_bar = ((Lam_bar - 1.0) / Lam)[:, None] * Bmat
        Bu = torch.einsum("bld,pd->blp", x + 0j, B_bar)
        h = torch.zeros_like(Bu[:, 0, :])
        hs = []
        for t in range(L):
            h = Lam_bar[None, :] * h + Bu[:, t, :]
            hs.append(h)
        hseq = torch.stack(hs, 1)
        y = 2.0 * torch.einsum("blp,dp->bld", hseq, Cmat).real + x * Dskip[None, None, :]
        return y

    def _B_ref(x, w_r, w_gate, w_up, w_down):
        B, S, D = x.shape
        C = 8
        r = x @ w_r
        idx = torch.argsort(-r, axis=-1)[:, :C]
        idx_e = (lambda x, n, axis: torch.repeat_interleave(x, n, dim=axis))(idx.reshape(B, C, 1), D, axis=2)
        x_sel = (lambda x, i, axis: torch.take_along_dim(x, i, dim=axis))(x, idx_e, axis=1)
        g = _silu(x_sel @ w_gate)
        u = x_sel @ w_up
        y = (g * u) @ w_down
        r_sel = _sigmoid((lambda x, i, axis: torch.take_along_dim(x, i, dim=axis))(r, idx, axis=1))
        y = y * r_sel.reshape(B, C, 1)
        b_off = (torch.arange(B) * S).reshape(B, 1)
        flat_idx = (idx + b_off).reshape(B * C)
        zero = (x * 0.0).reshape(B * S, D)
        upd = _scatter_add(zero, flat_idx, y.reshape(B * C, D), axis=0)
        return x + upd.reshape(B, S, D)

    x = A_x
    g = _sigmoid((x * gate_w).sum(-1, keepdims=True))
    a = _A_ref(x, A_Lambda_re, A_Lambda_im, A_B_re, A_B_im, A_C_re, A_C_im, A_log_dt, A_Dskip)
    b = _B_ref(x, B_w_r, B_w_gate, B_w_up, B_w_down)
    return g * a + (1 - g) * b
```

```cpp
#include <hip/hip_runtime.h>
#include <hip/hip_bf16.h>
#include <stdint.h>

typedef __attribute__((ext_vector_type(8))) short bf16x8;
typedef __attribute__((ext_vector_type(4))) float f32x4;
typedef __attribute__((ext_vector_type(2))) float f32x2;
typedef __attribute__((ext_vector_type(4))) unsigned u32x4;
typedef __attribute__((ext_vector_type(2))) unsigned u32x2;
typedef unsigned short u16;
typedef unsigned long long u64;
#define LAS __attribute__((address_space(3)))
#define DEV __device__ __forceinline__

#define ASM_MA \
  "s_nop 4\n\t" \
  "buffer_load_dword v228, off, %[srd], 0 sc1\n\t" \
  "buffer_load_dwordx2 %[dv], %[dofs], %[srdd], 0 offen\n\t" \
  "s_mov_b32 %[sk], 0\n\t" \
  "s_mov_b32 %[sx], 0x20000\n\t" \
  "s_branch Lsp0_%=\n\t" \
  "Lb0_%=:\n\t" \
  "ds_read_b128 v[128:131], %[pa0] offset:0\n\t" \
  "ds_read_b128 v[132:135], %[pa0] offset:8192\n\t" \
  "ds_read_b128 v[176:179], %[pa1] offset:0\n\t" \
  "ds_read_b128 v[180:183], %[pa1] offset:8192\n\t" \
  "ds_read_b128 v[144:147], %[pb0] offset:0\n\t" \
  "ds_read_b128 v[148:151], %[pb0] offset:2048\n\t" \
  "ds_read_b128 v[152:155], %[pb0] offset:4096\n\t" \
  "ds_read_b128 v[156:159], %[pb0] offset:6144\n\t" \
  "ds_read_b128 v[160:163], %[pb0] offset:8192\n\t" \
  "ds_read_b128 v[164:167], %[pb0] offset:10240\n\t" \
  "ds_read_b128 v[168:171], %[pb0] offset:12288\n\t" \
  "ds_read_b128 v[172:175], %[pb0] offset:14336\n\t" \
  "s_waitcnt lgkmcnt(4)\n\t" \
  "v_mfma_f32_16x16x32_bf16 v[64:67], v[128:131], v[144:147], 0\n\t" \
  "v_mfma_f32_16x16x32_bf16 v[96:99], v[132:135], v[144:147], 0\n\t" \
  "s_mov_b32 m0, %[scn]\n\t" \
  "s_mov_b64 exec, 1\n\t" \
  "buffer_load_dword %[l16], %[srz], 0 offen lds\n\t" \
  "s_mov_b64 exec, -1\n\t" \
  "s_add_u32 %[scn], %[scn], 16\n\t" \
  "ds_read_b128 v[144:147], %[pb1] offset:0\n\t" \
  "v_mov_b64 v[136:137], v[176:177]\n\t" \
  "v_mfma_f32_16x16x32_bf16 v[68:71], v[128:131], v[148:151], 0\n\t" \
  "v_mfma_f32_16x16x32_bf16 v[100:103], v[132:135], v[148:151], 0\n\t" \
  "ds_read_b128 v[148:151], %[pb1] offset:2048\n\t" \
  "v_mov_b64 v[138:139], v[178:179]\n\t" \
  "v_mfma_f32_16x16x32_bf16 v[72:75], v[128:131], v[152:155], 0\n\t" \
  "v_mfma_f32_16x16x32_bf16 v[104:107], v[132:135], v[152:155], 0\n\t" \
  "ds_read_b128 v[152:155], %[pb1] offset:4096\n\t" \
  "v_add_u32 %[fl], 8, %[fl]\n\t" \
  "v_mfma_f32_16x16x32_bf16 v[76:79], v[128:131], v[156:159], 0\n\t" \
  "v_mfma_f32_16x16x32_bf16 v[108:111], v[132:135], v[156:159], 0\n\t" \
  "ds_read_b128 v[156:159], %[pb1] offset:6144\n\t" \
  "ds_read_b64 v[224:225], %[fl]\n\t" \
  "s_waitcnt lgkmcnt(5)\n\t" \
  "v_mfma_f32_16x16x32_bf16 v[80:83], v[128:131], v[160:163], 0\n\t" \
  "v_mfma_f32_16x16x32_bf16 v[112:115], v[132:135], v[160:163], 0\n\t" \
  "ds_read_b128 v[160:163], %[pb1] offset:8192\n\t" \
  "v_mov_b64 v[140:141], v[180:181]\n\t" \
  "v_mfma_f32_16x16x32_bf16 v[84:87], v[128:131], v[164:167], 0\n\t" \
  "v_mfma_f32_16x16x32_bf16 v[116:119], v[132:135], v[164:167], 0\n\t" \
  "ds_read_b128 v[164:167], %[pb1] offset:10240\n\t" \
  "v_mov_b64 v[142:143], v[182:183]\n\t" \
  "v_mfma_f32_16x16x32_bf16 v[88:91], v[128:131], v[168:171], 0\n\t" \
  "v_mfma_f32_16x16x32_bf16 v[120:123], v[132:135], v[168:171], 0\n\t" \
  "ds_read_b128 v[168:171], %[pb1] offset:12288\n\t" \
  "v_mfma_f32_16x16x32_bf16 v[92:95], v[128:131], v[172:175], 0\n\t" \
  "v_mfma_f32_16x16x32_bf16 v[124:127], v[132:135], v[172:175], 0\n\t" \
  "ds_read_b128 v[172:175], %[pb1] offset:14336\n\t" \
  "Lloop_%=:\n\t" \
  "s_add_u32 %[sk], %[sk], 1\n\t" \
  "s_and_b32 %[s1], %[sk], 3\n\t" \
  "s_cselect_b32 %[s1], 0x8000, %[wrap]\n\t" \
  "s_add_u32 %[sx], %[sx], 0x8000\n\t" \
  "s_cmp_eq_u32 %[sx], 0x28000\n\t" \
  "s_cselect_b32 %[s2], %[wrap2], 0x8000\n\t" \
  "s_cselect_b32 %[sx], 0, %[sx]\n\t" \
  "s_waitcnt lgkmcnt(4)\n\t" \
  "v_or_b32 v224, v224, v225\n\t" \
  "v_mfma_f32_16x16x32_bf16 v[64:67], v[136:139], v[144:147], v[64:67]\n\t" \
  "v_readfirstlane_b32 %[s0], v224\n\t" \
  "v_mfma_f32_16x16x32_bf16 v[96:99], v[140:143], v[144:147], v[96:99]\n\t" \
  "v_add_u32 %[pa0], %[s1], %[pa0]\n\t" \
  "v_add_u32 %[pa1], %[s1], %[pa1]\n\t" \
  "v_add_u32 %[pb0], %[s2], %[pb0]\n\t" \
  "s_cmp_eq_u32 %[s0], 0\n\t" \
  "s_cbranch_scc0 Lsp1_%=\n\t" \
  "Lb1_%=:\n\t" \
  "ds_read_b128 v[128:131], %[pa0] offset:0\n\t" \
  "ds_read_b128 v[132:135], %[pa0] offset:8192\n\t" \
  "ds_read_b128 v[176:179], %[pa1] offset:0\n\t" \
  "ds_read_b128 v[180:183], %[pa1] offset:8192\n\t" \
  "ds_read_b128 v[144:147], %[pb0] offset:0\n\t" \
  "v_mfma_f32_16x16x32_bf16 v[68:71], v[136:139], v[148:151], v[68:71]\n\t" \
  "v_mfma_f32_16x16x32_bf16 v[100:103], v[140:143], v[148:151], v[100:103]\n\t" \
  "ds_read_b128 v[148:151], %[pb0] offset:2048\n\t" \
  "v_mfma_f32_16x16x32_bf16 v[72:75], v[136:139], v[152:155], v[72:75]\n\t" \
  "v_mfma_f32_16x16x32_bf16 v[104:107], v[140:143], v[152:155], v[104:107]\n\t" \
  "ds_read_b128 v[152:155], %[pb0] offset:4096\n\t" \
  "v_mfma_f32_16x16x32_bf16 v[76:79], v[136:139], v[156:159], v[76:79]\n\t" \
  "v_mfma_f32_16x16x32_bf16 v[108:111], v[140:143], v[156:159], v[108:111]\n\t" \
  "ds_read_b128 v[156:159], %[pb0] offset:6144\n\t" \
  "s_waitcnt lgkmcnt(4)\n\t" \
  "v_mfma_f32_16x16x32_bf16 v[80:83], v[136:139], v[160:163], v[80:83]\n\t" \
  "s_mov_b32 m0, %[scn]\n\t" \
  "s_mov_b64 exec, 1\n\t" \
  "buffer_load_dword %[l16], %[srz], 0 offen lds\n\t" \
  "s_mov_b64 exec, -1\n\t" \
  "v_mfma_f32_16x16x32_bf16 v[112:115], v[140:143], v[160:163], v[112:115]\n\t" \
  "ds_read_b128 v[160:163], %[pb0] offset:8192\n\t" \
  "v_mfma_f32_16x16x32_bf16 v[84:87], v[136:139], v[164:167], v[84:87]\n\t" \
  "v_mfma_f32_16x16x32_bf16 v[116:119], v[140:143], v[164:167], v[116:119]\n\t" \
  "ds_read_b128 v[164:167], %[pb0] offset:10240\n\t" \
  "v_add_u32 %[pb1], %[s2], %[pb1]\n\t" \
  "s_add_u32 %[scn], %[scn], 16\n\t" \
  "v_mfma_f32_16x16x32_bf16 v[88:91], v[136:139], v[168:171], v[88:91]\n\t" \
  "v_mfma_f32_16x16x32_bf16 v[120:123], v[140:143], v[168:171], v[120:123]\n\t" \
  "ds_read_b128 v[168:171], %[pb0] offset:12288\n\t" \
  "v_mfma_f32_16x16x32_bf16 v[92:95], v[136:139], v[172:175], v[92:95]\n\t" \
  "v_mfma_f32_16x16x32_bf16 v[124:127], v[140:143], v[172:175], v[124:127]\n\t" \
  "ds_read_b128 v[172:175], %[pb0] offset:14336\n\t" \
  "s_waitcnt lgkmcnt(4)\n\t" \
  "v_mfma_f32_16x16x32_bf16 v[64:67], v[128:131], v[144:147], v[64:67]\n\t" \
  "v_mfma_f32_16x16x32_bf16 v[96:99], v[132:135], v[144:147], v[96:99]\n\t" \
  "ds_read_b128 v[144:147], %[pb1] offset:0\n\t" \
  "v_mov_b64 v[136:137], v[176:177]\n\t" \
  "v_mfma_f32_16x16x32_bf16 v[68:71], v[128:131], v[148:151], v[68:71]\n\t" \
  "v_mfma_f32_16x16x32_bf16 v[100:103], v[132:135], v[148:151], v[100:103]\n\t" \
  "ds_read_b128 v[148:151], %[pb1] offset:2048\n\t" \
  "v_mov_b64 v[138:139], v[178:179]\n\t" \
  "v_mfma_f32_16x16x32_bf16 v[72:75], v[128:131], v[152:155], v[72:75]\n\t" \
  "v_mfma_f32_16x16x32_bf16 v[104:107], v[132:135], v[152:155], v[104:107]\n\t" \
  "ds_read_b128 v[152:155], %[pb1] offset:4096\n\t" \
  "v_add_u32 %[fl], 8, %[fl]\n\t" \
  "v_mfma_f32_16x16x32_bf16 v[76:79], v[128:131], v[156:159], v[76:79]\n\t" \
  "v_mfma_f32_16x16x32_bf16 v[108:111], v[132:135], v[156:159], v[108:111]\n\t" \
  "ds_read_b128 v[156:159], %[pb1] offset:6144\n\t" \
  "ds_read_b64 v[224:225], %[fl]\n\t" \
  "s_waitcnt lgkmcnt(5)\n\t" \
  "v_mfma_f32_16x16x32_bf16 v[80:83], v[128:131], v[160:163], v[80:83]\n\t" \
  "v_mfma_f32_16x16x32_bf16 v[112:115], v[132:135], v[160:163], v[112:115]\n\t" \
  "ds_read_b128 v[160:163], %[pb1] offset:8192\n\t" \
  "v_mov_b64 v[140:141], v[180:181]\n\t" \
  "v_mfma_f32_16x16x32_bf16 v[84:87], v[128:131], v[164:167], v[84:87]\n\t" \
  "v_mfma_f32_16x16x32_bf16 v[116:119], v[132:135], v[164:167], v[116:119]\n\t" \
  "ds_read_b128 v[164:167], %[pb1] offset:10240\n\t" \
  "v_mov_b64 v[142:143], v[182:183]\n\t" \
  "v_mfma_f32_16x16x32_bf16 v[88:91], v[128:131], v[168:171], v[88:91]\n\t" \
  "v_mfma_f32_16x16x32_bf16 v[120:123], v[132:135], v[168:171], v[120:123]\n\t" \
  "ds_read_b128 v[168:171], %[pb1] offset:12288\n\t" \
  "v_mfma_f32_16x16x32_bf16 v[92:95], v[128:131], v[172:175], v[92:95]\n\t" \
  "v_mfma_f32_16x16x32_bf16 v[124:127], v[132:135], v[172:175], v[124:127]\n\t" \
  "ds_read_b128 v[172:175], %[pb1] offset:14336\n\t" \
  "s_cmp_lt_u32 %[sk], 15\n\t" \
  "s_cbranch_scc1 Lloop_%=\n\t" \
  "s_waitcnt lgkmcnt(4)\n\t" \
  "v_or_b32 v224, v224, v225\n\t" \
  "v_mfma_f32_16x16x32_bf16 v[64:67], v[136:139], v[144:147], v[64:67]\n\t" \
  "v_readfirstlane_b32 %[s0], v224\n\t" \
  "v_mfma_f32_16x16x32_bf16 v[96:99], v[140:143], v[144:147], v[96:99]\n\t" \
  "s_cmp_eq_u32 %[s0], 0\n\t" \
  "s_cbranch_scc0 Lsp2_%=\n\t" \
  "Lb2_%=:\n\t" \
  "v_mfma_f32_16x16x32_bf16 v[68:71], v[136:139], v[148:151], v[68:71]\n\t" \
  "v_mfma_f32_16x16x32_bf16 v[100:103], v[140:143], v[148:151], v[100:103]\n\t" \
  "ds_read_b128 v[176:179], %[tq] offset:0\n\t" \
  "ds_read_b128 v[180:183], %[tq] offset:256\n\t" \
  "v_mfma_f32_16x16x32_bf16 v[72:75], v[136:139], v[152:155], v[72:75]\n\t" \
  "v_mfma_f32_16x16x32_bf16 v[104:107], v[140:143], v[152:155], v[104:107]\n\t" \
  "ds_read_b128 v[192:195], %[tq] offset:1024\n\t" \
  "ds_read_b128 v[196:199], %[tq] offset:1280\n\t" \
  "v_mfma_f32_16x16x32_bf16 v[76:79], v[136:139], v[156:159], v[76:79]\n\t" \
  "v_mfma_f32_16x16x32_bf16 v[108:111], v[140:143], v[156:159], v[108:111]\n\t" \
  "ds_read_b128 v[200:203], %[tq] offset:1536\n\t" \
  "ds_read_b128 v[204:207], %[tq] offset:1792\n\t" \
  "s_waitcnt lgkmcnt(6)\n\t" \
  "v_mfma_f32_16x16x32_bf16 v[80:83], v[136:139], v[160:163], v[80:83]\n\t" \
  "s_mov_b32 m0, %[scn]\n\t" \
  "s_mov_b64 exec, 1\n\t" \
  "buffer_load_dword %[l16], %[srz], 0 offen lds\n\t" \
  "s_mov_b64 exec, -1\n\t" \
  "v_mfma_f32_16x16x32_bf16 v[112:115], v[140:143], v[160:163], v[112:115]\n\t" \
  "ds_read_b128 v[208:211], %[tq] offset:2048\n\t" \
  "ds_read_b128 v[212:215], %[tq] offset:2304\n\t" \
  "v_mfma_f32_16x16x32_bf16 v[84:87], v[136:139], v[164:167], v[84:87]\n\t" \
  "v_mfma_f32_16x16x32_bf16 v[116:119], v[140:143], v[164:167], v[116:119]\n\t" \
  "ds_read_b128 v[216:219], %[tq] offset:2560\n\t" \
  "ds_read_b128 v[220:223], %[tq] offset:2816\n\t" \
  "v_mfma_f32_16x16x32_bf16 v[88:91], v[136:139], v[168:171], v[88:91]\n\t" \
  "v_mfma_f32_16x16x32_bf16 v[120:123], v[140:143], v[168:171], v[120:123]\n\t" \
  "ds_read_b128 v[184:187], %[tq] offset:512\n\t" \
  "ds_read_b128 v[188:191], %[tq] offset:768\n\t" \
  "v_mfma_f32_16x16x32_bf16 v[92:95], v[136:139], v[172:175], v[92:95]\n\t" \
  "v_mfma_f32_16x16x32_bf16 v[124:127], v[140:143], v[172:175], v[124:127]\n\t" \
  "ds_read2_b32 v[230:231], %[tcl] offset0:0 offset1:64\n\t" \
  "ds_read2_b32 v[232:233], %[tcw] offset0:0 offset1:64\n\t" \
  "s_setprio 3\n\t" \
  "s_waitcnt vmcnt(8) lgkmcnt(4)\n\t" \
  "v_readfirstlane_b32 %[tgB], v228\n\t" \
  "s_add_u32 %[tgA], %[tgB], 1\n\t" \
  "s_add_u32 %[tgB], %[tgB], 0x40000001\n\t" \
  "v_pk_fma_f32 v[244:245], v[180:181], v[96:97], v[68:69] neg_lo:[1,0,0] neg_hi:[1,0,0]\n\t" \
  "v_pk_fma_f32 v[246:247], v[182:183], v[98:99], v[70:71] neg_lo:[1,0,0] neg_hi:[1,0,0]\n\t" \
  "v_pk_fma_f32 v[248:249], v[180:181], v[64:65], v[100:101]\n\t" \
  "v_pk_fma_f32 v[250:251], v[182:183], v[66:67], v[102:103]\n\t" \
  "v_pk_fma_f32 v[236:237], v[176:177], v[64:65], v[244:245]\n\t" \
  "v_pk_fma_f32 v[238:239], v[178:179], v[66:67], v[246:247]\n\t" \
  "v_pk_fma_f32 v[240:241], v[176:177], v[96:97], v[248:249]\n\t" \
  "v_pk_fma_f32 v[242:243], v[178:179], v[98:99], v[250:251]\n\t" \
  "v_pk_fma_f32 v[244:245], v[180:181], v[240:241], v[72:73] neg_lo:[1,0,0] neg_hi:[1,0,0]\n\t" \
  "v_pk_fma_f32 v[246:247], v[182:183], v[242:243], v[74:75] neg_lo:[1,0,0] neg_hi:[1,0,0]\n\t" \
  "v_pk_fma_f32 v[248:249], v[180:181], v[236:237], v[104:105]\n\t" \
  "v_pk_fma_f32 v[250:251], v[182:183], v[238:239], v[106:107]\n\t" \
  "v_pk_fma_f32 v[236:237], v[176:177], v[236:237], v[244:245]\n\t" \
  "v_pk_fma_f32 v[238:239], v[178:179], v[238:239], v[246:247]\n\t" \
  "v_pk_fma_f32 v[240:241], v[176:177], v[240:241], v[248:249]\n\t" \
  "v_pk_fma_f32 v[242:243], v[178:179], v[242:243], v[250:251]\n\t" \
  "v_pk_fma_f32 v[244:245], v[180:181], v[240:241], v[76:77] neg_lo:[1,0,0] neg_hi:[1,0,0]\n\t" \
  "v_pk_fma_f32 v[246:247], v[182:183], v[242:243], v[78:79] neg_lo:[1,0,0] neg_hi:[1,0,0]\n\t" \
  "v_pk_fma_f32 v[248:249], v[180:181], v[236:237], v[108:109]\n\t" \
  "v_pk_fma_f32 v[250:251], v[182:183], v[238:239], v[110:111]\n\t" \
  "v_pk_fma_f32 v[236:237], v[176:177], v[236:237], v[244:245]\n\t" \
  "v_pk_fma_f32 v[238:239], v[178:179], v[238:239], v[246:247]\n\t" \
  "v_pk_fma_f32 v[240:241], v[176:177], v[240:241], v[248:249]\n\t" \
  "v_pk_fma_f32 v[242:243], v[178:179], v[242:243], v[250:251]\n\t" \
  "v_pk_fma_f32 v[244:245], v[180:181], v[240:241], v[80:81] neg_lo:[1,0,0] neg_hi:[1,0,0]\n\t" \
  "v_pk_fma_f32 v[246:247], v[182:183], v[242:243], v[82:83] neg_lo:[1,0,0] neg_hi:[1,0,0]\n\t" \
  "v_pk_fma_f32 v[248:249], v[180:181], v[236:237], v[112:113]\n\t" \
  "v_pk_fma_f32 v[250:251], v[182:183], v[238:239], v[114:115]\n\t" \
  "v_pk_fma_f32 v[236:237], v[176:177], v[236:237], v[244:245]\n\t" \
  "v_pk_fma_f32 v[238:239], v[178:179], v[238:239], v[246:247]\n\t" \
  "v_pk_fma_f32 v[240:241], v[176:177], v[240:241], v[248:249]\n\t" \
  "v_pk_fma_f32 v[242:243], v[178:179], v[242:243], v[250:251]\n\t" \
  "v_pk_fma_f32 v[244:245], v[180:181], v[240:241], v[84:85] neg_lo:[1,0,0] neg_hi:[1,0,0]\n\t" \
  "v_pk_fma_f32 v[246:247], v[182:183], v[242:243], v[86:87] neg_lo:[1,0,0] neg_hi:[1,0,0]\n\t" \
  "v_pk_fma_f32 v[248:249], v[180:181], v[236:237], v[116:117]\n\t" \
  "v_pk_fma_f32 v[250:251], v[182:183], v[238:239], v[118:119]\n\t" \
  "v_pk_fma_f32 v[236:237], v[176:177], v[236:237], v[244:245]\n\t" \
  "v_pk_fma_f32 v[238:239], v[178:179], v[238:239], v[246:247]\n\t" \
  "v_pk_fma_f32 v[240:241], v[176:177], v[240:241], v[248:249]\n\t" \
  "v_pk_fma_f32 v[242:243], v[178:179], v[242:243], v[250:251]\n\t" \
  "v_pk_fma_f32 v[244:245], v[180:181], v[240:241], v[88:89] neg_lo:[1,0,0] neg_hi:[1,0,0]\n\t" \
  "v_pk_fma_f32 v[246:247], v[182:183], v[242:243], v[90:91] neg_lo:[1,0,0] neg_hi:[1,0,0]\n\t" \
  "v_pk_fma_f32 v[248:249], v[180:181], v[236:237], v[120:121]\n\t" \
  "v_pk_fma_f32 v[250:251], v[182:183], v[238:239], v[122:123]\n\t" \
  "v_pk_fma_f32 v[236:237], v[176:177], v[236:237], v[244:245]\n\t" \
  "v_pk_fma_f32 v[238:239], v[178:179], v[238:239], v[246:247]\n\t" \
  "v_pk_fma_f32 v[240:241], v[176:177], v[240:241], v[248:249]\n\t" \
  "v_pk_fma_f32 v[242:243], v[178:179], v[242:243], v[250:251]\n\t" \
  "v_pk_fma_f32 v[244:245], v[180:181], v[240:241], v[92:93] neg_lo:[1,0,0] neg_hi:[1,0,0]\n\t" \
  "v_pk_fma_f32 v[246:247], v[182:183], v[242:243], v[94:95] neg_lo:[1,0,0] neg_hi:[1,0,0]\n\t" \
  "v_pk_fma_f32 v[248:249], v[180:181], v[236:237], v[124:125]\n\t" \
  "v_pk_fma_f32 v[250:251], v[182:183], v[238:239], v[126:127]\n\t" \
  "v_pk_fma_f32 v[236:237], v[176:177], v[236:237], v[244:245]\n\t" \
  "v_pk_fma_f32 v[238:239], v[178:179], v[238:239], v[246:247]\n\t" \
  "v_pk_fma_f32 v[240:241], v[176:177], v[240:241], v[248:249]\n\t" \
  "v_pk_fma_f32 v[242:243], v[178:179], v[242:243], v[250:251]\n\t" \
  "v_mov_b32 v253, %[tgA]\n\t" \
  "v_mov_b32 v255, %[tgB]\n\t" \
  "v_mov_b32_dpp v244, v236 row_shr:1 row_mask:0xf bank_mask:0xf bound_ctrl:0\n\t" \
  "v_mov_b32_dpp v245, v237 row_shr:1 row_mask:0xf bank_mask:0xf bound_ctrl:0\n\t" \
  "v_mov_b32_dpp v246, v238 row_shr:1 row_mask:0xf bank_mask:0xf bound_ctrl:0\n\t" \
  "v_mov_b32_dpp v247, v239 row_shr:1 row_mask:0xf bank_mask:0xf bound_ctrl:0\n\t" \
  "v_mov_b32_dpp v248, v240 row_shr:1 row_mask:0xf bank_mask:0xf bound_ctrl:0\n\t" \
  "v_mov_b32_dpp v249, v241 row_shr:1 row_mask:0xf bank_mask:0xf bound_ctrl:0\n\t" \
  "v_mov_b32_dpp v250, v242 row_shr:1 row_mask:0xf bank_mask:0xf bound_ctrl:0\n\t" \
  "v_mov_b32_dpp v251, v243 row_shr:1 row_mask:0xf bank_mask:0xf bound_ctrl:0\n\t" \
  "v_pk_fma_f32 v[236:237], v[192:193], v[244:245], v[236:237]\n\t" \
  "v_pk_fma_f32 v[238:239], v[194:195], v[246:247], v[238:239]\n\t" \
  "v_pk_fma_f32 v[240:241], v[192:193], v[248:249], v[240:241]\n\t" \
  "v_pk_fma_f32 v[242:243], v[194:195], v[250:251], v[242:243]\n\t" \
  "v_pk_fma_f32 v[236:237], v[196:197], v[248:249], v[236:237] neg_lo:[1,0,0] neg_hi:[1,0,0]\n\t" \
  "v_pk_fma_f32 v[238:239], v[198:199], v[250:251], v[238:239] neg_lo:[1,0,0] neg_hi:[1,0,0]\n\t" \
  "v_pk_fma_f32 v[240:241], v[196:197], v[244:245], v[240:241]\n\t" \
  "v_pk_fma_f32 v[242:243], v[198:199], v[246:247], v[242:243]\n\t" \
  "v_mov_b32_dpp v244, v236 row_shr:2 row_mask:0xf bank_mask:0xf bound_ctrl:0\n\t" \
  "v_mov_b32_dpp v245, v237 row_shr:2 row_mask:0xf bank_mask:0xf bound_ctrl:0\n\t" \
  "v_mov_b32_dpp v246, v238 row_shr:2 row_mask:0xf bank_mask:0xf bound_ctrl:0\n\t" \
  "v_mov_b32_dpp v247, v239 row_shr:2 row_mask:0xf bank_mask:0xf bound_ctrl:0\n\t" \
  "v_mov_b32_dpp v248, v240 row_shr:2 row_mask:0xf bank_mask:0xf bound_ctrl:0\n\t" \
  "v_mov_b32_dpp v249, v241 row_shr:2 row_mask:0xf bank_mask:0xf bound_ctrl:0\n\t" \
  "v_mov_b32_dpp v250, v242 row_shr:2 row_mask:0xf bank_mask:0xf bound_ctrl:0\n\t" \
  "v_mov_b32_dpp v251, v243 row_shr:2 row_mask:0xf bank_mask:0xf bound_ctrl:0\n\t" \
  "v_pk_fma_f32 v[236:237], v[200:201], v[244:245], v[236:237]\n\t" \
  "v_pk_fma_f32 v[238:239], v[202:203], v[246:247], v[238:239]\n\t" \
  "v_pk_fma_f32 v[240:241], v[200:201], v[248:249], v[240:241]\n\t" \
  "v_pk_fma_f32 v[242:243], v[202:203], v[250:251], v[242:243]\n\t" \
  "v_pk_fma_f32 v[236:237], v[204:205], v[248:249], v[236:237] neg_lo:[1,0,0] neg_hi:[1,0,0]\n\t" \
  "v_pk_fma_f32 v[238:239], v[206:207], v[250:251], v[238:239] neg_lo:[1,0,0] neg_hi:[1,0,0]\n\t" \
  "v_pk_fma_f32 v[240:241], v[204:205], v[244:245], v[240:241]\n\t" \
  "v_pk_fma_f32 v[242:243], v[206:207], v[246:247], v[242:243]\n\t" \
  "v_mov_b32_dpp v244, v236 row_shr:4 row_mask:0xf bank_mask:0xf bound_ctrl:0\n\t" \
  "v_mov_b32_dpp v245, v237 row_shr:4 row_mask:0xf bank_mask:0xf bound_ctrl:0\n\t" \
  "v_mov_b32_dpp v246, v238 row_shr:4 row_mask:0xf bank_mask:0xf bound_ctrl:0\n\t" \
  "v_mov_b32_dpp v247, v239 row_shr:4 row_mask:0xf bank_mask:0xf bound_ctrl:0\n\t" \
  "v_mov_b32_dpp v248, v240 row_shr:4 row_mask:0xf bank_mask:0xf bound_ctrl:0\n\t" \
  "v_mov_b32_dpp v249, v241 row_shr:4 row_mask:0xf bank_mask:0xf bound_ctrl:0\n\t" \
  "v_mov_b32_dpp v250, v242 row_shr:4 row_mask:0xf bank_mask:0xf bound_ctrl:0\n\t" \
  "v_mov_b32_dpp v251, v243 row_shr:4 row_mask:0xf bank_mask:0xf bound_ctrl:0\n\t" \
  "v_pk_fma_f32 v[236:237], v[208:209], v[244:245], v[236:237]\n\t" \
  "v_pk_fma_f32 v[238:239], v[210:211], v[246:247], v[238:239]\n\t" \
  "v_pk_fma_f32 v[240:241], v[208:209], v[248:249], v[240:241]\n\t" \
  "v_pk_fma_f32 v[242:243], v[210:211], v[250:251], v[242:243]\n\t" \
  "v_pk_fma_f32 v[236:237], v[212:213], v[248:249], v[236:237] neg_lo:[1,0,0] neg_hi:[1,0,0]\n\t" \
  "v_pk_fma_f32 v[238:239], v[214:215], v[250:251], v[238:239] neg_lo:[1,0,0] neg_hi:[1,0,0]\n\t" \
  "v_pk_fma_f32 v[240:241], v[212:213], v[244:245], v[240:241]\n\t" \
  "v_pk_fma_f32 v[242:243], v[214:215], v[246:247], v[242:243]\n\t" \
  "v_mov_b32_dpp v244, v236 row_shr:8 row_mask:0xf bank_mask:0xf bound_ctrl:0\n\t" \
  "v_mov_b32_dpp v245, v237 row_shr:8 row_mask:0xf bank_mask:0xf bound_ctrl:0\n\t" \
  "v_mov_b32_dpp v246, v238 row_shr:8 row_mask:0xf bank_mask:0xf bound_ctrl:0\n\t" \
  "v_mov_b32_dpp v247, v239 row_shr:8 row_mask:0xf bank_mask:0xf bound_ctrl:0\n\t" \
  "v_mov_b32_dpp v248, v240 row_shr:8 row_mask:0xf bank_mask:0xf bound_ctrl:0\n\t" \
  "v_mov_b32_dpp v249, v241 row_shr:8 row_mask:0xf bank_mask:0xf bound_ctrl:0\n\t" \
  "v_mov_b32_dpp v250, v242 row_shr:8 row_mask:0xf bank_mask:0xf bound_ctrl:0\n\t" \
  "v_mov_b32_dpp v251, v243 row_shr:8 row_mask:0xf bank_mask:0xf bound_ctrl:0\n\t" \
  "v_pk_fma_f32 v[236:237], v[216:217], v[244:245], v[236:237]\n\t" \
  "v_pk_fma_f32 v[238:239], v[218:219], v[246:247], v[238:239]\n\t" \
  "v_pk_fma_f32 v[240:241], v[216:217], v[248:249], v[240:241]\n\t" \
  "v_pk_fma_f32 v[242:243], v[218:219], v[250:251], v[242:243]\n\t" \
  "v_pk_fma_f32 v[236:237], v[220:221], v[248:249], v[236:237] neg_lo:[1,0,0] neg_hi:[1,0,0]\n\t" \
  "v_pk_fma_f32 v[238:239], v[222:223], v[250:251], v[238:239] neg_lo:[1,0,0] neg_hi:[1,0,0]\n\t" \
  "v_pk_fma_f32 v[240:241], v[220:221], v[244:245], v[240:241]\n\t" \
  "v_pk_fma_f32 v[242:243], v[222:223], v[246:247], v[242:243]\n\t" \
  "v_mov_b32_dpp v252, v236 row_newbcast:15 row_mask:0xf bank_mask:0x1\n\t" \
  "v_mov_b32_dpp v252, v237 row_newbcast:15 row_mask:0xf bank_mask:0x2\n\t" \
  "v_mov_b32_dpp v252, v238 row_newbcast:15 row_mask:0xf bank_mask:0x4\n\t" \
  "v_mov_b32_dpp v252, v239 row_newbcast:15 row_mask:0xf bank_mask:0x8\n\t" \
  "v_mov_b32_dpp v254, v240 row_newbcast:15 row_mask:0xf bank_mask:0x1\n\t" \
  "v_mov_b32_dpp v254, v241 row_newbcast:15 row_mask:0xf bank_mask:0x2\n\t" \
  "v_mov_b32_dpp v254, v242 row_newbcast:15 row_mask:0xf bank_mask:0x4\n\t" \
  "v_mov_b32_dpp v254, v243 row_newbcast:15 row_mask:0xf bank_mask:0x8\n\t" \
  "s_mov_b32 exec_lo, 0x11111111\n\t" \
  "s_mov_b32 exec_hi, 0x11111111\n\t" \
  "buffer_store_dwordx4 v[252:255], %[pubo], %[srd], %[sag] offen sc1\n\t" \
  "s_mov_b64 exec, -1\n\t" \
  "s_mov_b32 %[s1], 0\n\t" \
  "buffer_load_dwordx2 %[xe00], %[xo], %[srx], %[s1] offen offset:0\n\t" \
  "buffer_load_dwordx2 %[xe10], %[xo], %[srx], %[s1] offen offset:2048\n\t" \
  "s_mov_b32 %[s1], 16384\n\t" \
  "buffer_load_dwordx2 %[xe01], %[xo], %[srx], %[s1] offen offset:0\n\t" \
  "buffer_load_dwordx2 %[xe11], %[xo], %[srx], %[s1] offen offset:2048\n\t" \
  "s_mov_b32 %[s1], 32768\n\t" \
  "buffer_load_dwordx2 %[xe02], %[xo], %[srx], %[s1] offen offset:0\n\t" \
  "buffer_load_dwordx2 %[xe12], %[xo], %[srx], %[s1] offen offset:2048\n\t" \
  "s_mov_b32 %[s1], 49152\n\t" \
  "buffer_load_dwordx2 %[xe03], %[xo], %[srx], %[s1] offen offset:0\n\t" \
  "buffer_load_dwordx2 %[xe13], %[xo], %[srx], %[s1] offen offset:2048\n\t" \
  "s_waitcnt lgkmcnt(0)\n\t" \
  "v_mov_b32_dpp v244, v236 row_shr:1 row_mask:0xf bank_mask:0xf bound_ctrl:0\n\t" \
  "v_mov_b32_dpp v245, v237 row_shr:1 row_mask:0xf bank_mask:0xf bound_ctrl:0\n\t" \
  "v_mov_b32_dpp v246, v238 row_shr:1 row_mask:0xf bank_mask:0xf bound_ctrl:0\n\t" \
  "v_mov_b32_dpp v247, v239 row_shr:1 row_mask:0xf bank_mask:0xf bound_ctrl:0\n\t" \
  "v_mov_b32_dpp v248, v240 row_shr:1 row_mask:0xf bank_mask:0xf bound_ctrl:0\n\t" \
  "v_mov_b32_dpp v249, v241 row_shr:1 row_mask:0xf bank_mask:0xf bound_ctrl:0\n\t" \
  "v_mov_b32_dpp v250, v242 row_shr:1 row_mask:0xf bank_mask:0xf bound_ctrl:0\n\t" \
  "v_mov_b32_dpp v251, v243 row_shr:1 row_mask:0xf bank_mask:0xf bound_ctrl:0\n\t" \
  "v_pk_mul_f32 v[236:237], v[184:185], v[244:245]\n\t" \
  "v_pk_mul_f32 v[238:239], v[186:187], v[246:247]\n\t" \
  "v_pk_mul_f32 v[240:241], v[184:185], v[248:249]\n\t" \
  "v_pk_mul_f32 v[242:243], v[186:187], v[250:251]\n\t" \
  "v_pk_fma_f32 v[236:237], v[188:189], v[248:249], v[236:237] neg_lo:[1,0,0] neg_hi:[1,0,0]\n\t" \
  "v_pk_fma_f32 v[238:239], v[190:191], v[250:251], v[238:239] neg_lo:[1,0,0] neg_hi:[1,0,0]\n\t" \
  "v_pk_fma_f32 v[240:241], v[188:189], v[244:245], v[240:241]\n\t" \
  "v_pk_fma_f32 v[242:243], v[190:191], v[246:247], v[242:243]\n\t" \
  "v_pk_mul_f32 v[128:129], v[184:185], v[64:65]\n\t" \
  "v_pk_mul_f32 v[130:131], v[186:187], v[66:67]\n\t" \
  "v_pk_fma_f32 v[128:129], v[188:189], v[96:97], v[128:129] neg_lo:[1,0,0] neg_hi:[1,0,0]\n\t" \
  "v_pk_fma_f32 v[130:131], v[190:191], v[98:99], v[130:131] neg_lo:[1,0,0] neg_hi:[1,0,0]\n\t" \
  "v_pk_mul_f32 v[96:97], v[184:185], v[96:97]\n\t" \
  "v_pk_mul_f32 v[98:99], v[186:187], v[98:99]\n\t" \
  "v_pk_fma_f32 v[96:97], v[188:189], v[64:65], v[96:97] neg_lo:[1,0,1] neg_hi:[1,0,1]\n\t" \
  "v_pk_fma_f32 v[98:99], v[190:191], v[66:67], v[98:99] neg_lo:[1,0,1] neg_hi:[1,0,1]\n\t" \
  "v_pk_mul_f32 v[132:133], v[184:185], v[68:69]\n\t" \
  "v_pk_mul_f32 v[134:135], v[186:187], v[70:71]\n\t" \
  "v_pk_fma_f32 v[132:133], v[188:189], v[100:101], v[132:133] neg_lo:[1,0,0] neg_hi:[1,0,0]\n\t" \
  "v_pk_fma_f32 v[134:135], v[190:191], v[102:103], v[134:135] neg_lo:[1,0,0] neg_hi:[1,0,0]\n\t" \
  "v_pk_mul_f32 v[100:101], v[184:185], v[100:101]\n\t" \
  "v_pk_mul_f32 v[102:103], v[186:187], v[102:103]\n\t" \
  "v_pk_fma_f32 v[100:101], v[188:189], v[68:69], v[100:101] neg_lo:[1,0,1] neg_hi:[1,0,1]\n\t" \
  "v_pk_fma_f32 v[102:103], v[190:191], v[70:71], v[102:103] neg_lo:[1,0,1] neg_hi:[1,0,1]\n\t" \
  "v_pk_mul_f32 v[136:137], v[184:185], v[72:73]\n\t" \
  "v_pk_mul_f32 v[138:139], v[186:187], v[74:75]\n\t" \
  "v_pk_fma_f32 v[136:137], v[188:189], v[104:105], v[136:137] neg_lo:[1,0,0] neg_hi:[1,0,0]\n\t" \
  "v_pk_fma_f32 v[138:139], v[190:191], v[106:107], v[138:139] neg_lo:[1,0,0] neg_hi:[1,0,0]\n\t" \
  "v_pk_mul_f32 v[104:105], v[184:185], v[104:105]\n\t" \
  "v_pk_mul_f32 v[106:107], v[186:187], v[106:107]\n\t" \
  "v_pk_fma_f32 v[104:105], v[188:189], v[72:73], v[104:105] neg_lo:[1,0,1] neg_hi:[1,0,1]\n\t" \
  "v_pk_fma_f32 v[106:107], v[190:191], v[74:75], v[106:107] neg_lo:[1,0,1] neg_hi:[1,0,1]\n\t" \
  "v_pk_mul_f32 v[140:141], v[184:185], v[76:77]\n\t" \
  "v_pk_mul_f32 v[142:143], v[186:187], v[78:79]\n\t" \
  "v_pk_fma_f32 v[140:141], v[188:189], v[108:109], v[140:141] neg_lo:[1,0,0] neg_hi:[1,0,0]\n\t" \
  "v_pk_fma_f32 v[142:143], v[190:191], v[110:111], v[142:143] neg_lo:[1,0,0] neg_hi:[1,0,0]\n\t" \
  "v_pk_mul_f32 v[108:109], v[184:185], v[108:109]\n\t" \
  "v_pk_mul_f32 v[110:111], v[186:187], v[110:111]\n\t" \
  "v_pk_fma_f32 v[108:109], v[188:189], v[76:77], v[108:109] neg_lo:[1,0,1] neg_hi:[1,0,1]\n\t" \
  "v_pk_fma_f32 v[110:111], v[190:191], v[78:79], v[110:111] neg_lo:[1,0,1] neg_hi:[1,0,1]\n\t" \
  "v_pk_mul_f32 v[144:145], v[184:185], v[80:81]\n\t" \
  "v_pk_mul_f32 v[146:147], v[186:187], v[82:83]\n\t" \
  "v_pk_fma_f32 v[144:145], v[188:189], v[112:113], v[144:145] neg_lo:[1,0,0] neg_hi:[1,0,0]\n\t" \
  "v_pk_fma_f32 v[146:147], v[190:191], v[114:115], v[146:147] neg_lo:[1,0,0] neg_hi:[1,0,0]\n\t" \
  "v_pk_mul_f32 v[112:113], v[184:185], v[112:113]\n\t" \
  "v_pk_mul_f32 v[114:115], v[186:187], v[114:115]\n\t" \
  "v_pk_fma_f32 v[112:113], v[188:189], v[80:81], v[112:113] neg_lo:[1,0,1] neg_hi:[1,0,1]\n\t" \
  "v_pk_fma_f32 v[114:115], v[190:191], v[82:83], v[114:115] neg_lo:[1,0,1] neg_hi:[1,0,1]\n\t" \
  "v_pk_mul_f32 v[148:149], v[184:185], v[84:85]\n\t" \
  "v_pk_mul_f32 v[150:151], v[186:187], v[86:87]\n\t" \
  "v_pk_fma_f32 v[148:149], v[188:189], v[116:117], v[148:149] neg_lo:[1,0,0] neg_hi:[1,0,0]\n\t" \
  "v_pk_fma_f32 v[150:151], v[190:191], v[118:119], v[150:151] neg_lo:[1,0,0] neg_hi:[1,0,0]\n\t" \
  "v_pk_mul_f32 v[116:117], v[184:185], v[116:117]\n\t" \
  "v_pk_mul_f32 v[118:119], v[186:187], v[118:119]\n\t" \
  "v_pk_fma_f32 v[116:117], v[188:189], v[84:85], v[116:117] neg_lo:[1,0,1] neg_hi:[1,0,1]\n\t" \
  "v_pk_fma_f32 v[118:119], v[190:191], v[86:87], v[118:119] neg_lo:[1,0,1] neg_hi:[1,0,1]\n\t" \
  "v_pk_mul_f32 v[152:153], v[184:185], v[88:89]\n\t" \
  "v_pk_mul_f32 v[154:155], v[186:187], v[90:91]\n\t" \
  "v_pk_fma_f32 v[152:153], v[188:189], v[120:121], v[152:153] neg_lo:[1,0,0] neg_hi:[1,0,0]\n\t" \
  "v_pk_fma_f32 v[154:155], v[190:191], v[122:123], v[154:155] neg_lo:[1,0,0] neg_hi:[1,0,0]\n\t" \
  "v_pk_mul_f32 v[120:121], v[184:185], v[120:121]\n\t" \
  "v_pk_mul_f32 v[122:123], v[186:187], v[122:123]\n\t" \
  "v_pk_fma_f32 v[120:121], v[188:189], v[88:89], v[120:121] neg_lo:[1,0,1] neg_hi:[1,0,1]\n\t" \
  "v_pk_fma_f32 v[122:123], v[190:191], v[90:91], v[122:123] neg_lo:[1,0,1] neg_hi:[1,0,1]\n\t" \
  "v_pk_mul_f32 v[156:157], v[184:185], v[92:93]\n\t" \
  "v_pk_mul_f32 v[158:159], v[186:187], v[94:95]\n\t" \
  "v_pk_fma_f32 v[156:157], v[188:189], v[124:125], v[156:157] neg_lo:[1,0,0] neg_hi:[1,0,0]\n\t" \
  "v_pk_fma_f32 v[158:159], v[190:191], v[126:127], v[158:159] neg_lo:[1,0,0] neg_hi:[1,0,0]\n\t" \
  "v_pk_mul_f32 v[124:125], v[184:185], v[124:125]\n\t" \
  "v_pk_mul_f32 v[126:127], v[186:187], v[126:127]\n\t" \
  "v_pk_fma_f32 v[124:125], v[188:189], v[92:93], v[124:125] neg_lo:[1,0,1] neg_hi:[1,0,1]\n\t" \
  "v_pk_fma_f32 v[126:127], v[190:191], v[94:95], v[126:127] neg_lo:[1,0,1] neg_hi:[1,0,1]\n\t" \
  "v_mov_b32 v160, 0\n\t" \
  "v_mov_b32 v161, %[tgA]\n\t" \
  "v_mov_b32 v162, 0\n\t" \
  "v_mov_b32 v163, %[tgB]\n\t" \
  "v_mov_b32 v164, 0\n\t" \
  "v_mov_b32 v165, %[tgA]\n\t" \
  "v_mov_b32 v166, 0\n\t" \
  "v_mov_b32 v167, %[tgB]\n\t" \
  "v_mov_b32 v168, 0\n\t" \
  "v_mov_b32 v169, %[tgA]\n\t" \
  "v_mov_b32 v170, 0\n\t" \
  "v_mov_b32 v171, %[tgB]\n\t" \
  "v_mov_b32 v172, 0\n\t" \
  "v_mov_b32 v173, %[tgA]\n\t" \
  "v_mov_b32 v174, 0\n\t" \
  "v_mov_b32 v175, %[tgB]\n\t" \
  "s_mov_b64 exec, %[em0]\n\t" \
  "s_cbranch_execz Lpa_%=\n\t" \
  "buffer_load_dwordx4 v[160:163], %[plo], %[srd], %[sl0] offen sc1\n\t" \
  "s_mov_b64 exec, %[em1]\n\t" \
  "s_cbranch_execz Lpa_%=\n\t" \
  "buffer_load_dwordx4 v[164:167], %[plo], %[srd], %[sl1] offen sc1\n\t" \
  "s_mov_b64 exec, %[em2]\n\t" \
  "s_cbranch_execz Lpa_%=\n\t" \
  "buffer_load_dwordx4 v[168:171], %[plo], %[srd], %[sl2] offen sc1\n\t" \
  "s_mov_b64 exec, %[em3]\n\t" \
  "s_cbranch_execz Lpa_%=\n\t" \
  "buffer_load_dwordx4 v[172:175], %[plo], %[srd], %[sl3] offen sc1\n\t" \
  "Lpa_%=:\n\t" \
  "s_mov_b32 exec_lo, 0xaaaaaaaa\n\t" \
  "s_mov_b32 exec_hi, 0xaaaaaaaa\n\t" \
  "v_pk_mul_f32 v[244:245], v[196:197], v[188:189]\n\t" \
  "v_pk_mul_f32 v[246:247], v[198:199], v[190:191]\n\t" \
  "v_pk_mul_f32 v[248:249], v[196:197], v[184:185]\n\t" \
  "v_pk_mul_f32 v[250:251], v[198:199], v[186:187]\n\t" \
  "v_pk_fma_f32 v[184:185], v[192:193], v[184:185], v[244:245] neg_lo:[0,0,1] neg_hi:[0,0,1]\n\t" \
  "v_pk_fma_f32 v[186:187], v[194:195], v[186:187], v[246:247] neg_lo:[0,0,1] neg_hi:[0,0,1]\n\t" \
  "v_pk_fma_f32 v[188:189], v[192:193], v[188:189], v[248:249]\n\t" \
  "v_pk_fma_f32 v[190:191], v[194:195], v[190:191], v[250:251]\n\t" \
  "s_mov_b32 exec_lo, 0xcccccccc\n\t" \
  "s_mov_b32 exec_hi, 0xcccccccc\n\t" \
  "v_pk_mul_f32 v[244:245], v[204:205], v[188:189]\n\t" \
  "v_pk_mul_f32 v[246:247], v[206:207], v[190:191]\n\t" \
  "v_pk_mul_f32 v[248:249], v[204:205], v[184:185]\n\t" \
  "v_pk_mul_f32 v[250:251], v[206:207], v[186:187]\n\t" \
  "v_pk_fma_f32 v[184:185], v[200:201], v[184:185], v[244:245] neg_lo:[0,0,1] neg_hi:[0,0,1]\n\t" \
  "v_pk_fma_f32 v[186:187], v[202:203], v[186:187], v[246:247] neg_lo:[0,0,1] neg_hi:[0,0,1]\n\t" \
  "v_pk_fma_f32 v[188:189], v[200:201], v[188:189], v[248:249]\n\t" \
  "v_pk_fma_f32 v[190:191], v[202:203], v[190:191], v[250:251]\n\t" \
  "s_mov_b32 exec_lo, 0xf0f0f0f0\n\t" \
  "s_mov_b32 exec_hi, 0xf0f0f0f0\n\t" \
  "v_pk_mul_f32 v[244:245], v[212:213], v[188:189]\n\t" \
  "v_pk_mul_f32 v[246:247], v[214:215], v[190:191]\n\t" \
  "v_pk_mul_f32 v[248:249], v[212:213], v[184:185]\n\t" \
  "v_pk_mul_f32 v[250:251], v[214:215], v[186:187]\n\t" \
  "v_pk_fma_f32 v[184:185], v[208:209], v[184:185], v[244:245] neg_lo:[0,0,1] neg_hi:[0,0,1]\n\t" \
  "v_pk_fma_f32 v[186:187], v[210:211], v[186:187], v[246:247] neg_lo:[0,0,1] neg_hi:[0,0,1]\n\t" \
  "v_pk_fma_f32 v[188:189], v[208:209], v[188:189], v[248:249]\n\t" \
  "v_pk_fma_f32 v[190:191], v[210:211], v[190:191], v[250:251]\n\t" \
  "s_mov_b32 exec_lo, 0xff00ff00\n\t" \
  "s_mov_b32 exec_hi, 0xff00ff00\n\t" \
  "v_pk_mul_f32 v[244:245], v[220:221], v[188:189]\n\t" \
  "v_pk_mul_f32 v[246:247], v[222:223], v[190:191]\n\t" \
  "v_pk_mul_f32 v[248:249], v[220:221], v[184:185]\n\t" \
  "v_pk_mul_f32 v[250:251], v[222:223], v[186:187]\n\t" \
  "v_pk_fma_f32 v[184:185], v[216:217], v[184:185], v[244:245] neg_lo:[0,0,1] neg_hi:[0,0,1]\n\t" \
  "v_pk_fma_f32 v[186:187], v[218:219], v[186:187], v[246:247] neg_lo:[0,0,1] neg_hi:[0,0,1]\n\t" \
  "v_pk_fma_f32 v[188:189], v[216:217], v[188:189], v[248:249]\n\t" \
  "v_pk_fma_f32 v[190:191], v[218:219], v[190:191], v[250:251]\n\t" \
  "Lq_%=:\n\t" \
  "s_mov_b64 exec, -1\n\t" \
  "s_waitcnt vmcnt(0)\n\t" \
  "v_cmpx_eq_u32 vcc, %[tgA], v161\n\t" \
  "v_cmpx_eq_u32 vcc, %[tgB], v163\n\t" \
  "v_cmpx_eq_u32 vcc, %[tgA], v165\n\t" \
  "v_cmpx_eq_u32 vcc, %[tgB], v167\n\t" \
  "v_cmpx_eq_u32 vcc, %[tgA], v169\n\t" \
  "v_cmpx_eq_u32 vcc, %[tgB], v171\n\t" \
  "v_cmpx_eq_u32 vcc, %[tgA], v173\n\t" \
  "v_cmpx_eq_u32 vcc, %[tgB], v175\n\t" \
  "s_cmp_eq_u64 exec, -1\n\t" \
  "s_mov_b64 exec, -1\n\t" \
  "s_cbranch_scc1 Lq2_%=\n\t" \
  "s_sub_i32 %[bud], %[bud], 16\n\t" \
  "s_cmp_lt_i32 %[bud], 0\n\t" \
  "s_cbranch_scc1 Lq2_%=\n\t" \
  "s_sleep 4\n\t" \
  "s_mov_b64 exec, %[em0]\n\t" \
  "s_cbranch_execz Lpb_%=\n\t" \
  "buffer_load_dwordx4 v[160:163], %[plo], %[srd], %[sl0] offen sc1\n\t" \
  "s_mov_b64 exec, %[em1]\n\t" \
  "s_cbranch_execz Lpb_%=\n\t" \
  "buffer_load_dwordx4 v[164:167], %[plo], %[srd], %[sl1] offen sc1\n\t" \
  "s_mov_b64 exec, %[em2]\n\t" \
  "s_cbranch_execz Lpb_%=\n\t" \
  "buffer_load_dwordx4 v[168:171], %[plo], %[srd], %[sl2] offen sc1\n\t" \
  "s_mov_b64 exec, %[em3]\n\t" \
  "s_cbranch_execz Lpb_%=\n\t" \
  "buffer_load_dwordx4 v[172:175], %[plo], %[srd], %[sl3] offen sc1\n\t" \
  "Lpb_%=:\n\t" \
  "s_branch Lq_%=\n\t" \
  "Lq2_%=:\n\t" \
  "s_mov_b32 %[scn], %[mcar]\n\t" \
  "s_mov_b32 m0, %[scn]\n\t" \
  "s_mov_b64 exec, 1\n\t" \
  "buffer_load_dword %[l16], %[srz], 0 offen lds\n\t" \
  "s_mov_b64 exec, -1\n\t" \
  "v_fma_f32 v244, -v231, v174, v168\n\t" \
  "v_fma_f32 v245, v231, v172, v170\n\t" \
  "v_fma_f32 v252, v230, v172, v244\n\t" \
  "v_fma_f32 v253, v230, v174, v245\n\t" \
  "v_fma_f32 v244, -v231, v253, v164\n\t" \
  "v_fma_f32 v245, v231, v252, v166\n\t" \
  "v_fma_f32 v252, v230, v252, v244\n\t" \
  "v_fma_f32 v253, v230, v253, v245\n\t" \
  "v_fma_f32 v244, -v231, v253, v160\n\t" \
  "v_fma_f32 v245, v231, v252, v162\n\t" \
  "v_fma_f32 v252, v230, v252, v244\n\t" \
  "v_fma_f32 v253, v230, v253, v245\n\t" \
  "v_mul_f32 v244, v233, v253\n\t" \
  "v_mul_f32 v245, v233, v252\n\t" \
  "v_fma_f32 v254, v232, v252, -v244\n\t" \
  "v_fma_f32 v255, v232, v253, v245\n\t" \
  "v_fma_f32 v252, v232, v252, -v244\n\t" \
  "v_fma_f32 v253, v232, v253, v245\n\t" \
  "s_nop 1\n\t" \
  "v_permlane16_swap_b32 v252, v254\n\t" \
  "v_permlane16_swap_b32 v253, v255\n\t" \
  "s_nop 0\n\t" \
  "v_add_f32 v244, v252, v254\n\t" \
  "v_add_f32 v245, v253, v255\n\t" \
  "v_add_f32 v252, v252, v254\n\t" \
  "v_add_f32 v253, v253, v255\n\t" \
  "v_mov_b32 v254, v244\n\t" \
  "v_mov_b32 v255, v245\n\t" \
  "s_nop 1\n\t" \
  "v_permlane32_swap_b32 v252, v254\n\t" \
  "v_permlane32_swap_b32 v253, v255\n\t" \
  "s_nop 0\n\t" \
  "v_add_f32 v244, v252, v254\n\t" \
  "v_add_f32 v245, v253, v255\n\t" \
  "v_add_f32 v252, v252, v254\n\t" \
  "v_add_f32 v253, v253, v255\n\t" \
  "v_mov_b32 v254, v244\n\t" \
  "v_mov_b32 v255, v245\n\t" \
  "v_mov_b32_dpp v254, v252 row_ror:12 row_mask:0x2 bank_mask:0xf\n\t" \
  "v_mov_b32_dpp v255, v253 row_ror:12 row_mask:0x2 bank_mask:0xf\n\t" \
  "v_mov_b32_dpp v254, v252 row_ror:8 row_mask:0x4 bank_mask:0xf\n\t" \
  "v_mov_b32_dpp v255, v253 row_ror:8 row_mask:0x4 bank_mask:0xf\n\t" \
  "v_mov_b32_dpp v254, v252 row_ror:4 row_mask:0x8 bank_mask:0xf\n\t" \
  "v_mov_b32_dpp v255, v253 row_ror:4 row_mask:0x8 bank_mask:0xf\n\t" \
  "s_nop 1\n\t" \
  "v_mov_b32_dpp v244, v254 row_newbcast:0 row_mask:0xf bank_mask:0xf\n\t" \
  "v_mov_b32_dpp v245, v254 row_newbcast:1 row_mask:0xf bank_mask:0xf\n\t" \
  "v_mov_b32_dpp v246, v254 row_newbcast:2 row_mask:0xf bank_mask:0xf\n\t" \
  "v_mov_b32_dpp v247, v254 row_newbcast:3 row_mask:0xf bank_mask:0xf\n\t" \
  "v_mov_b32_dpp v248, v255 row_newbcast:0 row_mask:0xf bank_mask:0xf\n\t" \
  "v_mov_b32_dpp v249, v255 row_newbcast:1 row_mask:0xf bank_mask:0xf\n\t" \
  "v_mov_b32_dpp v250, v255 row_newbcast:2 row_mask:0xf bank_mask:0xf\n\t" \
  "v_mov_b32_dpp v251, v255 row_newbcast:3 row_mask:0xf bank_mask:0xf\n\t" \
  "v_pk_fma_f32 v[236:237], v[184:185], v[244:245], v[236:237]\n\t" \
  "v_pk_fma_f32 v[238:239], v[186:187], v[246:247], v[238:239]\n\t" \
  "v_pk_fma_f32 v[240:241], v[184:185], v[248:249], v[240:241]\n\t" \
  "v_pk_fma_f32 v[242:243], v[186:187], v[250:251], v[242:243]\n\t" \
  "v_pk_fma_f32 v[236:237], v[188:189], v[248:249], v[236:237] neg_lo:[1,0,0] neg_hi:[1,0,0]\n\t" \
  "v_pk_fma_f32 v[238:239], v[190:191], v[250:251], v[238:239] neg_lo:[1,0,0] neg_hi:[1,0,0]\n\t" \
  "v_pk_fma_f32 v[240:241], v[188:189], v[244:245], v[240:241]\n\t" \
  "v_pk_fma_f32 v[242:243], v[190:191], v[246:247], v[242:243]\n\t" \
  "v_pk_fma_f32 v[128:129], v[180:181], v[240:241], v[128:129] neg_lo:[1,0,0] neg_hi:[1,0,0]\n\t" \
  "v_pk_fma_f32 v[130:131], v[182:183], v[242:243], v[130:131] neg_lo:[1,0,0] neg_hi:[1,0,0]\n\t" \
  "v_pk_fma_f32 v[96:97], v[180:181], v[236:237], v[96:97] neg_lo:[1,0,0] neg_hi:[1,0,0]\n\t" \
  "v_pk_fma_f32 v[98:99], v[182:183], v[238:239], v[98:99] neg_lo:[1,0,0] neg_hi:[1,0,0]\n\t" \
  "v_pk_fma_f32 v[128:129], v[176:177], v[236:237], v[128:129]\n\t" \
  "v_pk_fma_f32 v[130:131], v[178:179], v[238:239], v[130:131]\n\t" \
  "v_pk_fma_f32 v[96:97], v[176:177], v[240:241], v[96:97] neg_lo:[1,0,0] neg_hi:[1,0,0]\n\t" \
  "v_pk_fma_f32 v[98:99], v[178:179], v[242:243], v[98:99] neg_lo:[1,0,0] neg_hi:[1,0,0]\n\t" \
  "v_cvt_pk_bf16_f32 v160, v128, v129\n\t" \
  "v_cvt_pk_bf16_f32 v161, v130, v131\n\t" \
  "v_cvt_pk_bf16_f32 v162, v96, v97\n\t" \
  "v_cvt_pk_bf16_f32 v163, v98, v99\n\t" \
  "v_pk_fma_f32 v[132:133], v[180:181], v[96:97], v[132:133]\n\t" \
  "v_pk_fma_f32 v[134:135], v[182:183], v[98:99], v[134:135]\n\t" \
  "v_permlane16_swap_b32 v160, v162\n\t" \
  "v_permlane16_swap_b32 v161, v163\n\t" \
  "buffer_store_dwordx4 v[160:163], %[l16], %[srd], %[sh] offen offset:0 sc1\n\t" \
  "v_pk_fma_f32 v[100:101], v[180:181], v[128:129], v[100:101] neg_lo:[1,0,0] neg_hi:[1,0,0]\n\t" \
  "v_pk_fma_f32 v[102:103], v[182:183], v[130:131], v[102:103] neg_lo:[1,0,0] neg_hi:[1,0,0]\n\t" \
  "v_pk_fma_f32 v[132:133], v[176:177], v[128:129], v[132:133]\n\t" \
  "v_pk_fma_f32 v[134:135], v[178:179], v[130:131], v[134:135]\n\t" \
  "v_pk_fma_f32 v[100:101], v[176:177], v[96:97], v[100:101]\n\t" \
  "v_pk_fma_f32 v[102:103], v[178:179], v[98:99], v[102:103]\n\t" \
  "v_cvt_pk_bf16_f32 v164, v132, v133\n\t" \
  "v_cvt_pk_bf16_f32 v165, v134, v135\n\t" \
  "v_cvt_pk_bf16_f32 v166, v100, v101\n\t" \
  "v_cvt_pk_bf16_f32 v167, v102, v103\n\t" \
  "v_pk_fma_f32 v[136:137], v[180:181], v[100:101], v[136:137]\n\t" \
  "v_pk_fma_f32 v[138:139], v[182:183], v[102:103], v[138:139]\n\t" \
  "v_permlane16_swap_b32 v164, v166\n\t" \
  "v_permlane16_swap_b32 v165, v167\n\t" \
  "buffer_store_dwordx4 v[164:167], %[l16], %[srd], %[sh] offen offset:1024 sc1\n\t" \
  "v_pk_fma_f32 v[104:105], v[180:181], v[132:133], v[104:105] neg_lo:[1,0,0] neg_hi:[1,0,0]\n\t" \
  "v_pk_fma_f32 v[106:107], v[182:183], v[134:135], v[106:107] neg_lo:[1,0,0] neg_hi:[1,0,0]\n\t" \
  "v_pk_fma_f32 v[136:137], v[176:177], v[132:133], v[136:137]\n\t" \
  "v_pk_fma_f32 v[138:139], v[178:179], v[134:135], v[138:139]\n\t" \
  "v_pk_fma_f32 v[104:105], v[176:177], v[100:101], v[104:105]\n\t" \
  "v_pk_fma_f32 v[106:107], v[178:179], v[102:103], v[106:107]\n\t" \
  "v_cvt_pk_bf16_f32 v168, v136, v137\n\t" \
  "v_cvt_pk_bf16_f32 v169, v138, v139\n\t" \
  "v_cvt_pk_bf16_f32 v170, v104, v105\n\t" \
  "v_cvt_pk_bf16_f32 v171, v106, v107\n\t" \
  "v_pk_fma_f32 v[140:141], v[180:181], v[104:105], v[140:141]\n\t" \
  "v_pk_fma_f32 v[142:143], v[182:183], v[106:107], v[142:143]\n\t" \
  "v_permlane16_swap_b32 v168, v170\n\t" \
  "v_permlane16_swap_b32 v169, v171\n\t" \
  "buffer_store_dwordx4 v[168:171], %[l16], %[srd], %[sh] offen offset:2048 sc1\n\t" \
  "v_pk_fma_f32 v[108:109], v[180:181], v[136:137], v[108:109] neg_lo:[1,0,0] neg_hi:[1,0,0]\n\t" \
  "v_pk_fma_f32 v[110:111], v[182:183], v[138:139], v[110:111] neg_lo:[1,0,0] neg_hi:[1,0,0]\n\t" \
  "v_pk_fma_f32 v[140:141], v[176:177], v[136:137], v[140:141]\n\t" \
  "v_pk_fma_f32 v[142:143], v[178:179], v[138:139], v[142:143]\n\t" \
  "v_pk_fma_f32 v[108:109], v[176:177], v[104:105], v[108:109]\n\t" \
  "v_pk_fma_f32 v[110:111], v[178:179], v[106:107], v[110:111]\n\t" \
  "v_cvt_pk_bf16_f32 v172, v140, v141\n\t" \
  "v_cvt_pk_bf16_f32 v173, v142, v143\n\t" \
  "v_cvt_pk_bf16_f32 v174, v108, v109\n\t" \
  "v_cvt_pk_bf16_f32 v175, v110, v111\n\t" \
  "v_pk_fma_f32 v[144:145], v[180:181], v[108:109], v[144:145]\n\t" \
  "v_pk_fma_f32 v[146:147], v[182:183], v[110:111], v[146:147]\n\t" \
  "v_permlane16_swap_b32 v172, v174\n\t" \
  "v_permlane16_swap_b32 v173, v175\n\t" \
  "buffer_store_dwordx4 v[172:175], %[l16], %[srd], %[sh] offen offset:3072 sc1\n\t" \
  "s_add_u32 %[sh], %[sh], 0x1000\n\t" \
  "v_pk_fma_f32 v[112:113], v[180:181], v[140:141], v[112:113] neg_lo:[1,0,0] neg_hi:[1,0,0]\n\t" \
  "v_pk_fma_f32 v[114:115], v[182:183], v[142:143], v[114:115] neg_lo:[1,0,0] neg_hi:[1,0,0]\n\t" \
  "v_pk_fma_f32 v[144:145], v[176:177], v[140:141], v[144:145]\n\t" \
  "v_pk_fma_f32 v[146:147], v[178:179], v[142:143], v[146:147]\n\t" \
  "v_pk_fma_f32 v[112:113], v[176:177], v[108:109], v[112:113]\n\t" \
  "v_pk_fma_f32 v[114:115], v[178:179], v[110:111], v[114:115]\n\t" \
  "v_cvt_pk_bf16_f32 v192, v144, v145\n\t" \
  "v_cvt_pk_bf16_f32 v193, v146, v147\n\t" \
  "v_cvt_pk_bf16_f32 v194, v112, v113\n\t" \
  "v_cvt_pk_bf16_f32 v195, v114, v115\n\t" \
  "v_pk_fma_f32 v[148:149], v[180:181], v[112:113], v[148:149]\n\t" \
  "v_pk_fma_f32 v[150:151], v[182:183], v[114:115], v[150:151]\n\t" \
  "v_permlane16_swap_b32 v192, v194\n\t" \
  "v_permlane16_swap_b32 v193, v195\n\t" \
  "buffer_store_dwordx4 v[192:195], %[l16], %[srd], %[sh] offen offset:0 sc1\n\t" \
  "v_pk_fma_f32 v[116:117], v[180:181], v[144:145], v[116:117] neg_lo:[1,0,0] neg_hi:[1,0,0]\n\t" \
  "v_pk_fma_f32 v[118:119], v[182:183], v[146:147], v[118:119] neg_lo:[1,0,0] neg_hi:[1,0,0]\n\t" \
  "v_pk_fma_f32 v[148:149], v[176:177], v[144:145], v[148:149]\n\t" \
  "v_pk_fma_f32 v[150:151], v[178:179], v[146:147], v[150:151]\n\t" \
  "v_pk_fma_f32 v[116:117], v[176:177], v[112:113], v[116:117]\n\t" \
  "v_pk_fma_f32 v[118:119], v[178:179], v[114:115], v[118:119]\n\t" \
  "v_cvt_pk_bf16_f32 v196, v148, v149\n\t" \
  "v_cvt_pk_bf16_f32 v197, v150, v151\n\t" \
  "v_cvt_pk_bf16_f32 v198, v116, v117\n\t" \
  "v_cvt_pk_bf16_f32 v199, v118, v119\n\t" \
  "v_pk_fma_f32 v[152:153], v[180:181], v[116:117], v[152:153]\n\t" \
  "v_pk_fma_f32 v[154:155], v[182:183], v[118:119], v[154:155]\n\t" \
  "v_permlane16_swap_b32 v196, v198\n\t" \
  "v_permlane16_swap_b32 v197, v199\n\t" \
  "buffer_store_dwordx4 v[196:199], %[l16], %[srd], %[sh] offen offset:1024 sc1\n\t" \
  "v_pk_fma_f32 v[120:121], v[180:181], v[148:149], v[120:121] neg_lo:[1,0,0] neg_hi:[1,0,0]\n\t" \
  "v_pk_fma_f32 v[122:123], v[182:183], v[150:151], v[122:123] neg_lo:[1,0,0] neg_hi:[1,0,0]\n\t" \
  "v_pk_fma_f32 v[152:153], v[176:177], v[148:149], v[152:153]\n\t" \
  "v_pk_fma_f32 v[154:155], v[178:179], v[150:151], v[154:155]\n\t" \
  "v_pk_fma_f32 v[120:121], v[176:177], v[116:117], v[120:121]\n\t" \
  "v_pk_fma_f32 v[122:123], v[178:179], v[118:119], v[122:123]\n\t" \
  "v_cvt_pk_bf16_f32 v200, v152, v153\n\t" \
  "v_cvt_pk_bf16_f32 v201, v154, v155\n\t" \
  "v_cvt_pk_bf16_f32 v202, v120, v121\n\t" \
  "v_cvt_pk_bf16_f32 v203, v122, v123\n\t" \
  "v_pk_fma_f32 v[156:157], v[180:181], v[120:121], v[156:157]\n\t" \
  "v_pk_fma_f32 v[158:159], v[182:183], v[122:123], v[158:159]\n\t" \
  "v_permlane16_swap_b32 v200, v202\n\t" \
  "v_permlane16_swap_b32 v201, v203\n\t" \
  "buffer_store_dwordx4 v[200:203], %[l16], %[srd], %[sh] offen offset:2048 sc1\n\t" \
  "v_pk_fma_f32 v[124:125], v[180:181], v[152:153], v[124:125] neg_lo:[1,0,0] neg_hi:[1,0,0]\n\t" \
  "v_pk_fma_f32 v[126:127], v[182:183], v[154:155], v[126:127] neg_lo:[1,0,0] neg_hi:[1,0,0]\n\t" \
  "v_pk_fma_f32 v[156:157], v[176:177], v[152:153], v[156:157]\n\t" \
  "v_pk_fma_f32 v[158:159], v[178:179], v[154:155], v[158:159]\n\t" \
  "v_pk_fma_f32 v[124:125], v[176:177], v[120:121], v[124:125]\n\t" \
  "v_pk_fma_f32 v[126:127], v[178:179], v[122:123], v[126:127]\n\t" \
  "v_cvt_pk_bf16_f32 v204, v156, v157\n\t" \
  "v_cvt_pk_bf16_f32 v205, v158, v159\n\t" \
  "v_cvt_pk_bf16_f32 v206, v124, v125\n\t" \
  "v_cvt_pk_bf16_f32 v207, v126, v127\n\t" \
  "s_nop 1\n\t" \
  "v_permlane16_swap_b32 v204, v206\n\t" \
  "v_permlane16_swap_b32 v205, v207\n\t" \
  "buffer_store_dwordx4 v[204:207], %[l16], %[srd], %[sh] offen offset:3072 sc1\n\t" \
  "s_add_u32 %[scn], %[scn], 8\n\t" \
  "s_mov_b32 m0, %[scn]\n\t" \
  "s_mov_b64 exec, 1\n\t" \
  "buffer_load_dword %[l16], %[srz], 0 offen lds\n\t" \
  "s_mov_b64 exec, -1\n\t" \
  "v_mov_b32 v252, %[tgA]\n\t" \
  "v_mov_b32 v253, %[tgB]\n\t" \
  "s_waitcnt vmcnt(0)\n\t" \
  "buffer_store_dwordx2 v[252:253], %[fso], %[srd], 0 offen sc1\n\t" \
  "s_branch Lend_%=\n\t" \
  "Lsp0_%=:\n\t" \
  "ds_read_b64 v[224:225], %[fl]\n\t" \
  "s_sub_i32 %[bud], %[bud], 1\n\t" \
  "s_cmp_lt_i32 %[bud], 0\n\t" \
  "s_cbranch_scc1 Lb0_%=\n\t" \
  "s_waitcnt lgkmcnt(0)\n\t" \
  "v_or_b32 v224, v224, v225\n\t" \
  "s_nop 0\n\t" \
  "v_readfirstlane_b32 %[s0], v224\n\t" \
  "s_cmp_eq_u32 %[s0], 0\n\t" \
  "s_cbranch_scc0 Lsp0_%=\n\t" \
  "s_branch Lb0_%=\n\t" \
  "Lsp1_%=:\n\t" \
  "ds_read_b64 v[224:225], %[fl]\n\t" \
  "s_sub_i32 %[bud], %[bud], 1\n\t" \
  "s_cmp_lt_i32 %[bud], 0\n\t" \
  "s_cbranch_scc1 Lb1_%=\n\t" \
  "s_waitcnt lgkmcnt(0)\n\t" \
  "v_or_b32 v224, v224, v225\n\t" \
  "s_nop 0\n\t" \
  "v_readfirstlane_b32 %[s0], v224\n\t" \
  "s_cmp_eq_u32 %[s0], 0\n\t" \
  "s_cbranch_scc0 Lsp1_%=\n\t" \
  "s_branch Lb1_%=\n\t" \
  "Lsp2_%=:\n\t" \
  "ds_read_b64 v[224:225], %[fl]\n\t" \
  "s_sub_i32 %[bud], %[bud], 1\n\t" \
  "s_cmp_lt_i32 %[bud], 0\n\t" \
  "s_cbranch_scc1 Lb2_%=\n\t" \
  "s_waitcnt lgkmcnt(0)\n\t" \
  "v_or_b32 v224, v224, v225\n\t" \
  "s_nop 0\n\t" \
  "v_readfirstlane_b32 %[s0], v224\n\t" \
  "s_cmp_eq_u32 %[s0], 0\n\t" \
  "s_cbranch_scc0 Lsp2_%=\n\t" \
  "s_branch Lb2_%=\n\t" \
  "Lend_%=:\n\t" \
  "s_nop 7\n\t" \
  "s_nop 7\n\t"
#define ASM_MB \
  "s_nop 4\n\t" \
  "s_mov_b32 %[sg], 0\n\t" \
  "s_mov_b32 %[sa], 0\n\t" \
  "s_branch Lc0_%=\n\t" \
  "Lc0b_%=:\n\t" \
  "ds_read_b128 v[128:131], %[pc0] offset:0\n\t" \
  "ds_read_b128 v[132:135], %[pc0] offset:2048\n\t" \
  "ds_read_b128 v[136:139], %[pc0] offset:4096\n\t" \
  "ds_read_b128 v[140:143], %[pc0] offset:6144\n\t" \
  "ds_read_b128 v[144:147], %[pc1] offset:0\n\t" \
  "ds_read_b128 v[148:151], %[pc1] offset:2048\n\t" \
  "ds_read_b128 v[152:155], %[pc1] offset:4096\n\t" \
  "ds_read_b128 v[156:159], %[pc1] offset:6144\n\t" \
  "v_add_u32 %[fl], 8, %[fl]\n\t" \
  "s_cmp_eq_u32 %[w], 0\n\t" \
  "s_cbranch_scc0 Lw_%=\n\t" \
  "Lf_%=:\n\t" \
  "buffer_load_dwordx2 v[196:197], %[flo], %[srd], %[sf] offen sc1\n\t" \
  "s_sub_i32 %[bud], %[bud], 16\n\t" \
  "s_cmp_lt_i32 %[bud], 0\n\t" \
  "s_cbranch_scc1 Lf2_%=\n\t" \
  "s_waitcnt vmcnt(0)\n\t" \
  "v_cmp_ne_u32 vcc, %[tgA], v196\n\t" \
  "v_cmp_ne_u32 %[sp], %[tgB], v197\n\t" \
  "s_or_b64 vcc, vcc, %[sp]\n\t" \
  "s_cbranch_scc0 Lf2_%=\n\t" \
  "s_sleep 4\n\t" \
  "s_branch Lf_%=\n\t" \
  "Lf2_%=:\n\t" \
  "v_mov_b32 v192, 0\n\t" \
  "v_mov_b32 v193, 0\n\t" \
  "ds_write_b64 %[hr], v[192:193]\n\t" \
  "Lf3_%=:\n\t" \
  "buffer_load_dwordx4 v[64:67], %[vo], %[srd], %[sb] offen offset:0 sc1\n\t" \
  "buffer_load_dwordx4 v[68:71], %[vo], %[srd], %[sb] offen offset:1024 sc1\n\t" \
  "s_add_u32 %[sb], %[sb], 0x2000\n\t" \
  "s_and_b32 %[sb], %[sb], 0x7ffff\n\t" \
  "buffer_load_dword v194, %[vo], %[srz], 0 offen\n\t" \
  "buffer_load_dwordx4 v[72:75], %[vo], %[srd], %[sb] offen offset:0 sc1\n\t" \
  "buffer_load_dwordx4 v[76:79], %[vo], %[srd], %[sb] offen offset:1024 sc1\n\t" \
  "s_add_u32 %[sb], %[sb], 0x2000\n\t" \
  "s_and_b32 %[sb], %[sb], 0x7ffff\n\t" \
  "buffer_load_dwordx4 v[80:83], %[vo], %[srd], %[sb] offen offset:0 sc1\n\t" \
  "buffer_load_dwordx4 v[84:87], %[vo], %[srd], %[sb] offen offset:1024 sc1\n\t" \
  "s_add_u32 %[sb], %[sb], 0x2000\n\t" \
  "s_and_b32 %[sb], %[sb], 0x7ffff\n\t" \
  "buffer_load_dwordx4 v[88:91], %[vo], %[srd], %[sb] offen offset:0 sc1\n\t" \
  "buffer_load_dwordx4 v[92:95], %[vo], %[srd], %[sb] offen offset:1024 sc1\n\t" \
  "s_add_u32 %[sb], %[sb], 0x2000\n\t" \
  "s_and_b32 %[sb], %[sb], 0x7ffff\n\t" \
  "buffer_load_dwordx4 v[96:99], %[vo], %[srd], %[sb] offen offset:0 sc1\n\t" \
  "buffer_load_dwordx4 v[100:103], %[vo], %[srd], %[sb] offen offset:1024 sc1\n\t" \
  "s_add_u32 %[sb], %[sb], 0x2000\n\t" \
  "s_and_b32 %[sb], %[sb], 0x7ffff\n\t" \
  "buffer_load_dword v194, %[vo], %[srz], 0 offen\n\t" \
  "buffer_load_dwordx4 v[104:107], %[vo], %[srd], %[sb] offen offset:0 sc1\n\t" \
  "buffer_load_dwordx4 v[108:111], %[vo], %[srd], %[sb] offen offset:1024 sc1\n\t" \
  "s_add_u32 %[sb], %[sb], 0x2000\n\t" \
  "s_and_b32 %[sb], %[sb], 0x7ffff\n\t" \
  "s_waitcnt lgkmcnt(0)\n\t" \
  "Lloop_%=:\n\t" \
  "s_add_u32 %[sa], %[sa], 0x4000\n\t" \
  "s_cmp_eq_u32 %[sa], 0x24000\n\t" \
  "s_cselect_b32 %[s1], %[wrap], 0x4000\n\t" \
  "s_cselect_b32 %[sa], 0, %[sa]\n\t" \
  "s_cmp_eq_u32 %[sg], 4\n\t" \
  "s_cbranch_scc1 Lh_%=\n\t" \
  "buffer_load_dword v194, %[vo], %[srz], 0 offen\n\t" \
  "Lhb_%=:\n\t" \
  "s_waitcnt vmcnt(13) lgkmcnt(4)\n\t" \
  "v_mfma_f32_16x16x32_bf16 %[c00], v[64:67], v[128:131], %[c00]\n\t" \
  "buffer_load_dwordx4 v[112:115], %[vo], %[srd], %[sb] offen offset:0 sc1\n\t" \
  "v_mfma_f32_16x16x32_bf16 %[c10], v[68:71], v[128:131], %[c10]\n\t" \
  "ds_read_b128 v[160:163], %[pc2] offset:0\n\t" \
  "v_mfma_f32_16x16x32_bf16 %[c01], v[64:67], v[132:135], %[c01]\n\t" \
  "v_add_u32 %[pc0], %[s1], %[pc0]\n\t" \
  "v_mfma_f32_16x16x32_bf16 %[c11], v[68:71], v[132:135], %[c11]\n\t" \
  "ds_read_b128 v[164:167], %[pc2] offset:2048\n\t" \
  "v_mfma_f32_16x16x32_bf16 %[c02], v[64:67], v[136:139], %[c02]\n\t" \
  "buffer_load_dwordx4 v[116:119], %[vo], %[srd], %[sb] offen offset:1024 sc1\n\t" \
  "s_add_u32 %[sb], %[sb], 0x2000\n\t" \
  "s_and_b32 %[sb], %[sb], 0x7ffff\n\t" \
  "v_mfma_f32_16x16x32_bf16 %[c12], v[68:71], v[136:139], %[c12]\n\t" \
  "ds_read_b128 v[168:171], %[pc2] offset:4096\n\t" \
  "v_mfma_f32_16x16x32_bf16 %[c03], v[64:67], v[140:143], %[c03]\n\t" \
  "v_mfma_f32_16x16x32_bf16 %[c13], v[68:71], v[140:143], %[c13]\n\t" \
  "ds_read_b128 v[172:175], %[pc2] offset:6144\n\t" \
  "s_waitcnt vmcnt(12) lgkmcnt(4)\n\t" \
  "ds_read_b64 v[192:193], %[fl]\n\t" \
  "v_mfma_f32_16x16x32_bf16 %[c00], v[72:75], v[144:147], %[c00]\n\t" \
  "buffer_load_dwordx4 v[120:123], %[vo], %[srd], %[sb] offen offset:0 sc1\n\t" \
  "v_mfma_f32_16x16x32_bf16 %[c10], v[76:79], v[144:147], %[c10]\n\t" \
  "ds_read_b128 v[176:179], %[pc3] offset:0\n\t" \
  "v_mfma_f32_16x16x32_bf16 %[c01], v[72:75], v[148:151], %[c01]\n\t" \
  "v_add_u32 %[pc1], %[s1], %[pc1]\n\t" \
  "v_mfma_f32_16x16x32_bf16 %[c11], v[76:79], v[148:151], %[c11]\n\t" \
  "ds_read_b128 v[180:183], %[pc3] offset:2048\n\t" \
  "v_mfma_f32_16x16x32_bf16 %[c02], v[72:75], v[152:155], %[c02]\n\t" \
  "buffer_load_dwordx4 v[124:127], %[vo], %[srd], %[sb] offen offset:1024 sc1\n\t" \
  "s_add_u32 %[sb], %[sb], 0x2000\n\t" \
  "s_and_b32 %[sb], %[sb], 0x7ffff\n\t" \
  "v_mfma_f32_16x16x32_bf16 %[c12], v[76:79], v[152:155], %[c12]\n\t" \
  "ds_read_b128 v[184:187], %[pc3] offset:4096\n\t" \
  "v_mfma_f32_16x16x32_bf16 %[c03], v[72:75], v[156:159], %[c03]\n\t" \
  "v_mfma_f32_16x16x32_bf16 %[c13], v[76:79], v[156:159], %[c13]\n\t" \
  "ds_read_b128 v[188:191], %[pc3] offset:6144\n\t" \
  "s_waitcnt vmcnt(12) lgkmcnt(4)\n\t" \
  "v_or_b32 v192, v192, v193\n\t" \
  "v_mfma_f32_16x16x32_bf16 %[c00], v[80:83], v[160:163], %[c00]\n\t" \
  "v_readfirstlane_b32 %[s0], v192\n\t" \
  "buffer_load_dwordx4 v[64:67], %[vo], %[srd], %[sb] offen offset:0 sc1\n\t" \
  "v_mfma_f32_16x16x32_bf16 %[c10], v[84:87], v[160:163], %[c10]\n\t" \
  "s_cmp_eq_u32 %[s0], 0\n\t" \
  "s_cbranch_scc0 Lca2_%=\n\t" \
  "Lca2b_%=:\n\t" \
  "ds_read_b128 v[128:131], %[pc0] offset:0\n\t" \
  "v_mfma_f32_16x16x32_bf16 %[c01], v[80:83], v[164:167], %[c01]\n\t" \
  "v_add_u32 %[pc2], %[s1], %[pc2]\n\t" \
  "v_mfma_f32_16x16x32_bf16 %[c11], v[84:87], v[164:167], %[c11]\n\t" \
  "ds_read_b128 v[132:135], %[pc0] offset:2048\n\t" \
  "v_mfma_f32_16x16x32_bf16 %[c02], v[80:83], v[168:171], %[c02]\n\t" \
  "buffer_load_dwordx4 v[68:71], %[vo], %[srd], %[sb] offen offset:1024 sc1\n\t" \
  "s_add_u32 %[sb], %[sb], 0x2000\n\t" \
  "s_and_b32 %[sb], %[sb], 0x7ffff\n\t" \
  "v_mfma_f32_16x16x32_bf16 %[c12], v[84:87], v[168:171], %[c12]\n\t" \
  "ds_read_b128 v[136:139], %[pc0] offset:4096\n\t" \
  "v_mfma_f32_16x16x32_bf16 %[c03], v[80:83], v[172:175], %[c03]\n\t" \
  "v_mfma_f32_16x16x32_bf16 %[c13], v[84:87], v[172:175], %[c13]\n\t" \
  "ds_read_b128 v[140:143], %[pc0] offset:6144\n\t" \
  "s_waitcnt vmcnt(12) lgkmcnt(4)\n\t" \
  "s_mov_b32 m0, %[scn]\n\t" \
  "s_mov_b64 exec, 1\n\t" \
  "buffer_load_dword %[vo], %[srz], 0 offen lds\n\t" \
  "s_mov_b64 exec, -1\n\t" \
  "s_add_u32 %[sg], %[sg], 1\n\t" \
  "s_add_u32 %[scn], %[scn], 16\n\t" \
  "v_mfma_f32_16x16x32_bf16 %[c00], v[88:91], v[176:179], %[c00]\n\t" \
  "buffer_load_dwordx4 v[72:75], %[vo], %[srd], %[sb] offen offset:0 sc1\n\t" \
  "v_mfma_f32_16x16x32_bf16 %[c10], v[92:95], v[176:179], %[c10]\n\t" \
  "ds_read_b128 v[144:147], %[pc1] offset:0\n\t" \
  "v_mfma_f32_16x16x32_bf16 %[c01], v[88:91], v[180:183], %[c01]\n\t" \
  "v_add_u32 %[pc3], %[s1], %[pc3]\n\t" \
  "v_add_u32 %[fl], 8, %[fl]\n\t" \
  "v_mfma_f32_16x16x32_bf16 %[c11], v[92:95], v[180:183], %[c11]\n\t" \
  "ds_read_b128 v[148:151], %[pc1] offset:2048\n\t" \
  "v_mfma_f32_16x16x32_bf16 %[c02], v[88:91], v[184:187], %[c02]\n\t" \
  "buffer_load_dwordx4 v[76:79], %[vo], %[srd], %[sb] offen offset:1024 sc1\n\t" \
  "s_add_u32 %[sb], %[sb], 0x2000\n\t" \
  "s_and_b32 %[sb], %[sb], 0x7ffff\n\t" \
  "v_mfma_f32_16x16x32_bf16 %[c12], v[92:95], v[184:187], %[c12]\n\t" \
  "ds_read_b128 v[152:155], %[pc1] offset:4096\n\t" \
  "v_mfma_f32_16x16x32_bf16 %[c03], v[88:91], v[188:191], %[c03]\n\t" \
  "v_mfma_f32_16x16x32_bf16 %[c13], v[92:95], v[188:191], %[c13]\n\t" \
  "ds_read_b128 v[156:159], %[pc1] offset:6144\n\t" \
  "s_add_u32 %[sa], %[sa], 0x4000\n\t" \
  "s_cmp_eq_u32 %[sa], 0x24000\n\t" \
  "s_cselect_b32 %[s1], %[wrap], 0x4000\n\t" \
  "s_cselect_b32 %[sa], 0, %[sa]\n\t" \
  "s_waitcnt vmcnt(13) lgkmcnt(4)\n\t" \
  "v_mfma_f32_16x16x32_bf16 %[c00], v[96:99], v[128:131], %[c00]\n\t" \
  "buffer_load_dwordx4 v[80:83], %[vo], %[srd], %[sb] offen offset:0 sc1\n\t" \
  "v_mfma_f32_16x16x32_bf16 %[c10], v[100:103], v[128:131], %[c10]\n\t" \
  "ds_read_b128 v[160:163], %[pc2] offset:0\n\t" \
  "v_mfma_f32_16x16x32_bf16 %[c01], v[96:99], v[132:135], %[c01]\n\t" \
  "v_add_u32 %[pc0], %[s1], %[pc0]\n\t" \
  "v_mfma_f32_16x16x32_bf16 %[c11], v[100:103], v[132:135], %[c11]\n\t" \
  "ds_read_b128 v[164:167], %[pc2] offset:2048\n\t" \
  "v_mfma_f32_16x16x32_bf16 %[c02], v[96:99], v[136:139], %[c02]\n\t" \
  "buffer_load_dwordx4 v[84:87], %[vo], %[srd], %[sb] offen offset:1024 sc1\n\t" \
  "s_add_u32 %[sb], %[sb], 0x2000\n\t" \
  "s_and_b32 %[sb], %[sb], 0x7ffff\n\t" \
  "v_mfma_f32_16x16x32_bf16 %[c12], v[100:103], v[136:139], %[c12]\n\t" \
  "ds_read_b128 v[168:171], %[pc2] offset:4096\n\t" \
  "v_mfma_f32_16x16x32_bf16 %[c03], v[96:99], v[140:143], %[c03]\n\t" \
  "v_mfma_f32_16x16x32_bf16 %[c13], v[100:103], v[140:143], %[c13]\n\t" \
  "ds_read_b128 v[172:175], %[pc2] offset:6144\n\t" \
  "s_waitcnt vmcnt(12) lgkmcnt(4)\n\t" \
  "ds_read_b64 v[192:193], %[fl]\n\t" \
  "v_mfma_f32_16x16x32_bf16 %[c00], v[104:107], v[144:147], %[c00]\n\t" \
  "buffer_load_dwordx4 v[88:91], %[vo], %[srd], %[sb] offen offset:0 sc1\n\t" \
  "v_mfma_f32_16x16x32_bf16 %[c10], v[108:111], v[144:147], %[c10]\n\t" \
  "ds_read_b128 v[176:179], %[pc3] offset:0\n\t" \
  "v_mfma_f32_16x16x32_bf16 %[c01], v[104:107], v[148:151], %[c01]\n\t" \
  "v_add_u32 %[pc1], %[s1], %[pc1]\n\t" \
  "v_mfma_f32_16x16x32_bf16 %[c11], v[108:111], v[148:151], %[c11]\n\t" \
  "ds_read_b128 v[180:183], %[pc3] offset:2048\n\t" \
  "v_mfma_f32_16x16x32_bf16 %[c02], v[104:107], v[152:155], %[c02]\n\t" \
  "buffer_load_dwordx4 v[92:95], %[vo], %[srd], %[sb] offen offset:1024 sc1\n\t" \
  "s_add_u32 %[sb], %[sb], 0x2000\n\t" \
  "s_and_b32 %[sb], %[sb], 0x7ffff\n\t" \
  "v_mfma_f32_16x16x32_bf16 %[c12], v[108:111], v[152:155], %[c12]\n\t" \
  "ds_read_b128 v[184:187], %[pc3] offset:4096\n\t" \
  "v_mfma_f32_16x16x32_bf16 %[c03], v[104:107], v[156:159], %[c03]\n\t" \
  "v_mfma_f32_16x16x32_bf16 %[c13], v[108:111], v[156:159], %[c13]\n\t" \
  "ds_read_b128 v[188:191], %[pc3] offset:6144\n\t" \
  "s_waitcnt vmcnt(11) lgkmcnt(4)\n\t" \
  "v_or_b32 v192, v192, v193\n\t" \
  "v_mfma_f32_16x16x32_bf16 %[c00], v[112:115], v[160:163], %[c00]\n\t" \
  "v_readfirstlane_b32 %[s0], v192\n\t" \
  "buffer_load_dwordx4 v[96:99], %[vo], %[srd], %[sb] offen offset:0 sc1\n\t" \
  "v_mfma_f32_16x16x32_bf16 %[c10], v[116:119], v[160:163], %[c10]\n\t" \
  "s_cmp_eq_u32 %[s0], 0\n\t" \
  "s_cbranch_scc0 Lca6_%=\n\t" \
  "Lca6b_%=:\n\t" \
  "ds_read_b128 v[128:131], %[pc0] offset:0\n\t" \
  "v_mfma_f32_16x16x32_bf16 %[c01], v[112:115], v[164:167], %[c01]\n\t" \
  "v_add_u32 %[pc2], %[s1], %[pc2]\n\t" \
  "v_mfma_f32_16x16x32_bf16 %[c11], v[116:119], v[164:167], %[c11]\n\t" \
  "ds_read_b128 v[132:135], %[pc0] offset:2048\n\t" \
  "v_mfma_f32_16x16x32_bf16 %[c02], v[112:115], v[168:171], %[c02]\n\t" \
  "buffer_load_dwordx4 v[100:103], %[vo], %[srd], %[sb] offen offset:1024 sc1\n\t" \
  "s_add_u32 %[sb], %[sb], 0x2000\n\t" \
  "s_and_b32 %[sb], %[sb], 0x7ffff\n\t" \
  "v_mfma_f32_16x16x32_bf16 %[c12], v[116:119], v[168:171], %[c12]\n\t" \
  "ds_read_b128 v[136:139], %[pc0] offset:4096\n\t" \
  "v_mfma_f32_16x16x32_bf16 %[c03], v[112:115], v[172:175], %[c03]\n\t" \
  "v_mfma_f32_16x16x32_bf16 %[c13], v[116:119], v[172:175], %[c13]\n\t" \
  "ds_read_b128 v[140:143], %[pc0] offset:6144\n\t" \
  "s_waitcnt vmcnt(11) lgkmcnt(4)\n\t" \
  "s_mov_b32 m0, %[scn]\n\t" \
  "s_mov_b64 exec, 1\n\t" \
  "buffer_load_dword %[vo], %[srz], 0 offen lds\n\t" \
  "s_mov_b64 exec, -1\n\t" \
  "s_add_u32 %[sg], %[sg], 1\n\t" \
  "s_add_u32 %[scn], %[scn], 16\n\t" \
  "v_mfma_f32_16x16x32_bf16 %[c00], v[120:123], v[176:179], %[c00]\n\t" \
  "buffer_load_dwordx4 v[104:107], %[vo], %[srd], %[sb] offen offset:0 sc1\n\t" \
  "v_mfma_f32_16x16x32_bf16 %[c10], v[124:127], v[176:179], %[c10]\n\t" \
  "ds_read_b128 v[144:147], %[pc1] offset:0\n\t" \
  "v_mfma_f32_16x16x32_bf16 %[c01], v[120:123], v[180:183], %[c01]\n\t" \
  "v_add_u32 %[pc3], %[s1], %[pc3]\n\t" \
  "v_add_u32 %[fl], 8, %[fl]\n\t" \
  "v_mfma_f32_16x16x32_bf16 %[c11], v[124:127], v[180:183], %[c11]\n\t" \
  "ds_read_b128 v[148:151], %[pc1] offset:2048\n\t" \
  "v_mfma_f32_16x16x32_bf16 %[c02], v[120:123], v[184:187], %[c02]\n\t" \
  "buffer_load_dwordx4 v[108:111], %[vo], %[srd], %[sb] offen offset:1024 sc1\n\t" \
  "s_add_u32 %[sb], %[sb], 0x2000\n\t" \
  "s_and_b32 %[sb], %[sb], 0x7ffff\n\t" \
  "v_mfma_f32_16x16x32_bf16 %[c12], v[124:127], v[184:187], %[c12]\n\t" \
  "ds_read_b128 v[152:155], %[pc1] offset:4096\n\t" \
  "v_mfma_f32_16x16x32_bf16 %[c03], v[120:123], v[188:191], %[c03]\n\t" \
  "v_mfma_f32_16x16x32_bf16 %[c13], v[124:127], v[188:191], %[c13]\n\t" \
  "ds_read_b128 v[156:159], %[pc1] offset:6144\n\t" \
  "s_cmp_eq_u32 %[sg], 6\n\t" \
  "s_cbranch_scc1 Lg_%=\n\t" \
  "s_cmp_lt_u32 %[sg], 14\n\t" \
  "s_cbranch_scc1 Lloop_%=\n\t" \
  "s_add_u32 %[sa], %[sa], 0x4000\n\t" \
  "s_cmp_eq_u32 %[sa], 0x24000\n\t" \
  "s_cselect_b32 %[s1], %[wrap], 0x4000\n\t" \
  "s_cselect_b32 %[sa], 0, %[sa]\n\t" \
  "s_waitcnt vmcnt(12) lgkmcnt(4)\n\t" \
  "v_mfma_f32_16x16x32_bf16 %[c00], v[64:67], v[128:131], %[c00]\n\t" \
  "buffer_load_dwordx4 v[112:115], %[vo], %[srd], %[sb] offen offset:0 sc1\n\t" \
  "v_mfma_f32_16x16x32_bf16 %[c10], v[68:71], v[128:131], %[c10]\n\t" \
  "ds_read_b128 v[160:163], %[pc2] offset:0\n\t" \
  "v_mfma_f32_16x16x32_bf16 %[c01], v[64:67], v[132:135], %[c01]\n\t" \
  "v_add_u32 %[pc0], %[s1], %[pc0]\n\t" \
  "v_mfma_f32_16x16x32_bf16 %[c11], v[68:71], v[132:135], %[c11]\n\t" \
  "ds_read_b128 v[164:167], %[pc2] offset:2048\n\t" \
  "v_mfma_f32_16x16x32_bf16 %[c02], v[64:67], v[136:139], %[c02]\n\t" \
  "buffer_load_dwordx4 v[116:119], %[vo], %[srd], %[sb] offen offset:1024 sc1\n\t" \
  "s_add_u32 %[sb], %[sb], 0x2000\n\t" \
  "s_and_b32 %[sb], %[sb], 0x7ffff\n\t" \
  "v_mfma_f32_16x16x32_bf16 %[c12], v[68:71], v[136:139], %[c12]\n\t" \
  "ds_read_b128 v[168:171], %[pc2] offset:4096\n\t" \
  "v_mfma_f32_16x16x32_bf16 %[c03], v[64:67], v[140:143], %[c03]\n\t" \
  "v_mfma_f32_16x16x32_bf16 %[c13], v[68:71], v[140:143], %[c13]\n\t" \
  "ds_read_b128 v[172:175], %[pc2] offset:6144\n\t" \
  "s_waitcnt vmcnt(11) lgkmcnt(4)\n\t" \
  "ds_read_b64 v[192:193], %[fl]\n\t" \
  "v_mfma_f32_16x16x32_bf16 %[c00], v[72:75], v[144:147], %[c00]\n\t" \
  "buffer_load_dwordx4 v[120:123], %[vo], %[srd], %[sb] offen offset:0 sc1\n\t" \
  "v_mfma_f32_16x16x32_bf16 %[c10], v[76:79], v[144:147], %[c10]\n\t" \
  "ds_read_b128 v[176:179], %[pc3] offset:0\n\t" \
  "v_mfma_f32_16x16x32_bf16 %[c01], v[72:75], v[148:151], %[c01]\n\t" \
  "v_add_u32 %[pc1], %[s1], %[pc1]\n\t" \
  "v_mfma_f32_16x16x32_bf16 %[c11], v[76:79], v[148:151], %[c11]\n\t" \
  "ds_read_b128 v[180:183], %[pc3] offset:2048\n\t" \
  "v_mfma_f32_16x16x32_bf16 %[c02], v[72:75], v[152:155], %[c02]\n\t" \
  "buffer_load_dwordx4 v[124:127], %[vo], %[srd], %[sb] offen offset:1024 sc1\n\t" \
  "s_add_u32 %[sb], %[sb], 0x2000\n\t" \
  "s_and_b32 %[sb], %[sb], 0x7ffff\n\t" \
  "v_mfma_f32_16x16x32_bf16 %[c12], v[76:79], v[152:155], %[c12]\n\t" \
  "ds_read_b128 v[184:187], %[pc3] offset:4096\n\t" \
  "v_mfma_f32_16x16x32_bf16 %[c03], v[72:75], v[156:159], %[c03]\n\t" \
  "v_mfma_f32_16x16x32_bf16 %[c13], v[76:79], v[156:159], %[c13]\n\t" \
  "ds_read_b128 v[188:191], %[pc3] offset:6144\n\t" \
  "s_waitcnt vmcnt(11) lgkmcnt(4)\n\t" \
  "v_or_b32 v192, v192, v193\n\t" \
  "v_mfma_f32_16x16x32_bf16 %[c00], v[80:83], v[160:163], %[c00]\n\t" \
  "v_readfirstlane_b32 %[s0], v192\n\t" \
  "v_mfma_f32_16x16x32_bf16 %[c10], v[84:87], v[160:163], %[c10]\n\t" \
  "s_cmp_eq_u32 %[s0], 0\n\t" \
  "s_cbranch_scc0 Lcb2_%=\n\t" \
  "Lcb2b_%=:\n\t" \
  "ds_read_b128 v[128:131], %[pc0] offset:0\n\t" \
  "v_mfma_f32_16x16x32_bf16 %[c01], v[80:83], v[164:167], %[c01]\n\t" \
  "v_add_u32 %[pc2], %[s1], %[pc2]\n\t" \
  "v_mfma_f32_16x16x32_bf16 %[c11], v[84:87], v[164:167], %[c11]\n\t" \
  "ds_read_b128 v[132:135], %[pc0] offset:2048\n\t" \
  "v_mfma_f32_16x16x32_bf16 %[c02], v[80:83], v[168:171], %[c02]\n\t" \
  "v_mfma_f32_16x16x32_bf16 %[c12], v[84:87], v[168:171], %[c12]\n\t" \
  "ds_read_b128 v[136:139], %[pc0] offset:4096\n\t" \
  "v_mfma_f32_16x16x32_bf16 %[c03], v[80:83], v[172:175], %[c03]\n\t" \
  "v_mfma_f32_16x16x32_bf16 %[c13], v[84:87], v[172:175], %[c13]\n\t" \
  "ds_read_b128 v[140:143], %[pc0] offset:6144\n\t" \
  "s_waitcnt vmcnt(9) lgkmcnt(4)\n\t" \
  "v_mfma_f32_16x16x32_bf16 %[c00], v[88:91], v[176:179], %[c00]\n\t" \
  "v_mfma_f32_16x16x32_bf16 %[c10], v[92:95], v[176:179], %[c10]\n\t" \
  "ds_read_b128 v[144:147], %[pc1] offset:0\n\t" \
  "v_mfma_f32_16x16x32_bf16 %[c01], v[88:91], v[180:183], %[c01]\n\t" \
  "v_add_u32 %[pc3], %[s1], %[pc3]\n\t" \
  "v_add_u32 %[fl], 8, %[fl]\n\t" \
  "v_mfma_f32_16x16x32_bf16 %[c11], v[92:95], v[180:183], %[c11]\n\t" \
  "ds_read_b128 v[148:151], %[pc1] offset:2048\n\t" \
  "v_mfma_f32_16x16x32_bf16 %[c02], v[88:91], v[184:187], %[c02]\n\t" \
  "v_mfma_f32_16x16x32_bf16 %[c12], v[92:95], v[184:187], %[c12]\n\t" \
  "ds_read_b128 v[152:155], %[pc1] offset:4096\n\t" \
  "v_mfma_f32_16x16x32_bf16 %[c03], v[88:91], v[188:191], %[c03]\n\t" \
  "v_mfma_f32_16x16x32_bf16 %[c13], v[92:95], v[188:191], %[c13]\n\t" \
  "ds_read_b128 v[156:159], %[pc1] offset:6144\n\t" \
  "s_add_u32 %[sa], %[sa], 0x4000\n\t" \
  "s_cmp_eq_u32 %[sa], 0x24000\n\t" \
  "s_cselect_b32 %[s1], %[wrap], 0x4000\n\t" \
  "s_cselect_b32 %[sa], 0, %[sa]\n\t" \
  "s_waitcnt vmcnt(7) lgkmcnt(4)\n\t" \
  "v_mfma_f32_16x16x32_bf16 %[c00], v[96:99], v[128:131], %[c00]\n\t" \
  "v_mfma_f32_16x16x32_bf16 %[c10], v[100:103], v[128:131], %[c10]\n\t" \
  "ds_read_b128 v[160:163], %[pc2] offset:0\n\t" \
  "v_mfma_f32_16x16x32_bf16 %[c01], v[96:99], v[132:135], %[c01]\n\t" \
  "v_add_u32 %[pc0], %[s1], %[pc0]\n\t" \
  "v_mfma_f32_16x16x32_bf16 %[c11], v[100:103], v[132:135], %[c11]\n\t" \
  "ds_read_b128 v[164:167], %[pc2] offset:2048\n\t" \
  "v_mfma_f32_16x16x32_bf16 %[c02], v[96:99], v[136:139], %[c02]\n\t" \
  "v_mfma_f32_16x16x32_bf16 %[c12], v[100:103], v[136:139], %[c12]\n\t" \
  "ds_read_b128 v[168:171], %[pc2] offset:4096\n\t" \
  "v_mfma_f32_16x16x32_bf16 %[c03], v[96:99], v[140:143], %[c03]\n\t" \
  "v_mfma_f32_16x16x32_bf16 %[c13], v[100:103], v[140:143], %[c13]\n\t" \
  "ds_read_b128 v[172:175], %[pc2] offset:6144\n\t" \
  "s_waitcnt vmcnt(4) lgkmcnt(4)\n\t" \
  "ds_read_b64 v[192:193], %[fl]\n\t" \
  "v_mfma_f32_16x16x32_bf16 %[c00], v[104:107], v[144:147], %[c00]\n\t" \
  "v_mfma_f32_16x16x32_bf16 %[c10], v[108:111], v[144:147], %[c10]\n\t" \
  "ds_read_b128 v[176:179], %[pc3] offset:0\n\t" \
  "v_mfma_f32_16x16x32_bf16 %[c01], v[104:107], v[148:151], %[c01]\n\t" \
  "v_add_u32 %[pc1], %[s1], %[pc1]\n\t" \
  "v_mfma_f32_16x16x32_bf16 %[c11], v[108:111], v[148:151], %[c11]\n\t" \
  "ds_read_b128 v[180:183], %[pc3] offset:2048\n\t" \
  "v_mfma_f32_16x16x32_bf16 %[c02], v[104:107], v[152:155], %[c02]\n\t" \
  "v_mfma_f32_16x16x32_bf16 %[c12], v[108:111], v[152:155], %[c12]\n\t" \
  "ds_read_b128 v[184:187], %[pc3] offset:4096\n\t" \
  "v_mfma_f32_16x16x32_bf16 %[c03], v[104:107], v[156:159], %[c03]\n\t" \
  "v_mfma_f32_16x16x32_bf16 %[c13], v[108:111], v[156:159], %[c13]\n\t" \
  "ds_read_b128 v[188:191], %[pc3] offset:6144\n\t" \
  "s_waitcnt vmcnt(2) lgkmcnt(4)\n\t" \
  "v_mfma_f32_16x16x32_bf16 %[c00], v[112:115], v[160:163], %[c00]\n\t" \
  "v_mfma_f32_16x16x32_bf16 %[c10], v[116:119], v[160:163], %[c10]\n\t" \
  "v_mfma_f32_16x16x32_bf16 %[c01], v[112:115], v[164:167], %[c01]\n\t" \
  "v_add_u32 %[pc2], %[s1], %[pc2]\n\t" \
  "v_mfma_f32_16x16x32_bf16 %[c11], v[116:119], v[164:167], %[c11]\n\t" \
  "v_mfma_f32_16x16x32_bf16 %[c02], v[112:115], v[168:171], %[c02]\n\t" \
  "v_mfma_f32_16x16x32_bf16 %[c12], v[116:119], v[168:171], %[c12]\n\t" \
  "v_mfma_f32_16x16x32_bf16 %[c03], v[112:115], v[172:175], %[c03]\n\t" \
  "v_mfma_f32_16x16x32_bf16 %[c13], v[116:119], v[172:175], %[c13]\n\t" \
  "s_waitcnt vmcnt(0) lgkmcnt(0)\n\t" \
  "v_mfma_f32_16x16x32_bf16 %[c00], v[120:123], v[176:179], %[c00]\n\t" \
  "v_mfma_f32_16x16x32_bf16 %[c10], v[124:127], v[176:179], %[c10]\n\t" \
  "v_mfma_f32_16x16x32_bf16 %[c01], v[120:123], v[180:183], %[c01]\n\t" \
  "v_add_u32 %[pc3], %[s1], %[pc3]\n\t" \
  "v_add_u32 %[fl], 8, %[fl]\n\t" \
  "v_mfma_f32_16x16x32_bf16 %[c11], v[124:127], v[180:183], %[c11]\n\t" \
  "v_mfma_f32_16x16x32_bf16 %[c02], v[120:123], v[184:187], %[c02]\n\t" \
  "v_mfma_f32_16x16x32_bf16 %[c12], v[124:127], v[184:187], %[c12]\n\t" \
  "v_mfma_f32_16x16x32_bf16 %[c03], v[120:123], v[188:191], %[c03]\n\t" \
  "v_mfma_f32_16x16x32_bf16 %[c13], v[124:127], v[188:191], %[c13]\n\t" \
  "s_branch Lend_%=\n\t" \
  "Lc0_%=:\n\t" \
  "ds_read_b64 v[192:193], %[fl]\n\t" \
  "s_sub_i32 %[bud], %[bud], 1\n\t" \
  "s_cmp_lt_i32 %[bud], 0\n\t" \
  "s_cbranch_scc1 Lc0b_%=\n\t" \
  "s_waitcnt lgkmcnt(0)\n\t" \
  "v_or_b32 v192, v192, v193\n\t" \
  "s_nop 0\n\t" \
  "v_readfirstlane_b32 %[s0], v192\n\t" \
  "s_cmp_eq_u32 %[s0], 0\n\t" \
  "s_cbranch_scc0 Lc0_%=\n\t" \
  "s_branch Lc0b_%=\n\t" \
  "Lw_%=:\n\t" \
  "ds_read_b64 v[192:193], %[hr]\n\t" \
  "s_sub_i32 %[bud], %[bud], 1\n\t" \
  "s_cmp_lt_i32 %[bud], 0\n\t" \
  "s_cbranch_scc1 Lf3_%=\n\t" \
  "s_waitcnt lgkmcnt(0)\n\t" \
  "v_or_b32 v192, v192, v193\n\t" \
  "s_nop 0\n\t" \
  "v_readfirstlane_b32 %[s0], v192\n\t" \
  "s_cmp_eq_u32 %[s0], 0\n\t" \
  "s_cbranch_scc0 Lw_%=\n\t" \
  "s_branch Lf3_%=\n\t" \
  "Lh_%=:\n\t" \
  "buffer_load_dwordx2 v[196:197], %[flo], %[srd], %[sf1] offen sc1\n\t" \
  "s_branch Lhb_%=\n\t" \
  "Lca2_%=:\n\t" \
  "ds_read_b64 v[192:193], %[fl]\n\t" \
  "s_sub_i32 %[bud], %[bud], 1\n\t" \
  "s_cmp_lt_i32 %[bud], 0\n\t" \
  "s_cbranch_scc1 Lca2b_%=\n\t" \
  "s_waitcnt lgkmcnt(0)\n\t" \
  "v_or_b32 v192, v192, v193\n\t" \
  "s_nop 0\n\t" \
  "v_readfirstlane_b32 %[s0], v192\n\t" \
  "s_cmp_eq_u32 %[s0], 0\n\t" \
  "s_cbranch_scc0 Lca2_%=\n\t" \
  "s_branch Lca2b_%=\n\t" \
  "Lca6_%=:\n\t" \
  "ds_read_b64 v[192:193], %[fl]\n\t" \
  "s_sub_i32 %[bud], %[bud], 1\n\t" \
  "s_cmp_lt_i32 %[bud], 0\n\t" \
  "s_cbranch_scc1 Lca6b_%=\n\t" \
  "s_waitcnt lgkmcnt(0)\n\t" \
  "v_or_b32 v192, v192, v193\n\t" \
  "s_nop 0\n\t" \
  "v_readfirstlane_b32 %[s0], v192\n\t" \
  "s_cmp_eq_u32 %[s0], 0\n\t" \
  "s_cbranch_scc0 Lca6_%=\n\t" \
  "s_branch Lca6b_%=\n\t" \
  "Lg1_%=:\n\t" \
  "buffer_load_dwordx2 v[196:197], %[flo], %[srd], %[sf1] offen sc1\n\t" \
  "s_sub_i32 %[bud], %[bud], 16\n\t" \
  "s_cmp_lt_i32 %[bud], 0\n\t" \
  "s_cbranch_scc1 Lloop_%=\n\t" \
  "s_waitcnt vmcnt(0)\n\t" \
  "Lg_%=:\n\t" \
  "v_cmp_ne_u32 vcc, %[tgA], v196\n\t" \
  "v_cmp_ne_u32 %[sp], %[tgB], v197\n\t" \
  "s_or_b64 vcc, vcc, %[sp]\n\t" \
  "s_cbranch_scc1 Lg1_%=\n\t" \
  "s_branch Lloop_%=\n\t" \
  "Lcb2_%=:\n\t" \
  "ds_read_b64 v[192:193], %[fl]\n\t" \
  "s_sub_i32 %[bud], %[bud], 1\n\t" \
  "s_cmp_lt_i32 %[bud], 0\n\t" \
  "s_cbranch_scc1 Lcb2b_%=\n\t" \
  "s_waitcnt lgkmcnt(0)\n\t" \
  "v_or_b32 v192, v192, v193\n\t" \
  "s_nop 0\n\t" \
  "v_readfirstlane_b32 %[s0], v192\n\t" \
  "s_cmp_eq_u32 %[s0], 0\n\t" \
  "s_cbranch_scc0 Lcb2_%=\n\t" \
  "s_branch Lcb2b_%=\n\t" \
  "Lend_%=:\n\t" \
  "s_nop 7\n\t"
#define ASM_LA \
  "s_nop 4\n\t" \
  "Ltop_%=:\n\t" \
  "s_mov_b32 m0, %[dst]\n\t" \
  "v_mov_b32 %[vc], %[ci]\n\t" \
  "buffer_load_dwordx4 %[v0], %[r0], %[so0] offen lds\n\t" \
  "s_add_u32 m0, m0, 0x400\n\t" \
  "s_add_u32 %[dst], %[dst], 0x8000\n\t" \
  "buffer_load_dwordx4 %[v0], %[r0], %[so1] offen lds\n\t" \
  "s_add_u32 m0, m0, 0x400\n\t" \
  "s_cmp_ge_u32 %[dst], 0x24000\n\t" \
  "s_cselect_b32 %[s0], %[wrap], 0\n\t" \
  "buffer_load_dwordx4 %[v0], %[r0], %[so2] offen lds\n\t" \
  "s_add_u32 m0, m0, 0x400\n\t" \
  "s_sub_u32 %[dst], %[dst], %[s0]\n\t" \
  "buffer_load_dwordx4 %[v0], %[r0], %[so3] offen lds\n\t" \
  "s_add_u32 m0, m0, 0x400\n\t" \
  "s_add_u32 %[sk], %[sk], 128\n\t" \
  "buffer_load_dwordx4 %[v0], %[r0], %[so4] offen lds\n\t" \
  "s_add_u32 m0, m0, 0x400\n\t" \
  "s_and_b32 %[sk], %[sk], 2047\n\t" \
  "buffer_load_dwordx4 %[v0], %[r0], %[so5] offen lds\n\t" \
  "s_add_u32 m0, m0, 0x400\n\t" \
  "s_nop 0\n\t" \
  "buffer_load_dwordx4 %[v0], %[r0], %[so6] offen lds\n\t" \
  "s_add_u32 m0, m0, 0x400\n\t" \
  "s_add_u32 %[ci], %[ci], 16\n\t" \
  "buffer_load_dwordx4 %[v0], %[r0], %[so7] offen lds\n\t" \
  "s_add_u32 m0, m0, 0x400\n\t" \
  "s_sub_u32 %[n], %[n], 1\n\t" \
  "buffer_load_dwordx4 %[v0], %[r1], %[so0] offen lds\n\t" \
  "s_add_u32 m0, m0, 0x400\n\t" \
  "s_nop 0\n\t" \
  "buffer_load_dwordx4 %[v0], %[r1], %[so1] offen lds\n\t" \
  "s_add_u32 m0, m0, 0x400\n\t" \
  "s_nop 0\n\t" \
  "buffer_load_dwordx4 %[v0], %[r1], %[so2] offen lds\n\t" \
  "s_add_u32 m0, m0, 0x400\n\t" \
  "s_nop 0\n\t" \
  "buffer_load_dwordx4 %[v0], %[r1], %[so3] offen lds\n\t" \
  "s_add_u32 m0, m0, 0x400\n\t" \
  "ds_read_b128 v[24:27], %[vc]\n\t" \
  "ds_read_b128 v[28:31], %[vw]\n\t" \
  "buffer_load_dwordx4 %[v0], %[r1], %[so4] offen lds\n\t" \
  "s_add_u32 m0, m0, 0x400\n\t" \
  "s_nop 0\n\t" \
  "buffer_load_dwordx4 %[v0], %[r1], %[so5] offen lds\n\t" \
  "s_add_u32 m0, m0, 0x400\n\t" \
  "s_nop 0\n\t" \
  "buffer_load_dwordx4 %[v0], %[r1], %[so6] offen lds\n\t" \
  "s_add_u32 m0, m0, 0x400\n\t" \
  "s_waitcnt lgkmcnt(0)\n\t" \
  "v_or3_b32 v24, v24, v25, v26\n\t" \
  "v_or_b32 v24, v24, v27\n\t" \
  "v_sub_u32 v28, v30, v28\n\t" \
  "v_sub_u32 v29, v31, v29\n\t" \
  "v_or3_b32 v24, v24, v28, v29\n\t" \
  "buffer_load_dwordx4 %[v0], %[r1], %[so7] offen lds\n\t" \
  "s_cmp_eq_u32 %[n], 15\n\t" \
  "s_cbranch_scc0 Lnb_%=\n\t" \
  "s_barrier\n\t" \
  "Lnb_%=:\n\t" \
  "s_mov_b32 m0, %[mk]\n\t" \
  "s_mov_b64 exec, 1\n\t" \
  "buffer_load_dword %[v0], %[rz], 0 offen lds\n\t" \
  "s_mov_b64 exec, -1\n\t" \
  "v_readfirstlane_b32 %[s0], v24\n\t" \
  "v_add_u32 %[v0], %[sk], %[b0]\n\t" \
  "s_add_u32 %[mk], %[mk], 8\n\t" \
  "s_cmp_eq_u32 %[n], 0\n\t" \
  "s_cbranch_scc1 Lend_%=\n\t" \
  "s_cmp_eq_u32 %[s0], 0\n\t" \
  "s_cbranch_scc1 Ltop_%=\n\t" \
  "Lsp_%=:\n\t" \
  "ds_read_b128 v[24:27], %[vc]\n\t" \
  "ds_read_b128 v[28:31], %[vw]\n\t" \
  "s_sub_i32 %[bud], %[bud], 1\n\t" \
  "s_cmp_lt_i32 %[bud], 0\n\t" \
  "s_cbranch_scc1 Ltop_%=\n\t" \
  "s_waitcnt lgkmcnt(0)\n\t" \
  "v_or3_b32 v24, v24, v25, v26\n\t" \
  "v_or_b32 v24, v24, v27\n\t" \
  "v_sub_u32 v28, v30, v28\n\t" \
  "v_sub_u32 v29, v31, v29\n\t" \
  "v_or3_b32 v24, v24, v28, v29\n\t" \
  "s_nop 0\n\t" \
  "v_readfirstlane_b32 %[s0], v24\n\t" \
  "s_cmp_eq_u32 %[s0], 0\n\t" \
  "s_cbranch_scc1 Ltop_%=\n\t" \
  "s_branch Lsp_%=\n\t" \
  "Lend_%=:\n\t"
#define ASM_LB \
  "s_nop 4\n\t" \
  "s_branch Lsp_%=\n\t" \
  "Ltop_%=:\n\t" \
  "s_mov_b32 m0, %[dst]\n\t" \
  "v_mov_b32 %[vc], %[ci]\n\t" \
  "buffer_load_dwordx4 %[v0], %[r0], %[so0] offen lds\n\t" \
  "s_add_u32 m0, m0, 0x400\n\t" \
  "s_add_u32 %[dst], %[dst], 0x8000\n\t" \
  "buffer_load_dwordx4 %[v1], %[r0], %[so1] offen lds\n\t" \
  "s_add_u32 m0, m0, 0x400\n\t" \
  "s_cmp_ge_u32 %[dst], 0x24000\n\t" \
  "s_cselect_b32 %[s0], %[wrap], 0\n\t" \
  "buffer_load_dwordx4 %[v0], %[r0], %[so2] offen lds\n\t" \
  "s_add_u32 m0, m0, 0x400\n\t" \
  "s_sub_u32 %[dst], %[dst], %[s0]\n\t" \
  "buffer_load_dwordx4 %[v1], %[r0], %[so3] offen lds\n\t" \
  "s_add_u32 m0, m0, 0x400\n\t" \
  "s_add_u32 %[sk], %[sk], 256\n\t" \
  "buffer_load_dwordx4 %[v0], %[r0], %[so4] offen lds\n\t" \
  "s_add_u32 m0, m0, 0x400\n\t" \
  "s_and_b32 %[sk], %[sk], 1023\n\t" \
  "buffer_load_dwordx4 %[v1], %[r0], %[so5] offen lds\n\t" \
  "s_add_u32 m0, m0, 0x400\n\t" \
  "s_cmp_ge_u32 %[ci], %[cth]\n\t" \
  "s_cselect_b32 %[s0], 32, 16\n\t" \
  "buffer_load_dwordx4 %[v0], %[r0], %[so6] offen lds\n\t" \
  "s_add_u32 m0, m0, 0x400\n\t" \
  "s_add_u32 %[ci], %[ci], %[s0]\n\t" \
  "buffer_load_dwordx4 %[v1], %[r0], %[so7] offen lds\n\t" \
  "s_add_u32 m0, m0, 0x400\n\t" \
  "s_sub_u32 %[n], %[n], 1\n\t" \
  "buffer_load_dwordx4 %[v0], %[r1], %[so0] offen lds\n\t" \
  "s_add_u32 m0, m0, 0x400\n\t" \
  "s_cmp_eq_u32 %[n], 4\n\t" \
  "s_cselect_b32 %[s0], 0x400, 0\n\t" \
  "buffer_load_dwordx4 %[v1], %[r1], %[so1] offen lds\n\t" \
  "s_add_u32 m0, m0, 0x400\n\t" \
  "s_xor_b32 %[hi], %[hi], %[s0]\n\t" \
  "buffer_load_dwordx4 %[v0], %[r1], %[so2] offen lds\n\t" \
  "s_add_u32 m0, m0, 0x400\n\t" \
  "s_or_b32 %[s1], %[sk], %[hi]\n\t" \
  "buffer_load_dwordx4 %[v1], %[r1], %[so3] offen lds\n\t" \
  "s_add_u32 m0, m0, 0x400\n\t" \
  "ds_read_b128 v[24:27], %[vc]\n\t" \
  "buffer_load_dwordx4 %[v0], %[r1], %[so4] offen lds\n\t" \
  "s_add_u32 m0, m0, 0x400\n\t" \
  "s_nop 0\n\t" \
  "buffer_load_dwordx4 %[v1], %[r1], %[so5] offen lds\n\t" \
  "s_add_u32 m0, m0, 0x400\n\t" \
  "s_nop 0\n\t" \
  "buffer_load_dwordx4 %[v0], %[r1], %[so6] offen lds\n\t" \
  "s_add_u32 m0, m0, 0x400\n\t" \
  "s_waitcnt lgkmcnt(0)\n\t" \
  "v_or3_b32 v24, v24, v25, v26\n\t" \
  "v_or_b32 v24, v24, v27\n\t" \
  "buffer_load_dwordx4 %[v1], %[r1], %[so7] offen lds\n\t" \
  "s_mov_b32 m0, %[mk]\n\t" \
  "s_mov_b64 exec, 3\n\t" \
  "buffer_load_dword %[v0], %[rz], 0 offen lds\n\t" \
  "s_mov_b64 exec, -1\n\t" \
  "v_readfirstlane_b32 %[s0], v24\n\t" \
  "v_add_u32 %[v0], %[s1], %[b0]\n\t" \
  "v_add_u32 %[v1], %[s1], %[b1]\n\t" \
  "s_add_u32 %[mk], %[mk], 16\n\t" \
  "s_cmp_eq_u32 %[n], %[nend]\n\t" \
  "s_cbranch_scc1 Lend_%=\n\t" \
  "s_cmp_eq_u32 %[s0], 0\n\t" \
  "s_cbranch_scc1 Ltop_%=\n\t" \
  "Lsp_%=:\n\t" \
  "ds_read_b128 v[24:27], %[vc]\n\t" \
  "s_sub_i32 %[bud], %[bud], 1\n\t" \
  "s_cmp_lt_i32 %[bud], 0\n\t" \
  "s_cbranch_scc1 Ltop_%=\n\t" \
  "s_waitcnt lgkmcnt(0)\n\t" \
  "v_or3_b32 v24, v24, v25, v26\n\t" \
  "v_or_b32 v24, v24, v27\n\t" \
  "s_nop 0\n\t" \
  "v_readfirstlane_b32 %[s0], v24\n\t" \
  "s_cmp_eq_u32 %[s0], 0\n\t" \
  "s_cbranch_scc1 Ltop_%=\n\t" \
  "s_sleep 1\n\t" \
  "s_branch Lsp_%=\n\t" \
  "Lend_%=:\n\t"

#define OST " sc1"
#define XPRIO0 3
#define XPRIO 1
#define CPRIO 2
#define LPRIO 2
#define FPRIO 1
#define MPRIO 2
#define WGRP (sg & 1)
#define WS0 1
#define WS1 5
#define WS2 9
#define WSLP 8
#define TKG0 3
#define GEXACT 0
#define GCG 0
#define YPRIO 3
#define GPRIO 1
#define GUG 39
#define TKG 4
#define YG 3
#define CGS 14
#define NOFFN 0
#define ROT1 0
#define CODEPF 28928
#define CPULL 1
#define WNT ""
#define PRET 1
constexpr int D_ = 1024, F_ = 3584;
constexpr int MISC = 147456;
constexpr int L_TAB = MISC;
constexpr int L_FLG = L_TAB + 11 * 512;
constexpr int L_ONE = L_FLG + 256;
constexpr int L_G = L_ONE + 1024;
constexpr int L_SEL = L_G + 512;
constexpr int L_BROW = L_SEL + 128;
constexpr int L_ACT = L_BROW + 2048;
constexpr int L_XS = L_ACT + 1024;
constexpr int L_JUNK = L_XS + 4096;
constexpr int LDS_BYTES = 163840;
static_assert(L_JUNK + 768 <= LDS_BYTES, "lds");
constexpr int M_CONS = L_ONE + 16, M_LAND = L_ONE + 544, M_GT = M_LAND + 288, M_HR = M_LAND + 296, M_CAR = M_LAND + 304, M_XS = M_LAND + 320, M_WR = M_LAND + 336;
constexpr int FL_XD = L_FLG + 16, FL_TK = L_FLG + 24;
constexpr unsigned W_EPOCH = 0;
constexpr unsigned W_RG = 256;
constexpr unsigned W_R2 = W_RG + 2048 * 8;
constexpr unsigned W_GF = W_R2 + 8192;
constexpr unsigned W_YF = W_GF + 56 * 128;
constexpr unsigned W_HF = 65536;
constexpr unsigned W_AGG = W_HF + 262144;
constexpr unsigned W_GU = W_AGG + 262144;
constexpr unsigned W_YP = W_GU + 56 * 16384;
constexpr unsigned W_H = 4u << 20;
constexpr unsigned W_DBG = 24u << 20;
constexpr unsigned W_END = 25u << 20;
static_assert(W_YF + 4096 <= W_HF && W_YP + 128 * 56 * 256 <= W_H, "ws");
#define WS_BASE(n) (((n) - W_END) & ~(size_t)0x1fffff)
constexpr int SPIN_MAX = 1 << 18;

DEV float bf2f(unsigned v) { return __uint_as_float(v << 16); }
DEV float bfhi(unsigned v) { return __uint_as_float(v & 0xffff0000u); }
DEV u16 f2bf(float f) { return __builtin_bit_cast(u16, (__bf16)f); }
DEV float rbf(float f) { return bf2f(f2bf(f)); }
typedef __bf16 bf16x2_t __attribute__((ext_vector_type(2)));
DEV unsigned pk2(float a, float b) { return __builtin_bit_cast(unsigned, __builtin_convertvector((f32x2){a, b}, bf16x2_t)); }
DEV float fma1(float a, float b, float c) { float d; asm("v_fma_f32 %0, %1, %2, %3" : "=v"(d) : "v"(a), "v"(b), "v"(c)); return d; }
DEV float fnma1(float a, float b, float c) { float d; asm("v_fma_f32 %0, -%1, %2, %3" : "=v"(d) : "v"(a), "v"(b), "v"(c)); return d; }
DEV float mul1(float a, float b) { float d; asm("v_mul_f32 %0, %1, %2" : "=v"(d) : "v"(a), "v"(b)); return d; }
DEV f32x2 cmul(f32x2 a, f32x2 b) { return (f32x2){fnma1(a.y, b.y, mul1(a.x, b.x)), fma1(a.y, b.x, mul1(a.x, b.y))}; }
template <int N> DEV void waitv() { asm volatile("s_waitcnt vmcnt(%0)" ::"n"(N) : "memory"); }
DEV void bar() { asm volatile("s_barrier" ::: "memory"); }
DEV unsigned lds_rd32(unsigned a) { unsigned v; asm volatile("ds_read_b32 %0, %1\n\ts_waitcnt lgkmcnt(0)" : "=v"(v) : "v"(a) : "memory"); return v; }
DEV u32x2 lds_rd64(unsigned a) { u32x2 v; asm volatile("ds_read_b64 %0, %1\n\ts_waitcnt lgkmcnt(0)" : "=v"(v) : "v"(a) : "memory"); return v; }
DEV u32x4 lds_rd128(unsigned a) { u32x4 v; asm volatile("ds_read_b128 %0, %1\n\ts_waitcnt lgkmcnt(0)" : "=v"(v) : "v"(a) : "memory"); return v; }
DEV void lds_wr32(unsigned a, unsigned v) { asm volatile("ds_write_b32 %0, %1" ::"v"(a), "v"(v) : "memory"); }
DEV void lds_wait_ge(unsigned a, unsigned need) {
#pragma unroll 1
  for (int it = 0; it < SPIN_MAX * 4; ++it) { if (__builtin_amdgcn_readfirstlane(lds_rd32(a)) >= need) break; __builtin_amdgcn_s_sleep(1); }
}
DEV void lds_wait2(unsigned a) {
#pragma unroll 1
  for (int it = 0; it < SPIN_MAX * 4; ++it) { const u32x2 v = lds_rd64(a); if (__builtin_amdgcn_readfirstlane(v.x & v.y)) break; __builtin_amdgcn_s_sleep(1); }
}
DEV void lds_wait0(unsigned a) {
#pragma unroll 1
  for (int it = 0; it < SPIN_MAX * 4; ++it) { if (__builtin_amdgcn_readfirstlane(lds_rd32(a)) == 0u) break; __builtin_amdgcn_s_sleep(1); }
}
typedef __amdgpu_buffer_rsrc_t rsrc_t;
DEV void mark(rsrc_t rz, unsigned a) {
  if ((threadIdx.x & 63) == 0) __builtin_amdgcn_raw_ptr_buffer_load_lds(rz, (LAS void*)(size_t)a, 4, 0, 0, 0, 0);
}
DEV u32x4 ld128(rsrc_t r, unsigned vo, unsigned so = 0) { return __builtin_amdgcn_raw_buffer_load_b128(r, vo, so, 0); }
#define FRESH() asm volatile("" ::: "memory")
DEV u32x4 ld128c(rsrc_t r, unsigned vo) { return __builtin_amdgcn_raw_buffer_load_b128(r, vo, 0, 16); }
DEV void st128c(rsrc_t r, unsigned vo, u32x4 v) {
  asm volatile("s_nop 4\n\tbuffer_store_dwordx4 %0, %1, %2, 0 offen sc1\n\ts_nop 7\n\ts_nop 7" ::"v"(v), "v"(vo), "s"(r) : "memory");
}
DEV void st64c(rsrc_t r, unsigned vo, u32x2 v) { __builtin_amdgcn_raw_buffer_store_b64(v, r, vo, 0, 16); }
DEV void ld8(u32x4 (&w)[8], rsrc_t r, unsigned vo, unsigned so, unsigned step) {
  asm volatile("s_nop 4\n\tbuffer_load_dwordx4 %0, %9, %10, %8 offen\n\ts_add_u32 %8, %8, %11\n\tbuffer_load_dwordx4 %1, %9, %10, %8 offen\n\ts_add_u32 %8, %8, %11\n\t"
               "buffer_load_dwordx4 %2, %9, %10, %8 offen\n\ts_add_u32 %8, %8, %11\n\tbuffer_load_dwordx4 %3, %9, %10, %8 offen\n\ts_add_u32 %8, %8, %11\n\t"
               "buffer_load_dwordx4 %4, %9, %10, %8 offen\n\ts_add_u32 %8, %8, %11\n\tbuffer_load_dwordx4 %5, %9, %10, %8 offen\n\ts_add_u32 %8, %8, %11\n\t"
               "buffer_load_dwordx4 %6, %9, %10, %8 offen\n\ts_add_u32 %8, %8, %11\n\tbuffer_load_dwordx4 %7, %9, %10, %8 offen"
               : "=&v"(w[0]), "=&v"(w[1]), "=&v"(w[2]), "=&v"(w[3]), "=&v"(w[4]), "=&v"(w[5]), "=&v"(w[6]), "=&v"(w[7]), "+&s"(so)
               : "v"(vo), "s"(r), "s"(step) : "memory", "scc");
}
DEV void ld8c(u32x4 (&w)[8], rsrc_t r, unsigned vo, unsigned so, unsigned step, int a, int b) {
  asm volatile("s_nop 4\n\ts_cmp_eq_u32 %12, %13\n\ts_cbranch_scc0 Lskip_%=\n\t"
               "buffer_load_dwordx4 %0, %9, %10, %8 offen" WNT "\n\ts_add_u32 %8, %8, %11\n\tbuffer_load_dwordx4 %1, %9, %10, %8 offen" WNT "\n\ts_add_u32 %8, %8, %11\n\t"
               "buffer_load_dwordx4 %2, %9, %10, %8 offen" WNT "\n\ts_add_u32 %8, %8, %11\n\tbuffer_load_dwordx4 %3, %9, %10, %8 offen" WNT "\n\ts_add_u32 %8, %8, %11\n\t"
               "buffer_load_dwordx4 %4, %9, %10, %8 offen" WNT "\n\ts_add_u32 %8, %8, %11\n\tbuffer_load_dwordx4 %5, %9, %10, %8 offen" WNT "\n\ts_add_u32 %8, %8, %11\n\t"
               "buffer_load_dwordx4 %6, %9, %10, %8 offen" WNT "\n\ts_add_u32 %8, %8, %11\n\tbuffer_load_dwordx4 %7, %9, %10, %8 offen" WNT "\n\tLskip_%=:"
               : "+&v"(w[0]), "+&v"(w[1]), "+&v"(w[2]), "+&v"(w[3]), "+&v"(w[4]), "+&v"(w[5]), "+&v"(w[6]), "+&v"(w[7]), "+&s"(so)
               : "v"(vo), "s"(r), "s"(step), "s"(a), "n"(b) : "memory", "scc");
}
DEV rsrc_t mk(const void* p, unsigned bytes) { return __builtin_amdgcn_make_buffer_rsrc((void*)p, 0, bytes, 0x00020000); }

DEV unsigned wave_maxu(unsigned v) {
  unsigned r;
  asm("s_nop 1\n\tv_max_u32_dpp %1, %1, %1 row_shr:1 row_mask:0xf bank_mask:0xf\n\ts_nop 1\n\tv_max_u32_dpp %1, %1, %1 row_shr:2 row_mask:0xf bank_mask:0xf\n\t"
      "s_nop 1\n\tv_max_u32_dpp %1, %1, %1 row_shr:4 row_mask:0xf bank_mask:0xf\n\ts_nop 1\n\tv_max_u32_dpp %1, %1, %1 row_shr:8 row_mask:0xf bank_mask:0xf\n\t"
      "s_nop 1\n\tv_max_u32_dpp %1, %1, %1 row_bcast:15 row_mask:0xa bank_mask:0xf\n\ts_nop 1\n\tv_max_u32_dpp %1, %1, %1 row_bcast:31 row_mask:0xc bank_mask:0xf\n\t"
      "s_nop 0\n\tv_readlane_b32 %0, %1, 63\n\ts_nop 1" : "=s"(r), "+v"(v));
  return r;
}
DEV float sum4(const float (&a)[4]) {
  auto p = __builtin_amdgcn_permlane16_swap(__float_as_uint(a[0]), __float_as_uint(a[1]), false, false);
  auto q = __builtin_amdgcn_permlane16_swap(__float_as_uint(a[2]), __float_as_uint(a[3]), false, false);
  const float u = __uint_as_float(p[0]) + __uint_as_float(p[1]), w = __uint_as_float(q[0]) + __uint_as_float(q[1]);
  auto r = __builtin_amdgcn_permlane32_swap(__float_as_uint(u), __float_as_uint(w), false, false);
  float v = __uint_as_float(r[0]) + __uint_as_float(r[1]);
  v += __uint_as_float((unsigned)__builtin_amdgcn_update_dpp(0, (int)__float_as_uint(v), 0x128, 0xf, 0xf, false));
  v += __uint_as_float((unsigned)__builtin_amdgcn_update_dpp(0, (int)__float_as_uint(v), 0x124, 0xf, 0xf, false));
  v += __uint_as_float((unsigned)__builtin_amdgcn_update_dpp(0, (int)__float_as_uint(v), 0x122, 0xf, 0xf, false));
  v += __uint_as_float((unsigned)__builtin_amdgcn_update_dpp(0, (int)__float_as_uint(v), 0x121, 0xf, 0xf, false));
  return v;
}
DEV float dot2(unsigned a, unsigned b, float c) { return __builtin_amdgcn_fdot2_f32_bf16(__builtin_bit_cast(bf16x2_t, a), __builtin_bit_cast(bf16x2_t, b), c, false); }
DEV unsigned route_code(unsigned u) { return (u & 0x8000u) ? (~u & 0xffffu) : (u | 0x8000u); }
DEV unsigned med3u(unsigned a, unsigned b, unsigned c) { unsigned d; asm("v_med3_u32 %0, %1, %2, %3" : "=v"(d) : "v"(a), "v"(b), "v"(c)); return d; }
DEV float key_val(unsigned key) {
  unsigned u = key >> 16;
  u = (u & 0x8000u) ? (u & 0x7fffu) : (~u & 0xffffu);
  return __uint_as_float(u << 16);
}
#define STAMP(k)
#define STAMPS()

DEV void unit_mma(const u32x4 (&w)[8], bf16x8 a, f32x4 (&d)[8]) {
#pragma unroll
  for (int j = 0; j < 8; ++j) {
    u32x4 b;
#pragma unroll
    for (int t = 0; t < 4; ++t) b[t] = __builtin_amdgcn_perm(w[2 * t + 1][j >> 1], w[2 * t][j >> 1], (j & 1) ? 0x07060302u : 0x05040100u);
    d[j] = __builtin_amdgcn_mfma_f32_16x16x32_bf16(a, __builtin_bit_cast(bf16x8, b), d[j], 0, 0, 0);
  }
}

DEV void fold(const f32x4 (&d)[8], f32x4 (&o)[2]) {
#pragma unroll
  for (int r = 0; r < 2; ++r)
#pragma unroll
    for (int q = 0; q < 4; ++q) {
      const float a0 = d[q][r], a1 = d[q][r + 2], b0 = d[q + 4][r], b1 = d[q + 4][r + 2];
      auto a = __builtin_amdgcn_permlane32_swap(__float_as_uint(a0), __float_as_uint(a1), false, false);
      auto b = __builtin_amdgcn_permlane32_swap(__float_as_uint(b0), __float_as_uint(b1), false, false);
      float t;
      asm("s_nop 1\n\tv_add_f32_dpp %0, %1, %2 row_ror:8 row_mask:0xf bank_mask:0x3\n\tv_add_f32_dpp %0, %3, %4 row_ror:8 row_mask:0xf bank_mask:0xc"
          : "=&v"(t) : "v"(a[1]), "v"(a[0]), "v"(b[0]), "v"(b[1]));
      o[r][q] = t;
    }
}
DEV void st2x128c(rsrc_t r, unsigned vo, unsigned so, const f32x4 (&o)[2]) {
  asm volatile("s_nop 4\n\tbuffer_store_dwordx4 %0, %2, %3, 0 offen sc1\n\tbuffer_store_dwordx4 %1, %2, %3, %4 offen sc1\n\ts_nop 7\n\ts_nop 7"
               ::"v"(o[0]), "v"(o[1]), "v"(vo), "s"(r), "s"(so) : "memory");
}
DEV void poll32(rsrc_t r, unsigned vo, unsigned tg, bool on) {
#pragma unroll 1
  for (int it = 0; it < SPIN_MAX; ++it) {
    FRESH();
    const unsigned v = __builtin_amdgcn_raw_buffer_load_b32(r, vo, 0, 16);
    if (__all(!on || v == tg)) break;
    __builtin_amdgcn_s_sleep(16);
  }
}

typedef __attribute__((ext_vector_type(8))) u64 u64x8;
__global__ __launch_bounds__(512, 2) void fused(const u16*, const u16*, const u16*, const u16*, const u16*, const u16*, const u16*, const u16*, const u16*,
                                                const u16*, const u16*, const u16*, const u16*, const u16*, char*, float*) {
#if __HIP_DEVICE_COMPILE__
  extern __shared__ __attribute__((aligned(16))) char smem_[];
  LAS char* smem = (LAS char*)smem_;
#define ARGREQ(W) u64x8 ka, kb; \
  asm volatile("s_load_dwordx16 %0, %2, 0x0\n\ts_load_dwordx16 %1, %2, 0x40" W : "=&s"(ka), "=&s"(kb) : "s"(__builtin_amdgcn_kernarg_segment_ptr()) : "memory")
#define ARGS() \
  const u16 *x = (const u16*)ka[0], *Lre = (const u16*)ka[1], *Lim = (const u16*)ka[2], *Bre = (const u16*)ka[3], *Bim = (const u16*)ka[4], *Cre = (const u16*)ka[5], \
            *Cim = (const u16*)ka[6], *logdt = (const u16*)ka[7], *Dskip = (const u16*)kb[0], *w_r = (const u16*)kb[1], *w_gate = (const u16*)kb[2], \
            *w_up = (const u16*)kb[3], *w_down = (const u16*)kb[4], *gate_w = (const u16*)kb[5]; \
  char* ws = (char*)kb[6]; float* out = (float*)kb[7]; \
  const rsrc_t rws = mk(ws, W_END), rx = mk(x, 2048 * 1024 * 2), rz = mk(x, 0)
  STAMP(0);
  const int tid = threadIdx.x, lane = tid & 63, wid = __builtin_amdgcn_readfirstlane(tid >> 6);
  const int bid = blockIdx.x, xcd = bid & 7, loc = bid >> 3;
  const int c = (xcd >> 1) * 4 + (loc >> 3), j = (xcd & 1) * 8 + (loc & 7);
  const int fr = lane & 15, fq = lane >> 4;

  if (__builtin_expect(wid == 4 || wid == 5, 1)) {
    ARGREQ("");
    const int l = wid - 4;
    if ((loc & 3) == l) {
      u64 pc;
      asm volatile("s_getpc_b64 %0" : "=s"(pc));
      const rsrc_t rc = mk((const void*)(pc & ~0xffull), CODEPF);
      __builtin_amdgcn_raw_ptr_buffer_load_lds(rc, (LAS void*)(smem + L_JUNK), 4, ((loc >> 1) & 6) * 8192 + l * 8192 + lane * 128, 0, 0, 0);
    }
    __builtin_amdgcn_s_setprio(LPRIO);
    const int l3 = lane >> 3;
    {
      const unsigned one = tid >= 272 ? 1u : 0u;
      asm volatile("ds_write_b128 %0, %1" ::"v"(L_FLG + min(tid - 256, 111) * 16), "v"((u32x4){one, one, one, one}) : "memory");
    }
    int bud = SPIN_MAX * 4;
    unsigned dst = 131072 - l * 114688, ci = M_CONS - 64, t_s0, vc = 0;
    unsigned b0 = (c * 128 + l * (j * 64 - c * 128) + l3 * (8 - 7 * l)) * 2048 + ((lane & 7) ^ l3) * 16, wrap = 163840 - l * 32768;
    unsigned so[8];
#pragma unroll
    for (int q = 0; q < 8; ++q) so[q] = (64 * (q & 1) + (q >> 1) + l * (8 * q - 64 * (q & 1) - (q >> 1))) * 2048;
    unsigned sk = ROT1 ? ((c + j) & 15) * 128 : (((xcd >> 1) + 2 * (xcd & 1)) & 3) * 512, n = 16, mka = M_LAND + l * 4, v0 = b0 + sk;
    asm volatile("" : "+s"(dst), "+v"(b0), "+s"(wrap), "+s"(so[1]), "+s"(so[2]), "+s"(so[3]), "+s"(so[4]), "+s"(so[5]), "+s"(so[6]), "+s"(so[7]),
                 "+s"(sk), "+s"(mka), "+v"(v0));
    asm volatile("s_waitcnt lgkmcnt(0)" : "+s"(ka), "+s"(kb)::"memory");
    ARGS();
    {
      const rsrc_t r0 = mk(l ? Bre : x, (1u << 22) >> l), r1 = mk(l ? Bim : x + 4096, l ? (1u << 21) : (1u << 22) - 8192);
      STAMP(8);
      asm volatile(ASM_LA : [dst] "+&s"(dst), [sk] "+&s"(sk), [ci] "+&s"(ci), [n] "+&s"(n), [mk] "+&s"(mka), [bud] "+&s"(bud), [s0] "=&s"(t_s0), [v0] "+&v"(v0), [vc] "+&v"(vc) : [r0] "s"(r0), [r1] "s"(r1), [rz] "s"(rz), [wrap] "s"(wrap), [b0] "v"(b0), [vw] "v"(M_WR), [so0] "s"(so[0]), [so1] "s"(so[1]), [so2] "s"(so[2]), [so3] "s"(so[3]), [so4] "s"(so[4]), [so5] "s"(so[5]), [so6] "s"(so[6]), [so7] "s"(so[7]) : "memory", "scc", "m0", "v24", "v25", "v26", "v27", "v28", "v29", "v30", "v31");
    }
    STAMP(1);
    {
      const rsrc_t r0 = mk(Cre, 1 << 21), r1 = mk(Cim, 1 << 21);
      const unsigned b0 = (j * 64 + 4 * l3) * 2048 + (((lane & 7) ^ (l3 >> 1)) * 16), b1 = b0 ^ 64, wrap = 147456, cth = M_CONS + (15 - l) * 16;
      unsigned so[8];
#pragma unroll
      for (int q = 0; q < 8; ++q) so[q] = (32 * (q & 1) + (q >> 1)) * 2048;
      unsigned sk = l * 128, hi = (j & 8) * 128, n = 8, mka = M_LAND + (17 + l) * 8, v0 = b0 + sk + hi, v1 = b1 + sk + hi, t_s1;
      const rsrc_t rp = mk(l ? Cim : Cre, 1 << 21);
      const unsigned p0 = b0 + (hi ^ 0x400);
      unsigned vd = l * 8192 + lane * 16;
      asm volatile(ASM_LB : [dst] "+&s"(dst), [sk] "+&s"(sk), [ci] "+&s"(ci), [n] "+&s"(n), [mk] "+&s"(mka), [bud] "+&s"(bud), [s0] "=&s"(t_s0), [v0] "+&v"(v0), [vc] "+&v"(vc), [v1] "+&v"(v1), [hi] "+&s"(hi), [s1] "=&s"(t_s1), [vd] "+&v"(vd) : [r0] "s"(r0), [r1] "s"(r1), [rz] "s"(rz), [wrap] "s"(wrap), [b0] "v"(b0), [cth] "s"(cth), [b1] "v"(b1), [nend] "s"(0), [rp] "s"(rp), [p0] "v"(p0), [p1] "v"(p0 ^ 64), [vm] "v"(M_CONS + 256), [vl] "v"(M_LAND + 26 * 8 + 4 * l), [so0] "s"(so[0]), [so1] "s"(so[1]), [so2] "s"(so[2]), [so3] "s"(so[3]), [so4] "s"(so[4]), [so5] "s"(so[5]), [so6] "s"(so[6]), [so7] "s"(so[7]) : "memory", "scc", "m0", "v24", "v25", "v26", "v27", "v28", "v29", "v30", "v31");
    }
    waitv<0>();
    STAMP(3);
    STAMPS();
    __builtin_amdgcn_endpgm();
  }

  if (__builtin_expect(wid >= 6, 1)) {
    const int xw = wid - 6;
    __builtin_amdgcn_s_setprio(XPRIO0);
    ARGREQ("\n\ts_waitcnt lgkmcnt(0)\n\ts_barrier");
    ARGS();
    const int row0 = c * 128 + j * 8 + xw * 4;
    const int l = xw;
    const bool ffn = !NOFFN && loc < 28;
    const int sg = xcd * 7 + (loc >> 2), kq = loc & 3;
    u32x4 w1[4][8], w2[2][8];
    unsigned tagA, tagB;
    const int grp = __builtin_amdgcn_readfirstlane(!ffn ? 9 : WGRP);
    auto wburst = [&](int p, unsigned gate) __attribute__((always_inline)) {
      const bool on = grp == p;
      const rsrc_t r1 = mk(xw ? w_up : w_gate, D_ * F_ * 2), r2 = mk(w_down, D_ * F_ * 2);
      const unsigned o1 = (kq * 256 + 32 * (fr >> 3) + 8 * fq) * (F_ * 2) + (sg * 64 + 8 * (fr & 7)) * 2;
      const unsigned o2 = (sg * 64 + 32 * (fr >> 3) + 8 * fq) * 2048 + (kq * 256 + xw * 128 + 8 * (fr & 7)) * 2;
      if (on) {
        lds_wait0(M_LAND + gate * 8);
        mark(rz, M_WR + xw * 4);
        __builtin_amdgcn_s_sleep(WSLP);
      }
#pragma unroll
      for (int u = 0; u < 4; ++u) ld8c(w1[u], r1, o1, u * 64 * (F_ * 2), F_ * 2, grp, p);
      if (on) mark(rz, M_WR + 8 + xw * 4);
#pragma unroll
      for (int u = 0; u < 2; ++u) ld8c(w2[u], r2, o2, u * 128, 2048, grp, p);
      if (on || (p == 0 && !ffn))
        __builtin_amdgcn_raw_ptr_buffer_load_lds(mk(xw ? Cim : Cre, 1 << 21), (LAS void*)(smem + L_JUNK + 256 + xw * 256), 4, (j * 64 + lane) * 2048 + c * 128, 0, 0, 0);
    };
    {
      unsigned ep;
      u32x4 xv[8], wv[2], gv[2];
      unsigned pl[3];
      const rsrc_t rw = mk(w_r, 2048), rg = mk(gate_w, 2048);
      const rsrc_t rp0 = mk(Lre, 2048), rp1 = mk(Lim, 2048), rp2 = mk(logdt, 2048);
      const unsigned zero = 0, l16 = lane * 16, xo = row0 * 2048 + lane * 16, po = (j * 64 + lane) * 2;
      asm volatile("s_nop 4\n\tbuffer_load_dword %0, %3, %5, 0 offen sc1\n\tbuffer_load_dwordx4 %1, %4, %6, 0 offen\n\tbuffer_load_dwordx4 %2, %4, %6, 0 offen offset:1024"
                   : "=&v"(ep), "=&v"(wv[0]), "=&v"(wv[1]) : "v"(zero), "v"(l16), "s"(rws), "s"(rw) : "memory");
      ld8(xv, rx, xo, 0, 1024);
      asm volatile("s_nop 4\n\tbuffer_load_dwordx4 %3, %9, %10, 0 offen\n\tbuffer_load_dwordx4 %4, %9, %10, 0 offen offset:1024\n\t"
                   "buffer_load_ushort %0, %5, %6, 0 offen\n\tbuffer_load_ushort %1, %5, %7, 0 offen\n\tbuffer_load_ushort %2, %5, %8, 0 offen"
                   : "=&v"(pl[0]), "=&v"(pl[1]), "=&v"(pl[2]), "=&v"(gv[0]), "=&v"(gv[1]) : "v"(po), "s"(rp0), "s"(rp1), "s"(rp2), "v"(l16), "s"(rg) : "memory");
      asm volatile("s_waitcnt vmcnt(0)" : "+v"(ep), "+v"(xv[0]), "+v"(xv[1]), "+v"(xv[2]), "+v"(xv[3]), "+v"(xv[4]), "+v"(xv[5]), "+v"(xv[6]),
                   "+v"(xv[7]), "+v"(wv[0]), "+v"(wv[1]), "+v"(gv[0]), "+v"(gv[1]), "+v"(pl[0]), "+v"(pl[1]), "+v"(pl[2])::"memory");
      tagA = ep + 1u; tagB = ep + 0x40000001u;
      float sr[4];
#pragma unroll
      for (int r = 0; r < 4; ++r) {
        sr[r] = 0.f;
#pragma unroll
        for (int h = 0; h < 2; ++h)
#pragma unroll
          for (int e = 0; e < 4; ++e) { const unsigned xu = xv[2 * r + h][e], wu = wv[h][e]; sr[r] = dot2(xu, wu, sr[r]); }
      }
      {
        const unsigned rb = route_code(f2bf(sum4(sr)));
        const unsigned r01 = (unsigned)__builtin_amdgcn_readlane((int)rb, 0) | ((unsigned)__builtin_amdgcn_readlane((int)rb, 16) << 16);
        const unsigned r23 = (unsigned)__builtin_amdgcn_readlane((int)rb, 32) | ((unsigned)__builtin_amdgcn_readlane((int)rb, 48) << 16);
        st128c(rws, lane == 0 ? W_R2 + row0 * 4 : 0xf0000000u, (u32x4){r01, tagA, r23, tagB});
      }
#pragma unroll
      for (int r = 0; r < 4; ++r) {
        sr[r] = 0.f;
#pragma unroll
        for (int h = 0; h < 2; ++h)
#pragma unroll
          for (int e = 0; e < 4; ++e) {
            const unsigned xu = xv[2 * r + h][e], gu = gv[h][e];
            sr[r] = dot2(xu, gu, sr[r]);
          }
      }
      {
        const float z = rbf(sum4(sr));
        st64c(rws, fr == 0 ? W_RG + (row0 + fq) * 8 : 0xf0000000u, (u32x2){__float_as_uint(rbf(1.f / (1.f + __expf(-z)))), tagB});
      }
      STAMP(8);
      __builtin_amdgcn_s_setprio(XPRIO);
#pragma unroll
      for (int u = 0; u < 4; ++u) asm volatile("" : "=v"(w1[u][0]), "=v"(w1[u][1]), "=v"(w1[u][2]), "=v"(w1[u][3]), "=v"(w1[u][4]), "=v"(w1[u][5]), "=v"(w1[u][6]), "=v"(w1[u][7]));
#pragma unroll
      for (int u = 0; u < 2; ++u) asm volatile("" : "=v"(w2[u][0]), "=v"(w2[u][1]), "=v"(w2[u][2]), "=v"(w2[u][3]), "=v"(w2[u][4]), "=v"(w2[u][5]), "=v"(w2[u][6]), "=v"(w2[u][7]));
      wburst(0, WS0);
      if (xw == 1) {
        const float lre = bf2f(pl[0]), lim = bf2f(pl[1]);
        const float dt = rbf(__expf(bf2f(pl[2])));
        const float er = expf(dt * lre);
        float sn, cs;
        sincosf(dt * lim, &sn, &cs);
        const f32x2 l1 = {mul1(er, cs), mul1(er, sn)};
        const float nr = l1.x - 1.f, ni = l1.y, den = fma1(lre, lre, mul1(lim, lim));
        const f32x2 cf = {fma1(nr, lre, mul1(ni, lim)) / den, fnma1(nr, lim, mul1(ni, lre)) / den};
        const f32x2 l2 = cmul(l1, l1), l4 = cmul(l2, l2), l8 = cmul(l4, l4), l16_ = cmul(l8, l8), l32 = cmul(l16_, l16_), l64 = cmul(l32, l32),
                    l128 = cmul(l64, l64), l256 = cmul(l128, l128), l384 = cmul(l256, l128), l512 = cmul(l256, l256);
        auto put = [&](int e, f32x2 v) { const float re = v.x, im = v.y; asm volatile("ds_write2_b32 %0, %1, %2 offset0:0 offset1:64" ::"v"(L_TAB + e * 512 + lane * 4), "v"(re), "v"(im) : "memory"); };
        put(0, l1); put(1, cf); put(2, l8); put(3, l16_); put(4, l32); put(5, l64); put(6, l512); put(7, (f32x2){1.f, 0.f}); put(8, l128); put(9, l256); put(10, l384);
        asm volatile("ds_write_b64 %0, %1" ::"v"(M_LAND + 16 * 8), "v"((u32x2){0u, 0u}) : "memory");
      }
    }
    unsigned m = 0, m2 = 0, m3 = 0;
    auto gkey = [&](unsigned k) { return (k & 0xffff0000u) | ((k & 28u) << 6) | (k & 3u) | (unsigned)(252 - lane * 4); };
    lds_wait0(M_LAND + (grp == 0 ? TKG0 : TKG) * 8);
    if (xw == 1) {
#pragma unroll
      for (int h = 0; h < 2; ++h) {
        poll32(rws, W_RG + (c * 128 + h * 64 + lane) * 8 + 4, tagB, true);
        __builtin_amdgcn_raw_ptr_buffer_load_lds(rws, (LAS void*)(smem + L_G + h * 256), 4, W_RG + (c * 128 + h * 64 + lane) * 8, 0, 0, 16);
      }
      mark(rz, M_GT);
      STAMP(1);
    } else {
      {
        u32x4 g[8];
#pragma unroll 1
        for (int it = 0; it < SPIN_MAX; ++it) {
          FRESH();
#pragma unroll
          for (int q = 0; q < 8; ++q) g[q] = ld128c(rws, W_R2 + lane * 16 + q * 1024);
          bool ok = true;
#pragma unroll
          for (int q = 0; q < 8; ++q) ok = ok && g[q].y == tagA && g[q].w == tagB;
          if (__all(ok)) break;
          __builtin_amdgcn_s_sleep(16);
        }
        STAMP(1);
        auto lkey = [&](int i) { const unsigned w = g[i >> 2][i & 2]; return ((i & 1) ? (w & 0xffff0000u) : (w << 16)) | (unsigned)(31 - i); };
        m = lkey(0); m2 = 0; m3 = 0;
#pragma unroll
        for (int i = 1; i < 32; ++i) {
          const unsigned k = lkey(i);
          m3 = med3u(m2, m3, k); m2 = med3u(m, m2, k); m = max(m, k);
        }
      }
      m = gkey(m); m2 = gkey(m2); m3 = gkey(m3);
    }
    asm volatile("" : "+v"(m), "+v"(m2), "+v"(m3));
    wburst(1, WS1);
    if (xw == 0) {
      unsigned mine = 0;
#pragma unroll 1
      for (int s = 0; s < 8; ++s) {
        const unsigned mm = wave_maxu(m);
        mine = (lane >> 3) == s ? mm : mine;
        const bool hit = m == mm;
        m = hit ? m2 : m; m2 = hit ? m3 : m2; m3 = hit ? 0u : m3;
      }
      if (__ballot(m == 0u)) {
        unsigned lim = 0xffffffffu;
#pragma unroll 1
        for (int s = 0; s < 8; ++s) {
          unsigned mm = 0;
#pragma unroll 1
          for (int q = 0; q < 8; ++q) {
            const u32x4 w = ld128c(rws, W_R2 + lane * 16 + q * 1024);
#pragma unroll
            for (int e = 0; e < 4; ++e) {
              const unsigned k = gkey(((e & 1) ? (w[e & 2] & 0xffff0000u) : (w[e & 2] << 16)) | (unsigned)(31 - 4 * q - e));
              mm = max(mm, k < lim || s == 0 ? k : 0u);
            }
          }
          lim = wave_maxu(mm);
          mine = (lane >> 3) == s ? lim : mine;
        }
      }
      const unsigned myidx = 2047u - (mine & 0xffffu);
      if (loc < 28) {
#pragma unroll
        for (int u = 0; u < 4; ++u)
          __builtin_amdgcn_raw_ptr_buffer_load_lds(rx, (LAS void*)(smem + L_XS + u * 1024), 16, myidx * 2048 + (loc & 3) * 512 + (lane & 7) * 16, u * 128, 0, 0);
      }
      const float myrs = rbf(1.f / (1.f + __expf(-key_val(mine))));
      if ((lane & 7) == 0) asm volatile("ds_write_b64 %0, %1\n\ts_waitcnt lgkmcnt(0)" ::"v"(L_SEL + lane), "v"((u32x2){myidx, __float_as_uint(myrs)}) : "memory");
      mark(rz, M_XS);
    }
    STAMP(2);
    wburst(2, WS2);
    STAMP(7);
    __builtin_amdgcn_s_setprio(FPRIO);
    lds_wait0(M_XS);
    const u32x2 sel = lds_rd64(L_SEL + (lane & 7) * 8);
    const unsigned tg = (lane & 1) ? tagB : tagA;
    const unsigned fa = ((fr & 7) * 8 + 4 * (fr >> 3) + fq) * 16, lp = (fr & 7) * 32 + (fr >> 3) * 16, tk = 4 * (fq & 1) + 2 * (fq >> 1);
    if (ffn) {
      f32x4 d[8], o[2];
      u32x4 xa[4];
#pragma unroll
      for (int u = 0; u < 4; ++u) xa[u] = *(const LAS u32x4*)(smem + L_XS + u * 1024 + fa);
#pragma unroll
      for (int q = 0; q < 8; ++q) d[q] = (f32x4){0.f, 0.f, 0.f, 0.f};
      waitv<0>();
      if (GCG) lds_wait0(M_LAND + GCG * 8);
#pragma unroll
      for (int u = 0; u < 4; ++u) {
        asm volatile("" : "+v"(w1[u][0]), "+v"(w1[u][1]), "+v"(w1[u][2]), "+v"(w1[u][3]), "+v"(w1[u][4]), "+v"(w1[u][5]), "+v"(w1[u][6]), "+v"(w1[u][7]));
        unit_mma(w1[u], __builtin_bit_cast(bf16x8, xa[u]), d);
        asm volatile("" : "+v"(d[0]), "+v"(d[1]), "+v"(d[2]), "+v"(d[3]), "+v"(d[4]), "+v"(d[5]), "+v"(d[6]), "+v"(d[7]));
      }
      fold(d, o);
      st2x128c(rws, W_GU + ((sg * 8 + tk) * 8 + kq * 2 + l) * 256 + lp, 2048, o);
      waitv<0>();
      __builtin_amdgcn_raw_buffer_store_b32(tg, rws, lane < 2 ? W_GF + sg * 128 + (kq * 2 + l) * 8 + lane * 4 : 0xf0000000u, 0, 16);
      STAMP(3);
      {
        if (GUG) lds_wait0(M_LAND + GUG * 8);
        __builtin_amdgcn_s_setprio(GPRIO);
        poll32(rws, W_GF + sg * 128 + fr * 4, tg, true);
        f32x4 a[8];
#pragma unroll
        for (int q = 0; q < 8; ++q) a[q] = __builtin_bit_cast(f32x4, ld128c(rws, W_GU + (sg * 8 + 4 * l + fq) * 2048 + fr * 16 + q * 256));
        float a2[4];
#pragma unroll
        for (int e = 0; e < 4; ++e) {
          const float g = rbf((a[0][e] + a[2][e]) + (a[4][e] + a[6][e])), u = rbf((a[1][e] + a[3][e]) + (a[5][e] + a[7][e]));
          a2[e] = rbf(g / (1.f + __expf(-g))) * u;
        }
        asm volatile("ds_write_b64 %0, %1\n\ts_nop 3" ::"v"(L_ACT + (4 * l + fq) * 128 + fr * 8), "v"((u32x2){pk2(a2[0], a2[1]), pk2(a2[2], a2[3])}) : "memory");
        lds_wr32(FL_TK + l * 4, 1u);
        lds_wait_ge(FL_TK + (l ^ 1) * 4, 1u);
      }
      const u32x4 af = lds_rd128(L_ACT + fa);
      STAMP(4);
#pragma unroll
      for (int u = 0; u < 2; ++u)
        asm volatile("" : "+v"(w2[u][0]), "+v"(w2[u][1]), "+v"(w2[u][2]), "+v"(w2[u][3]), "+v"(w2[u][4]), "+v"(w2[u][5]), "+v"(w2[u][6]), "+v"(w2[u][7]));
#pragma unroll
      for (int u = 0; u < 2; ++u) {
#pragma unroll
        for (int q = 0; q < 8; ++q) d[q] = (f32x4){0.f, 0.f, 0.f, 0.f};
        unit_mma(w2[u], __builtin_bit_cast(bf16x8, af), d);
        fold(d, o);
        st2x128c(rws, W_YP + ((tk * 16 + kq * 4 + l * 2 + u) * 56 + sg) * 256 + lp, 16 * 56 * 256, o);
      }
      waitv<0>();
      __builtin_amdgcn_raw_buffer_store_b32(tg, rws, lane < 2 ? W_YF + (kq * 2 + l) * 512 + sg * 8 + lane * 4 : 0xf0000000u, 0, 16);
      STAMP(5);
    }
    u64 rows = __ballot(lane < 8 && (sel.x >> 7) == (unsigned)c);
    if (l) rows &= rows - 1;
    if (!NOFFN && rows) {
      lds_wait0(M_CONS + (16 + YG) * 16);
      __builtin_amdgcn_s_setprio(YPRIO);
#pragma unroll 1
      for (int it = 0; it < SPIN_MAX; ++it) {
        FRESH();
        const u32x2 v = __builtin_amdgcn_raw_buffer_load_b64(rws, W_YF + (j >> 1) * 512 + lane * 8, 0, 16);
        if (__all(lane >= 56 || (v.x == tagA && v.y == tagB))) break;
        __builtin_amdgcn_s_sleep(16);
      }
      STAMP(9);
#pragma unroll 1
      for (; rows; rows &= rows - 1, rows &= rows - 1) {
        const int s = __builtin_ctzll(rows);
        const int t = __builtin_amdgcn_readlane(sel.x, s);
        const float rs = __uint_as_float(__builtin_amdgcn_readlane(sel.y, s));
        const u32x2 xr = __builtin_amdgcn_raw_buffer_load_b64(rx, t * 2048 + j * 128 + fr * 8, 0, 0);
        f32x4 a[14];
#pragma unroll
        for (int k = 0; k < 14; ++k) a[k] = __builtin_bit_cast(f32x4, ld128c(rws, W_YP + (s * 16 + j) * (56 * 256) + lane * 16 + k * 1024));
        const float omg = rbf(1.f - __uint_as_float(lds_rd32(L_G + (t & 127) * 4)));
        f32x4 dl;
#pragma unroll
        for (int e = 0; e < 4; ++e) {
          float y = a[0][e];
#pragma unroll
          for (int k = 1; k < 14; ++k) y += a[k][e];
          { auto p = __builtin_amdgcn_permlane16_swap(__float_as_uint(y), __float_as_uint(y), false, false); y = __uint_as_float(p[0]) + __uint_as_float(p[1]); }
          { auto p = __builtin_amdgcn_permlane32_swap(__float_as_uint(y), __float_as_uint(y), false, false); y = __uint_as_float(p[0]) + __uint_as_float(p[1]); }
          const unsigned xw_ = xr[e >> 1];
          const float xf = (e & 1) ? bfhi(xw_) : bf2f(xw_ & 0xffffu);
          dl[e] = rbf(mul1(omg, rbf(xf + rbf(mul1(rbf(y), rs))))) - rbf(mul1(omg, xf));
        }
        if (lane < 16) asm volatile("ds_write_b128 %0, %1\n\ts_nop 3" ::"v"(L_BROW + s * 256 + fr * 16), "v"(dl) : "memory");
      }
    }
    lds_wr32(FL_XD + l * 4, 1u);
    waitv<0>();
    STAMP(6);
    STAMPS();
    __builtin_amdgcn_endpgm();
  }

  __builtin_amdgcn_s_setprio(MPRIO);
  ARGREQ("\n\ts_waitcnt lgkmcnt(0)");
  ARGS();
  if (wid == 3) {
    const int sg = min(xcd * 7 + (loc >> 2), 55), kq = loc & 3;
    const unsigned wo = kq * (256 * F_) + sg * 64;
    const void* q[14] = {Bim + j * 65536, w_gate + wo, w_gate + wo + 255 * F_, w_up + wo, w_up + wo + 255 * F_, w_down + sg * 65536, Cre + j * 65536, Cim + j * 65536,
                         x + (1 << 20), x + (2 << 20) - 2, ws + W_H - 128, ws + W_H + (c * 64 + 4 * j) * 8192, out + c * 131072 + j * 64, Dskip};
    unsigned lo = (unsigned)(size_t)x, hi = (unsigned)((size_t)x >> 32);
#pragma unroll
    for (int i = 0; i < 14; ++i)
      asm("v_writelane_b32 %0, %2, %4\n\tv_writelane_b32 %1, %3, %4" : "+v"(lo), "+v"(hi) : "s"((unsigned)(size_t)q[i]), "s"((unsigned)((size_t)q[i] >> 32)), "n"(i));
    asm volatile("s_nop 1\n\ts_mov_b64 exec, 0x7fff\n\tglobal_load_dword v229, %0, off\n\ts_mov_b64 exec, -1" ::"v"((u32x2){lo, hi}) : "memory", "v229");
  }
  bar();
  unsigned tagA, tagB;
  int bud = SPIN_MAX * 4;
  const int tl0 = 32 * fq + 2 * wid;
  u32x2 xe[2][4], dv;
  {
    const rsrc_t rd = mk(Dskip, 2048);
    const unsigned dofs = (j * 64 + 4 * fr) * 2, xo = (c * 128 + tl0) * 2048 + dofs;
    const int x0 = (fq ^ (fr & 7)) * 16, x1 = ((4 + fq) ^ (fr & 7)) * 16;
    const unsigned pa = 16384 + (wid * 16 + fr) * 128, pb = 131072 + fr * 128;
    unsigned pa0 = pa + x0, pa1 = pa + x1, pb0 = pb + x0, pb1 = pb + x1;
    const unsigned tq = L_TAB + (wid * 16 + fq * 4) * 4, tcl = L_TAB + 6 * 512 + (wid * 16 + fr) * 4, tcw = tcl + 512 + fq * 512;
    const unsigned l16 = lane * 16, pubo = (fq * 4 + (fr >> 2)) * 16, plo = fr * 16 + (3 - fq) * 1024;
    const unsigned sag = W_AGG + (j * 16 + c) * 1024 + wid * 256;
    unsigned sh = W_H + (c * 64 + 4 * j + wid) * 8192, fl = M_LAND, scn = M_CONS - 16 + wid * 4;
    const unsigned fso = lane < 2 ? W_HF + (c * 2 + ((j >> 3) ^ lane)) * 8192 + lane * 4096 + (4 * (j & 7) + wid) * 128 : 0xf0000000u;
    unsigned sl[4];
    u64 em[4];
#pragma unroll
    for (int q = 0; q < 4; ++q) {
      sl[q] = sag - (4 + 4 * q) * 1024;
      const int n = min(max(c - 4 * q, 0), 4);
      em[q] = n == 4 ? ~0ull : (1ull << (16 * n)) - 1ull;
    }
    unsigned t_sk, t_s0, t_s1, t_s2, t_sx;
    asm volatile(ASM_MA : [pa0] "+&v"(pa0), [pa1] "+&v"(pa1), [pb0] "+&v"(pb0), [pb1] "+&v"(pb1), [fl] "+&v"(fl), [sk] "=&s"(t_sk), [s0] "=&s"(t_s0), [s1] "=&s"(t_s1), [s2] "=&s"(t_s2), [sx] "=&s"(t_sx), [bud] "+&s"(bud), [scn] "+&s"(scn), [sh] "+&s"(sh), [tgA] "=&s"(tagA), [tgB] "=&s"(tagB), [xe00] "=&v"(xe[0][0]), [xe01] "=&v"(xe[0][1]), [xe02] "=&v"(xe[0][2]), [xe03] "=&v"(xe[0][3]), [xe10] "=&v"(xe[1][0]), [xe11] "=&v"(xe[1][1]), [xe12] "=&v"(xe[1][2]), [xe13] "=&v"(xe[1][3]), [dv] "=&v"(dv) : [tq] "v"(tq), [tcl] "v"(tcl), [tcw] "v"(tcw), [pubo] "v"(pubo), [plo] "v"(plo), [l16] "v"(l16), [xo] "v"(xo), [dofs] "v"(dofs), [fso] "v"(fso), [wrap] "s"(-98304), [wrap2] "s"(-131072), [srd] "s"(rws), [srx] "s"(rx), [srdd] "s"(rd), [srz] "s"(rz), [sag] "s"(sag), [mcar] "s"(M_CAR), [sl0] "s"(sl[0]), [em0] "s"(em[0]), [sl1] "s"(sl[1]), [em1] "s"(em[1]), [sl2] "s"(sl[2]), [em2] "s"(em[2]), [sl3] "s"(sl[3]), [em3] "s"(em[3]) : "memory", "scc", "vcc", "m0", "v64", "v65", "v66", "v67", "v68", "v69", "v70", "v71", "v72", "v73", "v74", "v75", "v76", "v77", "v78", "v79", "v80", "v81", "v82", "v83", "v84", "v85", "v86", "v87", "v88", "v89", "v90", "v91", "v92", "v93", "v94", "v95", "v96", "v97", "v98", "v99", "v100", "v101", "v102", "v103", "v104", "v105", "v106", "v107", "v108", "v109", "v110", "v111", "v112", "v113", "v114", "v115", "v116", "v117", "v118", "v119", "v120", "v121", "v122", "v123", "v124", "v125", "v126", "v127", "v128", "v129", "v130", "v131", "v132", "v133", "v134", "v135", "v136", "v137", "v138", "v139", "v140", "v141", "v142", "v143", "v144", "v145", "v146", "v147", "v148", "v149", "v150", "v151", "v152", "v153", "v154", "v155", "v156", "v157", "v158", "v159", "v160", "v161", "v162", "v163", "v164", "v165", "v166", "v167", "v168", "v169", "v170", "v171", "v172", "v173", "v174", "v175", "v176", "v177", "v178", "v179", "v180", "v181", "v182", "v183", "v184", "v185", "v186", "v187", "v188", "v189", "v190", "v191", "v192", "v193", "v194", "v195", "v196", "v197", "v198", "v199", "v200", "v201", "v202", "v203", "v204", "v205", "v206", "v207", "v208", "v209", "v210", "v211", "v212", "v213", "v214", "v215", "v216", "v217", "v218", "v219", "v220", "v221", "v222", "v223", "v224", "v225", "v226", "v227", "v228", "v229", "v230", "v231", "v232", "v233", "v234", "v235", "v236", "v237", "v238", "v239", "v240", "v241", "v242", "v243", "v244", "v245", "v246", "v247", "v248", "v249", "v250", "v251", "v252", "v253", "v254", "v255");
  }
  STAMP(9);
  float ge[2][4];
  f32x4 base[2][4];
  lds_wait0(M_GT); lds_wait0(M_XS);
  const unsigned selu = lds_rd32(L_SEL + (lane & 7) * 8);
  u64 mine = __ballot(lane < 8 && (selu >> 7) == (unsigned)c && ((selu >> 1) & 3u) == (unsigned)wid);
#pragma unroll
  for (int r = 0; r < 4; ++r) {
    const f32x2 g2 = *(const LAS f32x2*)(smem + L_G + (tl0 + 8 * r) * 4);
    ge[0][r] = g2.x; ge[1][r] = g2.y;
  }
#pragma unroll
  for (int e = 0; e < 2; ++e)
#pragma unroll
    for (int r = 0; r < 4; ++r) {
      const float omg = rbf(1.f - ge[e][r]);
#pragma unroll
      for (int n = 0; n < 4; ++n) {
        const unsigned xw_ = xe[e][r][n >> 1], dw_ = dv[n >> 1];
        const float xf = (n & 1) ? bfhi(xw_) : bf2f(xw_ & 0xffffu), df = (n & 1) ? bfhi(dw_) : bf2f(dw_ & 0xffffu);
        base[e][r][n] = fma1(ge[e][r], rbf(xf * df), rbf(omg * xf));
      }
    }
  asm volatile("" : "+v"(base[0][0]), "+v"(base[0][1]), "+v"(base[0][2]), "+v"(base[0][3]), "+v"(base[1][0]), "+v"(base[1][1]), "+v"(base[1][2]), "+v"(base[1][3]));
  f32x4 ac2[2][4];
  {
#pragma unroll
    for (int m = 0; m < 2; ++m)
#pragma unroll
      for (int n = 0; n < 4; ++n) ac2[m][n] = (f32x4){0.f, 0.f, 0.f, 0.f};
    unsigned pc[4], sb = (j & 8) * 32768, fl2 = M_LAND + 17 * 8, scn2 = M_CONS + 256 + wid * 4;
    const unsigned vo = W_H + c * 524288 + wid * 2048 + lane * 16, hr = M_HR;
#pragma unroll
    for (int q = 0; q < 4; ++q) pc[q] = fr * 128 + (((2 * q + (fq >> 1)) ^ ((fr >> 1) & 7)) * 16) + 8192 * (fq & 1);
    const unsigned flo = (lane >> 1) * 128, sf = __builtin_amdgcn_readfirstlane(W_HF + (c * 2 + (j >> 3)) * 8192);
    unsigned t_sk, t_sa, t_s0, t_s1;
    u64 t_sp;
    asm volatile(ASM_MB : [c00] "+&v"(ac2[0][0]), [c01] "+&v"(ac2[0][1]), [c02] "+&v"(ac2[0][2]), [c03] "+&v"(ac2[0][3]), [c10] "+&v"(ac2[1][0]), [c11] "+&v"(ac2[1][1]), [c12] "+&v"(ac2[1][2]), [c13] "+&v"(ac2[1][3]), [pc0] "+&v"(pc[0]), [pc1] "+&v"(pc[1]), [pc2] "+&v"(pc[2]), [pc3] "+&v"(pc[3]), [fl] "+&v"(fl2), [sb] "+&s"(sb), [scn] "+&s"(scn2), [sg] "=&s"(t_sk), [sa] "=&s"(t_sa), [s0] "=&s"(t_s0), [s1] "=&s"(t_s1), [sp] "=&s"(t_sp), [bud] "+&s"(bud) : [vo] "v"(vo), [srd] "s"(rws), [srz] "s"(rz), [hr] "v"(hr), [w] "s"(wid), [wrap] "s"(-131072), [tgA] "s"(tagA), [tgB] "s"(tagB), [flo] "v"(flo), [sf] "s"(sf), [sf1] "s"(sf + 4096) : "memory", "scc", "vcc", "m0", "v64", "v65", "v66", "v67", "v68", "v69", "v70", "v71", "v72", "v73", "v74", "v75", "v76", "v77", "v78", "v79", "v80", "v81", "v82", "v83", "v84", "v85", "v86", "v87", "v88", "v89", "v90", "v91", "v92", "v93", "v94", "v95", "v96", "v97", "v98", "v99", "v100", "v101", "v102", "v103", "v104", "v105", "v106", "v107", "v108", "v109", "v110", "v111", "v112", "v113", "v114", "v115", "v116", "v117", "v118", "v119", "v120", "v121", "v122", "v123", "v124", "v125", "v126", "v127", "v128", "v129", "v130", "v131", "v132", "v133", "v134", "v135", "v136", "v137", "v138", "v139", "v140", "v141", "v142", "v143", "v144", "v145", "v146", "v147", "v148", "v149", "v150", "v151", "v152", "v153", "v154", "v155", "v156", "v157", "v158", "v159", "v160", "v161", "v162", "v163", "v164", "v165", "v166", "v167", "v168", "v169", "v170", "v171", "v172", "v173", "v174", "v175", "v176", "v177", "v178", "v179", "v180", "v181", "v182", "v183", "v184", "v185", "v186", "v187", "v188", "v189", "v190", "v191", "v192", "v193", "v194", "v195", "v196", "v197");
  }
  STAMP(6);
  asm volatile("" : "+v"(ge[0][0]), "+v"(ge[0][1]), "+v"(ge[0][2]), "+v"(ge[0][3]), "+v"(ge[1][0]), "+v"(ge[1][1]), "+v"(ge[1][2]), "+v"(ge[1][3]));
  int ln2 = lane;
  asm volatile("" : "+v"(ln2));
  const int fr2 = ln2 & 15, tl2 = 32 * (ln2 >> 4) + 2 * wid;
  if (__builtin_expect(mine != 0, 0)) {
    lds_wait2(FL_XD);
#pragma unroll 1
    for (; mine; mine &= mine - 1) {
      const int s = __builtin_ctzll(mine);
      const int t = lds_rd32(L_SEL + s * 8) & 127;
      const f32x4 dl = *(const LAS f32x4*)(smem + L_BROW + s * 256 + fr2 * 16);
#pragma unroll
      for (int e = 0; e < 2; ++e)
#pragma unroll
        for (int r = 0; r < 4; ++r) {
          const bool m = t == tl2 + 8 * r + e;
#pragma unroll
          for (int n = 0; n < 4; ++n) base[e][r][n] += m ? dl[n] : 0.f;
        }
    }
  }
  f32x4 ov[2][4];
#pragma unroll
  for (int e = 0; e < 2; ++e)
#pragma unroll
    for (int r = 0; r < 4; ++r) {
      const float g2 = 2.f * ge[e][r];
#pragma unroll
      for (int n = 0; n < 4; ++n) ov[e][r][n] = fma1(g2, ac2[e][n][r], base[e][r][n]);
    }
  {
    const rsrc_t ro = mk(out, 2048 * 1024 * 4);
    const unsigned oo = (c * 128 + tl2) * 4096 + (j * 64 + 4 * fr2) * 4, oo2 = oo + 4096;
    asm volatile("s_nop 4\n\t"
                 "buffer_store_dwordx4 %0, %8, %10, 0 offen" OST "\n\tbuffer_store_dwordx4 %4, %9, %10, 0 offen" OST "\n\t"
                 "buffer_store_dwordx4 %1, %8, %10, %11 offen" OST "\n\tbuffer_store_dwordx4 %5, %9, %10, %11 offen" OST "\n\t"
                 "buffer_store_dwordx4 %2, %8, %10, %12 offen" OST "\n\tbuffer_store_dwordx4 %6, %9, %10, %12 offen" OST "\n\t"
                 "buffer_store_dwordx4 %3, %8, %10, %13 offen" OST "\n\tbuffer_store_dwordx4 %7, %9, %10, %13 offen" OST "\n\t"
                 "s_nop 7\n\ts_nop 7"
                 ::"v"(ov[0][0]), "v"(ov[0][1]), "v"(ov[0][2]), "v"(ov[0][3]), "v"(ov[1][0]), "v"(ov[1][1]), "v"(ov[1][2]), "v"(ov[1][3]), "v"(oo), "v"(oo2), "s"(ro),
                 "s"(32768), "s"(65536), "s"(98304) : "memory");
  }
  if (c == 15 && j == 0 && wid == 0 && lane == 0) st64c(rws, W_EPOCH, (u32x2){tagA, tagB});
  STAMP(7);
  STAMPS();
#endif
}

extern "C" void kernel_launch(void* const* d_in, const int* in_sizes, int n_in, void* d_out, int out_size, void* d_ws, size_t ws_size,
                              hipStream_t stream) {
  (void)hipFuncSetAttribute((const void*)fused, hipFuncAttributeMaxDynamicSharedMemorySize, LDS_BYTES);
  fused<<<256, 512, LDS_BYTES, stream>>>((const u16*)d_in[0], (const u16*)d_in[1], (const u16*)d_in[2], (const u16*)d_in[3], (const u16*)d_in[4],
                                         (const u16*)d_in[5], (const u16*)d_in[6], (const u16*)d_in[7], (const u16*)d_in[8], (const u16*)d_in[10],
                                         (const u16*)d_in[11], (const u16*)d_in[12], (const u16*)d_in[13], (const u16*)d_in[14], (char*)d_ws + WS_BASE(ws_size),
                                         (float*)d_out);
}
```

```cpp
#include <hip/hip_runtime.h>
#include <hip/hip_bf16.h>
#include <stdint.h>

typedef __attribute__((ext_vector_type(8))) short bf16x8;
typedef __attribute__((ext_vector_type(4))) float f32x4;
typedef __attribute__((ext_vector_type(2))) float f32x2;
typedef __attribute__((ext_vector_type(4))) unsigned u32x4;
typedef __attribute__((ext_vector_type(2))) unsigned u32x2;
typedef unsigned short u16;
typedef unsigned long long u64;
#define LAS __attribute__((address_space(3)))
#define DEV __device__ __forceinline__

#define ASM_MA \
  "s_nop 4\n\t" \
  "buffer_load_dword v228, off, %[srd], 0 sc1\n\t" \
  "buffer_load_dwordx2 %[dv], %[dofs], %[srdd], 0 offen\n\t" \
  "s_mov_b32 %[sk], 0\n\t" \
  "s_mov_b32 %[sx], 0x20000\n\t" \
  "s_branch Lsp0_%=\n\t" \
  "Lb0_%=:\n\t" \
  "s_cmp_eq_u32 %[sk], %[ks]\n\t" \
  "s_cbranch_scc1 Lx0_%=\n\t" \
  "Lxb0_%=:\n\t" \
  "ds_read_b128 v[128:131], %[pa0] offset:0\n\t" \
  "ds_read_b128 v[132:135], %[pa0] offset:8192\n\t" \
  "ds_read_b128 v[176:179], %[pa1] offset:0\n\t" \
  "ds_read_b128 v[180:183], %[pa1] offset:8192\n\t" \
  "ds_read_b128 v[144:147], %[pb0] offset:0\n\t" \
  "ds_read_b128 v[148:151], %[pb0] offset:2048\n\t" \
  "ds_read_b128 v[152:155], %[pb0] offset:4096\n\t" \
  "ds_read_b128 v[156:159], %[pb0] offset:6144\n\t" \
  "ds_read_b128 v[160:163], %[pb0] offset:8192\n\t" \
  "ds_read_b128 v[164:167], %[pb0] offset:10240\n\t" \
  "ds_read_b128 v[168:171], %[pb0] offset:12288\n\t" \
  "ds_read_b128 v[172:175], %[pb0] offset:14336\n\t" \
  "s_waitcnt lgkmcnt(4)\n\t" \
  "v_mfma_f32_16x16x32_bf16 v[64:67], v[128:131], v[144:147], 0\n\t" \
  "v_mfma_f32_16x16x32_bf16 v[96:99], v[132:135], v[144:147], 0\n\t" \
  "s_mov_b32 m0, %[scn]\n\t" \
  "s_mov_b64 exec, 1\n\t" \
  "buffer_load_dword %[l16], %[srz], 0 offen lds\n\t" \
  "s_mov_b64 exec, -1\n\t" \
  "s_add_u32 %[scn], %[scn], 16\n\t" \
  "ds_read_b128 v[144:147], %[pb1] offset:0\n\t" \
  "v_mov_b64 v[136:137], v[176:177]\n\t" \
  "v_mfma_f32_16x16x32_bf16 v[68:71], v[128:131], v[148:151], 0\n\t" \
  "v_mfma_f32_16x16x32_bf16 v[100:103], v[132:135], v[148:151], 0\n\t" \
  "ds_read_b128 v[148:151], %[pb1] offset:2048\n\t" \
  "v_mov_b64 v[138:139], v[178:179]\n\t" \
  "v_mfma_f32_16x16x32_bf16 v[72:75], v[128:131], v[152:155], 0\n\t" \
  "v_mfma_f32_16x16x32_bf16 v[104:107], v[132:135], v[152:155], 0\n\t" \
  "ds_read_b128 v[152:155], %[pb1] offset:4096\n\t" \
  "v_add_u32 %[fl], 8, %[fl]\n\t" \
  "v_mfma_f32_16x16x32_bf16 v[76:79], v[128:131], v[156:159], 0\n\t" \
  "v_mfma_f32_16x16x32_bf16 v[108:111], v[132:135], v[156:159], 0\n\t" \
  "ds_read_b128 v[156:159], %[pb1] offset:6144\n\t" \
  "ds_read_b64 v[224:225], %[fl]\n\t" \
  "s_waitcnt lgkmcnt(5)\n\t" \
  "v_mfma_f32_16x16x32_bf16 v[80:83], v[128:131], v[160:163], 0\n\t" \
  "v_mfma_f32_16x16x32_bf16 v[112:115], v[132:135], v[160:163], 0\n\t" \
  "ds_read_b128 v[160:163], %[pb1] offset:8192\n\t" \
  "v_mov_b64 v[140:141], v[180:181]\n\t" \
  "v_mfma_f32_16x16x32_bf16 v[84:87], v[128:131], v[164:167], 0\n\t" \
  "v_mfma_f32_16x16x32_bf16 v[116:119], v[132:135], v[164:167], 0\n\t" \
  "ds_read_b128 v[164:167], %[pb1] offset:10240\n\t" \
  "v_mov_b64 v[142:143], v[182:183]\n\t" \
  "v_mfma_f32_16x16x32_bf16 v[88:91], v[128:131], v[168:171], 0\n\t" \
  "v_mfma_f32_16x16x32_bf16 v[120:123], v[132:135], v[168:171], 0\n\t" \
  "ds_read_b128 v[168:171], %[pb1] offset:12288\n\t" \
  "v_mfma_f32_16x16x32_bf16 v[92:95], v[128:131], v[172:175], 0\n\t" \
  "v_mfma_f32_16x16x32_bf16 v[124:127], v[132:135], v[172:175], 0\n\t" \
  "ds_read_b128 v[172:175], %[pb1] offset:14336\n\t" \
  "Lloop_%=:\n\t" \
  "s_add_u32 %[sk], %[sk], 1\n\t" \
  "s_and_b32 %[s1], %[sk], 3\n\t" \
  "s_cselect_b32 %[s1], 0x8000, %[wrap]\n\t" \
  "s_add_u32 %[sx], %[sx], 0x8000\n\t" \
  "s_cmp_eq_u32 %[sx], 0x28000\n\t" \
  "s_cselect_b32 %[s2], %[wrap2], 0x8000\n\t" \
  "s_cselect_b32 %[sx], 0, %[sx]\n\t" \
  "s_waitcnt lgkmcnt(4)\n\t" \
  "v_or_b32 v224, v224, v225\n\t" \
  "v_mfma_f32_16x16x32_bf16 v[64:67], v[136:139], v[144:147], v[64:67]\n\t" \
  "v_readfirstlane_b32 %[s0], v224\n\t" \
  "v_mfma_f32_16x16x32_bf16 v[96:99], v[140:143], v[144:147], v[96:99]\n\t" \
  "v_add_u32 %[pa0], %[s1], %[pa0]\n\t" \
  "v_add_u32 %[pa1], %[s1], %[pa1]\n\t" \
  "v_add_u32 %[pb0], %[s2], %[pb0]\n\t" \
  "s_cmp_eq_u32 %[s0], 0\n\t" \
  "s_cbranch_scc0 Lsp1_%=\n\t" \
  "Lb1_%=:\n\t" \
  "ds_read_b128 v[128:131], %[pa0] offset:0\n\t" \
  "ds_read_b128 v[132:135], %[pa0] offset:8192\n\t" \
  "ds_read_b128 v[176:179], %[pa1] offset:0\n\t" \
  "ds_read_b128 v[180:183], %[pa1] offset:8192\n\t" \
  "ds_read_b128 v[144:147], %[pb0] offset:0\n\t" \
  "v_mfma_f32_16x16x32_bf16 v[68:71], v[136:139], v[148:151], v[68:71]\n\t" \
  "v_mfma_f32_16x16x32_bf16 v[100:103], v[140:143], v[148:151], v[100:103]\n\t" \
  "ds_read_b128 v[148:151], %[pb0] offset:2048\n\t" \
  "v_mfma_f32_16x16x32_bf16 v[72:75], v[136:139], v[152:155], v[72:75]\n\t" \
  "v_mfma_f32_16x16x32_bf16 v[104:107], v[140:143], v[152:155], v[104:107]\n\t" \
  "ds_read_b128 v[152:155], %[pb0] offset:4096\n\t" \
  "v_mfma_f32_16x16x32_bf16 v[76:79], v[136:139], v[156:159], v[76:79]\n\t" \
  "v_mfma_f32_16x16x32_bf16 v[108:111], v[140:143], v[156:159], v[108:111]\n\t" \
  "ds_read_b128 v[156:159], %[pb0] offset:6144\n\t" \
  "s_waitcnt lgkmcnt(4)\n\t" \
  "v_mfma_f32_16x16x32_bf16 v[80:83], v[136:139], v[160:163], v[80:83]\n\t" \
  "s_mov_b32 m0, %[scn]\n\t" \
  "s_mov_b64 exec, 1\n\t" \
  "buffer_load_dword %[l16], %[srz], 0 offen lds\n\t" \
  "s_mov_b64 exec, -1\n\t" \
  "v_mfma_f32_16x16x32_bf16 v[112:115], v[140:143], v[160:163], v[112:115]\n\t" \
  "ds_read_b128 v[160:163], %[pb0] offset:8192\n\t" \
  "v_mfma_f32_16x16x32_bf16 v[84:87], v[136:139], v[164:167], v[84:87]\n\t" \
  "v_mfma_f32_16x16x32_bf16 v[116:119], v[140:143], v[164:167], v[116:119]\n\t" \
  "ds_read_b128 v[164:167], %[pb0] offset:10240\n\t" \
  "v_add_u32 %[pb1], %[s2], %[pb1]\n\t" \
  "s_add_u32 %[scn], %[scn], 16\n\t" \
  "v_mfma_f32_16x16x32_bf16 v[88:91], v[136:139], v[168:171], v[88:91]\n\t" \
  "v_mfma_f32_16x16x32_bf16 v[120:123], v[140:143], v[168:171], v[120:123]\n\t" \
  "ds_read_b128 v[168:171], %[pb0] offset:12288\n\t" \
  "s_cmp_eq_u32 %[sk], %[ks]\n\t" \
  "s_cbranch_scc1 Lx1_%=\n\t" \
  "Lxb1_%=:\n\t" \
  "v_mfma_f32_16x16x32_bf16 v[92:95], v[136:139], v[172:175], v[92:95]\n\t" \
  "v_mfma_f32_16x16x32_bf16 v[124:127], v[140:143], v[172:175], v[124:127]\n\t" \
  "ds_read_b128 v[172:175], %[pb0] offset:14336\n\t" \
  "s_waitcnt lgkmcnt(4)\n\t" \
  "v_mfma_f32_16x16x32_bf16 v[64:67], v[128:131], v[144:147], v[64:67]\n\t" \
  "v_mfma_f32_16x16x32_bf16 v[96:99], v[132:135], v[144:147], v[96:99]\n\t" \
  "ds_read_b128 v[144:147], %[pb1] offset:0\n\t" \
  "v_mov_b64 v[136:137], v[176:177]\n\t" \
  "v_mfma_f32_16x16x32_bf16 v[68:71], v[128:131], v[148:151], v[68:71]\n\t" \
  "v_mfma_f32_16x16x32_bf16 v[100:103], v[132:135], v[148:151], v[100:103]\n\t" \
  "ds_read_b128 v[148:151], %[pb1] offset:2048\n\t" \
  "v_mov_b64 v[138:139], v[178:179]\n\t" \
  "v_mfma_f32_16x16x32_bf16 v[72:75], v[128:131], v[152:155], v[72:75]\n\t" \
  "v_mfma_f32_16x16x32_bf16 v[104:107], v[132:135], v[152:155], v[104:107]\n\t" \
  "ds_read_b128 v[152:155], %[pb1] offset:4096\n\t" \
  "v_add_u32 %[fl], 8, %[fl]\n\t" \
  "v_mfma_f32_16x16x32_bf16 v[76:79], v[128:131], v[156:159], v[76:79]\n\t" \
  "v_mfma_f32_16x16x32_bf16 v[108:111], v[132:135], v[156:159], v[108:111]\n\t" \
  "ds_read_b128 v[156:159], %[pb1] offset:6144\n\t" \
  "ds_read_b64 v[224:225], %[fl]\n\t" \
  "s_waitcnt lgkmcnt(5)\n\t" \
  "v_mfma_f32_16x16x32_bf16 v[80:83], v[128:131], v[160:163], v[80:83]\n\t" \
  "v_mfma_f32_16x16x32_bf16 v[112:115], v[132:135], v[160:163], v[112:115]\n\t" \
  "ds_read_b128 v[160:163], %[pb1] offset:8192\n\t" \
  "v_mov_b64 v[140:141], v[180:181]\n\t" \
  "v_mfma_f32_16x16x32_bf16 v[84:87], v[128:131], v[164:167], v[84:87]\n\t" \
  "v_mfma_f32_16x16x32_bf16 v[116:119], v[132:135], v[164:167], v[116:119]\n\t" \
  "ds_read_b128 v[164:167], %[pb1] offset:10240\n\t" \
  "v_mov_b64 v[142:143], v[182:183]\n\t" \
  "v_mfma_f32_16x16x32_bf16 v[88:91], v[128:131], v[168:171], v[88:91]\n\t" \
  "v_mfma_f32_16x16x32_bf16 v[120:123], v[132:135], v[168:171], v[120:123]\n\t" \
  "ds_read_b128 v[168:171], %[pb1] offset:12288\n\t" \
  "v_mfma_f32_16x16x32_bf16 v[92:95], v[128:131], v[172:175], v[92:95]\n\t" \
  "v_mfma_f32_16x16x32_bf16 v[124:127], v[132:135], v[172:175], v[124:127]\n\t" \
  "ds_read_b128 v[172:175], %[pb1] offset:14336\n\t" \
  "s_cmp_lt_u32 %[sk], 15\n\t" \
  "s_cbranch_scc1 Lloop_%=\n\t" \
  "s_waitcnt lgkmcnt(4)\n\t" \
  "v_or_b32 v224, v224, v225\n\t" \
  "v_mfma_f32_16x16x32_bf16 v[64:67], v[136:139], v[144:147], v[64:67]\n\t" \
  "v_readfirstlane_b32 %[s0], v224\n\t" \
  "v_mfma_f32_16x16x32_bf16 v[96:99], v[140:143], v[144:147], v[96:99]\n\t" \
  "s_cmp_eq_u32 %[s0], 0\n\t" \
  "s_cbranch_scc0 Lsp2_%=\n\t" \
  "Lb2_%=:\n\t" \
  "v_mfma_f32_16x16x32_bf16 v[68:71], v[136:139], v[148:151], v[68:71]\n\t" \
  "v_mfma_f32_16x16x32_bf16 v[100:103], v[140:143], v[148:151], v[100:103]\n\t" \
  "ds_read_b128 v[176:179], %[tq] offset:0\n\t" \
  "ds_read_b128 v[180:183], %[tq] offset:256\n\t" \
  "v_mfma_f32_16x16x32_bf16 v[72:75], v[136:139], v[152:155], v[72:75]\n\t" \
  "v_mfma_f32_16x16x32_bf16 v[104:107], v[140:143], v[152:155], v[104:107]\n\t" \
  "ds_read_b128 v[192:195], %[tq] offset:1024\n\t" \
  "ds_read_b128 v[196:199], %[tq] offset:1280\n\t" \
  "v_mfma_f32_16x16x32_bf16 v[76:79], v[136:139], v[156:159], v[76:79]\n\t" \
  "v_mfma_f32_16x16x32_bf16 v[108:111], v[140:143], v[156:159], v[108:111]\n\t" \
  "ds_read_b128 v[200:203], %[tq] offset:1536\n\t" \
  "ds_read_b128 v[204:207], %[tq] offset:1792\n\t" \
  "s_waitcnt lgkmcnt(6)\n\t" \
  "v_mfma_f32_16x16x32_bf16 v[80:83], v[136:139], v[160:163], v[80:83]\n\t" \
  "s_mov_b32 m0, %[scn]\n\t" \
  "s_mov_b64 exec, 1\n\t" \
  "buffer_load_dword %[l16], %[srz], 0 offen lds\n\t" \
  "s_mov_b64 exec, -1\n\t" \
  "v_mfma_f32_16x16x32_bf16 v[112:115], v[140:143], v[160:163], v[112:115]\n\t" \
  "ds_read_b128 v[208:211], %[tq] offset:2048\n\t" \
  "ds_read_b128 v[212:215], %[tq] offset:2304\n\t" \
  "v_mfma_f32_16x16x32_bf16 v[84:87], v[136:139], v[164:167], v[84:87]\n\t" \
  "v_mfma_f32_16x16x32_bf16 v[116:119], v[140:143], v[164:167], v[116:119]\n\t" \
  "ds_read_b128 v[216:219], %[tq] offset:2560\n\t" \
  "ds_read_b128 v[220:223], %[tq] offset:2816\n\t" \
  "v_mfma_f32_16x16x32_bf16 v[88:91], v[136:139], v[168:171], v[88:91]\n\t" \
  "v_mfma_f32_16x16x32_bf16 v[120:123], v[140:143], v[168:171], v[120:123]\n\t" \
  "ds_read_b128 v[184:187], %[tq] offset:512\n\t" \
  "ds_read_b128 v[188:191], %[tq] offset:768\n\t" \
  "v_mfma_f32_16x16x32_bf16 v[92:95], v[136:139], v[172:175], v[92:95]\n\t" \
  "v_mfma_f32_16x16x32_bf16 v[124:127], v[140:143], v[172:175], v[124:127]\n\t" \
  "ds_read2_b32 v[230:231], %[tcl] offset0:0 offset1:64\n\t" \
  "ds_read2_b32 v[232:233], %[tcw] offset0:0 offset1:64\n\t" \
  "s_setprio 3\n\t" \
  "s_waitcnt vmcnt(8) lgkmcnt(4)\n\t" \
  "v_readfirstlane_b32 %[tgB], v228\n\t" \
  "s_add_u32 %[tgA], %[tgB], 1\n\t" \
  "s_add_u32 %[tgB], %[tgB], 0x40000001\n\t" \
  "v_pk_fma_f32 v[244:245], v[180:181], v[96:97], v[68:69] neg_lo:[1,0,0] neg_hi:[1,0,0]\n\t" \
  "v_pk_fma_f32 v[246:247], v[182:183], v[98:99], v[70:71] neg_lo:[1,0,0] neg_hi:[1,0,0]\n\t" \
  "v_pk_fma_f32 v[248:249], v[180:181], v[64:65], v[100:101]\n\t" \
  "v_pk_fma_f32 v[250:251], v[182:183], v[66:67], v[102:103]\n\t" \
  "v_pk_fma_f32 v[236:237], v[176:177], v[64:65], v[244:245]\n\t" \
  "v_pk_fma_f32 v[238:239], v[178:179], v[66:67], v[246:247]\n\t" \
  "v_pk_fma_f32 v[240:241], v[176:177], v[96:97], v[248:249]\n\t" \
  "v_pk_fma_f32 v[242:243], v[178:179], v[98:99], v[250:251]\n\t" \
  "v_pk_fma_f32 v[244:245], v[180:181], v[240:241], v[72:73] neg_lo:[1,0,0] neg_hi:[1,0,0]\n\t" \
  "v_pk_fma_f32 v[246:247], v[182:183], v[242:243], v[74:75] neg_lo:[1,0,0] neg_hi:[1,0,0]\n\t" \
  "v_pk_fma_f32 v[248:249], v[180:181], v[236:237], v[104:105]\n\t" \
  "v_pk_fma_f32 v[250:251], v[182:183], v[238:239], v[106:107]\n\t" \
  "v_pk_fma_f32 v[236:237], v[176:177], v[236:237], v[244:245]\n\t" \
  "v_pk_fma_f32 v[238:239], v[178:179], v[238:239], v[246:247]\n\t" \
  "v_pk_fma_f32 v[240:241], v[176:177], v[240:241], v[248:249]\n\t" \
  "v_pk_fma_f32 v[242:243], v[178:179], v[242:243], v[250:251]\n\t" \
  "v_pk_fma_f32 v[244:245], v[180:181], v[240:241], v[76:77] neg_lo:[1,0,0] neg_hi:[1,0,0]\n\t" \
  "v_pk_fma_f32 v[246:247], v[182:183], v[242:243], v[78:79] neg_lo:[1,0,0] neg_hi:[1,0,0]\n\t" \
  "v_pk_fma_f32 v[248:249], v[180:181], v[236:237], v[108:109]\n\t" \
  "v_pk_fma_f32 v[250:251], v[182:183], v[238:239], v[110:111]\n\t" \
  "v_pk_fma_f32 v[236:237], v[176:177], v[236:237], v[244:245]\n\t" \
  "v_pk_fma_f32 v[238:239], v[178:179], v[238:239], v[246:247]\n\t" \
  "v_pk_fma_f32 v[240:241], v[176:177], v[240:241], v[248:249]\n\t" \
  "v_pk_fma_f32 v[242:243], v[178:179], v[242:243], v[250:251]\n\t" \
  "v_pk_fma_f32 v[244:245], v[180:181], v[240:241], v[80:81] neg_lo:[1,0,0] neg_hi:[1,0,0]\n\t" \
  "v_pk_fma_f32 v[246:247], v[182:183], v[242:243], v[82:83] neg_lo:[1,0,0] neg_hi:[1,0,0]\n\t" \
  "v_pk_fma_f32 v[248:249], v[180:181], v[236:237], v[112:113]\n\t" \
  "v_pk_fma_f32 v[250:251], v[182:183], v[238:239], v[114:115]\n\t" \
  "v_pk_fma_f32 v[236:237], v[176:177], v[236:237], v[244:245]\n\t" \
  "v_pk_fma_f32 v[238:239], v[178:179], v[238:239], v[246:247]\n\t" \
  "v_pk_fma_f32 v[240:241], v[176:177], v[240:241], v[248:249]\n\t" \
  "v_pk_fma_f32 v[242:243], v[178:179], v[242:243], v[250:251]\n\t" \
  "v_pk_fma_f32 v[244:245], v[180:181], v[240:241], v[84:85] neg_lo:[1,0,0] neg_hi:[1,0,0]\n\t" \
  "v_pk_fma_f32 v[246:247], v[182:183], v[242:243], v[86:87] neg_lo:[1,0,0] neg_hi:[1,0,0]\n\t" \
  "v_pk_fma_f32 v[248:249], v[180:181], v[236:237], v[116:117]\n\t" \
  "v_pk_fma_f32 v[250:251], v[182:183], v[238:239], v[118:119]\n\t" \
  "v_pk_fma_f32 v[236:237], v[176:177], v[236:237], v[244:245]\n\t" \
  "v_pk_fma_f32 v[238:239], v[178:179], v[238:239], v[246:247]\n\t" \
  "v_pk_fma_f32 v[240:241], v[176:177], v[240:241], v[248:249]\n\t" \
  "v_pk_fma_f32 v[242:243], v[178:179], v[242:243], v[250:251]\n\t" \
  "v_pk_fma_f32 v[244:245], v[180:181], v[240:241], v[88:89] neg_lo:[1,0,0] neg_hi:[1,0,0]\n\t" \
  "v_pk_fma_f32 v[246:247], v[182:183], v[242:243], v[90:91] neg_lo:[1,0,0] neg_hi:[1,0,0]\n\t" \
  "v_pk_fma_f32 v[248:249], v[180:181], v[236:237], v[120:121]\n\t" \
  "v_pk_fma_f32 v[250:251], v[182:183], v[238:239], v[122:123]\n\t" \
  "v_pk_fma_f32 v[236:237], v[176:177], v[236:237], v[244:245]\n\t" \
  "v_pk_fma_f32 v[238:239], v[178:179], v[238:239], v[246:247]\n\t" \
  "v_pk_fma_f32 v[240:241], v[176:177], v[240:241], v[248:249]\n\t" \
  "v_pk_fma_f32 v[242:243], v[178:179], v[242:243], v[250:251]\n\t" \
  "v_pk_fma_f32 v[244:245], v[180:181], v[240:241], v[92:93] neg_lo:[1,0,0] neg_hi:[1,0,0]\n\t" \
  "v_pk_fma_f32 v[246:247], v[182:183], v[242:243], v[94:95] neg_lo:[1,0,0] neg_hi:[1,0,0]\n\t" \
  "v_pk_fma_f32 v[248:249], v[180:181], v[236:237], v[124:125]\n\t" \
  "v_pk_fma_f32 v[250:251], v[182:183], v[238:239], v[126:127]\n\t" \
  "v_pk_fma_f32 v[236:237], v[176:177], v[236:237], v[244:245]\n\t" \
  "v_pk_fma_f32 v[238:239], v[178:179], v[238:239], v[246:247]\n\t" \
  "v_pk_fma_f32 v[240:241], v[176:177], v[240:241], v[248:249]\n\t" \
  "v_pk_fma_f32 v[242:243], v[178:179], v[242:243], v[250:251]\n\t" \
  "v_mov_b32 v253, %[tgA]\n\t" \
  "v_mov_b32 v255, %[tgB]\n\t" \
  "v_mov_b32_dpp v244, v236 row_shr:1 row_mask:0xf bank_mask:0xf bound_ctrl:0\n\t" \
  "v_mov_b32_dpp v245, v237 row_shr:1 row_mask:0xf bank_mask:0xf bound_ctrl:0\n\t" \
  "v_mov_b32_dpp v246, v238 row_shr:1 row_mask:0xf bank_mask:0xf bound_ctrl:0\n\t" \
  "v_mov_b32_dpp v247, v239 row_shr:1 row_mask:0xf bank_mask:0xf bound_ctrl:0\n\t" \
  "v_mov_b32_dpp v248, v240 row_shr:1 row_mask:0xf bank_mask:0xf bound_ctrl:0\n\t" \
  "v_mov_b32_dpp v249, v241 row_shr:1 row_mask:0xf bank_mask:0xf bound_ctrl:0\n\t" \
  "v_mov_b32_dpp v250, v242 row_shr:1 row_mask:0xf bank_mask:0xf bound_ctrl:0\n\t" \
  "v_mov_b32_dpp v251, v243 row_shr:1 row_mask:0xf bank_mask:0xf bound_ctrl:0\n\t" \
  "v_pk_fma_f32 v[236:237], v[192:193], v[244:245], v[236:237]\n\t" \
  "v_pk_fma_f32 v[238:239], v[194:195], v[246:247], v[238:239]\n\t" \
  "v_pk_fma_f32 v[240:241], v[192:193], v[248:249], v[240:241]\n\t" \
  "v_pk_fma_f32 v[242:243], v[194:195], v[250:251], v[242:243]\n\t" \
  "v_pk_fma_f32 v[236:237], v[196:197], v[248:249], v[236:237] neg_lo:[1,0,0] neg_hi:[1,0,0]\n\t" \
  "v_pk_fma_f32 v[238:239], v[198:199], v[250:251], v[238:239] neg_lo:[1,0,0] neg_hi:[1,0,0]\n\t" \
  "v_pk_fma_f32 v[240:241], v[196:197], v[244:245], v[240:241]\n\t" \
  "v_pk_fma_f32 v[242:243], v[198:199], v[246:247], v[242:243]\n\t" \
  "v_mov_b32_dpp v244, v236 row_shr:2 row_mask:0xf bank_mask:0xf bound_ctrl:0\n\t" \
  "v_mov_b32_dpp v245, v237 row_shr:2 row_mask:0xf bank_mask:0xf bound_ctrl:0\n\t" \
  "v_mov_b32_dpp v246, v238 row_shr:2 row_mask:0xf bank_mask:0xf bound_ctrl:0\n\t" \
  "v_mov_b32_dpp v247, v239 row_shr:2 row_mask:0xf bank_mask:0xf bound_ctrl:0\n\t" \
  "v_mov_b32_dpp v248, v240 row_shr:2 row_mask:0xf bank_mask:0xf bound_ctrl:0\n\t" \
  "v_mov_b32_dpp v249, v241 row_shr:2 row_mask:0xf bank_mask:0xf bound_ctrl:0\n\t" \
  "v_mov_b32_dpp v250, v242 row_shr:2 row_mask:0xf bank_mask:0xf bound_ctrl:0\n\t" \
  "v_mov_b32_dpp v251, v243 row_shr:2 row_mask:0xf bank_mask:0xf bound_ctrl:0\n\t" \
  "v_pk_fma_f32 v[236:237], v[200:201], v[244:245], v[236:237]\n\t" \
  "v_pk_fma_f32 v[238:239], v[202:203], v[246:247], v[238:239]\n\t" \
  "v_pk_fma_f32 v[240:241], v[200:201], v[248:249], v[240:241]\n\t" \
  "v_pk_fma_f32 v[242:243], v[202:203], v[250:251], v[242:243]\n\t" \
  "v_pk_fma_f32 v[236:237], v[204:205], v[248:249], v[236:237] neg_lo:[1,0,0] neg_hi:[1,0,0]\n\t" \
  "v_pk_fma_f32 v[238:239], v[206:207], v[250:251], v[238:239] neg_lo:[1,0,0] neg_hi:[1,0,0]\n\t" \
  "v_pk_fma_f32 v[240:241], v[204:205], v[244:245], v[240:241]\n\t" \
  "v_pk_fma_f32 v[242:243], v[206:207], v[246:247], v[242:243]\n\t" \
  "v_mov_b32_dpp v244, v236 row_shr:4 row_mask:0xf bank_mask:0xf bound_ctrl:0\n\t" \
  "v_mov_b32_dpp v245, v237 row_shr:4 row_mask:0xf bank_mask:0xf bound_ctrl:0\n\t" \
  "v_mov_b32_dpp v246, v238 row_shr:4 row_mask:0xf bank_mask:0xf bound_ctrl:0\n\t" \
  "v_mov_b32_dpp v247, v239 row_shr:4 row_mask:0xf bank_mask:0xf bound_ctrl:0\n\t" \
  "v_mov_b32_dpp v248, v240 row_shr:4 row_mask:0xf bank_mask:0xf bound_ctrl:0\n\t" \
  "v_mov_b32_dpp v249, v241 row_shr:4 row_mask:0xf bank_mask:0xf bound_ctrl:0\n\t" \
  "v_mov_b32_dpp v250, v242 row_shr:4 row_mask:0xf bank_mask:0xf bound_ctrl:0\n\t" \
  "v_mov_b32_dpp v251, v243 row_shr:4 row_mask:0xf bank_mask:0xf bound_ctrl:0\n\t" \
  "v_pk_fma_f32 v[236:237], v[208:209], v[244:245], v[236:237]\n\t" \
  "v_pk_fma_f32 v[238:239], v[210:211], v[246:247], v[238:239]\n\t" \
  "v_pk_fma_f32 v[240:241], v[208:209], v[248:249], v[240:241]\n\t" \
  "v_pk_fma_f32 v[242:243], v[210:211], v[250:251], v[242:243]\n\t" \
  "v_pk_fma_f32 v[236:237], v[212:213], v[248:249], v[236:237] neg_lo:[1,0,0] neg_hi:[1,0,0]\n\t" \
  "v_pk_fma_f32 v[238:239], v[214:215], v[250:251], v[238:239] neg_lo:[1,0,0] neg_hi:[1,0,0]\n\t" \
  "v_pk_fma_f32 v[240:241], v[212:213], v[244:245], v[240:241]\n\t" \
  "v_pk_fma_f32 v[242:243], v[214:215], v[246:247], v[242:243]\n\t" \
  "v_mov_b32_dpp v244, v236 row_shr:8 row_mask:0xf bank_mask:0xf bound_ctrl:0\n\t" \
  "v_mov_b32_dpp v245, v237 row_shr:8 row_mask:0xf bank_mask:0xf bound_ctrl:0\n\t" \
  "v_mov_b32_dpp v246, v238 row_shr:8 row_mask:0xf bank_mask:0xf bound_ctrl:0\n\t" \
  "v_mov_b32_dpp v247, v239 row_shr:8 row_mask:0xf bank_mask:0xf bound_ctrl:0\n\t" \
  "v_mov_b32_dpp v248, v240 row_shr:8 row_mask:0xf bank_mask:0xf bound_ctrl:0\n\t" \
  "v_mov_b32_dpp v249, v241 row_shr:8 row_mask:0xf bank_mask:0xf bound_ctrl:0\n\t" \
  "v_mov_b32_dpp v250, v242 row_shr:8 row_mask:0xf bank_mask:0xf bound_ctrl:0\n\t" \
  "v_mov_b32_dpp v251, v243 row_shr:8 row_mask:0xf bank_mask:0xf bound_ctrl:0\n\t" \
  "v_pk_fma_f32 v[236:237], v[216:217], v[244:245], v[236:237]\n\t" \
  "v_pk_fma_f32 v[238:239], v[218:219], v[246:247], v[238:239]\n\t" \
  "v_pk_fma_f32 v[240:241], v[216:217], v[248:249], v[240:241]\n\t" \
  "v_pk_fma_f32 v[242:243], v[218:219], v[250:251], v[242:243]\n\t" \
  "v_pk_fma_f32 v[236:237], v[220:221], v[248:249], v[236:237] neg_lo:[1,0,0] neg_hi:[1,0,0]\n\t" \
  "v_pk_fma_f32 v[238:239], v[222:223], v[250:251], v[238:239] neg_lo:[1,0,0] neg_hi:[1,0,0]\n\t" \
  "v_pk_fma_f32 v[240:241], v[220:221], v[244:245], v[240:241]\n\t" \
  "v_pk_fma_f32 v[242:243], v[222:223], v[246:247], v[242:243]\n\t" \
  "v_mov_b32_dpp v252, v236 row_newbcast:15 row_mask:0xf bank_mask:0x1\n\t" \
  "v_mov_b32_dpp v252, v237 row_newbcast:15 row_mask:0xf bank_mask:0x2\n\t" \
  "v_mov_b32_dpp v252, v238 row_newbcast:15 row_mask:0xf bank_mask:0x4\n\t" \
  "v_mov_b32_dpp v252, v239 row_newbcast:15 row_mask:0xf bank_mask:0x8\n\t" \
  "v_mov_b32_dpp v254, v240 row_newbcast:15 row_mask:0xf bank_mask:0x1\n\t" \
  "v_mov_b32_dpp v254, v241 row_newbcast:15 row_mask:0xf bank_mask:0x2\n\t" \
  "v_mov_b32_dpp v254, v242 row_newbcast:15 row_mask:0xf bank_mask:0x4\n\t" \
  "v_mov_b32_dpp v254, v243 row_newbcast:15 row_mask:0xf bank_mask:0x8\n\t" \
  "s_mov_b32 exec_lo, 0x11111111\n\t" \
  "s_mov_b32 exec_hi, 0x11111111\n\t" \
  "buffer_store_dwordx4 v[252:255], %[pubo], %[srd], %[sag] offen sc1\n\t" \
  "s_mov_b64 exec, 1\n\t" \
  "buffer_atomic_swap v253, off, %[srd], %[shc] sc1\n\t" \
  "s_mov_b64 exec, -1\n\t" \
  "s_mov_b64 exec, %[eh]\n\t" \
  "v_mov_b32 v226, 0\n\t" \
  "v_lshrrev_b32 v227, 2, %[l16]\n\t" \
  "s_cbranch_execz Lhza_%=\n\t" \
  "buffer_atomic_or v226, v227, %[srd], %[sht] offen sc0 sc1\n\t" \
  "s_branch Lhya_%=\n\t" \
  "Lhza_%=:\n\t" \
  "s_mov_b64 exec, 1\n\t" \
  "buffer_load_dword v226, %[l16], %[srz], 0 offen\n\t" \
  "Lhya_%=:\n\t" \
  "s_mov_b64 exec, -1\n\t" \
  "s_waitcnt lgkmcnt(0)\n\t" \
  "v_mov_b32_dpp v244, v236 row_shr:1 row_mask:0xf bank_mask:0xf bound_ctrl:0\n\t" \
  "v_mov_b32_dpp v245, v237 row_shr:1 row_mask:0xf bank_mask:0xf bound_ctrl:0\n\t" \
  "v_mov_b32_dpp v246, v238 row_shr:1 row_mask:0xf bank_mask:0xf bound_ctrl:0\n\t" \
  "v_mov_b32_dpp v247, v239 row_shr:1 row_mask:0xf bank_mask:0xf bound_ctrl:0\n\t" \
  "v_mov_b32_dpp v248, v240 row_shr:1 row_mask:0xf bank_mask:0xf bound_ctrl:0\n\t" \
  "v_mov_b32_dpp v249, v241 row_shr:1 row_mask:0xf bank_mask:0xf bound_ctrl:0\n\t" \
  "v_mov_b32_dpp v250, v242 row_shr:1 row_mask:0xf bank_mask:0xf bound_ctrl:0\n\t" \
  "v_mov_b32_dpp v251, v243 row_shr:1 row_mask:0xf bank_mask:0xf bound_ctrl:0\n\t" \
  "v_pk_mul_f32 v[236:237], v[184:185], v[244:245]\n\t" \
  "v_pk_mul_f32 v[238:239], v[186:187], v[246:247]\n\t" \
  "v_pk_mul_f32 v[240:241], v[184:185], v[248:249]\n\t" \
  "v_pk_mul_f32 v[242:243], v[186:187], v[250:251]\n\t" \
  "v_pk_fma_f32 v[236:237], v[188:189], v[248:249], v[236:237] neg_lo:[1,0,0] neg_hi:[1,0,0]\n\t" \
  "v_pk_fma_f32 v[238:239], v[190:191], v[250:251], v[238:239] neg_lo:[1,0,0] neg_hi:[1,0,0]\n\t" \
  "v_pk_fma_f32 v[240:241], v[188:189], v[244:245], v[240:241]\n\t" \
  "v_pk_fma_f32 v[242:243], v[190:191], v[246:247], v[242:243]\n\t" \
  "v_pk_mul_f32 v[128:129], v[184:185], v[64:65]\n\t" \
  "v_pk_mul_f32 v[130:131], v[186:187], v[66:67]\n\t" \
  "v_pk_fma_f32 v[128:129], v[188:189], v[96:97], v[128:129] neg_lo:[1,0,0] neg_hi:[1,0,0]\n\t" \
  "v_pk_fma_f32 v[130:131], v[190:191], v[98:99], v[130:131] neg_lo:[1,0,0] neg_hi:[1,0,0]\n\t" \
  "v_pk_mul_f32 v[96:97], v[184:185], v[96:97]\n\t" \
  "v_pk_mul_f32 v[98:99], v[186:187], v[98:99]\n\t" \
  "v_pk_fma_f32 v[96:97], v[188:189], v[64:65], v[96:97] neg_lo:[1,0,1] neg_hi:[1,0,1]\n\t" \
  "v_pk_fma_f32 v[98:99], v[190:191], v[66:67], v[98:99] neg_lo:[1,0,1] neg_hi:[1,0,1]\n\t" \
  "v_pk_mul_f32 v[132:133], v[184:185], v[68:69]\n\t" \
  "v_pk_mul_f32 v[134:135], v[186:187], v[70:71]\n\t" \
  "v_pk_fma_f32 v[132:133], v[188:189], v[100:101], v[132:133] neg_lo:[1,0,0] neg_hi:[1,0,0]\n\t" \
  "v_pk_fma_f32 v[134:135], v[190:191], v[102:103], v[134:135] neg_lo:[1,0,0] neg_hi:[1,0,0]\n\t" \
  "v_pk_mul_f32 v[100:101], v[184:185], v[100:101]\n\t" \
  "v_pk_mul_f32 v[102:103], v[186:187], v[102:103]\n\t" \
  "v_pk_fma_f32 v[100:101], v[188:189], v[68:69], v[100:101] neg_lo:[1,0,1] neg_hi:[1,0,1]\n\t" \
  "v_pk_fma_f32 v[102:103], v[190:191], v[70:71], v[102:103] neg_lo:[1,0,1] neg_hi:[1,0,1]\n\t" \
  "v_pk_mul_f32 v[136:137], v[184:185], v[72:73]\n\t" \
  "v_pk_mul_f32 v[138:139], v[186:187], v[74:75]\n\t" \
  "v_pk_fma_f32 v[136:137], v[188:189], v[104:105], v[136:137] neg_lo:[1,0,0] neg_hi:[1,0,0]\n\t" \
  "v_pk_fma_f32 v[138:139], v[190:191], v[106:107], v[138:139] neg_lo:[1,0,0] neg_hi:[1,0,0]\n\t" \
  "v_pk_mul_f32 v[104:105], v[184:185], v[104:105]\n\t" \
  "v_pk_mul_f32 v[106:107], v[186:187], v[106:107]\n\t" \
  "v_pk_fma_f32 v[104:105], v[188:189], v[72:73], v[104:105] neg_lo:[1,0,1] neg_hi:[1,0,1]\n\t" \
  "v_pk_fma_f32 v[106:107], v[190:191], v[74:75], v[106:107] neg_lo:[1,0,1] neg_hi:[1,0,1]\n\t" \
  "v_pk_mul_f32 v[140:141], v[184:185], v[76:77]\n\t" \
  "v_pk_mul_f32 v[142:143], v[186:187], v[78:79]\n\t" \
  "v_pk_fma_f32 v[140:141], v[188:189], v[108:109], v[140:141] neg_lo:[1,0,0] neg_hi:[1,0,0]\n\t" \
  "v_pk_fma_f32 v[142:143], v[190:191], v[110:111], v[142:143] neg_lo:[1,0,0] neg_hi:[1,0,0]\n\t" \
  "v_pk_mul_f32 v[108:109], v[184:185], v[108:109]\n\t" \
  "v_pk_mul_f32 v[110:111], v[186:187], v[110:111]\n\t" \
  "v_pk_fma_f32 v[108:109], v[188:189], v[76:77], v[108:109] neg_lo:[1,0,1] neg_hi:[1,0,1]\n\t" \
  "v_pk_fma_f32 v[110:111], v[190:191], v[78:79], v[110:111] neg_lo:[1,0,1] neg_hi:[1,0,1]\n\t" \
  "v_pk_mul_f32 v[144:145], v[184:185], v[80:81]\n\t" \
  "v_pk_mul_f32 v[146:147], v[186:187], v[82:83]\n\t" \
  "v_pk_fma_f32 v[144:145], v[188:189], v[112:113], v[144:145] neg_lo:[1,0,0] neg_hi:[1,0,0]\n\t" \
  "v_pk_fma_f32 v[146:147], v[190:191], v[114:115], v[146:147] neg_lo:[1,0,0] neg_hi:[1,0,0]\n\t" \
  "v_pk_mul_f32 v[112:113], v[184:185], v[112:113]\n\t" \
  "v_pk_mul_f32 v[114:115], v[186:187], v[114:115]\n\t" \
  "v_pk_fma_f32 v[112:113], v[188:189], v[80:81], v[112:113] neg_lo:[1,0,1] neg_hi:[1,0,1]\n\t" \
  "v_pk_fma_f32 v[114:115], v[190:191], v[82:83], v[114:115] neg_lo:[1,0,1] neg_hi:[1,0,1]\n\t" \
  "v_pk_mul_f32 v[148:149], v[184:185], v[84:85]\n\t" \
  "v_pk_mul_f32 v[150:151], v[186:187], v[86:87]\n\t" \
  "v_pk_fma_f32 v[148:149], v[188:189], v[116:117], v[148:149] neg_lo:[1,0,0] neg_hi:[1,0,0]\n\t" \
  "v_pk_fma_f32 v[150:151], v[190:191], v[118:119], v[150:151] neg_lo:[1,0,0] neg_hi:[1,0,0]\n\t" \
  "v_pk_mul_f32 v[116:117], v[184:185], v[116:117]\n\t" \
  "v_pk_mul_f32 v[118:119], v[186:187], v[118:119]\n\t" \
  "v_pk_fma_f32 v[116:117], v[188:189], v[84:85], v[116:117] neg_lo:[1,0,1] neg_hi:[1,0,1]\n\t" \
  "v_pk_fma_f32 v[118:119], v[190:191], v[86:87], v[118:119] neg_lo:[1,0,1] neg_hi:[1,0,1]\n\t" \
  "v_pk_mul_f32 v[152:153], v[184:185], v[88:89]\n\t" \
  "v_pk_mul_f32 v[154:155], v[186:187], v[90:91]\n\t" \
  "v_pk_fma_f32 v[152:153], v[188:189], v[120:121], v[152:153] neg_lo:[1,0,0] neg_hi:[1,0,0]\n\t" \
  "v_pk_fma_f32 v[154:155], v[190:191], v[122:123], v[154:155] neg_lo:[1,0,0] neg_hi:[1,0,0]\n\t" \
  "v_pk_mul_f32 v[120:121], v[184:185], v[120:121]\n\t" \
  "v_pk_mul_f32 v[122:123], v[186:187], v[122:123]\n\t" \
  "v_pk_fma_f32 v[120:121], v[188:189], v[88:89], v[120:121] neg_lo:[1,0,1] neg_hi:[1,0,1]\n\t" \
  "v_pk_fma_f32 v[122:123], v[190:191], v[90:91], v[122:123] neg_lo:[1,0,1] neg_hi:[1,0,1]\n\t" \
  "v_pk_mul_f32 v[156:157], v[184:185], v[92:93]\n\t" \
  "v_pk_mul_f32 v[158:159], v[186:187], v[94:95]\n\t" \
  "v_pk_fma_f32 v[156:157], v[188:189], v[124:125], v[156:157] neg_lo:[1,0,0] neg_hi:[1,0,0]\n\t" \
  "v_pk_fma_f32 v[158:159], v[190:191], v[126:127], v[158:159] neg_lo:[1,0,0] neg_hi:[1,0,0]\n\t" \
  "v_pk_mul_f32 v[124:125], v[184:185], v[124:125]\n\t" \
  "v_pk_mul_f32 v[126:127], v[186:187], v[126:127]\n\t" \
  "v_pk_fma_f32 v[124:125], v[188:189], v[92:93], v[124:125] neg_lo:[1,0,1] neg_hi:[1,0,1]\n\t" \
  "v_pk_fma_f32 v[126:127], v[190:191], v[94:95], v[126:127] neg_lo:[1,0,1] neg_hi:[1,0,1]\n\t" \
  "v_mov_b32 v160, 0\n\t" \
  "v_mov_b32 v161, %[tgA]\n\t" \
  "v_mov_b32 v162, 0\n\t" \
  "v_mov_b32 v163, %[tgB]\n\t" \
  "v_mov_b32 v164, 0\n\t" \
  "v_mov_b32 v165, %[tgA]\n\t" \
  "v_mov_b32 v166, 0\n\t" \
  "v_mov_b32 v167, %[tgB]\n\t" \
  "v_mov_b32 v168, 0\n\t" \
  "v_mov_b32 v169, %[tgA]\n\t" \
  "v_mov_b32 v170, 0\n\t" \
  "v_mov_b32 v171, %[tgB]\n\t" \
  "v_mov_b32 v172, 0\n\t" \
  "v_mov_b32 v173, %[tgA]\n\t" \
  "v_mov_b32 v174, 0\n\t" \
  "v_mov_b32 v175, %[tgB]\n\t" \
  "s_waitcnt vmcnt(0)\n\t" \
  "v_cmp_ne_u32 vcc, %[tgA], v226\n\t" \
  "s_and_b64 vcc, vcc, %[eh]\n\t" \
  "s_cselect_b32 %[s0], 0, 1\n\t" \
  "s_cbranch_scc0 Lh1_%=\n\t" \
  "s_mov_b64 exec, %[eh]\n\t" \
  "v_mov_b32 v226, 0\n\t" \
  "v_lshrrev_b32 v227, 2, %[l16]\n\t" \
  "s_cbranch_execz Lhzb_%=\n\t" \
  "buffer_atomic_or v226, v227, %[srd], %[sht] offen sc0 sc1\n\t" \
  "s_branch Lhyb_%=\n\t" \
  "Lhzb_%=:\n\t" \
  "s_mov_b64 exec, 1\n\t" \
  "buffer_load_dword v226, %[l16], %[srz], 0 offen\n\t" \
  "Lhyb_%=:\n\t" \
  "s_mov_b64 exec, -1\n\t" \
  "s_branch Lpa_%=\n\t" \
  "Lh1_%=:\n\t" \
  "s_mov_b64 exec, %[em0]\n\t" \
  "s_cbranch_execz Lpa_%=\n\t" \
  "buffer_load_dwordx4 v[160:163], %[plo], %[srd], %[sl0] offen sc1\n\t" \
  "s_mov_b64 exec, %[em1]\n\t" \
  "s_cbranch_execz Lpa_%=\n\t" \
  "buffer_load_dwordx4 v[164:167], %[plo], %[srd], %[sl1] offen sc1\n\t" \
  "s_mov_b64 exec, %[em2]\n\t" \
  "s_cbranch_execz Lpa_%=\n\t" \
  "buffer_load_dwordx4 v[168:171], %[plo], %[srd], %[sl2] offen sc1\n\t" \
  "s_mov_b64 exec, %[em3]\n\t" \
  "s_cbranch_execz Lpa_%=\n\t" \
  "buffer_load_dwordx4 v[172:175], %[plo], %[srd], %[sl3] offen sc1\n\t" \
  "Lpa_%=:\n\t" \
  "s_mov_b32 exec_lo, 0xaaaaaaaa\n\t" \
  "s_mov_b32 exec_hi, 0xaaaaaaaa\n\t" \
  "v_pk_mul_f32 v[244:245], v[196:197], v[188:189]\n\t" \
  "v_pk_mul_f32 v[246:247], v[198:199], v[190:191]\n\t" \
  "v_pk_mul_f32 v[248:249], v[196:197], v[184:185]\n\t" \
  "v_pk_mul_f32 v[250:251], v[198:199], v[186:187]\n\t" \
  "v_pk_fma_f32 v[184:185], v[192:193], v[184:185], v[244:245] neg_lo:[0,0,1] neg_hi:[0,0,1]\n\t" \
  "v_pk_fma_f32 v[186:187], v[194:195], v[186:187], v[246:247] neg_lo:[0,0,1] neg_hi:[0,0,1]\n\t" \
  "v_pk_fma_f32 v[188:189], v[192:193], v[188:189], v[248:249]\n\t" \
  "v_pk_fma_f32 v[190:191], v[194:195], v[190:191], v[250:251]\n\t" \
  "s_mov_b32 exec_lo, 0xcccccccc\n\t" \
  "s_mov_b32 exec_hi, 0xcccccccc\n\t" \
  "v_pk_mul_f32 v[244:245], v[204:205], v[188:189]\n\t" \
  "v_pk_mul_f32 v[246:247], v[206:207], v[190:191]\n\t" \
  "v_pk_mul_f32 v[248:249], v[204:205], v[184:185]\n\t" \
  "v_pk_mul_f32 v[250:251], v[206:207], v[186:187]\n\t" \
  "v_pk_fma_f32 v[184:185], v[200:201], v[184:185], v[244:245] neg_lo:[0,0,1] neg_hi:[0,0,1]\n\t" \
  "v_pk_fma_f32 v[186:187], v[202:203], v[186:187], v[246:247] neg_lo:[0,0,1] neg_hi:[0,0,1]\n\t" \
  "v_pk_fma_f32 v[188:189], v[200:201], v[188:189], v[248:249]\n\t" \
  "v_pk_fma_f32 v[190:191], v[202:203], v[190:191], v[250:251]\n\t" \
  "s_mov_b32 exec_lo, 0xf0f0f0f0\n\t" \
  "s_mov_b32 exec_hi, 0xf0f0f0f0\n\t" \
  "v_pk_mul_f32 v[244:245], v[212:213], v[188:189]\n\t" \
  "v_pk_mul_f32 v[246:247], v[214:215], v[190:191]\n\t" \
  "v_pk_mul_f32 v[248:249], v[212:213], v[184:185]\n\t" \
  "v_pk_mul_f32 v[250:251], v[214:215], v[186:187]\n\t" \
  "v_pk_fma_f32 v[184:185], v[208:209], v[184:185], v[244:245] neg_lo:[0,0,1] neg_hi:[0,0,1]\n\t" \
  "v_pk_fma_f32 v[186:187], v[210:211], v[186:187], v[246:247] neg_lo:[0,0,1] neg_hi:[0,0,1]\n\t" \
  "v_pk_fma_f32 v[188:189], v[208:209], v[188:189], v[248:249]\n\t" \
  "v_pk_fma_f32 v[190:191], v[210:211], v[190:191], v[250:251]\n\t" \
  "s_mov_b32 exec_lo, 0xff00ff00\n\t" \
  "s_mov_b32 exec_hi, 0xff00ff00\n\t" \
  "v_pk_mul_f32 v[244:245], v[220:221], v[188:189]\n\t" \
  "v_pk_mul_f32 v[246:247], v[222:223], v[190:191]\n\t" \
  "v_pk_mul_f32 v[248:249], v[220:221], v[184:185]\n\t" \
  "v_pk_mul_f32 v[250:251], v[222:223], v[186:187]\n\t" \
  "v_pk_fma_f32 v[184:185], v[216:217], v[184:185], v[244:245] neg_lo:[0,0,1] neg_hi:[0,0,1]\n\t" \
  "v_pk_fma_f32 v[186:187], v[218:219], v[186:187], v[246:247] neg_lo:[0,0,1] neg_hi:[0,0,1]\n\t" \
  "v_pk_fma_f32 v[188:189], v[216:217], v[188:189], v[248:249]\n\t" \
  "v_pk_fma_f32 v[190:191], v[218:219], v[190:191], v[250:251]\n\t" \
  "s_mov_b64 exec, -1\n\t" \
  "s_cmp_eq_u32 %[s0], 1\n\t" \
  "s_cbranch_scc1 Lq_%=\n\t" \
  "Lh2_%=:\n\t" \
  "s_waitcnt vmcnt(0)\n\t" \
  "v_cmp_ne_u32 vcc, %[tgA], v226\n\t" \
  "s_and_b64 vcc, vcc, %[eh]\n\t" \
  "s_cbranch_scc0 Lh3_%=\n\t" \
  "s_sub_i32 %[bud], %[bud], 16\n\t" \
  "s_cmp_lt_i32 %[bud], 0\n\t" \
  "s_cbranch_scc1 Lh3_%=\n\t" \
  "s_sleep 1\n\t" \
  "s_mov_b64 exec, %[eh]\n\t" \
  "v_mov_b32 v226, 0\n\t" \
  "v_lshrrev_b32 v227, 2, %[l16]\n\t" \
  "s_cbranch_execz Lhzc_%=\n\t" \
  "buffer_atomic_or v226, v227, %[srd], %[sht] offen sc0 sc1\n\t" \
  "s_branch Lhyc_%=\n\t" \
  "Lhzc_%=:\n\t" \
  "s_mov_b64 exec, 1\n\t" \
  "buffer_load_dword v226, %[l16], %[srz], 0 offen\n\t" \
  "Lhyc_%=:\n\t" \
  "s_mov_b64 exec, -1\n\t" \
  "s_branch Lh2_%=\n\t" \
  "Lh3_%=:\n\t" \
  "s_mov_b64 exec, %[em0]\n\t" \
  "s_cbranch_execz Lpc_%=\n\t" \
  "buffer_load_dwordx4 v[160:163], %[plo], %[srd], %[sl0] offen sc1\n\t" \
  "s_mov_b64 exec, %[em1]\n\t" \
  "s_cbranch_execz Lpc_%=\n\t" \
  "buffer_load_dwordx4 v[164:167], %[plo], %[srd], %[sl1] offen sc1\n\t" \
  "s_mov_b64 exec, %[em2]\n\t" \
  "s_cbranch_execz Lpc_%=\n\t" \
  "buffer_load_dwordx4 v[168:171], %[plo], %[srd], %[sl2] offen sc1\n\t" \
  "s_mov_b64 exec, %[em3]\n\t" \
  "s_cbranch_execz Lpc_%=\n\t" \
  "buffer_load_dwordx4 v[172:175], %[plo], %[srd], %[sl3] offen sc1\n\t" \
  "Lpc_%=:\n\t" \
  "Lq_%=:\n\t" \
  "s_mov_b64 exec, -1\n\t" \
  "s_waitcnt vmcnt(0)\n\t" \
  "v_cmpx_eq_u32 vcc, %[tgA], v161\n\t" \
  "v_cmpx_eq_u32 vcc, %[tgB], v163\n\t" \
  "v_cmpx_eq_u32 vcc, %[tgA], v165\n\t" \
  "v_cmpx_eq_u32 vcc, %[tgB], v167\n\t" \
  "v_cmpx_eq_u32 vcc, %[tgA], v169\n\t" \
  "v_cmpx_eq_u32 vcc, %[tgB], v171\n\t" \
  "v_cmpx_eq_u32 vcc, %[tgA], v173\n\t" \
  "v_cmpx_eq_u32 vcc, %[tgB], v175\n\t" \
  "s_cmp_eq_u64 exec, -1\n\t" \
  "s_mov_b64 exec, -1\n\t" \
  "s_cbranch_scc1 Lq2_%=\n\t" \
  "s_sub_i32 %[bud], %[bud], 16\n\t" \
  "s_cmp_lt_i32 %[bud], 0\n\t" \
  "s_cbranch_scc1 Lq2_%=\n\t" \
  "s_sleep 4\n\t" \
  "s_mov_b64 exec, %[em0]\n\t" \
  "s_cbranch_execz Lpb_%=\n\t" \
  "buffer_load_dwordx4 v[160:163], %[plo], %[srd], %[sl0] offen sc1\n\t" \
  "s_mov_b64 exec, %[em1]\n\t" \
  "s_cbranch_execz Lpb_%=\n\t" \
  "buffer_load_dwordx4 v[164:167], %[plo], %[srd], %[sl1] offen sc1\n\t" \
  "s_mov_b64 exec, %[em2]\n\t" \
  "s_cbranch_execz Lpb_%=\n\t" \
  "buffer_load_dwordx4 v[168:171], %[plo], %[srd], %[sl2] offen sc1\n\t" \
  "s_mov_b64 exec, %[em3]\n\t" \
  "s_cbranch_execz Lpb_%=\n\t" \
  "buffer_load_dwordx4 v[172:175], %[plo], %[srd], %[sl3] offen sc1\n\t" \
  "Lpb_%=:\n\t" \
  "s_branch Lq_%=\n\t" \
  "Lq2_%=:\n\t" \
  "s_mov_b32 %[scn], %[mcar]\n\t" \
  "s_mov_b32 m0, %[scn]\n\t" \
  "s_mov_b64 exec, 1\n\t" \
  "buffer_load_dword %[l16], %[srz], 0 offen lds\n\t" \
  "s_mov_b64 exec, -1\n\t" \
  "v_fma_f32 v244, -v231, v174, v168\n\t" \
  "v_fma_f32 v245, v231, v172, v170\n\t" \
  "v_fma_f32 v252, v230, v172, v244\n\t" \
  "v_fma_f32 v253, v230, v174, v245\n\t" \
  "v_fma_f32 v244, -v231, v253, v164\n\t" \
  "v_fma_f32 v245, v231, v252, v166\n\t" \
  "v_fma_f32 v252, v230, v252, v244\n\t" \
  "v_fma_f32 v253, v230, v253, v245\n\t" \
  "v_fma_f32 v244, -v231, v253, v160\n\t" \
  "v_fma_f32 v245, v231, v252, v162\n\t" \
  "v_fma_f32 v252, v230, v252, v244\n\t" \
  "v_fma_f32 v253, v230, v253, v245\n\t" \
  "v_mul_f32 v244, v233, v253\n\t" \
  "v_mul_f32 v245, v233, v252\n\t" \
  "v_fma_f32 v254, v232, v252, -v244\n\t" \
  "v_fma_f32 v255, v232, v253, v245\n\t" \
  "v_fma_f32 v252, v232, v252, -v244\n\t" \
  "v_fma_f32 v253, v232, v253, v245\n\t" \
  "s_nop 1\n\t" \
  "v_permlane16_swap_b32 v252, v254\n\t" \
  "v_permlane16_swap_b32 v253, v255\n\t" \
  "s_nop 0\n\t" \
  "v_add_f32 v244, v252, v254\n\t" \
  "v_add_f32 v245, v253, v255\n\t" \
  "v_add_f32 v252, v252, v254\n\t" \
  "v_add_f32 v253, v253, v255\n\t" \
  "v_mov_b32 v254, v244\n\t" \
  "v_mov_b32 v255, v245\n\t" \
  "s_nop 1\n\t" \
  "v_permlane32_swap_b32 v252, v254\n\t" \
  "v_permlane32_swap_b32 v253, v255\n\t" \
  "s_nop 0\n\t" \
  "v_add_f32 v244, v252, v254\n\t" \
  "v_add_f32 v245, v253, v255\n\t" \
  "v_add_f32 v252, v252, v254\n\t" \
  "v_add_f32 v253, v253, v255\n\t" \
  "v_mov_b32 v254, v244\n\t" \
  "v_mov_b32 v255, v245\n\t" \
  "v_mov_b32_dpp v254, v252 row_ror:12 row_mask:0x2 bank_mask:0xf\n\t" \
  "v_mov_b32_dpp v255, v253 row_ror:12 row_mask:0x2 bank_mask:0xf\n\t" \
  "v_mov_b32_dpp v254, v252 row_ror:8 row_mask:0x4 bank_mask:0xf\n\t" \
  "v_mov_b32_dpp v255, v253 row_ror:8 row_mask:0x4 bank_mask:0xf\n\t" \
  "v_mov_b32_dpp v254, v252 row_ror:4 row_mask:0x8 bank_mask:0xf\n\t" \
  "v_mov_b32_dpp v255, v253 row_ror:4 row_mask:0x8 bank_mask:0xf\n\t" \
  "s_nop 1\n\t" \
  "v_mov_b32_dpp v244, v254 row_newbcast:0 row_mask:0xf bank_mask:0xf\n\t" \
  "v_mov_b32_dpp v245, v254 row_newbcast:1 row_mask:0xf bank_mask:0xf\n\t" \
  "v_mov_b32_dpp v246, v254 row_newbcast:2 row_mask:0xf bank_mask:0xf\n\t" \
  "v_mov_b32_dpp v247, v254 row_newbcast:3 row_mask:0xf bank_mask:0xf\n\t" \
  "v_mov_b32_dpp v248, v255 row_newbcast:0 row_mask:0xf bank_mask:0xf\n\t" \
  "v_mov_b32_dpp v249, v255 row_newbcast:1 row_mask:0xf bank_mask:0xf\n\t" \
  "v_mov_b32_dpp v250, v255 row_newbcast:2 row_mask:0xf bank_mask:0xf\n\t" \
  "v_mov_b32_dpp v251, v255 row_newbcast:3 row_mask:0xf bank_mask:0xf\n\t" \
  "v_pk_fma_f32 v[236:237], v[184:185], v[244:245], v[236:237]\n\t" \
  "v_pk_fma_f32 v[238:239], v[186:187], v[246:247], v[238:239]\n\t" \
  "v_pk_fma_f32 v[240:241], v[184:185], v[248:249], v[240:241]\n\t" \
  "v_pk_fma_f32 v[242:243], v[186:187], v[250:251], v[242:243]\n\t" \
  "v_pk_fma_f32 v[236:237], v[188:189], v[248:249], v[236:237] neg_lo:[1,0,0] neg_hi:[1,0,0]\n\t" \
  "v_pk_fma_f32 v[238:239], v[190:191], v[250:251], v[238:239] neg_lo:[1,0,0] neg_hi:[1,0,0]\n\t" \
  "v_pk_fma_f32 v[240:241], v[188:189], v[244:245], v[240:241]\n\t" \
  "v_pk_fma_f32 v[242:243], v[190:191], v[246:247], v[242:243]\n\t" \
  "v_pk_fma_f32 v[128:129], v[180:181], v[240:241], v[128:129] neg_lo:[1,0,0] neg_hi:[1,0,0]\n\t" \
  "v_pk_fma_f32 v[130:131], v[182:183], v[242:243], v[130:131] neg_lo:[1,0,0] neg_hi:[1,0,0]\n\t" \
  "v_pk_fma_f32 v[96:97], v[180:181], v[236:237], v[96:97] neg_lo:[1,0,0] neg_hi:[1,0,0]\n\t" \
  "v_pk_fma_f32 v[98:99], v[182:183], v[238:239], v[98:99] neg_lo:[1,0,0] neg_hi:[1,0,0]\n\t" \
  "v_pk_fma_f32 v[128:129], v[176:177], v[236:237], v[128:129]\n\t" \
  "v_pk_fma_f32 v[130:131], v[178:179], v[238:239], v[130:131]\n\t" \
  "v_pk_fma_f32 v[96:97], v[176:177], v[240:241], v[96:97] neg_lo:[1,0,0] neg_hi:[1,0,0]\n\t" \
  "v_pk_fma_f32 v[98:99], v[178:179], v[242:243], v[98:99] neg_lo:[1,0,0] neg_hi:[1,0,0]\n\t" \
  "v_cvt_pk_bf16_f32 v160, v128, v129\n\t" \
  "v_cvt_pk_bf16_f32 v161, v130, v131\n\t" \
  "v_cvt_pk_bf16_f32 v162, v96, v97\n\t" \
  "v_cvt_pk_bf16_f32 v163, v98, v99\n\t" \
  "v_pk_fma_f32 v[132:133], v[180:181], v[96:97], v[132:133]\n\t" \
  "v_pk_fma_f32 v[134:135], v[182:183], v[98:99], v[134:135]\n\t" \
  "v_permlane16_swap_b32 v160, v162\n\t" \
  "v_permlane16_swap_b32 v161, v163\n\t" \
  "buffer_store_dwordx4 v[160:163], %[l16], %[srd], %[sh] offen offset:0 sc1\n\t" \
  "v_pk_fma_f32 v[100:101], v[180:181], v[128:129], v[100:101] neg_lo:[1,0,0] neg_hi:[1,0,0]\n\t" \
  "v_pk_fma_f32 v[102:103], v[182:183], v[130:131], v[102:103] neg_lo:[1,0,0] neg_hi:[1,0,0]\n\t" \
  "v_pk_fma_f32 v[132:133], v[176:177], v[128:129], v[132:133]\n\t" \
  "v_pk_fma_f32 v[134:135], v[178:179], v[130:131], v[134:135]\n\t" \
  "v_pk_fma_f32 v[100:101], v[176:177], v[96:97], v[100:101]\n\t" \
  "v_pk_fma_f32 v[102:103], v[178:179], v[98:99], v[102:103]\n\t" \
  "v_cvt_pk_bf16_f32 v164, v132, v133\n\t" \
  "v_cvt_pk_bf16_f32 v165, v134, v135\n\t" \
  "v_cvt_pk_bf16_f32 v166, v100, v101\n\t" \
  "v_cvt_pk_bf16_f32 v167, v102, v103\n\t" \
  "v_pk_fma_f32 v[136:137], v[180:181], v[100:101], v[136:137]\n\t" \
  "v_pk_fma_f32 v[138:139], v[182:183], v[102:103], v[138:139]\n\t" \
  "v_permlane16_swap_b32 v164, v166\n\t" \
  "v_permlane16_swap_b32 v165, v167\n\t" \
  "buffer_store_dwordx4 v[164:167], %[l16], %[srd], %[sh] offen offset:1024 sc1\n\t" \
  "v_pk_fma_f32 v[104:105], v[180:181], v[132:133], v[104:105] neg_lo:[1,0,0] neg_hi:[1,0,0]\n\t" \
  "v_pk_fma_f32 v[106:107], v[182:183], v[134:135], v[106:107] neg_lo:[1,0,0] neg_hi:[1,0,0]\n\t" \
  "v_pk_fma_f32 v[136:137], v[176:177], v[132:133], v[136:137]\n\t" \
  "v_pk_fma_f32 v[138:139], v[178:179], v[134:135], v[138:139]\n\t" \
  "v_pk_fma_f32 v[104:105], v[176:177], v[100:101], v[104:105]\n\t" \
  "v_pk_fma_f32 v[106:107], v[178:179], v[102:103], v[106:107]\n\t" \
  "v_cvt_pk_bf16_f32 v168, v136, v137\n\t" \
  "v_cvt_pk_bf16_f32 v169, v138, v139\n\t" \
  "v_cvt_pk_bf16_f32 v170, v104, v105\n\t" \
  "v_cvt_pk_bf16_f32 v171, v106, v107\n\t" \
  "v_pk_fma_f32 v[140:141], v[180:181], v[104:105], v[140:141]\n\t" \
  "v_pk_fma_f32 v[142:143], v[182:183], v[106:107], v[142:143]\n\t" \
  "v_permlane16_swap_b32 v168, v170\n\t" \
  "v_permlane16_swap_b32 v169, v171\n\t" \
  "buffer_store_dwordx4 v[168:171], %[l16], %[srd], %[sh] offen offset:2048 sc1\n\t" \
  "v_pk_fma_f32 v[108:109], v[180:181], v[136:137], v[108:109] neg_lo:[1,0,0] neg_hi:[1,0,0]\n\t" \
  "v_pk_fma_f32 v[110:111], v[182:183], v[138:139], v[110:111] neg_lo:[1,0,0] neg_hi:[1,0,0]\n\t" \
  "v_pk_fma_f32 v[140:141], v[176:177], v[136:137], v[140:141]\n\t" \
  "v_pk_fma_f32 v[142:143], v[178:179], v[138:139], v[142:143]\n\t" \
  "v_pk_fma_f32 v[108:109], v[176:177], v[104:105], v[108:109]\n\t" \
  "v_pk_fma_f32 v[110:111], v[178:179], v[106:107], v[110:111]\n\t" \
  "v_cvt_pk_bf16_f32 v172, v140, v141\n\t" \
  "v_cvt_pk_bf16_f32 v173, v142, v143\n\t" \
  "v_cvt_pk_bf16_f32 v174, v108, v109\n\t" \
  "v_cvt_pk_bf16_f32 v175, v110, v111\n\t" \
  "v_pk_fma_f32 v[144:145], v[180:181], v[108:109], v[144:145]\n\t" \
  "v_pk_fma_f32 v[146:147], v[182:183], v[110:111], v[146:147]\n\t" \
  "v_permlane16_swap_b32 v172, v174\n\t" \
  "v_permlane16_swap_b32 v173, v175\n\t" \
  "buffer_store_dwordx4 v[172:175], %[l16], %[srd], %[sh] offen offset:3072 sc1\n\t" \
  "s_add_u32 %[sh], %[sh], 0x1000\n\t" \
  "v_pk_fma_f32 v[112:113], v[180:181], v[140:141], v[112:113] neg_lo:[1,0,0] neg_hi:[1,0,0]\n\t" \
  "v_pk_fma_f32 v[114:115], v[182:183], v[142:143], v[114:115] neg_lo:[1,0,0] neg_hi:[1,0,0]\n\t" \
  "v_pk_fma_f32 v[144:145], v[176:177], v[140:141], v[144:145]\n\t" \
  "v_pk_fma_f32 v[146:147], v[178:179], v[142:143], v[146:147]\n\t" \
  "v_pk_fma_f32 v[112:113], v[176:177], v[108:109], v[112:113]\n\t" \
  "v_pk_fma_f32 v[114:115], v[178:179], v[110:111], v[114:115]\n\t" \
  "v_cvt_pk_bf16_f32 v192, v144, v145\n\t" \
  "v_cvt_pk_bf16_f32 v193, v146, v147\n\t" \
  "v_cvt_pk_bf16_f32 v194, v112, v113\n\t" \
  "v_cvt_pk_bf16_f32 v195, v114, v115\n\t" \
  "v_pk_fma_f32 v[148:149], v[180:181], v[112:113], v[148:149]\n\t" \
  "v_pk_fma_f32 v[150:151], v[182:183], v[114:115], v[150:151]\n\t" \
  "v_permlane16_swap_b32 v192, v194\n\t" \
  "v_permlane16_swap_b32 v193, v195\n\t" \
  "buffer_store_dwordx4 v[192:195], %[l16], %[srd], %[sh] offen offset:0 sc1\n\t" \
  "v_pk_fma_f32 v[116:117], v[180:181], v[144:145], v[116:117] neg_lo:[1,0,0] neg_hi:[1,0,0]\n\t" \
  "v_pk_fma_f32 v[118:119], v[182:183], v[146:147], v[118:119] neg_lo:[1,0,0] neg_hi:[1,0,0]\n\t" \
  "v_pk_fma_f32 v[148:149], v[176:177], v[144:145], v[148:149]\n\t" \
  "v_pk_fma_f32 v[150:151], v[178:179], v[146:147], v[150:151]\n\t" \
  "v_pk_fma_f32 v[116:117], v[176:177], v[112:113], v[116:117]\n\t" \
  "v_pk_fma_f32 v[118:119], v[178:179], v[114:115], v[118:119]\n\t" \
  "v_cvt_pk_bf16_f32 v196, v148, v149\n\t" \
  "v_cvt_pk_bf16_f32 v197, v150, v151\n\t" \
  "v_cvt_pk_bf16_f32 v198, v116, v117\n\t" \
  "v_cvt_pk_bf16_f32 v199, v118, v119\n\t" \
  "v_pk_fma_f32 v[152:153], v[180:181], v[116:117], v[152:153]\n\t" \
  "v_pk_fma_f32 v[154:155], v[182:183], v[118:119], v[154:155]\n\t" \
  "v_permlane16_swap_b32 v196, v198\n\t" \
  "v_permlane16_swap_b32 v197, v199\n\t" \
  "buffer_store_dwordx4 v[196:199], %[l16], %[srd], %[sh] offen offset:1024 sc1\n\t" \
  "v_pk_fma_f32 v[120:121], v[180:181], v[148:149], v[120:121] neg_lo:[1,0,0] neg_hi:[1,0,0]\n\t" \
  "v_pk_fma_f32 v[122:123], v[182:183], v[150:151], v[122:123] neg_lo:[1,0,0] neg_hi:[1,0,0]\n\t" \
  "v_pk_fma_f32 v[152:153], v[176:177], v[148:149], v[152:153]\n\t" \
  "v_pk_fma_f32 v[154:155], v[178:179], v[150:151], v[154:155]\n\t" \
  "v_pk_fma_f32 v[120:121], v[176:177], v[116:117], v[120:121]\n\t" \
  "v_pk_fma_f32 v[122:123], v[178:179], v[118:119], v[122:123]\n\t" \
  "v_cvt_pk_bf16_f32 v200, v152, v153\n\t" \
  "v_cvt_pk_bf16_f32 v201, v154, v155\n\t" \
  "v_cvt_pk_bf16_f32 v202, v120, v121\n\t" \
  "v_cvt_pk_bf16_f32 v203, v122, v123\n\t" \
  "v_pk_fma_f32 v[156:157], v[180:181], v[120:121], v[156:157]\n\t" \
  "v_pk_fma_f32 v[158:159], v[182:183], v[122:123], v[158:159]\n\t" \
  "v_permlane16_swap_b32 v200, v202\n\t" \
  "v_permlane16_swap_b32 v201, v203\n\t" \
  "buffer_store_dwordx4 v[200:203], %[l16], %[srd], %[sh] offen offset:2048 sc1\n\t" \
  "v_pk_fma_f32 v[124:125], v[180:181], v[152:153], v[124:125] neg_lo:[1,0,0] neg_hi:[1,0,0]\n\t" \
  "v_pk_fma_f32 v[126:127], v[182:183], v[154:155], v[126:127] neg_lo:[1,0,0] neg_hi:[1,0,0]\n\t" \
  "v_pk_fma_f32 v[156:157], v[176:177], v[152:153], v[156:157]\n\t" \
  "v_pk_fma_f32 v[158:159], v[178:179], v[154:155], v[158:159]\n\t" \
  "v_pk_fma_f32 v[124:125], v[176:177], v[120:121], v[124:125]\n\t" \
  "v_pk_fma_f32 v[126:127], v[178:179], v[122:123], v[126:127]\n\t" \
  "v_cvt_pk_bf16_f32 v204, v156, v157\n\t" \
  "v_cvt_pk_bf16_f32 v205, v158, v159\n\t" \
  "v_cvt_pk_bf16_f32 v206, v124, v125\n\t" \
  "v_cvt_pk_bf16_f32 v207, v126, v127\n\t" \
  "s_nop 1\n\t" \
  "v_permlane16_swap_b32 v204, v206\n\t" \
  "v_permlane16_swap_b32 v205, v207\n\t" \
  "buffer_store_dwordx4 v[204:207], %[l16], %[srd], %[sh] offen offset:3072 sc1\n\t" \
  "s_add_u32 %[scn], %[scn], 8\n\t" \
  "s_mov_b32 m0, %[scn]\n\t" \
  "s_mov_b64 exec, 1\n\t" \
  "buffer_load_dword %[l16], %[srz], 0 offen lds\n\t" \
  "s_mov_b64 exec, -1\n\t" \
  "s_branch Lend_%=\n\t" \
  "Lsp0_%=:\n\t" \
  "ds_read_b64 v[224:225], %[fl]\n\t" \
  "s_sub_i32 %[bud], %[bud], 1\n\t" \
  "s_cmp_lt_i32 %[bud], 0\n\t" \
  "s_cbranch_scc1 Lb0_%=\n\t" \
  "s_waitcnt lgkmcnt(0)\n\t" \
  "v_or_b32 v224, v224, v225\n\t" \
  "s_nop 0\n\t" \
  "v_readfirstlane_b32 %[s0], v224\n\t" \
  "s_cmp_eq_u32 %[s0], 0\n\t" \
  "s_cbranch_scc0 Lsp0_%=\n\t" \
  "s_branch Lb0_%=\n\t" \
  "Lx0_%=:\n\t" \
  "v_add_u32 v244, %[sx], %[xo]\n\t" \
  "v_xor_b32 v245, 16, v244\n\t" \
  "v_xor_b32 v246, 32, v244\n\t" \
  "v_xor_b32 v247, 48, v244\n\t" \
  "ds_read_b64 %[xe00], v244 offset:0\n\t" \
  "ds_read_b64 %[xe10], v244 offset:2048\n\t" \
  "ds_read_b64 %[xe01], v245 offset:128\n\t" \
  "ds_read_b64 %[xe11], v245 offset:2176\n\t" \
  "ds_read_b64 %[xe02], v246 offset:256\n\t" \
  "ds_read_b64 %[xe12], v246 offset:2304\n\t" \
  "ds_read_b64 %[xe03], v247 offset:384\n\t" \
  "ds_read_b64 %[xe13], v247 offset:2432\n\t" \
  "s_branch Lxb0_%=\n\t" \
  "Lsp1_%=:\n\t" \
  "ds_read_b64 v[224:225], %[fl]\n\t" \
  "s_sub_i32 %[bud], %[bud], 1\n\t" \
  "s_cmp_lt_i32 %[bud], 0\n\t" \
  "s_cbranch_scc1 Lb1_%=\n\t" \
  "s_waitcnt lgkmcnt(0)\n\t" \
  "v_or_b32 v224, v224, v225\n\t" \
  "s_nop 0\n\t" \
  "v_readfirstlane_b32 %[s0], v224\n\t" \
  "s_cmp_eq_u32 %[s0], 0\n\t" \
  "s_cbranch_scc0 Lsp1_%=\n\t" \
  "s_branch Lb1_%=\n\t" \
  "Lx1_%=:\n\t" \
  "v_add_u32 v244, %[sx], %[xo]\n\t" \
  "v_xor_b32 v245, 16, v244\n\t" \
  "v_xor_b32 v246, 32, v244\n\t" \
  "v_xor_b32 v247, 48, v244\n\t" \
  "ds_read_b64 %[xe00], v244 offset:0\n\t" \
  "ds_read_b64 %[xe10], v244 offset:2048\n\t" \
  "ds_read_b64 %[xe01], v245 offset:128\n\t" \
  "ds_read_b64 %[xe11], v245 offset:2176\n\t" \
  "ds_read_b64 %[xe02], v246 offset:256\n\t" \
  "ds_read_b64 %[xe12], v246 offset:2304\n\t" \
  "ds_read_b64 %[xe03], v247 offset:384\n\t" \
  "ds_read_b64 %[xe13], v247 offset:2432\n\t" \
  "s_branch Lxb1_%=\n\t" \
  "Lsp2_%=:\n\t" \
  "ds_read_b64 v[224:225], %[fl]\n\t" \
  "s_sub_i32 %[bud], %[bud], 1\n\t" \
  "s_cmp_lt_i32 %[bud], 0\n\t" \
  "s_cbranch_scc1 Lb2_%=\n\t" \
  "s_waitcnt lgkmcnt(0)\n\t" \
  "v_or_b32 v224, v224, v225\n\t" \
  "s_nop 0\n\t" \
  "v_readfirstlane_b32 %[s0], v224\n\t" \
  "s_cmp_eq_u32 %[s0], 0\n\t" \
  "s_cbranch_scc0 Lsp2_%=\n\t" \
  "s_branch Lb2_%=\n\t" \
  "Lend_%=:\n\t" \
  "s_nop 7\n\t" \
  "s_nop 7\n\t"
#define ASM_MB \
  "s_nop 4\n\t" \
  "s_mov_b32 %[sg], 0\n\t" \
  "s_mov_b32 m0, %[scn]\n\t" \
  "Lc0_%=:\n\t" \
  "ds_read_b32 v192, %[f9]\n\t" \
  "s_sub_i32 %[bud], %[bud], 1\n\t" \
  "s_cmp_lt_i32 %[bud], 0\n\t" \
  "s_cbranch_scc1 Lc0b_%=\n\t" \
  "s_waitcnt lgkmcnt(0)\n\t" \
  "v_cmp_ne_u32 vcc, 0, v192\n\t" \
  "s_cbranch_vccnz Lc0_%=\n\t" \
  "Lc0b_%=:\n\t" \
  "ds_read_b128 v[128:131], %[pc0] offset:0\n\t" \
  "ds_read_b128 v[132:135], %[pc0] offset:2048\n\t" \
  "ds_read_b128 v[136:139], %[pc0] offset:4096\n\t" \
  "ds_read_b128 v[140:143], %[pc0] offset:6144\n\t" \
  "ds_read_b128 v[144:147], %[pc1] offset:0\n\t" \
  "ds_read_b128 v[148:151], %[pc1] offset:2048\n\t" \
  "ds_read_b128 v[152:155], %[pc1] offset:4096\n\t" \
  "ds_read_b128 v[156:159], %[pc1] offset:6144\n\t" \
  "s_cmp_eq_u32 %[w], 0\n\t" \
  "s_cbranch_scc0 Lw_%=\n\t" \
  "Lf_%=:\n\t" \
  "v_mov_b32 v196, 0\n\t" \
  "s_mov_b64 exec, 1\n\t" \
  "buffer_atomic_or v196, off, %[srd], %[sf] sc0 sc1\n\t" \
  "s_mov_b64 exec, -1\n\t" \
  "s_sub_i32 %[bud], %[bud], 16\n\t" \
  "s_cmp_lt_i32 %[bud], 0\n\t" \
  "s_cbranch_scc1 Lf2_%=\n\t" \
  "s_waitcnt vmcnt(0)\n\t" \
  "v_readfirstlane_b32 %[s0], v196\n\t" \
  "s_cmp_lg_u32 %[s0], 32\n\t" \
  "s_cbranch_scc0 Lf2_%=\n\t" \
  "s_sleep 1\n\t" \
  "s_branch Lf_%=\n\t" \
  "Lf2_%=:\n\t" \
  "v_mov_b32 v192, 0\n\t" \
  "v_mov_b32 v193, 0\n\t" \
  "ds_write_b64 %[hr], v[192:193]\n\t" \
  "Lf3_%=:\n\t" \
  "s_setprio 2\n\t" \
  "buffer_load_dwordx4 v[64:67], %[vo], %[srd], %[h0] offen offset:0 sc1\n\t" \
  "buffer_load_dwordx4 v[68:71], %[vo], %[srd], %[h0] offen offset:1024 sc1\n\t" \
  "buffer_load_dwordx4 v[72:75], %[vo], %[srd], %[h1] offen offset:0 sc1\n\t" \
  "buffer_load_dwordx4 v[76:79], %[vo], %[srd], %[h1] offen offset:1024 sc1\n\t" \
  "buffer_load_dwordx4 v[80:83], %[vo], %[srd], %[h2] offen offset:0 sc1\n\t" \
  "buffer_load_dword v194, off, %[srz], 0\n\t" \
  "buffer_load_dwordx4 v[84:87], %[vo], %[srd], %[h2] offen offset:1024 sc1\n\t" \
  "buffer_load_dwordx4 v[88:91], %[vo], %[srd], %[h3] offen offset:0 sc1\n\t" \
  "buffer_load_dwordx4 v[92:95], %[vo], %[srd], %[h3] offen offset:1024 sc1\n\t" \
  "buffer_load_dwordx4 v[96:99], %[vo], %[srd], %[h4] offen offset:0 sc1\n\t" \
  "buffer_load_dwordx4 v[100:103], %[vo], %[srd], %[h4] offen offset:1024 sc1\n\t" \
  "buffer_load_dwordx4 v[104:107], %[vo], %[srd], %[h5] offen offset:0 sc1\n\t" \
  "buffer_load_dwordx4 v[108:111], %[vo], %[srd], %[h5] offen offset:1024 sc1\n\t" \
  "buffer_load_dwordx4 v[112:115], %[vo], %[srd], %[h6] offen offset:0 sc1\n\t" \
  "buffer_load_dword v194, off, %[srz], 0\n\t" \
  "buffer_load_dwordx4 v[116:119], %[vo], %[srd], %[h6] offen offset:1024 sc1\n\t" \
  "s_waitcnt lgkmcnt(0)\n\t" \
  "Lloop_%=:\n\t" \
  "s_waitcnt vmcnt(14) lgkmcnt(4)\n\t" \
  "v_mfma_f32_16x16x32_bf16 %[c00], v[64:67], v[128:131], %[c00]\n\t" \
  "buffer_load_dwordx4 v[120:123], %[vo], %[srd], %[h7] offen offset:0 sc1\n\t" \
  "v_mfma_f32_16x16x32_bf16 %[c10], v[68:71], v[128:131], %[c10]\n\t" \
  "ds_read_b128 v[160:163], %[pc2] offset:0\n\t" \
  "v_add_u32 %[pc0], %[k2], %[pc0]\n\t" \
  "v_mfma_f32_16x16x32_bf16 %[c01], v[64:67], v[132:135], %[c01]\n\t" \
  "v_mfma_f32_16x16x32_bf16 %[c11], v[68:71], v[132:135], %[c11]\n\t" \
  "ds_read_b128 v[164:167], %[pc2] offset:2048\n\t" \
  "v_mfma_f32_16x16x32_bf16 %[c02], v[64:67], v[136:139], %[c02]\n\t" \
  "buffer_load_dwordx4 v[124:127], %[vo], %[srd], %[h7] offen offset:1024 sc1\n\t" \
  "v_mfma_f32_16x16x32_bf16 %[c12], v[68:71], v[136:139], %[c12]\n\t" \
  "ds_read_b128 v[168:171], %[pc2] offset:4096\n\t" \
  "v_mfma_f32_16x16x32_bf16 %[c03], v[64:67], v[140:143], %[c03]\n\t" \
  "v_add_u32 %[vo], 0x10000, %[vo]\n\t" \
  "v_and_b32 %[vo], 0x7ffff, %[vo]\n\t" \
  "v_mfma_f32_16x16x32_bf16 %[c13], v[68:71], v[140:143], %[c13]\n\t" \
  "ds_read_b128 v[172:175], %[pc2] offset:6144\n\t" \
  "s_waitcnt vmcnt(14) lgkmcnt(4)\n\t" \
  "v_mfma_f32_16x16x32_bf16 %[c00], v[72:75], v[144:147], %[c00]\n\t" \
  "buffer_load_dwordx4 v[64:67], %[vo], %[srd], %[h0] offen offset:0 sc1\n\t" \
  "v_mfma_f32_16x16x32_bf16 %[c10], v[76:79], v[144:147], %[c10]\n\t" \
  "ds_read_b128 v[176:179], %[pc3] offset:0\n\t" \
  "v_add_u32 %[pc1], %[k2], %[pc1]\n\t" \
  "v_mfma_f32_16x16x32_bf16 %[c01], v[72:75], v[148:151], %[c01]\n\t" \
  "s_cmp_eq_u32 %[sg], 2\n\t" \
  "s_cbranch_scc1 Lh_%=\n\t" \
  "Lhb_%=:\n\t" \
  "v_mfma_f32_16x16x32_bf16 %[c11], v[76:79], v[148:151], %[c11]\n\t" \
  "ds_read_b128 v[180:183], %[pc3] offset:2048\n\t" \
  "v_mfma_f32_16x16x32_bf16 %[c02], v[72:75], v[152:155], %[c02]\n\t" \
  "buffer_load_dwordx4 v[68:71], %[vo], %[srd], %[h0] offen offset:1024 sc1\n\t" \
  "v_mfma_f32_16x16x32_bf16 %[c12], v[76:79], v[152:155], %[c12]\n\t" \
  "ds_read_b128 v[184:187], %[pc3] offset:4096\n\t" \
  "v_mfma_f32_16x16x32_bf16 %[c03], v[72:75], v[156:159], %[c03]\n\t" \
  "v_mfma_f32_16x16x32_bf16 %[c13], v[76:79], v[156:159], %[c13]\n\t" \
  "ds_read_b128 v[188:191], %[pc3] offset:6144\n\t" \
  "s_waitcnt vmcnt(13) lgkmcnt(4)\n\t" \
  "v_mfma_f32_16x16x32_bf16 %[c00], v[80:83], v[160:163], %[c00]\n\t" \
  "buffer_load_dwordx4 v[72:75], %[vo], %[srd], %[h1] offen offset:0 sc1\n\t" \
  "v_mfma_f32_16x16x32_bf16 %[c10], v[84:87], v[160:163], %[c10]\n\t" \
  "ds_read_b128 v[128:131], %[pc0] offset:0\n\t" \
  "v_add_u32 %[pc2], %[k2], %[pc2]\n\t" \
  "v_mfma_f32_16x16x32_bf16 %[c01], v[80:83], v[164:167], %[c01]\n\t" \
  "v_mfma_f32_16x16x32_bf16 %[c11], v[84:87], v[164:167], %[c11]\n\t" \
  "ds_read_b128 v[132:135], %[pc0] offset:2048\n\t" \
  "v_mfma_f32_16x16x32_bf16 %[c02], v[80:83], v[168:171], %[c02]\n\t" \
  "buffer_load_dwordx4 v[76:79], %[vo], %[srd], %[h1] offen offset:1024 sc1\n\t" \
  "v_mfma_f32_16x16x32_bf16 %[c12], v[84:87], v[168:171], %[c12]\n\t" \
  "ds_read_b128 v[136:139], %[pc0] offset:4096\n\t" \
  "v_mfma_f32_16x16x32_bf16 %[c03], v[80:83], v[172:175], %[c03]\n\t" \
  "v_mfma_f32_16x16x32_bf16 %[c13], v[84:87], v[172:175], %[c13]\n\t" \
  "ds_read_b128 v[140:143], %[pc0] offset:6144\n\t" \
  "s_waitcnt vmcnt(13) lgkmcnt(4)\n\t" \
  "v_mfma_f32_16x16x32_bf16 %[c00], v[88:91], v[176:179], %[c00]\n\t" \
  "buffer_load_dwordx4 v[80:83], %[vo], %[srd], %[h2] offen offset:0 sc1\n\t" \
  "v_mfma_f32_16x16x32_bf16 %[c10], v[92:95], v[176:179], %[c10]\n\t" \
  "ds_read_b128 v[144:147], %[pc1] offset:0\n\t" \
  "v_add_u32 %[pc3], %[k2], %[pc3]\n\t" \
  "v_mfma_f32_16x16x32_bf16 %[c01], v[88:91], v[180:183], %[c01]\n\t" \
  "s_mov_b64 exec, 1\n\t" \
  "buffer_load_dword off, %[srz], 0 lds\n\t" \
  "s_mov_b64 exec, -1\n\t" \
  "v_mfma_f32_16x16x32_bf16 %[c11], v[92:95], v[180:183], %[c11]\n\t" \
  "ds_read_b128 v[148:151], %[pc1] offset:2048\n\t" \
  "v_mfma_f32_16x16x32_bf16 %[c02], v[88:91], v[184:187], %[c02]\n\t" \
  "buffer_load_dwordx4 v[84:87], %[vo], %[srd], %[h2] offen offset:1024 sc1\n\t" \
  "v_mfma_f32_16x16x32_bf16 %[c12], v[92:95], v[184:187], %[c12]\n\t" \
  "ds_read_b128 v[152:155], %[pc1] offset:4096\n\t" \
  "s_add_u32 m0, m0, 16\n\t" \
  "v_mfma_f32_16x16x32_bf16 %[c03], v[88:91], v[188:191], %[c03]\n\t" \
  "v_mfma_f32_16x16x32_bf16 %[c13], v[92:95], v[188:191], %[c13]\n\t" \
  "ds_read_b128 v[156:159], %[pc1] offset:6144\n\t" \
  "s_waitcnt vmcnt(14) lgkmcnt(4)\n\t" \
  "v_mfma_f32_16x16x32_bf16 %[c00], v[96:99], v[128:131], %[c00]\n\t" \
  "buffer_load_dwordx4 v[88:91], %[vo], %[srd], %[h3] offen offset:0 sc1\n\t" \
  "v_mfma_f32_16x16x32_bf16 %[c10], v[100:103], v[128:131], %[c10]\n\t" \
  "ds_read_b128 v[160:163], %[pc2] offset:0\n\t" \
  "v_add_u32 %[pc0], %[k2], %[pc0]\n\t" \
  "v_mfma_f32_16x16x32_bf16 %[c01], v[96:99], v[132:135], %[c01]\n\t" \
  "v_mfma_f32_16x16x32_bf16 %[c11], v[100:103], v[132:135], %[c11]\n\t" \
  "ds_read_b128 v[164:167], %[pc2] offset:2048\n\t" \
  "v_mfma_f32_16x16x32_bf16 %[c02], v[96:99], v[136:139], %[c02]\n\t" \
  "buffer_load_dwordx4 v[92:95], %[vo], %[srd], %[h3] offen offset:1024 sc1\n\t" \
  "v_mfma_f32_16x16x32_bf16 %[c12], v[100:103], v[136:139], %[c12]\n\t" \
  "ds_read_b128 v[168:171], %[pc2] offset:4096\n\t" \
  "v_mfma_f32_16x16x32_bf16 %[c03], v[96:99], v[140:143], %[c03]\n\t" \
  "v_mfma_f32_16x16x32_bf16 %[c13], v[100:103], v[140:143], %[c13]\n\t" \
  "ds_read_b128 v[172:175], %[pc2] offset:6144\n\t" \
  "s_waitcnt vmcnt(14) lgkmcnt(4)\n\t" \
  "v_mfma_f32_16x16x32_bf16 %[c00], v[104:107], v[144:147], %[c00]\n\t" \
  "buffer_load_dwordx4 v[96:99], %[vo], %[srd], %[h4] offen offset:0 sc1\n\t" \
  "v_mfma_f32_16x16x32_bf16 %[c10], v[108:111], v[144:147], %[c10]\n\t" \
  "ds_read_b128 v[176:179], %[pc3] offset:0\n\t" \
  "v_add_u32 %[pc1], %[k2], %[pc1]\n\t" \
  "v_mfma_f32_16x16x32_bf16 %[c01], v[104:107], v[148:151], %[c01]\n\t" \
  "v_mfma_f32_16x16x32_bf16 %[c11], v[108:111], v[148:151], %[c11]\n\t" \
  "ds_read_b128 v[180:183], %[pc3] offset:2048\n\t" \
  "v_mfma_f32_16x16x32_bf16 %[c02], v[104:107], v[152:155], %[c02]\n\t" \
  "buffer_load_dwordx4 v[100:103], %[vo], %[srd], %[h4] offen offset:1024 sc1\n\t" \
  "v_mfma_f32_16x16x32_bf16 %[c12], v[108:111], v[152:155], %[c12]\n\t" \
  "ds_read_b128 v[184:187], %[pc3] offset:4096\n\t" \
  "v_mfma_f32_16x16x32_bf16 %[c03], v[104:107], v[156:159], %[c03]\n\t" \
  "v_mfma_f32_16x16x32_bf16 %[c13], v[108:111], v[156:159], %[c13]\n\t" \
  "ds_read_b128 v[188:191], %[pc3] offset:6144\n\t" \
  "s_waitcnt vmcnt(13) lgkmcnt(4)\n\t" \
  "v_mfma_f32_16x16x32_bf16 %[c00], v[112:115], v[160:163], %[c00]\n\t" \
  "buffer_load_dwordx4 v[104:107], %[vo], %[srd], %[h5] offen offset:0 sc1\n\t" \
  "v_mfma_f32_16x16x32_bf16 %[c10], v[116:119], v[160:163], %[c10]\n\t" \
  "ds_read_b128 v[128:131], %[pc0] offset:0\n\t" \
  "v_add_u32 %[pc2], %[k2], %[pc2]\n\t" \
  "v_mfma_f32_16x16x32_bf16 %[c01], v[112:115], v[164:167], %[c01]\n\t" \
  "v_mfma_f32_16x16x32_bf16 %[c11], v[116:119], v[164:167], %[c11]\n\t" \
  "ds_read_b128 v[132:135], %[pc0] offset:2048\n\t" \
  "v_mfma_f32_16x16x32_bf16 %[c02], v[112:115], v[168:171], %[c02]\n\t" \
  "buffer_load_dwordx4 v[108:111], %[vo], %[srd], %[h5] offen offset:1024 sc1\n\t" \
  "v_mfma_f32_16x16x32_bf16 %[c12], v[116:119], v[168:171], %[c12]\n\t" \
  "ds_read_b128 v[136:139], %[pc0] offset:4096\n\t" \
  "v_mfma_f32_16x16x32_bf16 %[c03], v[112:115], v[172:175], %[c03]\n\t" \
  "v_mfma_f32_16x16x32_bf16 %[c13], v[116:119], v[172:175], %[c13]\n\t" \
  "ds_read_b128 v[140:143], %[pc0] offset:6144\n\t" \
  "s_waitcnt vmcnt(13) lgkmcnt(4)\n\t" \
  "v_mfma_f32_16x16x32_bf16 %[c00], v[120:123], v[176:179], %[c00]\n\t" \
  "buffer_load_dwordx4 v[112:115], %[vo], %[srd], %[h6] offen offset:0 sc1\n\t" \
  "v_mfma_f32_16x16x32_bf16 %[c10], v[124:127], v[176:179], %[c10]\n\t" \
  "ds_read_b128 v[144:147], %[pc1] offset:0\n\t" \
  "v_add_u32 %[pc3], %[k2], %[pc3]\n\t" \
  "v_mfma_f32_16x16x32_bf16 %[c01], v[120:123], v[180:183], %[c01]\n\t" \
  "s_mov_b64 exec, 1\n\t" \
  "buffer_load_dword off, %[srz], 0 lds\n\t" \
  "s_mov_b64 exec, -1\n\t" \
  "v_mfma_f32_16x16x32_bf16 %[c11], v[124:127], v[180:183], %[c11]\n\t" \
  "ds_read_b128 v[148:151], %[pc1] offset:2048\n\t" \
  "s_add_u32 %[sg], %[sg], 1\n\t" \
  "v_mfma_f32_16x16x32_bf16 %[c02], v[120:123], v[184:187], %[c02]\n\t" \
  "buffer_load_dwordx4 v[116:119], %[vo], %[srd], %[h6] offen offset:1024 sc1\n\t" \
  "v_mfma_f32_16x16x32_bf16 %[c12], v[124:127], v[184:187], %[c12]\n\t" \
  "ds_read_b128 v[152:155], %[pc1] offset:4096\n\t" \
  "s_add_u32 m0, m0, 16\n\t" \
  "v_mfma_f32_16x16x32_bf16 %[c03], v[120:123], v[188:191], %[c03]\n\t" \
  "s_cmp_eq_u32 %[sg], 3\n\t" \
  "s_cbranch_scc1 Lg_%=\n\t" \
  "Lgb_%=:\n\t" \
  "v_mfma_f32_16x16x32_bf16 %[c13], v[124:127], v[188:191], %[c13]\n\t" \
  "s_cmp_lt_u32 %[sg], 4\n\t" \
  "ds_read_b128 v[156:159], %[pc1] offset:6144\n\t" \
  "s_cbranch_scc1 Lloop_%=\n\t" \
  "s_mov_b32 %[s1], 0xfffe0000\n\t" \
  "Lloop2_%=:\n\t" \
  "s_waitcnt vmcnt(12) lgkmcnt(4)\n\t" \
  "v_mfma_f32_16x16x32_bf16 %[c00], v[64:67], v[128:131], %[c00]\n\t" \
  "ds_read_b64 v[192:193], %[fl] offset:0\n\t" \
  "buffer_load_dwordx4 v[120:123], %[vo], %[srd], %[h7] offen offset:0 sc1\n\t" \
  "v_mfma_f32_16x16x32_bf16 %[c10], v[68:71], v[128:131], %[c10]\n\t" \
  "ds_read_b128 v[160:163], %[pc2] offset:0\n\t" \
  "v_add_u32 %[pc0], %[s1], %[pc0]\n\t" \
  "v_mfma_f32_16x16x32_bf16 %[c01], v[64:67], v[132:135], %[c01]\n\t" \
  "v_mfma_f32_16x16x32_bf16 %[c11], v[68:71], v[132:135], %[c11]\n\t" \
  "ds_read_b128 v[164:167], %[pc2] offset:2048\n\t" \
  "v_mfma_f32_16x16x32_bf16 %[c02], v[64:67], v[136:139], %[c02]\n\t" \
  "buffer_load_dwordx4 v[124:127], %[vo], %[srd], %[h7] offen offset:1024 sc1\n\t" \
  "v_mfma_f32_16x16x32_bf16 %[c12], v[68:71], v[136:139], %[c12]\n\t" \
  "ds_read_b128 v[168:171], %[pc2] offset:4096\n\t" \
  "v_mfma_f32_16x16x32_bf16 %[c03], v[64:67], v[140:143], %[c03]\n\t" \
  "v_add_u32 %[vo], 0x10000, %[vo]\n\t" \
  "v_and_b32 %[vo], 0x7ffff, %[vo]\n\t" \
  "v_mfma_f32_16x16x32_bf16 %[c13], v[68:71], v[140:143], %[c13]\n\t" \
  "ds_read_b128 v[172:175], %[pc2] offset:6144\n\t" \
  "s_waitcnt vmcnt(12) lgkmcnt(4)\n\t" \
  "v_mfma_f32_16x16x32_bf16 %[c00], v[72:75], v[144:147], %[c00]\n\t" \
  "buffer_load_dwordx4 v[64:67], %[vo], %[srd], %[h0] offen offset:0 sc1\n\t" \
  "v_mfma_f32_16x16x32_bf16 %[c10], v[76:79], v[144:147], %[c10]\n\t" \
  "ds_read_b128 v[176:179], %[pc3] offset:0\n\t" \
  "v_add_u32 %[pc1], %[s1], %[pc1]\n\t" \
  "v_mfma_f32_16x16x32_bf16 %[c01], v[72:75], v[148:151], %[c01]\n\t" \
  "v_or_b32 v192, v192, v193\n\t" \
  "v_mfma_f32_16x16x32_bf16 %[c11], v[76:79], v[148:151], %[c11]\n\t" \
  "ds_read_b128 v[180:183], %[pc3] offset:2048\n\t" \
  "v_readfirstlane_b32 %[s0], v192\n\t" \
  "v_mfma_f32_16x16x32_bf16 %[c02], v[72:75], v[152:155], %[c02]\n\t" \
  "buffer_load_dwordx4 v[68:71], %[vo], %[srd], %[h0] offen offset:1024 sc1\n\t" \
  "v_mfma_f32_16x16x32_bf16 %[c12], v[76:79], v[152:155], %[c12]\n\t" \
  "ds_read_b128 v[184:187], %[pc3] offset:4096\n\t" \
  "v_mfma_f32_16x16x32_bf16 %[c03], v[72:75], v[156:159], %[c03]\n\t" \
  "s_cmp_eq_u32 %[s0], 0\n\t" \
  "s_cbranch_scc0 Lcb1_%=\n\t" \
  "Lcb1b_%=:\n\t" \
  "v_mfma_f32_16x16x32_bf16 %[c13], v[76:79], v[156:159], %[c13]\n\t" \
  "ds_read_b128 v[188:191], %[pc3] offset:6144\n\t" \
  "s_waitcnt vmcnt(12) lgkmcnt(4)\n\t" \
  "v_mfma_f32_16x16x32_bf16 %[c00], v[80:83], v[160:163], %[c00]\n\t" \
  "buffer_load_dwordx4 v[72:75], %[vo], %[srd], %[h1] offen offset:0 sc1\n\t" \
  "v_mfma_f32_16x16x32_bf16 %[c10], v[84:87], v[160:163], %[c10]\n\t" \
  "ds_read_b128 v[128:131], %[pc0] offset:0\n\t" \
  "v_add_u32 %[pc2], %[s1], %[pc2]\n\t" \
  "v_mfma_f32_16x16x32_bf16 %[c01], v[80:83], v[164:167], %[c01]\n\t" \
  "v_mfma_f32_16x16x32_bf16 %[c11], v[84:87], v[164:167], %[c11]\n\t" \
  "ds_read_b128 v[132:135], %[pc0] offset:2048\n\t" \
  "v_mfma_f32_16x16x32_bf16 %[c02], v[80:83], v[168:171], %[c02]\n\t" \
  "buffer_load_dwordx4 v[76:79], %[vo], %[srd], %[h1] offen offset:1024 sc1\n\t" \
  "v_mfma_f32_16x16x32_bf16 %[c12], v[84:87], v[168:171], %[c12]\n\t" \
  "ds_read_b128 v[136:139], %[pc0] offset:4096\n\t" \
  "v_mfma_f32_16x16x32_bf16 %[c03], v[80:83], v[172:175], %[c03]\n\t" \
  "v_mfma_f32_16x16x32_bf16 %[c13], v[84:87], v[172:175], %[c13]\n\t" \
  "ds_read_b128 v[140:143], %[pc0] offset:6144\n\t" \
  "s_waitcnt vmcnt(12) lgkmcnt(4)\n\t" \
  "v_mfma_f32_16x16x32_bf16 %[c00], v[88:91], v[176:179], %[c00]\n\t" \
  "buffer_load_dwordx4 v[80:83], %[vo], %[srd], %[h2] offen offset:0 sc1\n\t" \
  "v_mfma_f32_16x16x32_bf16 %[c10], v[92:95], v[176:179], %[c10]\n\t" \
  "ds_read_b128 v[144:147], %[pc1] offset:0\n\t" \
  "v_add_u32 %[pc3], %[s1], %[pc3]\n\t" \
  "v_mfma_f32_16x16x32_bf16 %[c01], v[88:91], v[180:183], %[c01]\n\t" \
  "v_mfma_f32_16x16x32_bf16 %[c11], v[92:95], v[180:183], %[c11]\n\t" \
  "ds_read_b128 v[148:151], %[pc1] offset:2048\n\t" \
  "v_mfma_f32_16x16x32_bf16 %[c02], v[88:91], v[184:187], %[c02]\n\t" \
  "buffer_load_dwordx4 v[84:87], %[vo], %[srd], %[h2] offen offset:1024 sc1\n\t" \
  "v_mfma_f32_16x16x32_bf16 %[c12], v[92:95], v[184:187], %[c12]\n\t" \
  "ds_read_b128 v[152:155], %[pc1] offset:4096\n\t" \
  "v_mfma_f32_16x16x32_bf16 %[c03], v[88:91], v[188:191], %[c03]\n\t" \
  "v_mfma_f32_16x16x32_bf16 %[c13], v[92:95], v[188:191], %[c13]\n\t" \
  "ds_read_b128 v[156:159], %[pc1] offset:6144\n\t" \
  "s_waitcnt vmcnt(12) lgkmcnt(4)\n\t" \
  "v_mfma_f32_16x16x32_bf16 %[c00], v[96:99], v[128:131], %[c00]\n\t" \
  "ds_read_b64 v[192:193], %[fl] offset:8\n\t" \
  "buffer_load_dwordx4 v[88:91], %[vo], %[srd], %[h3] offen offset:0 sc1\n\t" \
  "v_mfma_f32_16x16x32_bf16 %[c10], v[100:103], v[128:131], %[c10]\n\t" \
  "ds_read_b128 v[160:163], %[pc2] offset:0\n\t" \
  "v_add_u32 %[pc0], %[k2], %[pc0]\n\t" \
  "v_mfma_f32_16x16x32_bf16 %[c01], v[96:99], v[132:135], %[c01]\n\t" \
  "v_mfma_f32_16x16x32_bf16 %[c11], v[100:103], v[132:135], %[c11]\n\t" \
  "ds_read_b128 v[164:167], %[pc2] offset:2048\n\t" \
  "v_mfma_f32_16x16x32_bf16 %[c02], v[96:99], v[136:139], %[c02]\n\t" \
  "buffer_load_dwordx4 v[92:95], %[vo], %[srd], %[h3] offen offset:1024 sc1\n\t" \
  "v_mfma_f32_16x16x32_bf16 %[c12], v[100:103], v[136:139], %[c12]\n\t" \
  "ds_read_b128 v[168:171], %[pc2] offset:4096\n\t" \
  "v_mfma_f32_16x16x32_bf16 %[c03], v[96:99], v[140:143], %[c03]\n\t" \
  "v_mfma_f32_16x16x32_bf16 %[c13], v[100:103], v[140:143], %[c13]\n\t" \
  "ds_read_b128 v[172:175], %[pc2] offset:6144\n\t" \
  "s_waitcnt vmcnt(12) lgkmcnt(4)\n\t" \
  "v_mfma_f32_16x16x32_bf16 %[c00], v[104:107], v[144:147], %[c00]\n\t" \
  "buffer_load_dwordx4 v[96:99], %[vo], %[srd], %[h4] offen offset:0 sc1\n\t" \
  "v_mfma_f32_16x16x32_bf16 %[c10], v[108:111], v[144:147], %[c10]\n\t" \
  "ds_read_b128 v[176:179], %[pc3] offset:0\n\t" \
  "v_add_u32 %[pc1], %[k2], %[pc1]\n\t" \
  "v_mfma_f32_16x16x32_bf16 %[c01], v[104:107], v[148:151], %[c01]\n\t" \
  "v_or_b32 v192, v192, v193\n\t" \
  "v_mfma_f32_16x16x32_bf16 %[c11], v[108:111], v[148:151], %[c11]\n\t" \
  "ds_read_b128 v[180:183], %[pc3] offset:2048\n\t" \
  "v_readfirstlane_b32 %[s0], v192\n\t" \
  "v_mfma_f32_16x16x32_bf16 %[c02], v[104:107], v[152:155], %[c02]\n\t" \
  "buffer_load_dwordx4 v[100:103], %[vo], %[srd], %[h4] offen offset:1024 sc1\n\t" \
  "v_mfma_f32_16x16x32_bf16 %[c12], v[108:111], v[152:155], %[c12]\n\t" \
  "ds_read_b128 v[184:187], %[pc3] offset:4096\n\t" \
  "v_mfma_f32_16x16x32_bf16 %[c03], v[104:107], v[156:159], %[c03]\n\t" \
  "s_cmp_eq_u32 %[s0], 0\n\t" \
  "s_cbranch_scc0 Lcb5_%=\n\t" \
  "Lcb5b_%=:\n\t" \
  "v_mfma_f32_16x16x32_bf16 %[c13], v[108:111], v[156:159], %[c13]\n\t" \
  "ds_read_b128 v[188:191], %[pc3] offset:6144\n\t" \
  "s_waitcnt vmcnt(12) lgkmcnt(4)\n\t" \
  "v_mfma_f32_16x16x32_bf16 %[c00], v[112:115], v[160:163], %[c00]\n\t" \
  "buffer_load_dwordx4 v[104:107], %[vo], %[srd], %[h5] offen offset:0 sc1\n\t" \
  "v_mfma_f32_16x16x32_bf16 %[c10], v[116:119], v[160:163], %[c10]\n\t" \
  "ds_read_b128 v[128:131], %[pc0] offset:0\n\t" \
  "v_add_u32 %[pc2], %[k2], %[pc2]\n\t" \
  "v_mfma_f32_16x16x32_bf16 %[c01], v[112:115], v[164:167], %[c01]\n\t" \
  "v_add_u32 %[fl], 16, %[fl]\n\t" \
  "s_mov_b32 %[s1], %[k2]\n\t" \
  "v_mfma_f32_16x16x32_bf16 %[c11], v[116:119], v[164:167], %[c11]\n\t" \
  "ds_read_b128 v[132:135], %[pc0] offset:2048\n\t" \
  "v_mfma_f32_16x16x32_bf16 %[c02], v[112:115], v[168:171], %[c02]\n\t" \
  "buffer_load_dwordx4 v[108:111], %[vo], %[srd], %[h5] offen offset:1024 sc1\n\t" \
  "v_mfma_f32_16x16x32_bf16 %[c12], v[116:119], v[168:171], %[c12]\n\t" \
  "ds_read_b128 v[136:139], %[pc0] offset:4096\n\t" \
  "v_mfma_f32_16x16x32_bf16 %[c03], v[112:115], v[172:175], %[c03]\n\t" \
  "v_mfma_f32_16x16x32_bf16 %[c13], v[116:119], v[172:175], %[c13]\n\t" \
  "ds_read_b128 v[140:143], %[pc0] offset:6144\n\t" \
  "s_waitcnt vmcnt(12) lgkmcnt(4)\n\t" \
  "v_mfma_f32_16x16x32_bf16 %[c00], v[120:123], v[176:179], %[c00]\n\t" \
  "buffer_load_dwordx4 v[112:115], %[vo], %[srd], %[h6] offen offset:0 sc1\n\t" \
  "v_mfma_f32_16x16x32_bf16 %[c10], v[124:127], v[176:179], %[c10]\n\t" \
  "ds_read_b128 v[144:147], %[pc1] offset:0\n\t" \
  "v_add_u32 %[pc3], %[k2], %[pc3]\n\t" \
  "v_mfma_f32_16x16x32_bf16 %[c01], v[120:123], v[180:183], %[c01]\n\t" \
  "s_add_u32 %[sg], %[sg], 1\n\t" \
  "s_cmp_lt_u32 %[sg], 7\n\t" \
  "v_mfma_f32_16x16x32_bf16 %[c11], v[124:127], v[180:183], %[c11]\n\t" \
  "ds_read_b128 v[148:151], %[pc1] offset:2048\n\t" \
  "v_mfma_f32_16x16x32_bf16 %[c02], v[120:123], v[184:187], %[c02]\n\t" \
  "buffer_load_dwordx4 v[116:119], %[vo], %[srd], %[h6] offen offset:1024 sc1\n\t" \
  "v_mfma_f32_16x16x32_bf16 %[c12], v[124:127], v[184:187], %[c12]\n\t" \
  "ds_read_b128 v[152:155], %[pc1] offset:4096\n\t" \
  "v_mfma_f32_16x16x32_bf16 %[c03], v[120:123], v[188:191], %[c03]\n\t" \
  "v_mfma_f32_16x16x32_bf16 %[c13], v[124:127], v[188:191], %[c13]\n\t" \
  "ds_read_b128 v[156:159], %[pc1] offset:6144\n\t" \
  "s_cbranch_scc1 Lloop2_%=\n\t" \
  "s_waitcnt vmcnt(12) lgkmcnt(4)\n\t" \
  "v_mfma_f32_16x16x32_bf16 %[c00], v[64:67], v[128:131], %[c00]\n\t" \
  "ds_read_b64 v[192:193], %[fl] offset:0\n\t" \
  "buffer_load_dwordx4 v[120:123], %[vo], %[srd], %[h7] offen offset:0 sc1\n\t" \
  "v_mfma_f32_16x16x32_bf16 %[c10], v[68:71], v[128:131], %[c10]\n\t" \
  "ds_read_b128 v[160:163], %[pc2] offset:0\n\t" \
  "v_add_u32 %[pc0], %[k2], %[pc0]\n\t" \
  "v_mfma_f32_16x16x32_bf16 %[c01], v[64:67], v[132:135], %[c01]\n\t" \
  "v_mfma_f32_16x16x32_bf16 %[c11], v[68:71], v[132:135], %[c11]\n\t" \
  "ds_read_b128 v[164:167], %[pc2] offset:2048\n\t" \
  "v_mfma_f32_16x16x32_bf16 %[c02], v[64:67], v[136:139], %[c02]\n\t" \
  "buffer_load_dwordx4 v[124:127], %[vo], %[srd], %[h7] offen offset:1024 sc1\n\t" \
  "v_mfma_f32_16x16x32_bf16 %[c12], v[68:71], v[136:139], %[c12]\n\t" \
  "ds_read_b128 v[168:171], %[pc2] offset:4096\n\t" \
  "v_mfma_f32_16x16x32_bf16 %[c03], v[64:67], v[140:143], %[c03]\n\t" \
  "v_mfma_f32_16x16x32_bf16 %[c13], v[68:71], v[140:143], %[c13]\n\t" \
  "ds_read_b128 v[172:175], %[pc2] offset:6144\n\t" \
  "s_waitcnt vmcnt(12) lgkmcnt(4)\n\t" \
  "v_mfma_f32_16x16x32_bf16 %[c00], v[72:75], v[144:147], %[c00]\n\t" \
  "v_mfma_f32_16x16x32_bf16 %[c10], v[76:79], v[144:147], %[c10]\n\t" \
  "ds_read_b128 v[176:179], %[pc3] offset:0\n\t" \
  "v_add_u32 %[pc1], %[k2], %[pc1]\n\t" \
  "v_mfma_f32_16x16x32_bf16 %[c01], v[72:75], v[148:151], %[c01]\n\t" \
  "v_or_b32 v192, v192, v193\n\t" \
  "v_mfma_f32_16x16x32_bf16 %[c11], v[76:79], v[148:151], %[c11]\n\t" \
  "ds_read_b128 v[180:183], %[pc3] offset:2048\n\t" \
  "v_readfirstlane_b32 %[s0], v192\n\t" \
  "v_mfma_f32_16x16x32_bf16 %[c02], v[72:75], v[152:155], %[c02]\n\t" \
  "v_mfma_f32_16x16x32_bf16 %[c12], v[76:79], v[152:155], %[c12]\n\t" \
  "ds_read_b128 v[184:187], %[pc3] offset:4096\n\t" \
  "v_mfma_f32_16x16x32_bf16 %[c03], v[72:75], v[156:159], %[c03]\n\t" \
  "s_cmp_eq_u32 %[s0], 0\n\t" \
  "s_cbranch_scc0 Lcf1_%=\n\t" \
  "Lcf1b_%=:\n\t" \
  "v_mfma_f32_16x16x32_bf16 %[c13], v[76:79], v[156:159], %[c13]\n\t" \
  "ds_read_b128 v[188:191], %[pc3] offset:6144\n\t" \
  "s_waitcnt vmcnt(10) lgkmcnt(4)\n\t" \
  "v_mfma_f32_16x16x32_bf16 %[c00], v[80:83], v[160:163], %[c00]\n\t" \
  "v_mfma_f32_16x16x32_bf16 %[c10], v[84:87], v[160:163], %[c10]\n\t" \
  "ds_read_b128 v[128:131], %[pc0] offset:0\n\t" \
  "v_add_u32 %[pc2], %[k2], %[pc2]\n\t" \
  "v_mfma_f32_16x16x32_bf16 %[c01], v[80:83], v[164:167], %[c01]\n\t" \
  "v_mfma_f32_16x16x32_bf16 %[c11], v[84:87], v[164:167], %[c11]\n\t" \
  "ds_read_b128 v[132:135], %[pc0] offset:2048\n\t" \
  "v_mfma_f32_16x16x32_bf16 %[c02], v[80:83], v[168:171], %[c02]\n\t" \
  "v_mfma_f32_16x16x32_bf16 %[c12], v[84:87], v[168:171], %[c12]\n\t" \
  "ds_read_b128 v[136:139], %[pc0] offset:4096\n\t" \
  "v_mfma_f32_16x16x32_bf16 %[c03], v[80:83], v[172:175], %[c03]\n\t" \
  "v_mfma_f32_16x16x32_bf16 %[c13], v[84:87], v[172:175], %[c13]\n\t" \
  "ds_read_b128 v[140:143], %[pc0] offset:6144\n\t" \
  "s_waitcnt vmcnt(8) lgkmcnt(4)\n\t" \
  "v_mfma_f32_16x16x32_bf16 %[c00], v[88:91], v[176:179], %[c00]\n\t" \
  "v_mfma_f32_16x16x32_bf16 %[c10], v[92:95], v[176:179], %[c10]\n\t" \
  "ds_read_b128 v[144:147], %[pc1] offset:0\n\t" \
  "v_add_u32 %[pc3], %[k2], %[pc3]\n\t" \
  "v_mfma_f32_16x16x32_bf16 %[c01], v[88:91], v[180:183], %[c01]\n\t" \
  "v_mfma_f32_16x16x32_bf16 %[c11], v[92:95], v[180:183], %[c11]\n\t" \
  "ds_read_b128 v[148:151], %[pc1] offset:2048\n\t" \
  "v_mfma_f32_16x16x32_bf16 %[c02], v[88:91], v[184:187], %[c02]\n\t" \
  "v_mfma_f32_16x16x32_bf16 %[c12], v[92:95], v[184:187], %[c12]\n\t" \
  "ds_read_b128 v[152:155], %[pc1] offset:4096\n\t" \
  "v_mfma_f32_16x16x32_bf16 %[c03], v[88:91], v[188:191], %[c03]\n\t" \
  "v_mfma_f32_16x16x32_bf16 %[c13], v[92:95], v[188:191], %[c13]\n\t" \
  "ds_read_b128 v[156:159], %[pc1] offset:6144\n\t" \
  "s_waitcnt vmcnt(6) lgkmcnt(4)\n\t" \
  "v_mfma_f32_16x16x32_bf16 %[c00], v[96:99], v[128:131], %[c00]\n\t" \
  "v_mfma_f32_16x16x32_bf16 %[c10], v[100:103], v[128:131], %[c10]\n\t" \
  "ds_read_b128 v[160:163], %[pc2] offset:0\n\t" \
  "v_mfma_f32_16x16x32_bf16 %[c01], v[96:99], v[132:135], %[c01]\n\t" \
  "v_mfma_f32_16x16x32_bf16 %[c11], v[100:103], v[132:135], %[c11]\n\t" \
  "ds_read_b128 v[164:167], %[pc2] offset:2048\n\t" \
  "v_mfma_f32_16x16x32_bf16 %[c02], v[96:99], v[136:139], %[c02]\n\t" \
  "v_mfma_f32_16x16x32_bf16 %[c12], v[100:103], v[136:139], %[c12]\n\t" \
  "ds_read_b128 v[168:171], %[pc2] offset:4096\n\t" \
  "v_mfma_f32_16x16x32_bf16 %[c03], v[96:99], v[140:143], %[c03]\n\t" \
  "v_mfma_f32_16x16x32_bf16 %[c13], v[100:103], v[140:143], %[c13]\n\t" \
  "ds_read_b128 v[172:175], %[pc2] offset:6144\n\t" \
  "s_waitcnt vmcnt(4) lgkmcnt(4)\n\t" \
  "v_mfma_f32_16x16x32_bf16 %[c00], v[104:107], v[144:147], %[c00]\n\t" \
  "v_mfma_f32_16x16x32_bf16 %[c10], v[108:111], v[144:147], %[c10]\n\t" \
  "ds_read_b128 v[176:179], %[pc3] offset:0\n\t" \
  "v_mfma_f32_16x16x32_bf16 %[c01], v[104:107], v[148:151], %[c01]\n\t" \
  "v_mfma_f32_16x16x32_bf16 %[c11], v[108:111], v[148:151], %[c11]\n\t" \
  "ds_read_b128 v[180:183], %[pc3] offset:2048\n\t" \
  "v_mfma_f32_16x16x32_bf16 %[c02], v[104:107], v[152:155], %[c02]\n\t" \
  "v_mfma_f32_16x16x32_bf16 %[c12], v[108:111], v[152:155], %[c12]\n\t" \
  "ds_read_b128 v[184:187], %[pc3] offset:4096\n\t" \
  "v_mfma_f32_16x16x32_bf16 %[c03], v[104:107], v[156:159], %[c03]\n\t" \
  "v_mfma_f32_16x16x32_bf16 %[c13], v[108:111], v[156:159], %[c13]\n\t" \
  "ds_read_b128 v[188:191], %[pc3] offset:6144\n\t" \
  "s_waitcnt vmcnt(2) lgkmcnt(4)\n\t" \
  "v_mfma_f32_16x16x32_bf16 %[c00], v[112:115], v[160:163], %[c00]\n\t" \
  "ds_read_b64 v[192:193], %[xd]\n\t" \
  "v_mfma_f32_16x16x32_bf16 %[c10], v[116:119], v[160:163], %[c10]\n\t" \
  "ds_read_b128 v[128:131], %[d0]\n\t" \
  "v_mfma_f32_16x16x32_bf16 %[c01], v[112:115], v[164:167], %[c01]\n\t" \
  "ds_read_b128 v[132:135], %[d1]\n\t" \
  "v_mfma_f32_16x16x32_bf16 %[c11], v[116:119], v[164:167], %[c11]\n\t" \
  "ds_read_b128 v[136:139], %[d2]\n\t" \
  "v_mfma_f32_16x16x32_bf16 %[c02], v[112:115], v[168:171], %[c02]\n\t" \
  "ds_read_b128 v[140:143], %[d3]\n\t" \
  "v_mfma_f32_16x16x32_bf16 %[c12], v[116:119], v[168:171], %[c12]\n\t" \
  "ds_read_b128 v[144:147], %[d4]\n\t" \
  "v_mfma_f32_16x16x32_bf16 %[c03], v[112:115], v[172:175], %[c03]\n\t" \
  "ds_read_b128 v[148:151], %[d5]\n\t" \
  "v_mfma_f32_16x16x32_bf16 %[c13], v[116:119], v[172:175], %[c13]\n\t" \
  "ds_read_b128 v[152:155], %[d6]\n\t" \
  "ds_read_b128 v[156:159], %[d7]\n\t" \
  "s_waitcnt vmcnt(0) lgkmcnt(9)\n\t" \
  "v_mfma_f32_16x16x32_bf16 %[c00], v[120:123], v[176:179], %[c00]\n\t" \
  "v_mfma_f32_16x16x32_bf16 %[c10], v[124:127], v[176:179], %[c10]\n\t" \
  "v_mfma_f32_16x16x32_bf16 %[c01], v[120:123], v[180:183], %[c01]\n\t" \
  "v_mfma_f32_16x16x32_bf16 %[c11], v[124:127], v[180:183], %[c11]\n\t" \
  "v_mfma_f32_16x16x32_bf16 %[c02], v[120:123], v[184:187], %[c02]\n\t" \
  "v_mfma_f32_16x16x32_bf16 %[c12], v[124:127], v[184:187], %[c12]\n\t" \
  "v_mfma_f32_16x16x32_bf16 %[c03], v[120:123], v[188:191], %[c03]\n\t" \
  "v_mfma_f32_16x16x32_bf16 %[c13], v[124:127], v[188:191], %[c13]\n\t" \
  "s_cmp_eq_u32 %[mine], 0\n\t" \
  "s_cbranch_scc1 Lend_%=\n\t" \
  "Ld_%=:\n\t" \
  "s_waitcnt lgkmcnt(0)\n\t" \
  "v_and_b32 v192, v192, v193\n\t" \
  "s_nop 0\n\t" \
  "v_readfirstlane_b32 %[s0], v192\n\t" \
  "s_cmp_eq_u32 %[s0], 0\n\t" \
  "s_cbranch_scc1 Ld1_%=\n\t" \
  "Ld2_%=:\n\t" \
  "v_add_f32 %[b00], %[b00], v128\n\t" \
  "v_add_f32 %[b01], %[b01], v129\n\t" \
  "v_add_f32 %[b02], %[b02], v130\n\t" \
  "v_add_f32 %[b03], %[b03], v131\n\t" \
  "v_add_f32 %[b10], %[b10], v132\n\t" \
  "v_add_f32 %[b11], %[b11], v133\n\t" \
  "v_add_f32 %[b12], %[b12], v134\n\t" \
  "v_add_f32 %[b13], %[b13], v135\n\t" \
  "v_add_f32 %[b20], %[b20], v136\n\t" \
  "v_add_f32 %[b21], %[b21], v137\n\t" \
  "v_add_f32 %[b22], %[b22], v138\n\t" \
  "v_add_f32 %[b23], %[b23], v139\n\t" \
  "v_add_f32 %[b30], %[b30], v140\n\t" \
  "v_add_f32 %[b31], %[b31], v141\n\t" \
  "v_add_f32 %[b32], %[b32], v142\n\t" \
  "v_add_f32 %[b33], %[b33], v143\n\t" \
  "v_add_f32 %[b40], %[b40], v144\n\t" \
  "v_add_f32 %[b41], %[b41], v145\n\t" \
  "v_add_f32 %[b42], %[b42], v146\n\t" \
  "v_add_f32 %[b43], %[b43], v147\n\t" \
  "v_add_f32 %[b50], %[b50], v148\n\t" \
  "v_add_f32 %[b51], %[b51], v149\n\t" \
  "v_add_f32 %[b52], %[b52], v150\n\t" \
  "v_add_f32 %[b53], %[b53], v151\n\t" \
  "v_add_f32 %[b60], %[b60], v152\n\t" \
  "v_add_f32 %[b61], %[b61], v153\n\t" \
  "v_add_f32 %[b62], %[b62], v154\n\t" \
  "v_add_f32 %[b63], %[b63], v155\n\t" \
  "v_add_f32 %[b70], %[b70], v156\n\t" \
  "v_add_f32 %[b71], %[b71], v157\n\t" \
  "v_add_f32 %[b72], %[b72], v158\n\t" \
  "v_add_f32 %[b73], %[b73], v159\n\t" \
  "s_branch Lend_%=\n\t" \
  "Lw_%=:\n\t" \
  "ds_read_b64 v[192:193], %[hr]\n\t" \
  "s_sub_i32 %[bud], %[bud], 1\n\t" \
  "s_cmp_lt_i32 %[bud], 0\n\t" \
  "s_cbranch_scc1 Lf3_%=\n\t" \
  "s_waitcnt lgkmcnt(0)\n\t" \
  "v_or_b32 v192, v192, v193\n\t" \
  "s_nop 0\n\t" \
  "v_readfirstlane_b32 %[s0], v192\n\t" \
  "s_cmp_eq_u32 %[s0], 0\n\t" \
  "s_cbranch_scc0 Lw_%=\n\t" \
  "s_branch Lf3_%=\n\t" \
  "Lh_%=:\n\t" \
  "buffer_load_dword v196, off, %[srd], %[sf1] sc1\n\t" \
  "s_branch Lhb_%=\n\t" \
  "Lg1_%=:\n\t" \
  "v_mov_b32 v196, 0\n\t" \
  "s_mov_b64 exec, 1\n\t" \
  "buffer_atomic_or v196, off, %[srd], %[sf1] sc0 sc1\n\t" \
  "s_mov_b64 exec, -1\n\t" \
  "s_sub_i32 %[bud], %[bud], 16\n\t" \
  "s_cmp_lt_i32 %[bud], 0\n\t" \
  "s_cbranch_scc1 Lgb_%=\n\t" \
  "s_waitcnt vmcnt(0)\n\t" \
  "s_branch Lg2_%=\n\t" \
  "Lg_%=:\n\t" \
  "s_waitcnt vmcnt(12)\n\t" \
  "Lg2_%=:\n\t" \
  "v_readfirstlane_b32 %[s0], v196\n\t" \
  "s_cmp_lg_u32 %[s0], 32\n\t" \
  "s_cbranch_scc1 Lg1_%=\n\t" \
  "s_branch Lgb_%=\n\t" \
  "Lcb1_%=:\n\t" \
  "ds_read_b64 v[192:193], %[fl] offset:0\n\t" \
  "s_sub_i32 %[bud], %[bud], 1\n\t" \
  "s_cmp_lt_i32 %[bud], 0\n\t" \
  "s_cbranch_scc1 Lcb1b_%=\n\t" \
  "s_waitcnt lgkmcnt(0)\n\t" \
  "v_or_b32 v192, v192, v193\n\t" \
  "s_nop 0\n\t" \
  "v_readfirstlane_b32 %[s0], v192\n\t" \
  "s_cmp_eq_u32 %[s0], 0\n\t" \
  "s_cbranch_scc0 Lcb1_%=\n\t" \
  "s_branch Lcb1b_%=\n\t" \
  "Lcb5_%=:\n\t" \
  "ds_read_b64 v[192:193], %[fl] offset:8\n\t" \
  "s_sub_i32 %[bud], %[bud], 1\n\t" \
  "s_cmp_lt_i32 %[bud], 0\n\t" \
  "s_cbranch_scc1 Lcb5b_%=\n\t" \
  "s_waitcnt lgkmcnt(0)\n\t" \
  "v_or_b32 v192, v192, v193\n\t" \
  "s_nop 0\n\t" \
  "v_readfirstlane_b32 %[s0], v192\n\t" \
  "s_cmp_eq_u32 %[s0], 0\n\t" \
  "s_cbranch_scc0 Lcb5_%=\n\t" \
  "s_branch Lcb5b_%=\n\t" \
  "Lcf1_%=:\n\t" \
  "ds_read_b64 v[192:193], %[fl] offset:0\n\t" \
  "s_sub_i32 %[bud], %[bud], 1\n\t" \
  "s_cmp_lt_i32 %[bud], 0\n\t" \
  "s_cbranch_scc1 Lcf1b_%=\n\t" \
  "s_waitcnt lgkmcnt(0)\n\t" \
  "v_or_b32 v192, v192, v193\n\t" \
  "s_nop 0\n\t" \
  "v_readfirstlane_b32 %[s0], v192\n\t" \
  "s_cmp_eq_u32 %[s0], 0\n\t" \
  "s_cbranch_scc0 Lcf1_%=\n\t" \
  "s_branch Lcf1b_%=\n\t" \
  "Ld1_%=:\n\t" \
  "s_sub_i32 %[bud], %[bud], 1\n\t" \
  "s_cmp_lt_i32 %[bud], 0\n\t" \
  "s_cbranch_scc1 Ld2_%=\n\t" \
  "ds_read_b64 v[192:193], %[xd]\n\t" \
  "ds_read_b128 v[128:131], %[d0]\n\t" \
  "ds_read_b128 v[132:135], %[d1]\n\t" \
  "ds_read_b128 v[136:139], %[d2]\n\t" \
  "ds_read_b128 v[140:143], %[d3]\n\t" \
  "ds_read_b128 v[144:147], %[d4]\n\t" \
  "ds_read_b128 v[148:151], %[d5]\n\t" \
  "ds_read_b128 v[152:155], %[d6]\n\t" \
  "ds_read_b128 v[156:159], %[d7]\n\t" \
  "s_branch Ld_%=\n\t" \
  "Lend_%=:\n\t" \
  "s_waitcnt lgkmcnt(0)\n\t" \
  "s_nop 6\n\t"
#define ASM_LA \
  "s_nop 4\n\t" \
  "Ltop_%=:\n\t" \
  "s_mov_b32 m0, %[dst]\n\t" \
  "v_mov_b32 %[vc], %[ci]\n\t" \
  "buffer_load_dwordx4 %[v0], %[r0], %[so0] offen lds\n\t" \
  "s_add_u32 m0, m0, 0x400\n\t" \
  "s_add_u32 %[dst], %[dst], 0x8000\n\t" \
  "buffer_load_dwordx4 %[v0], %[r0], %[so1] offen lds\n\t" \
  "s_add_u32 m0, m0, 0x400\n\t" \
  "s_cmp_ge_u32 %[dst], 0x24000\n\t" \
  "s_cselect_b32 %[s0], %[wrap], 0\n\t" \
  "buffer_load_dwordx4 %[v0], %[r0], %[so2] offen lds\n\t" \
  "s_add_u32 m0, m0, 0x400\n\t" \
  "s_sub_u32 %[dst], %[dst], %[s0]\n\t" \
  "buffer_load_dwordx4 %[v0], %[r0], %[so3] offen lds\n\t" \
  "s_add_u32 m0, m0, 0x400\n\t" \
  "s_add_u32 %[sk], %[sk], 128\n\t" \
  "buffer_load_dwordx4 %[v0], %[r0], %[so4] offen lds\n\t" \
  "s_add_u32 m0, m0, 0x400\n\t" \
  "s_and_b32 %[sk], %[sk], 2047\n\t" \
  "buffer_load_dwordx4 %[v0], %[r0], %[so5] offen lds\n\t" \
  "s_add_u32 m0, m0, 0x400\n\t" \
  "s_nop 0\n\t" \
  "buffer_load_dwordx4 %[v0], %[r0], %[so6] offen lds\n\t" \
  "s_add_u32 m0, m0, 0x400\n\t" \
  "s_add_u32 %[ci], %[ci], 16\n\t" \
  "buffer_load_dwordx4 %[v0], %[r0], %[so7] offen lds\n\t" \
  "s_add_u32 m0, m0, 0x400\n\t" \
  "s_sub_u32 %[n], %[n], 1\n\t" \
  "buffer_load_dwordx4 %[v0], %[r1], %[so0] offen lds\n\t" \
  "s_add_u32 m0, m0, 0x400\n\t" \
  "s_nop 0\n\t" \
  "buffer_load_dwordx4 %[v0], %[r1], %[so1] offen lds\n\t" \
  "s_add_u32 m0, m0, 0x400\n\t" \
  "s_nop 0\n\t" \
  "buffer_load_dwordx4 %[v0], %[r1], %[so2] offen lds\n\t" \
  "s_add_u32 m0, m0, 0x400\n\t" \
  "s_nop 0\n\t" \
  "buffer_load_dwordx4 %[v0], %[r1], %[so3] offen lds\n\t" \
  "s_add_u32 m0, m0, 0x400\n\t" \
  "ds_read_b128 v[24:27], %[vc]\n\t" \
  "ds_read_b128 v[28:31], %[vw]\n\t" \
  "buffer_load_dwordx4 %[v0], %[r1], %[so4] offen lds\n\t" \
  "s_add_u32 m0, m0, 0x400\n\t" \
  "s_nop 0\n\t" \
  "buffer_load_dwordx4 %[v0], %[r1], %[so5] offen lds\n\t" \
  "s_add_u32 m0, m0, 0x400\n\t" \
  "s_nop 0\n\t" \
  "buffer_load_dwordx4 %[v0], %[r1], %[so6] offen lds\n\t" \
  "s_add_u32 m0, m0, 0x400\n\t" \
  "s_waitcnt lgkmcnt(0)\n\t" \
  "v_or3_b32 v24, v24, v25, v26\n\t" \
  "v_or_b32 v24, v24, v27\n\t" \
  "v_sub_u32 v28, v30, v28\n\t" \
  "v_sub_u32 v29, v31, v29\n\t" \
  "v_or3_b32 v24, v24, v28, v29\n\t" \
  "buffer_load_dwordx4 %[v0], %[r1], %[so7] offen lds\n\t" \
  "s_cmp_eq_u32 %[n], 15\n\t" \
  "s_cbranch_scc0 Lnb_%=\n\t" \
  "s_barrier\n\t" \
  "Lnb_%=:\n\t" \
  "s_mov_b32 m0, %[mk]\n\t" \
  "s_mov_b64 exec, 1\n\t" \
  "buffer_load_dword %[v0], %[rz], 0 offen lds\n\t" \
  "s_mov_b64 exec, -1\n\t" \
  "v_readfirstlane_b32 %[s0], v24\n\t" \
  "v_add_u32 %[v0], %[sk], %[b0]\n\t" \
  "s_add_u32 %[mk], %[mk], 8\n\t" \
  "s_cmp_eq_u32 %[n], 0\n\t" \
  "s_cbranch_scc1 Lend_%=\n\t" \
  "s_cmp_eq_u32 %[s0], 0\n\t" \
  "s_cbranch_scc1 Ltop_%=\n\t" \
  "Lsp_%=:\n\t" \
  "ds_read_b128 v[24:27], %[vc]\n\t" \
  "ds_read_b128 v[28:31], %[vw]\n\t" \
  "s_sub_i32 %[bud], %[bud], 1\n\t" \
  "s_cmp_lt_i32 %[bud], 0\n\t" \
  "s_cbranch_scc1 Ltop_%=\n\t" \
  "s_waitcnt lgkmcnt(0)\n\t" \
  "v_or3_b32 v24, v24, v25, v26\n\t" \
  "v_or_b32 v24, v24, v27\n\t" \
  "v_sub_u32 v28, v30, v28\n\t" \
  "v_sub_u32 v29, v31, v29\n\t" \
  "v_or3_b32 v24, v24, v28, v29\n\t" \
  "s_nop 0\n\t" \
  "v_readfirstlane_b32 %[s0], v24\n\t" \
  "s_cmp_eq_u32 %[s0], 0\n\t" \
  "s_cbranch_scc1 Ltop_%=\n\t" \
  "s_branch Lsp_%=\n\t" \
  "Lend_%=:\n\t"
#define ASM_LB \
  "s_nop 4\n\t" \
  "s_branch Lsp_%=\n\t" \
  "Ltop_%=:\n\t" \
  "s_mov_b32 m0, %[dst]\n\t" \
  "v_mov_b32 %[vc], %[ci]\n\t" \
  "buffer_load_dwordx4 %[v0], %[r0], %[so0] offen lds\n\t" \
  "s_add_u32 m0, m0, 0x400\n\t" \
  "s_add_u32 %[dst], %[dst], 0x8000\n\t" \
  "buffer_load_dwordx4 %[v1], %[r0], %[so1] offen lds\n\t" \
  "s_add_u32 m0, m0, 0x400\n\t" \
  "s_cmp_ge_u32 %[dst], 0x24000\n\t" \
  "s_cselect_b32 %[s0], %[wrap], 0\n\t" \
  "buffer_load_dwordx4 %[v0], %[r0], %[so2] offen lds\n\t" \
  "s_add_u32 m0, m0, 0x400\n\t" \
  "s_sub_u32 %[dst], %[dst], %[s0]\n\t" \
  "buffer_load_dwordx4 %[v1], %[r0], %[so3] offen lds\n\t" \
  "s_add_u32 m0, m0, 0x400\n\t" \
  "s_add_u32 %[sk], %[sk], 256\n\t" \
  "buffer_load_dwordx4 %[v0], %[r0], %[so4] offen lds\n\t" \
  "s_add_u32 m0, m0, 0x400\n\t" \
  "s_and_b32 %[sk], %[sk], 1023\n\t" \
  "buffer_load_dwordx4 %[v1], %[r0], %[so5] offen lds\n\t" \
  "s_add_u32 m0, m0, 0x400\n\t" \
  "s_cmp_ge_u32 %[ci], %[cth]\n\t" \
  "s_cselect_b32 %[s0], 32, 16\n\t" \
  "buffer_load_dwordx4 %[v0], %[r0], %[so6] offen lds\n\t" \
  "s_add_u32 m0, m0, 0x400\n\t" \
  "s_add_u32 %[ci], %[ci], %[s0]\n\t" \
  "buffer_load_dwordx4 %[v1], %[r0], %[so7] offen lds\n\t" \
  "s_add_u32 m0, m0, 0x400\n\t" \
  "s_sub_u32 %[n], %[n], 1\n\t" \
  "buffer_load_dwordx4 %[v0], %[r1], %[so0] offen lds\n\t" \
  "s_add_u32 m0, m0, 0x400\n\t" \
  "s_cmp_eq_u32 %[n], 4\n\t" \
  "s_cselect_b32 %[s0], 0x400, 0\n\t" \
  "buffer_load_dwordx4 %[v1], %[r1], %[so1] offen lds\n\t" \
  "s_add_u32 m0, m0, 0x400\n\t" \
  "s_xor_b32 %[hi], %[hi], %[s0]\n\t" \
  "buffer_load_dwordx4 %[v0], %[r1], %[so2] offen lds\n\t" \
  "s_add_u32 m0, m0, 0x400\n\t" \
  "s_or_b32 %[s1], %[sk], %[hi]\n\t" \
  "buffer_load_dwordx4 %[v1], %[r1], %[so3] offen lds\n\t" \
  "s_add_u32 m0, m0, 0x400\n\t" \
  "ds_read_b128 v[24:27], %[vc]\n\t" \
  "buffer_load_dwordx4 %[v0], %[r1], %[so4] offen lds\n\t" \
  "s_add_u32 m0, m0, 0x400\n\t" \
  "s_nop 0\n\t" \
  "buffer_load_dwordx4 %[v1], %[r1], %[so5] offen lds\n\t" \
  "s_add_u32 m0, m0, 0x400\n\t" \
  "s_nop 0\n\t" \
  "buffer_load_dwordx4 %[v0], %[r1], %[so6] offen lds\n\t" \
  "s_add_u32 m0, m0, 0x400\n\t" \
  "s_waitcnt lgkmcnt(0)\n\t" \
  "v_or3_b32 v24, v24, v25, v26\n\t" \
  "v_or_b32 v24, v24, v27\n\t" \
  "buffer_load_dwordx4 %[v1], %[r1], %[so7] offen lds\n\t" \
  "s_mov_b32 m0, %[mk]\n\t" \
  "s_mov_b64 exec, 3\n\t" \
  "buffer_load_dword %[v0], %[rz], 0 offen lds\n\t" \
  "s_mov_b64 exec, -1\n\t" \
  "v_readfirstlane_b32 %[s0], v24\n\t" \
  "v_add_u32 %[v0], %[s1], %[b0]\n\t" \
  "v_add_u32 %[v1], %[s1], %[b1]\n\t" \
  "s_add_u32 %[mk], %[mk], 16\n\t" \
  "s_cmp_eq_u32 %[n], %[nend]\n\t" \
  "s_cbranch_scc1 Lend_%=\n\t" \
  "s_cmp_eq_u32 %[s0], 0\n\t" \
  "s_cbranch_scc1 Ltop_%=\n\t" \
  "Lsp_%=:\n\t" \
  "ds_read_b128 v[24:27], %[vc]\n\t" \
  "s_sub_i32 %[bud], %[bud], 1\n\t" \
  "s_cmp_lt_i32 %[bud], 0\n\t" \
  "s_cbranch_scc1 Ltop_%=\n\t" \
  "s_waitcnt lgkmcnt(0)\n\t" \
  "v_or3_b32 v24, v24, v25, v26\n\t" \
  "v_or_b32 v24, v24, v27\n\t" \
  "s_nop 0\n\t" \
  "v_readfirstlane_b32 %[s0], v24\n\t" \
  "s_cmp_eq_u32 %[s0], 0\n\t" \
  "s_cbranch_scc1 Ltop_%=\n\t" \
  "s_sleep 1\n\t" \
  "s_branch Lsp_%=\n\t" \
  "Lend_%=:\n\t" \
  "buffer_load_dwordx4 v[32:35], %[p0], %[rp], %[so0] offen offset:128\n\t" \
  "buffer_load_dwordx4 v[36:39], %[p1], %[rp], %[so1] offen offset:128\n\t" \
  "buffer_load_dwordx4 v[40:43], %[p0], %[rp], %[so2] offen offset:128\n\t" \
  "buffer_load_dwordx4 v[44:47], %[p1], %[rp], %[so3] offen offset:128\n\t" \
  "buffer_load_dwordx4 v[48:51], %[p0], %[rp], %[so4] offen offset:128\n\t" \
  "buffer_load_dwordx4 v[52:55], %[p1], %[rp], %[so5] offen offset:128\n\t" \
  "buffer_load_dwordx4 v[56:59], %[p0], %[rp], %[so6] offen offset:128\n\t" \
  "buffer_load_dwordx4 v[60:63], %[p1], %[rp], %[so7] offen offset:128\n\t" \
  "buffer_load_dwordx4 v[64:67], %[p0], %[rp], %[so0] offen offset:256\n\t" \
  "buffer_load_dwordx4 v[68:71], %[p1], %[rp], %[so1] offen offset:256\n\t" \
  "buffer_load_dwordx4 v[72:75], %[p0], %[rp], %[so2] offen offset:256\n\t" \
  "buffer_load_dwordx4 v[76:79], %[p1], %[rp], %[so3] offen offset:256\n\t" \
  "buffer_load_dwordx4 v[80:83], %[p0], %[rp], %[so4] offen offset:256\n\t" \
  "buffer_load_dwordx4 v[84:87], %[p1], %[rp], %[so5] offen offset:256\n\t" \
  "buffer_load_dwordx4 v[88:91], %[p0], %[rp], %[so6] offen offset:256\n\t" \
  "buffer_load_dwordx4 v[92:95], %[p1], %[rp], %[so7] offen offset:256\n\t" \
  "buffer_load_dwordx4 v[96:99], %[p0], %[rp], %[so0] offen offset:384\n\t" \
  "buffer_load_dwordx4 v[100:103], %[p1], %[rp], %[so1] offen offset:384\n\t" \
  "buffer_load_dwordx4 v[104:107], %[p0], %[rp], %[so2] offen offset:384\n\t" \
  "buffer_load_dwordx4 v[108:111], %[p1], %[rp], %[so3] offen offset:384\n\t" \
  "buffer_load_dwordx4 v[112:115], %[p0], %[rp], %[so4] offen offset:384\n\t" \
  "buffer_load_dwordx4 v[116:119], %[p1], %[rp], %[so5] offen offset:384\n\t" \
  "buffer_load_dwordx4 v[120:123], %[p0], %[rp], %[so6] offen offset:384\n\t" \
  "buffer_load_dwordx4 v[124:127], %[p1], %[rp], %[so7] offen offset:384\n\t" \
  "buffer_load_dwordx4 v[128:131], %[p0], %[rp], %[so0] offen offset:512\n\t" \
  "buffer_load_dwordx4 v[132:135], %[p1], %[rp], %[so1] offen offset:512\n\t" \
  "buffer_load_dwordx4 v[136:139], %[p0], %[rp], %[so2] offen offset:512\n\t" \
  "buffer_load_dwordx4 v[140:143], %[p1], %[rp], %[so3] offen offset:512\n\t" \
  "buffer_load_dwordx4 v[144:147], %[p0], %[rp], %[so4] offen offset:512\n\t" \
  "buffer_load_dwordx4 v[148:151], %[p1], %[rp], %[so5] offen offset:512\n\t" \
  "buffer_load_dwordx4 v[152:155], %[p0], %[rp], %[so6] offen offset:512\n\t" \
  "buffer_load_dwordx4 v[156:159], %[p1], %[rp], %[so7] offen offset:512\n\t" \
  "buffer_load_dwordx4 v[160:163], %[p0], %[rp], %[so0] offen offset:640\n\t" \
  "buffer_load_dwordx4 v[164:167], %[p1], %[rp], %[so1] offen offset:640\n\t" \
  "buffer_load_dwordx4 v[168:171], %[p0], %[rp], %[so2] offen offset:640\n\t" \
  "buffer_load_dwordx4 v[172:175], %[p1], %[rp], %[so3] offen offset:640\n\t" \
  "buffer_load_dwordx4 v[176:179], %[p0], %[rp], %[so4] offen offset:640\n\t" \
  "buffer_load_dwordx4 v[180:183], %[p1], %[rp], %[so5] offen offset:640\n\t" \
  "buffer_load_dwordx4 v[184:187], %[p0], %[rp], %[so6] offen offset:640\n\t" \
  "buffer_load_dwordx4 v[188:191], %[p1], %[rp], %[so7] offen offset:640\n\t" \
  "buffer_load_dwordx4 v[192:195], %[p0], %[rp], %[so0] offen offset:768\n\t" \
  "buffer_load_dwordx4 v[196:199], %[p1], %[rp], %[so1] offen offset:768\n\t" \
  "buffer_load_dwordx4 v[200:203], %[p0], %[rp], %[so2] offen offset:768\n\t" \
  "buffer_load_dwordx4 v[204:207], %[p1], %[rp], %[so3] offen offset:768\n\t" \
  "buffer_load_dwordx4 v[208:211], %[p0], %[rp], %[so4] offen offset:768\n\t" \
  "buffer_load_dwordx4 v[212:215], %[p1], %[rp], %[so5] offen offset:768\n\t" \
  "buffer_load_dwordx4 v[216:219], %[p0], %[rp], %[so6] offen offset:768\n\t" \
  "buffer_load_dwordx4 v[220:223], %[p1], %[rp], %[so7] offen offset:768\n\t" \
  "buffer_load_dwordx4 v[224:227], %[p0], %[rp], %[so0] offen offset:896\n\t" \
  "buffer_load_dwordx4 v[228:231], %[p1], %[rp], %[so1] offen offset:896\n\t" \
  "buffer_load_dwordx4 v[232:235], %[p0], %[rp], %[so2] offen offset:896\n\t" \
  "buffer_load_dwordx4 v[236:239], %[p1], %[rp], %[so3] offen offset:896\n\t" \
  "buffer_load_dwordx4 v[240:243], %[p0], %[rp], %[so4] offen offset:896\n\t" \
  "buffer_load_dwordx4 v[244:247], %[p1], %[rp], %[so5] offen offset:896\n\t" \
  "buffer_load_dwordx4 v[248:251], %[p0], %[rp], %[so6] offen offset:896\n\t" \
  "buffer_load_dwordx4 v[252:255], %[p1], %[rp], %[so7] offen offset:896\n\t" \
  "v_mov_b32 v28, 0\n\t" \
  "Lw0_%=:\n\t" \
  "ds_read_b128 v[24:27], %[vm] offset:0\n\t" \
  "s_sub_i32 %[bud], %[bud], 1\n\t" \
  "s_cmp_lt_i32 %[bud], 0\n\t" \
  "s_cbranch_scc1 Lx0_%=\n\t" \
  "s_waitcnt lgkmcnt(0)\n\t" \
  "v_or3_b32 v24, v24, v25, v26\n\t" \
  "v_or_b32 v24, v24, v27\n\t" \
  "s_nop 0\n\t" \
  "v_readfirstlane_b32 %[s0], v24\n\t" \
  "s_cmp_eq_u32 %[s0], 0\n\t" \
  "s_cbranch_scc1 Lx0_%=\n\t" \
  "s_sleep 2\n\t" \
  "s_branch Lw0_%=\n\t" \
  "Lx0_%=:\n\t" \
  "s_waitcnt vmcnt(0)\n\t" \
  "ds_write_b128 %[vd], v[32:35] offset:0\n\t" \
  "ds_write_b128 %[vd], v[36:39] offset:1024\n\t" \
  "ds_write_b128 %[vd], v[40:43] offset:2048\n\t" \
  "ds_write_b128 %[vd], v[44:47] offset:3072\n\t" \
  "ds_write_b128 %[vd], v[48:51] offset:4096\n\t" \
  "ds_write_b128 %[vd], v[52:55] offset:5120\n\t" \
  "ds_write_b128 %[vd], v[56:59] offset:6144\n\t" \
  "ds_write_b128 %[vd], v[60:63] offset:7168\n\t" \
  "s_waitcnt lgkmcnt(0)\n\t" \
  "ds_write_b32 %[vl], v28 offset:0\n\t" \
  "Lw1_%=:\n\t" \
  "ds_read_b128 v[24:27], %[vm] offset:16\n\t" \
  "s_sub_i32 %[bud], %[bud], 1\n\t" \
  "s_cmp_lt_i32 %[bud], 0\n\t" \
  "s_cbranch_scc1 Lx1_%=\n\t" \
  "s_waitcnt lgkmcnt(0)\n\t" \
  "v_or3_b32 v24, v24, v25, v26\n\t" \
  "v_or_b32 v24, v24, v27\n\t" \
  "s_nop 0\n\t" \
  "v_readfirstlane_b32 %[s0], v24\n\t" \
  "s_cmp_eq_u32 %[s0], 0\n\t" \
  "s_cbranch_scc1 Lx1_%=\n\t" \
  "s_sleep 2\n\t" \
  "s_branch Lw1_%=\n\t" \
  "Lx1_%=:\n\t" \
  "ds_write_b128 %[vd], v[64:67] offset:16384\n\t" \
  "ds_write_b128 %[vd], v[68:71] offset:17408\n\t" \
  "ds_write_b128 %[vd], v[72:75] offset:18432\n\t" \
  "ds_write_b128 %[vd], v[76:79] offset:19456\n\t" \
  "ds_write_b128 %[vd], v[80:83] offset:20480\n\t" \
  "ds_write_b128 %[vd], v[84:87] offset:21504\n\t" \
  "ds_write_b128 %[vd], v[88:91] offset:22528\n\t" \
  "ds_write_b128 %[vd], v[92:95] offset:23552\n\t" \
  "s_waitcnt lgkmcnt(0)\n\t" \
  "ds_write_b32 %[vl], v28 offset:8\n\t" \
  "Lw2_%=:\n\t" \
  "ds_read_b128 v[24:27], %[vm] offset:32\n\t" \
  "s_sub_i32 %[bud], %[bud], 1\n\t" \
  "s_cmp_lt_i32 %[bud], 0\n\t" \
  "s_cbranch_scc1 Lx2_%=\n\t" \
  "s_waitcnt lgkmcnt(0)\n\t" \
  "v_or3_b32 v24, v24, v25, v26\n\t" \
  "v_or_b32 v24, v24, v27\n\t" \
  "s_nop 0\n\t" \
  "v_readfirstlane_b32 %[s0], v24\n\t" \
  "s_cmp_eq_u32 %[s0], 0\n\t" \
  "s_cbranch_scc1 Lx2_%=\n\t" \
  "s_sleep 2\n\t" \
  "s_branch Lw2_%=\n\t" \
  "Lx2_%=:\n\t" \
  "ds_write_b128 %[vd], v[96:99] offset:32768\n\t" \
  "ds_write_b128 %[vd], v[100:103] offset:33792\n\t" \
  "ds_write_b128 %[vd], v[104:107] offset:34816\n\t" \
  "ds_write_b128 %[vd], v[108:111] offset:35840\n\t" \
  "ds_write_b128 %[vd], v[112:115] offset:36864\n\t" \
  "ds_write_b128 %[vd], v[116:119] offset:37888\n\t" \
  "ds_write_b128 %[vd], v[120:123] offset:38912\n\t" \
  "ds_write_b128 %[vd], v[124:127] offset:39936\n\t" \
  "s_waitcnt lgkmcnt(0)\n\t" \
  "ds_write_b32 %[vl], v28 offset:16\n\t" \
  "Lw3_%=:\n\t" \
  "ds_read_b128 v[24:27], %[vm] offset:48\n\t" \
  "s_sub_i32 %[bud], %[bud], 1\n\t" \
  "s_cmp_lt_i32 %[bud], 0\n\t" \
  "s_cbranch_scc1 Lx3_%=\n\t" \
  "s_waitcnt lgkmcnt(0)\n\t" \
  "v_or3_b32 v24, v24, v25, v26\n\t" \
  "v_or_b32 v24, v24, v27\n\t" \
  "s_nop 0\n\t" \
  "v_readfirstlane_b32 %[s0], v24\n\t" \
  "s_cmp_eq_u32 %[s0], 0\n\t" \
  "s_cbranch_scc1 Lx3_%=\n\t" \
  "s_sleep 2\n\t" \
  "s_branch Lw3_%=\n\t" \
  "Lx3_%=:\n\t" \
  "ds_write_b128 %[vd], v[128:131] offset:49152\n\t" \
  "ds_write_b128 %[vd], v[132:135] offset:50176\n\t" \
  "ds_write_b128 %[vd], v[136:139] offset:51200\n\t" \
  "ds_write_b128 %[vd], v[140:143] offset:52224\n\t" \
  "ds_write_b128 %[vd], v[144:147] offset:53248\n\t" \
  "ds_write_b128 %[vd], v[148:151] offset:54272\n\t" \
  "ds_write_b128 %[vd], v[152:155] offset:55296\n\t" \
  "ds_write_b128 %[vd], v[156:159] offset:56320\n\t" \
  "v_add_u32 %[vd], 0x10000, %[vd]\n\t" \
  "s_waitcnt lgkmcnt(0)\n\t" \
  "ds_write_b32 %[vl], v28 offset:24\n\t" \
  "Lw4_%=:\n\t" \
  "ds_read_b128 v[24:27], %[vm] offset:64\n\t" \
  "s_sub_i32 %[bud], %[bud], 1\n\t" \
  "s_cmp_lt_i32 %[bud], 0\n\t" \
  "s_cbranch_scc1 Lx4_%=\n\t" \
  "s_waitcnt lgkmcnt(0)\n\t" \
  "v_or3_b32 v24, v24, v25, v26\n\t" \
  "v_or_b32 v24, v24, v27\n\t" \
  "s_nop 0\n\t" \
  "v_readfirstlane_b32 %[s0], v24\n\t" \
  "s_cmp_eq_u32 %[s0], 0\n\t" \
  "s_cbranch_scc1 Lx4_%=\n\t" \
  "s_sleep 2\n\t" \
  "s_branch Lw4_%=\n\t" \
  "Lx4_%=:\n\t" \
  "ds_write_b128 %[vd], v[160:163] offset:0\n\t" \
  "ds_write_b128 %[vd], v[164:167] offset:1024\n\t" \
  "ds_write_b128 %[vd], v[168:171] offset:2048\n\t" \
  "ds_write_b128 %[vd], v[172:175] offset:3072\n\t" \
  "ds_write_b128 %[vd], v[176:179] offset:4096\n\t" \
  "ds_write_b128 %[vd], v[180:183] offset:5120\n\t" \
  "ds_write_b128 %[vd], v[184:187] offset:6144\n\t" \
  "ds_write_b128 %[vd], v[188:191] offset:7168\n\t" \
  "s_waitcnt lgkmcnt(0)\n\t" \
  "ds_write_b32 %[vl], v28 offset:32\n\t" \
  "Lw5_%=:\n\t" \
  "ds_read_b128 v[24:27], %[vm] offset:80\n\t" \
  "s_sub_i32 %[bud], %[bud], 1\n\t" \
  "s_cmp_lt_i32 %[bud], 0\n\t" \
  "s_cbranch_scc1 Lx5_%=\n\t" \
  "s_waitcnt lgkmcnt(0)\n\t" \
  "v_or3_b32 v24, v24, v25, v26\n\t" \
  "v_or_b32 v24, v24, v27\n\t" \
  "s_nop 0\n\t" \
  "v_readfirstlane_b32 %[s0], v24\n\t" \
  "s_cmp_eq_u32 %[s0], 0\n\t" \
  "s_cbranch_scc1 Lx5_%=\n\t" \
  "s_sleep 2\n\t" \
  "s_branch Lw5_%=\n\t" \
  "Lx5_%=:\n\t" \
  "ds_write_b128 %[vd], v[192:195] offset:16384\n\t" \
  "ds_write_b128 %[vd], v[196:199] offset:17408\n\t" \
  "ds_write_b128 %[vd], v[200:203] offset:18432\n\t" \
  "ds_write_b128 %[vd], v[204:207] offset:19456\n\t" \
  "ds_write_b128 %[vd], v[208:211] offset:20480\n\t" \
  "ds_write_b128 %[vd], v[212:215] offset:21504\n\t" \
  "ds_write_b128 %[vd], v[216:219] offset:22528\n\t" \
  "ds_write_b128 %[vd], v[220:223] offset:23552\n\t" \
  "s_waitcnt lgkmcnt(0)\n\t" \
  "ds_write_b32 %[vl], v28 offset:40\n\t" \
  "Lw6_%=:\n\t" \
  "ds_read_b128 v[24:27], %[vm] offset:96\n\t" \
  "s_sub_i32 %[bud], %[bud], 1\n\t" \
  "s_cmp_lt_i32 %[bud], 0\n\t" \
  "s_cbranch_scc1 Lx6_%=\n\t" \
  "s_waitcnt lgkmcnt(0)\n\t" \
  "v_or3_b32 v24, v24, v25, v26\n\t" \
  "v_or_b32 v24, v24, v27\n\t" \
  "s_nop 0\n\t" \
  "v_readfirstlane_b32 %[s0], v24\n\t" \
  "s_cmp_eq_u32 %[s0], 0\n\t" \
  "s_cbranch_scc1 Lx6_%=\n\t" \
  "s_sleep 2\n\t" \
  "s_branch Lw6_%=\n\t" \
  "Lx6_%=:\n\t" \
  "ds_write_b128 %[vd], v[224:227] offset:32768\n\t" \
  "ds_write_b128 %[vd], v[228:231] offset:33792\n\t" \
  "ds_write_b128 %[vd], v[232:235] offset:34816\n\t" \
  "ds_write_b128 %[vd], v[236:239] offset:35840\n\t" \
  "ds_write_b128 %[vd], v[240:243] offset:36864\n\t" \
  "ds_write_b128 %[vd], v[244:247] offset:37888\n\t" \
  "ds_write_b128 %[vd], v[248:251] offset:38912\n\t" \
  "ds_write_b128 %[vd], v[252:255] offset:39936\n\t" \
  "s_waitcnt lgkmcnt(0)\n\t" \
  "ds_write_b32 %[vl], v28 offset:48\n\t" \
  "s_waitcnt lgkmcnt(0)\n\t"

#define OST " sc1"
#define XPRIO0 3
#define XPRIO 1
#define CPRIO 2
#define LPRIO 2
#define FPRIO 1
#define MPRIO 2
#define WGRP (sg & 1)
#define WS0 1
#define WS1 5
#define WS2 9
#define WSLP 8
#define TKG0 3
#define GEXACT 0
#define GCG 0
#define YPRIO 3
#define GPRIO 1
#define W2G 14
#define W2P 1
#define XP 0
#define XPB 1
#define XPD 0
#define CWS 16
#define GUG 25
#define TKG 4
#define YG 1
#define CGS 14
#define NOFFN 0
#define ROT1 0
#define CODEPF 29568
#define CPULL 1
#define WNT ""
#define PRET 1
constexpr int D_ = 1024, F_ = 3584;
constexpr int MISC = 147456;
constexpr int L_TAB = MISC;
constexpr int L_FLG = L_TAB + 11 * 512;
constexpr int L_ONE = L_FLG + 256;
constexpr int L_G = L_ONE + 1024;
constexpr int L_SEL = L_G + 512;
constexpr int L_BROW = L_SEL + 128;
constexpr int L_ACT = L_BROW + 2048;
constexpr int L_XS = L_ACT + 1024;
constexpr int L_JUNK = L_XS + 4096;
constexpr int LDS_BYTES = 163840;
static_assert(L_JUNK + 768 <= LDS_BYTES, "lds");
constexpr int M_CONS = L_ONE + 16, M_LAND = L_ONE + 544, M_GT = M_LAND + 288, M_HR = M_LAND + 296, M_CAR = M_LAND + 304, M_XS = M_LAND + 320, M_WR = M_LAND + 336, M_OWN = M_LAND + 352;
constexpr int FL_XD = L_FLG + 16, FL_TK = L_FLG + 24;
constexpr unsigned W_EPOCH = 0;
constexpr unsigned W_RG = 256;
constexpr unsigned W_R2 = W_RG + 2048 * 8;
constexpr unsigned W_HF = 32768;
constexpr unsigned W_GF = W_HF + 32 * 128;
constexpr unsigned W_YF = W_GF + 56 * 128;
constexpr unsigned W_HT = 49152;
constexpr unsigned W_AGG = 65536;
constexpr unsigned W_GU = W_AGG + 262144;
constexpr unsigned W_YP = W_GU + 56 * 16384;
constexpr unsigned W_H = 4u << 20;
constexpr unsigned W_DBG = 24u << 20;
constexpr unsigned W_END = 25u << 20;
static_assert(W_R2 + 8192 <= W_HF && W_YF + 1024 <= W_HT && W_YP + 128 * 56 * 256 <= W_H, "ws");
#define WS_BASE(n) (((n) - W_END) & ~(size_t)0x1fffff)
constexpr int SPIN_MAX = 1 << 18;

DEV float bf2f(unsigned v) { return __uint_as_float(v << 16); }
DEV float bfhi(unsigned v) { return __uint_as_float(v & 0xffff0000u); }
DEV u16 f2bf(float f) { return __builtin_bit_cast(u16, (__bf16)f); }
DEV float rbf(float f) { return bf2f(f2bf(f)); }
typedef __bf16 bf16x2_t __attribute__((ext_vector_type(2)));
DEV unsigned pk2(float a, float b) { return __builtin_bit_cast(unsigned, __builtin_convertvector((f32x2){a, b}, bf16x2_t)); }
DEV float fma1(float a, float b, float c) { float d; asm("v_fma_f32 %0, %1, %2, %3" : "=v"(d) : "v"(a), "v"(b), "v"(c)); return d; }
DEV float fnma1(float a, float b, float c) { float d; asm("v_fma_f32 %0, -%1, %2, %3" : "=v"(d) : "v"(a), "v"(b), "v"(c)); return d; }
DEV float mul1(float a, float b) { float d; asm("v_mul_f32 %0, %1, %2" : "=v"(d) : "v"(a), "v"(b)); return d; }
DEV f32x2 pkfma(f32x2 a, f32x2 b, f32x2 c) { f32x2 d; asm("v_pk_fma_f32 %0, %1, %2, %3" : "=v"(d) : "v"(a), "v"(b), "v"(c)); return d; }
DEV float add1(float a, float b) { float d; asm("v_add_f32 %0, %1, %2" : "=v"(d) : "v"(a), "v"(b)); return d; }
DEV f32x2 pkmul(f32x2 a, f32x2 b) { f32x2 d; asm("v_pk_mul_f32 %0, %1, %2" : "=v"(d) : "v"(a), "v"(b)); return d; }
DEV f32x2 cmul(f32x2 a, f32x2 b) { return (f32x2){fnma1(a.y, b.y, mul1(a.x, b.x)), fma1(a.y, b.x, mul1(a.x, b.y))}; }
template <int N> DEV void waitv() { asm volatile("s_waitcnt vmcnt(%0)" ::"n"(N) : "memory"); }
DEV void bar() { asm volatile("s_barrier" ::: "memory"); }
DEV unsigned lds_rd32(unsigned a) { unsigned v; asm volatile("ds_read_b32 %0, %1\n\ts_waitcnt lgkmcnt(0)" : "=v"(v) : "v"(a) : "memory"); return v; }
DEV u32x2 lds_rd64(unsigned a) { u32x2 v; asm volatile("ds_read_b64 %0, %1\n\ts_waitcnt lgkmcnt(0)" : "=v"(v) : "v"(a) : "memory"); return v; }
DEV u32x4 lds_rd128(unsigned a) { u32x4 v; asm volatile("ds_read_b128 %0, %1\n\ts_waitcnt lgkmcnt(0)" : "=v"(v) : "v"(a) : "memory"); return v; }
DEV void lds_wr32(unsigned a, unsigned v) { asm volatile("ds_write_b32 %0, %1" ::"v"(a), "v"(v) : "memory"); }
DEV void lds_wait_ge(unsigned a, unsigned need) {
#pragma unroll 1
  for (int it = 0; it < SPIN_MAX * 4; ++it) { if (__builtin_amdgcn_readfirstlane(lds_rd32(a)) >= need) break; __builtin_amdgcn_s_sleep(1); }
}
DEV void lds_wait2(unsigned a) {
#pragma unroll 1
  for (int it = 0; it < SPIN_MAX * 4; ++it) { const u32x2 v = lds_rd64(a); if (__builtin_amdgcn_readfirstlane(v.x & v.y)) break; __builtin_amdgcn_s_sleep(1); }
}
DEV void lds_wait0(unsigned a) {
#pragma unroll 1
  for (int it = 0; it < SPIN_MAX * 4; ++it) { if (__builtin_amdgcn_readfirstlane(lds_rd32(a)) == 0u) break; __builtin_amdgcn_s_sleep(1); }
}
typedef __amdgpu_buffer_rsrc_t rsrc_t;
DEV void mark(rsrc_t rz, unsigned a) {
  if ((threadIdx.x & 63) == 0) __builtin_amdgcn_raw_ptr_buffer_load_lds(rz, (LAS void*)(size_t)a, 4, 0, 0, 0, 0);
}
DEV u32x4 ld128(rsrc_t r, unsigned vo, unsigned so = 0) { return __builtin_amdgcn_raw_buffer_load_b128(r, vo, so, 0); }
#define FRESH() asm volatile("" ::: "memory")
DEV u32x4 ld128c(rsrc_t r, unsigned vo) { return __builtin_amdgcn_raw_buffer_load_b128(r, vo, 0, 16); }
DEV void st128c(rsrc_t r, unsigned vo, u32x4 v) {
  asm volatile("s_nop 4\n\tbuffer_store_dwordx4 %0, %1, %2, 0 offen sc1\n\ts_nop 7\n\ts_nop 7" ::"v"(v), "v"(vo), "s"(r) : "memory");
}
DEV void st64c(rsrc_t r, unsigned vo, u32x2 v) { __builtin_amdgcn_raw_buffer_store_b64(v, r, vo, 0, 16); }
DEV void ld8(u32x4 (&w)[8], rsrc_t r, unsigned vo, unsigned so, unsigned step) {
  asm volatile("s_nop 4\n\tbuffer_load_dwordx4 %0, %9, %10, %8 offen\n\ts_add_u32 %8, %8, %11\n\tbuffer_load_dwordx4 %1, %9, %10, %8 offen\n\ts_add_u32 %8, %8, %11\n\t"
               "buffer_load_dwordx4 %2, %9, %10, %8 offen\n\ts_add_u32 %8, %8, %11\n\tbuffer_load_dwordx4 %3, %9, %10, %8 offen\n\ts_add_u32 %8, %8, %11\n\t"
               "buffer_load_dwordx4 %4, %9, %10, %8 offen\n\ts_add_u32 %8, %8, %11\n\tbuffer_load_dwordx4 %5, %9, %10, %8 offen\n\ts_add_u32 %8, %8, %11\n\t"
               "buffer_load_dwordx4 %6, %9, %10, %8 offen\n\ts_add_u32 %8, %8, %11\n\tbuffer_load_dwordx4 %7, %9, %10, %8 offen"
               : "=&v"(w[0]), "=&v"(w[1]), "=&v"(w[2]), "=&v"(w[3]), "=&v"(w[4]), "=&v"(w[5]), "=&v"(w[6]), "=&v"(w[7]), "+&s"(so)
               : "v"(vo), "s"(r), "s"(step) : "memory", "scc");
}
DEV void ld8c(u32x4 (&w)[8], rsrc_t r, unsigned vo, unsigned so, unsigned step, int a, int b) {
  asm volatile("s_nop 4\n\ts_cmp_eq_u32 %12, %13\n\ts_cbranch_scc0 Lskip_%=\n\t"
               "buffer_load_dwordx4 %0, %9, %10, %8 offen" WNT "\n\ts_add_u32 %8, %8, %11\n\tbuffer_load_dwordx4 %1, %9, %10, %8 offen" WNT "\n\ts_add_u32 %8, %8, %11\n\t"
               "buffer_load_dwordx4 %2, %9, %10, %8 offen" WNT "\n\ts_add_u32 %8, %8, %11\n\tbuffer_load_dwordx4 %3, %9, %10, %8 offen" WNT "\n\ts_add_u32 %8, %8, %11\n\t"
               "buffer_load_dwordx4 %4, %9, %10, %8 offen" WNT "\n\ts_add_u32 %8, %8, %11\n\tbuffer_load_dwordx4 %5, %9, %10, %8 offen" WNT "\n\ts_add_u32 %8, %8, %11\n\t"
               "buffer_load_dwordx4 %6, %9, %10, %8 offen" WNT "\n\ts_add_u32 %8, %8, %11\n\tbuffer_load_dwordx4 %7, %9, %10, %8 offen" WNT "\n\tLskip_%=:"
               : "+&v"(w[0]), "+&v"(w[1]), "+&v"(w[2]), "+&v"(w[3]), "+&v"(w[4]), "+&v"(w[5]), "+&v"(w[6]), "+&v"(w[7]), "+&s"(so)
               : "v"(vo), "s"(r), "s"(step), "s"(a), "n"(b) : "memory", "scc");
}
DEV rsrc_t mk(const void* p, unsigned bytes) { return __builtin_amdgcn_make_buffer_rsrc((void*)p, 0, bytes, 0x00020000); }

DEV unsigned wave_maxu(unsigned v) {
  unsigned r;
  asm("s_nop 1\n\tv_max_u32_dpp %1, %1, %1 row_shr:1 row_mask:0xf bank_mask:0xf\n\ts_nop 1\n\tv_max_u32_dpp %1, %1, %1 row_shr:2 row_mask:0xf bank_mask:0xf\n\t"
      "s_nop 1\n\tv_max_u32_dpp %1, %1, %1 row_shr:4 row_mask:0xf bank_mask:0xf\n\ts_nop 1\n\tv_max_u32_dpp %1, %1, %1 row_shr:8 row_mask:0xf bank_mask:0xf\n\t"
      "s_nop 1\n\tv_max_u32_dpp %1, %1, %1 row_bcast:15 row_mask:0xa bank_mask:0xf\n\ts_nop 1\n\tv_max_u32_dpp %1, %1, %1 row_bcast:31 row_mask:0xc bank_mask:0xf\n\t"
      "s_nop 0\n\tv_readlane_b32 %0, %1, 63\n\ts_nop 1" : "=s"(r), "+v"(v));
  return r;
}
DEV float sum4(const float (&a)[4]) {
  auto p = __builtin_amdgcn_permlane16_swap(__float_as_uint(a[0]), __float_as_uint(a[1]), false, false);
  auto q = __builtin_amdgcn_permlane16_swap(__float_as_uint(a[2]), __float_as_uint(a[3]), false, false);
  const float u = __uint_as_float(p[0]) + __uint_as_float(p[1]), w = __uint_as_float(q[0]) + __uint_as_float(q[1]);
  auto r = __builtin_amdgcn_permlane32_swap(__float_as_uint(u), __float_as_uint(w), false, false);
  float v = __uint_as_float(r[0]) + __uint_as_float(r[1]);
  v += __uint_as_float((unsigned)__builtin_amdgcn_update_dpp(0, (int)__float_as_uint(v), 0x128, 0xf, 0xf, false));
  v += __uint_as_float((unsigned)__builtin_amdgcn_update_dpp(0, (int)__float_as_uint(v), 0x124, 0xf, 0xf, false));
  v += __uint_as_float((unsigned)__builtin_amdgcn_update_dpp(0, (int)__float_as_uint(v), 0x122, 0xf, 0xf, false));
  v += __uint_as_float((unsigned)__builtin_amdgcn_update_dpp(0, (int)__float_as_uint(v), 0x121, 0xf, 0xf, false));
  return v;
}
DEV float dot2(unsigned a, unsigned b, float c) { return __builtin_amdgcn_fdot2_f32_bf16(__builtin_bit_cast(bf16x2_t, a), __builtin_bit_cast(bf16x2_t, b), c, false); }
DEV unsigned route_code(unsigned u) { return (u & 0x8000u) ? (~u & 0xffffu) : (u | 0x8000u); }
DEV unsigned med3u(unsigned a, unsigned b, unsigned c) { unsigned d; asm("v_med3_u32 %0, %1, %2, %3" : "=v"(d) : "v"(a), "v"(b), "v"(c)); return d; }
DEV float key_val(unsigned key) {
  unsigned u = key >> 16;
  u = (u & 0x8000u) ? (u & 0x7fffu) : (~u & 0xffffu);
  return __uint_as_float(u << 16);
}
#define STAMP(k)
#define STAMPS()

DEV void unit_mma(const u32x4 (&w)[8], bf16x8 a, f32x4 (&d)[8]) {
#pragma unroll
  for (int j = 0; j < 8; ++j) {
    u32x4 b;
#pragma unroll
    for (int t = 0; t < 4; ++t) b[t] = __builtin_amdgcn_perm(w[2 * t + 1][j >> 1], w[2 * t][j >> 1], (j & 1) ? 0x07060302u : 0x05040100u);
    d[j] = __builtin_amdgcn_mfma_f32_16x16x32_bf16(a, __builtin_bit_cast(bf16x8, b), d[j], 0, 0, 0);
  }
}

DEV void fold(const f32x4 (&d)[8], f32x4 (&o)[2]) {
#pragma unroll
  for (int r = 0; r < 2; ++r)
#pragma unroll
    for (int q = 0; q < 4; ++q) {
      const float a0 = d[q][r], a1 = d[q][r + 2], b0 = d[q + 4][r], b1 = d[q + 4][r + 2];
      auto a = __builtin_amdgcn_permlane32_swap(__float_as_uint(a0), __float_as_uint(a1), false, false);
      auto b = __builtin_amdgcn_permlane32_swap(__float_as_uint(b0), __float_as_uint(b1), false, false);
      float t;
      asm("s_nop 1\n\tv_add_f32_dpp %0, %1, %2 row_ror:8 row_mask:0xf bank_mask:0x3\n\tv_add_f32_dpp %0, %3, %4 row_ror:8 row_mask:0xf bank_mask:0xc"
          : "=&v"(t) : "v"(a[1]), "v"(a[0]), "v"(b[0]), "v"(b[1]));
      o[r][q] = t;
    }
}
DEV void st2x128c(rsrc_t r, unsigned vo, unsigned so, const f32x4 (&o)[2]) {
  asm volatile("s_nop 4\n\tbuffer_store_dwordx4 %0, %2, %3, 0 offen sc1\n\tbuffer_store_dwordx4 %1, %2, %3, %4 offen sc1\n\ts_nop 7\n\ts_nop 7"
               ::"v"(o[0]), "v"(o[1]), "v"(vo), "s"(r), "s"(so) : "memory");
}
DEV void poll32(rsrc_t r, unsigned vo, unsigned tg, bool on) {
#pragma unroll 1
  for (int it = 0; it < SPIN_MAX; ++it) {
    FRESH();
    const unsigned v = __builtin_amdgcn_raw_buffer_load_b32(r, vo, 0, 16);
    if (__all(!on || v == tg)) break;
    __builtin_amdgcn_s_sleep(16);
  }
}

DEV void count1(rsrc_t r, unsigned so) {
  asm volatile("s_nop 4\n\ts_waitcnt vmcnt(0)\n\ts_mov_b64 exec, 1\n\tbuffer_atomic_add %0, off, %1, %2 sc1\n\ts_mov_b64 exec, -1" ::"v"(1u), "s"(r), "s"(so) : "memory");
}
DEV void count_wait(rsrc_t r, unsigned so, unsigned n) {
#pragma unroll 1
  for (int it = 0; it < SPIN_MAX; ++it) {
    unsigned v = 0;
    asm volatile("s_nop 4\n\ts_mov_b64 exec, 1\n\tbuffer_atomic_or %0, off, %1, %2 sc0 sc1\n\ts_mov_b64 exec, -1\n\ts_waitcnt vmcnt(0)" : "+v"(v) : "s"(r), "s"(so) : "memory");
    if (__builtin_amdgcn_readfirstlane(v) == n) break;
    __builtin_amdgcn_s_sleep(CWS);
  }
}

typedef __attribute__((ext_vector_type(8))) u64 u64x8;
__global__ __launch_bounds__(512, 2) void fused(const u16*, const u16*, const u16*, const u16*, const u16*, const u16*, const u16*, const u16*, const u16*,
                                                const u16*, const u16*, const u16*, const u16*, const u16*, char*, float*) {
#if __HIP_DEVICE_COMPILE__
  extern __shared__ __attribute__((aligned(16))) char smem_[];
  LAS char* smem = (LAS char*)smem_;
#define ARGREQ(W) u64x8 ka, kb; \
  asm volatile("s_load_dwordx16 %0, %2, 0x0\n\ts_load_dwordx16 %1, %2, 0x40" W : "=&s"(ka), "=&s"(kb) : "s"(__builtin_amdgcn_kernarg_segment_ptr()) : "memory")
#define ARGS() \
  const u16 *x = (const u16*)ka[0], *Lre = (const u16*)ka[1], *Lim = (const u16*)ka[2], *Bre = (const u16*)ka[3], *Bim = (const u16*)ka[4], *Cre = (const u16*)ka[5], \
            *Cim = (const u16*)ka[6], *logdt = (const u16*)ka[7], *Dskip = (const u16*)kb[0], *w_r = (const u16*)kb[1], *w_gate = (const u16*)kb[2], \
            *w_up = (const u16*)kb[3], *w_down = (const u16*)kb[4], *gate_w = (const u16*)kb[5]; \
  char* ws = (char*)kb[6]; float* out = (float*)kb[7]; \
  const rsrc_t rws = mk(ws, W_END), rx = mk(x, 2048 * 1024 * 2), rz = mk(x, 0)
  STAMP(0);
  const int tid = threadIdx.x, lane = tid & 63, wid = __builtin_amdgcn_readfirstlane(tid >> 6);
  const int bid = blockIdx.x, xcd = bid & 7, loc = bid >> 3;
  const int c = (xcd >> 1) * 4 + (loc >> 3), j = (xcd & 1) * 8 + (loc & 7);
  const int fr = lane & 15, fq = lane >> 4;

  if (__builtin_expect(wid == 4 || wid == 5, 1)) {
    ARGREQ("");
    const int l = wid - 4;
    if ((loc & 3) == l) {
      u64 pc;
      asm volatile("s_getpc_b64 %0" : "=s"(pc));
      const rsrc_t rc = mk((const void*)(pc & ~0xffull), CODEPF);
      __builtin_amdgcn_raw_ptr_buffer_load_lds(rc, (LAS void*)(smem + L_JUNK), 4, ((loc >> 1) & 6) * 8192 + l * 8192 + lane * 128, 0, 0, 0);
    }
    __builtin_amdgcn_s_setprio(LPRIO);
    const int l3 = lane >> 3;
    {
      const unsigned one = tid >= 272 ? 1u : 0u;
      asm volatile("ds_write_b128 %0, %1" ::"v"(L_FLG + min(tid - 256, 111) * 16), "v"((u32x4){one, one, one, one}) : "memory");
    }
    int bud = SPIN_MAX * 4;
    unsigned dst = 131072 - l * 114688, ci = M_CONS - 64, t_s0, vc = 0;
    unsigned b0 = (c * 128 + l * (j * 64 - c * 128) + l3 * (8 - 7 * l)) * 2048 + ((lane & 7) ^ l3) * 16, wrap = 163840 - l * 32768;
    unsigned so[8];
#pragma unroll
    for (int q = 0; q < 8; ++q) so[q] = (64 * (q & 1) + (q >> 1) + l * (8 * q - 64 * (q & 1) - (q >> 1))) * 2048;
    unsigned sk = ROT1 ? ((c + j) & 15) * 128 : (((xcd >> 1) + 2 * (xcd & 1)) & 3) * 512, n = 16, mka = M_LAND + l * 4, v0 = b0 + sk;
    asm volatile("" : "+s"(dst), "+v"(b0), "+s"(wrap), "+s"(so[1]), "+s"(so[2]), "+s"(so[3]), "+s"(so[4]), "+s"(so[5]), "+s"(so[6]), "+s"(so[7]),
                 "+s"(sk), "+s"(mka), "+v"(v0));
    asm volatile("s_waitcnt lgkmcnt(0)" : "+s"(ka), "+s"(kb)::"memory");
    ARGS();
    {
      const rsrc_t r0 = mk(l ? Bre : x, (1u << 22) >> l), r1 = mk(l ? Bim : x + 4096, l ? (1u << 21) : (1u << 22) - 8192);
      STAMP(8);
      asm volatile(ASM_LA : [dst] "+&s"(dst), [sk] "+&s"(sk), [ci] "+&s"(ci), [n] "+&s"(n), [mk] "+&s"(mka), [bud] "+&s"(bud), [s0] "=&s"(t_s0), [v0] "+&v"(v0), [vc] "+&v"(vc) : [r0] "s"(r0), [r1] "s"(r1), [rz] "s"(rz), [wrap] "s"(wrap), [b0] "v"(b0), [vw] "v"(M_WR), [so0] "s"(so[0]), [so1] "s"(so[1]), [so2] "s"(so[2]), [so3] "s"(so[3]), [so4] "s"(so[4]), [so5] "s"(so[5]), [so6] "s"(so[6]), [so7] "s"(so[7]) : "memory", "scc", "m0", "v24", "v25", "v26", "v27", "v28", "v29", "v30", "v31");
    }
    STAMP(1);
    {
      const rsrc_t r0 = mk(Cre, 1 << 21), r1 = mk(Cim, 1 << 21);
      const unsigned b0 = (j * 64 + 4 * l3) * 2048 + (((lane & 7) ^ (l3 >> 1)) * 16), b1 = b0 ^ 64, wrap = 147456, cth = M_CONS + (15 - l) * 16;
      unsigned so[8];
#pragma unroll
      for (int q = 0; q < 8; ++q) so[q] = (32 * (q & 1) + (q >> 1)) * 2048;
      unsigned sk = ((0 * j + l) & 7) * 128, hi = (j & 8) * 128, n = 8, mka = M_LAND + (17 + l) * 8, v0 = b0 + sk + hi, v1 = b1 + sk + hi, t_s1;
      const rsrc_t rp = mk(l ? Cim : Cre, 1 << 21);
      const unsigned p0 = b0 + (hi ^ 0x400);
      unsigned vd = l * 8192 + lane * 16;
      asm volatile(ASM_LB : [dst] "+&s"(dst), [sk] "+&s"(sk), [ci] "+&s"(ci), [n] "+&s"(n), [mk] "+&s"(mka), [bud] "+&s"(bud), [s0] "=&s"(t_s0), [v0] "+&v"(v0), [vc] "+&v"(vc), [v1] "+&v"(v1), [hi] "+&s"(hi), [s1] "=&s"(t_s1), [vd] "+&v"(vd) : [r0] "s"(r0), [r1] "s"(r1), [rz] "s"(rz), [wrap] "s"(wrap), [b0] "v"(b0), [sk2] "s"(l * 128), [cth] "s"(cth), [b1] "v"(b1), [nend] "s"(3 + l), [rp] "s"(rp), [p0] "v"(p0), [p1] "v"(p0 ^ 64), [vm] "v"(M_CONS + 256), [vl] "v"(M_LAND + 26 * 8 + 4 * l), [so0] "s"(so[0]), [so1] "s"(so[1]), [so2] "s"(so[2]), [so3] "s"(so[3]), [so4] "s"(so[4]), [so5] "s"(so[5]), [so6] "s"(so[6]), [so7] "s"(so[7]) : "memory", "scc", "m0", "v24", "v25", "v26", "v27", "v28", "v29", "v30", "v31", "v32", "v33", "v34", "v35", "v36", "v37", "v38", "v39", "v40", "v41", "v42", "v43", "v44", "v45", "v46", "v47", "v48", "v49", "v50", "v51", "v52", "v53", "v54", "v55", "v56", "v57", "v58", "v59", "v60", "v61", "v62", "v63", "v64", "v65", "v66", "v67", "v68", "v69", "v70", "v71", "v72", "v73", "v74", "v75", "v76", "v77", "v78", "v79", "v80", "v81", "v82", "v83", "v84", "v85", "v86", "v87", "v88", "v89", "v90", "v91", "v92", "v93", "v94", "v95", "v96", "v97", "v98", "v99", "v100", "v101", "v102", "v103", "v104", "v105", "v106", "v107", "v108", "v109", "v110", "v111", "v112", "v113", "v114", "v115", "v116", "v117", "v118", "v119", "v120", "v121", "v122", "v123", "v124", "v125", "v126", "v127", "v128", "v129", "v130", "v131", "v132", "v133", "v134", "v135", "v136", "v137", "v138", "v139", "v140", "v141", "v142", "v143", "v144", "v145", "v146", "v147", "v148", "v149", "v150", "v151", "v152", "v153", "v154", "v155", "v156", "v157", "v158", "v159", "v160", "v161", "v162", "v163", "v164", "v165", "v166", "v167", "v168", "v169", "v170", "v171", "v172", "v173", "v174", "v175", "v176", "v177", "v178", "v179", "v180", "v181", "v182", "v183", "v184", "v185", "v186", "v187", "v188", "v189", "v190", "v191", "v192", "v193", "v194", "v195", "v196", "v197", "v198", "v199", "v200", "v201", "v202", "v203", "v204", "v205", "v206", "v207", "v208", "v209", "v210", "v211", "v212", "v213", "v214", "v215", "v216", "v217", "v218", "v219", "v220", "v221", "v222", "v223", "v224", "v225", "v226", "v227", "v228", "v229", "v230", "v231", "v232", "v233", "v234", "v235", "v236", "v237", "v238", "v239", "v240", "v241", "v242", "v243", "v244", "v245", "v246", "v247", "v248", "v249", "v250", "v251", "v252", "v253", "v254", "v255");
    }
    waitv<0>();
    STAMP(3);
    STAMPS();
    __builtin_amdgcn_endpgm();
  }

  if (__builtin_expect(wid >= 6, 1)) {
    const int xw = wid - 6;
    __builtin_amdgcn_s_setprio(XPRIO0);
    ARGREQ("\n\ts_waitcnt lgkmcnt(0)\n\ts_barrier");
    ARGS();
    const int row0 = c * 128 + j * 8 + xw * 4;
    const int l = xw;
    const bool ffn = !NOFFN && loc < 28;
    const int sg = xcd * 7 + (loc >> 2), kq = loc & 3;
    u32x4 w1[4][8], w2[2][8];
    unsigned tagA, tagB;
    const int grp = __builtin_amdgcn_readfirstlane(!ffn ? 9 : WGRP);
    auto wburst = [&](int p, unsigned gate) __attribute__((always_inline)) {
      const bool on = grp == p;
      const rsrc_t r1 = mk(xw ? w_up : w_gate, D_ * F_ * 2), r2 = mk(w_down, D_ * F_ * 2);
      const unsigned o1 = (kq * 256 + 32 * (fr >> 3) + 8 * fq) * (F_ * 2) + (sg * 64 + 8 * (fr & 7)) * 2;
      const unsigned o2 = (sg * 64 + 32 * (fr >> 3) + 8 * fq) * 2048 + (kq * 256 + xw * 128 + 8 * (fr & 7)) * 2;
      if (on) {
        lds_wait0(M_LAND + gate * 8);
        mark(rz, M_WR + xw * 4);
        __builtin_amdgcn_s_sleep(WSLP);
      }
#pragma unroll
      for (int u = 0; u < 4; ++u) {
        ld8c(w1[u], r1, o1, u * 64 * (F_ * 2), F_ * 2, grp, p);
        if (W2G >= 0 && u == 1 && on) mark(rz, M_WR + 8 + xw * 4);
      }
      if (on || (p == 0 && !ffn))
        __builtin_amdgcn_raw_ptr_buffer_load_lds(mk(xw ? Cim : Cre, 1 << 21), (LAS void*)(smem + L_JUNK + 256 + xw * 256), 4, (j * 64 + lane) * 2048 + c * 128, 0, 0, 0);
    };
    {
      unsigned ep;
      u32x4 xv[8], wv[2], gv[2];
      unsigned pl[3];
      const rsrc_t rw = mk(w_r, 2048), rg = mk(gate_w, 2048);
      const rsrc_t rp0 = mk(Lre, 2048), rp1 = mk(Lim, 2048), rp2 = mk(logdt, 2048);
      const unsigned zero = 0, l16 = lane * 16, xo = row0 * 2048 + lane * 16, po = (j * 64 + lane) * 2;
      asm volatile("s_nop 4\n\tbuffer_load_dword %0, %3, %5, 0 offen sc1\n\tbuffer_load_dwordx4 %1, %4, %6, 0 offen\n\tbuffer_load_dwordx4 %2, %4, %6, 0 offen offset:1024"
                   : "=&v"(ep), "=&v"(wv[0]), "=&v"(wv[1]) : "v"(zero), "v"(l16), "s"(rws), "s"(rw) : "memory");
      ld8(xv, rx, xo, 0, 1024);
      asm volatile("s_nop 4\n\tbuffer_load_dwordx4 %3, %9, %10, 0 offen\n\tbuffer_load_dwordx4 %4, %9, %10, 0 offen offset:1024\n\t"
                   "buffer_load_ushort %0, %5, %6, 0 offen\n\tbuffer_load_ushort %1, %5, %7, 0 offen\n\tbuffer_load_ushort %2, %5, %8, 0 offen"
                   : "=&v"(pl[0]), "=&v"(pl[1]), "=&v"(pl[2]), "=&v"(gv[0]), "=&v"(gv[1]) : "v"(po), "s"(rp0), "s"(rp1), "s"(rp2), "v"(l16), "s"(rg) : "memory");
      asm volatile("s_nop 4\n\tbuffer_atomic_swap %0, %1, %2, 0 offen sc1" ::"v"(zero), "v"(tid == 384 && bid < 96 ? W_HF + bid * 128 : 0xf0000000u), "s"(rws) : "memory");
      asm volatile("s_waitcnt vmcnt(0)" : "+v"(ep), "+v"(xv[0]), "+v"(xv[1]), "+v"(xv[2]), "+v"(xv[3]), "+v"(xv[4]), "+v"(xv[5]), "+v"(xv[6]),
                   "+v"(xv[7]), "+v"(wv[0]), "+v"(wv[1]), "+v"(gv[0]), "+v"(gv[1]), "+v"(pl[0]), "+v"(pl[1]), "+v"(pl[2])::"memory");
      tagA = ep + 1u; tagB = ep + 0x40000001u;
      float sr[4];
#pragma unroll
      for (int r = 0; r < 4; ++r) {
        sr[r] = 0.f;
#pragma unroll
        for (int h = 0; h < 2; ++h)
#pragma unroll
          for (int e = 0; e < 4; ++e) { const unsigned xu = xv[2 * r + h][e], wu = wv[h][e]; sr[r] = dot2(xu, wu, sr[r]); }
      }
      {
        const unsigned rb = route_code(f2bf(sum4(sr)));
        const unsigned r01 = (unsigned)__builtin_amdgcn_readlane((int)rb, 0) | ((unsigned)__builtin_amdgcn_readlane((int)rb, 16) << 16);
        const unsigned r23 = (unsigned)__builtin_amdgcn_readlane((int)rb, 32) | ((unsigned)__builtin_amdgcn_readlane((int)rb, 48) << 16);
        st128c(rws, lane == 0 ? W_R2 + row0 * 4 : 0xf0000000u, (u32x4){r01, tagA, r23, tagB});
      }
#pragma unroll
      for (int r = 0; r < 4; ++r) {
        sr[r] = 0.f;
#pragma unroll
        for (int h = 0; h < 2; ++h)
#pragma unroll
          for (int e = 0; e < 4; ++e) {
            const unsigned xu = xv[2 * r + h][e], gu = gv[h][e];
            sr[r] = dot2(xu, gu, sr[r]);
          }
      }
      {
        const float z = rbf(sum4(sr));
        st64c(rws, fr == 0 ? W_RG + (row0 + fq) * 8 : 0xf0000000u, (u32x2){__float_as_uint(rbf(1.f / (1.f + __expf(-z)))), tagB});
      }
      STAMP(8);
      __builtin_amdgcn_s_setprio(XPRIO);
#pragma unroll
      for (int u = 0; u < 4; ++u) asm volatile("" : "=v"(w1[u][0]), "=v"(w1[u][1]), "=v"(w1[u][2]), "=v"(w1[u][3]), "=v"(w1[u][4]), "=v"(w1[u][5]), "=v"(w1[u][6]), "=v"(w1[u][7]));
#pragma unroll
      for (int u = 0; u < 2; ++u) asm volatile("" : "=v"(w2[u][0]), "=v"(w2[u][1]), "=v"(w2[u][2]), "=v"(w2[u][3]), "=v"(w2[u][4]), "=v"(w2[u][5]), "=v"(w2[u][6]), "=v"(w2[u][7]));
      wburst(0, WS0);
      if (xw == 1) {
        const float lre = bf2f(pl[0]), lim = bf2f(pl[1]);
        const float dt = rbf(__expf(bf2f(pl[2])));
        const float er = expf(dt * lre);
        float sn, cs;
        sincosf(dt * lim, &sn, &cs);
        const f32x2 l1 = {mul1(er, cs), mul1(er, sn)};
        const float nr = l1.x - 1.f, ni = l1.y, den = fma1(lre, lre, mul1(lim, lim));
        const f32x2 cf = {fma1(nr, lre, mul1(ni, lim)) / den, fnma1(nr, lim, mul1(ni, lre)) / den};
        const f32x2 l2 = cmul(l1, l1), l4 = cmul(l2, l2), l8 = cmul(l4, l4), l16_ = cmul(l8, l8), l32 = cmul(l16_, l16_), l64 = cmul(l32, l32),
                    l128 = cmul(l64, l64), l256 = cmul(l128, l128), l384 = cmul(l256, l128), l512 = cmul(l256, l256);
        auto put = [&](int e, f32x2 v) { const float re = v.x, im = v.y; asm volatile("ds_write2_b32 %0, %1, %2 offset0:0 offset1:64" ::"v"(L_TAB + e * 512 + lane * 4), "v"(re), "v"(im) : "memory"); };
        put(0, l1); put(1, cf); put(2, l8); put(3, l16_); put(4, l32); put(5, l64); put(6, l512); put(7, (f32x2){1.f, 0.f}); put(8, l128); put(9, l256); put(10, l384);
        asm volatile("ds_write_b64 %0, %1" ::"v"(M_LAND + 16 * 8), "v"((u32x2){0u, 0u}) : "memory");
      }
    }
    unsigned m = 0, m2 = 0, m3 = 0;
    auto gkey = [&](unsigned k) { return (k & 0xffff0000u) | ((k & 28u) << 6) | (k & 3u) | (unsigned)(252 - lane * 4); };
    lds_wait0(M_LAND + (grp == 0 ? TKG0 : TKG) * 8);
    if (xw == 1) {
#pragma unroll
      for (int h = 0; h < 2; ++h) {
        poll32(rws, W_RG + (c * 128 + h * 64 + lane) * 8 + 4, tagB, true);
        __builtin_amdgcn_raw_ptr_buffer_load_lds(rws, (LAS void*)(smem + L_G + h * 256), 4, W_RG + (c * 128 + h * 64 + lane) * 8, 0, 0, 16);
      }
      mark(rz, M_GT);
      STAMP(1);
    } else {
      {
        u32x4 g[8];
#pragma unroll 1
        for (int it = 0; it < SPIN_MAX; ++it) {
          FRESH();
#pragma unroll
          for (int q = 0; q < 8; ++q) g[q] = ld128c(rws, W_R2 + lane * 16 + q * 1024);
          bool ok = true;
#pragma unroll
          for (int q = 0; q < 8; ++q) ok = ok && g[q].y == tagA && g[q].w == tagB;
          if (__all(ok)) break;
          __builtin_amdgcn_s_sleep(16);
        }
        STAMP(1);
        auto lkey = [&](int i) { const unsigned w = g[i >> 2][i & 2]; return ((i & 1) ? (w & 0xffff0000u) : (w << 16)) | (unsigned)(31 - i); };
        m = lkey(0); m2 = 0; m3 = 0;
#pragma unroll
        for (int i = 1; i < 32; ++i) {
          const unsigned k = lkey(i);
          m3 = med3u(m2, m3, k); m2 = med3u(m, m2, k); m = max(m, k);
        }
      }
      m = gkey(m); m2 = gkey(m2); m3 = gkey(m3);
    }
    asm volatile("" : "+v"(m), "+v"(m2), "+v"(m3));
    wburst(1, WS1);
    if (xw == 0) {
      unsigned mine = 0;
#pragma unroll 1
      for (int s = 0; s < 8; ++s) {
        const unsigned mm = wave_maxu(m);
        mine = (lane >> 3) == s ? mm : mine;
        const bool hit = m == mm;
        m = hit ? m2 : m; m2 = hit ? m3 : m2; m3 = hit ? 0u : m3;
      }
      if (__ballot(m == 0u)) {
        unsigned lim = 0xffffffffu;
#pragma unroll 1
        for (int s = 0; s < 8; ++s) {
          unsigned mm = 0;
#pragma unroll 1
          for (int q = 0; q < 8; ++q) {
            const u32x4 w = ld128c(rws, W_R2 + lane * 16 + q * 1024);
#pragma unroll
            for (int e = 0; e < 4; ++e) {
              const unsigned k = gkey(((e & 1) ? (w[e & 2] & 0xffff0000u) : (w[e & 2] << 16)) | (unsigned)(31 - 4 * q - e));
              mm = max(mm, k < lim || s == 0 ? k : 0u);
            }
          }
          lim = wave_maxu(mm);
          mine = (lane >> 3) == s ? lim : mine;
        }
      }
      const unsigned myidx = 2047u - (mine & 0xffffu);
      if (loc < 28) {
#pragma unroll
        for (int u = 0; u < 4; ++u)
          __builtin_amdgcn_raw_ptr_buffer_load_lds(rx, (LAS void*)(smem + L_XS + u * 1024), 16, myidx * 2048 + (loc & 3) * 512 + (lane & 7) * 16, u * 128, 0, 0);
      }
      const float myrs = rbf(1.f / (1.f + __expf(-key_val(mine))));
      if ((lane & 7) == 0) asm volatile("ds_write_b64 %0, %1\n\ts_waitcnt lgkmcnt(0)" ::"v"(L_SEL + lane), "v"((u32x2){myidx, __float_as_uint(myrs)}) : "memory");
      mark(rz, M_XS);
    }
    STAMP(2);
    wburst(2, WS2);
    STAMP(7);
    __builtin_amdgcn_s_setprio(FPRIO);
    lds_wait0(M_XS);
    const u32x2 sel = lds_rd64(L_SEL + (lane & 7) * 8);
    const unsigned fa = ((fr & 7) * 8 + 4 * (fr >> 3) + fq) * 16, lp = (fr & 7) * 32 + (fr >> 3) * 16, tk = 4 * (fq & 1) + 2 * (fq >> 1);
    if (ffn) {
      f32x4 d[8], o[2];
      u32x4 xa[4];
#pragma unroll
      for (int u = 0; u < 4; ++u) xa[u] = *(const LAS u32x4*)(smem + L_XS + u * 1024 + fa);
#pragma unroll
      for (int q = 0; q < 8; ++q) d[q] = (f32x4){0.f, 0.f, 0.f, 0.f};
      waitv<0>();
      STAMP(10);
      {
        if (W2G) lds_wait0(M_LAND + W2G * 8);
        __builtin_amdgcn_s_setprio(3);
        const rsrc_t r2 = mk(w_down, D_ * F_ * 2);
        const unsigned o2 = (sg * 64 + 32 * (fr >> 3) + 8 * fq) * 2048 + (kq * 256 + xw * 128 + 8 * (fr & 7)) * 2;
#pragma unroll
        for (int u = 0; u < 2; ++u) ld8(w2[u], r2, o2, u * 128, 2048);
        __builtin_amdgcn_s_setprio(FPRIO);
      }
      if (GCG) lds_wait0(M_LAND + GCG * 8);
#pragma unroll
      for (int u = 0; u < 4; ++u) {
        asm volatile("" : "+v"(w1[u][0]), "+v"(w1[u][1]), "+v"(w1[u][2]), "+v"(w1[u][3]), "+v"(w1[u][4]), "+v"(w1[u][5]), "+v"(w1[u][6]), "+v"(w1[u][7]));
        unit_mma(w1[u], __builtin_bit_cast(bf16x8, xa[u]), d);
        asm volatile("" : "+v"(d[0]), "+v"(d[1]), "+v"(d[2]), "+v"(d[3]), "+v"(d[4]), "+v"(d[5]), "+v"(d[6]), "+v"(d[7]));
      }
      fold(d, o);
      STAMP(11);
      st2x128c(rws, W_GU + ((sg * 8 + tk) * 8 + kq * 2 + l) * 256 + lp, 2048, o);
      count1(rws, W_GF + sg * 128);
      STAMP(3);
      {
        if (GUG) lds_wait0(M_LAND + GUG * 8);
        __builtin_amdgcn_s_setprio(GPRIO);
        count_wait(rws, W_GF + sg * 128, 8);
        f32x4 a[8];
#pragma unroll
        for (int q = 0; q < 8; ++q) a[q] = __builtin_bit_cast(f32x4, ld128c(rws, W_GU + (sg * 8 + 4 * l + fq) * 2048 + fr * 16 + q * 256));
        float a2[4];
#pragma unroll
        for (int e = 0; e < 4; ++e) {
          const float g = rbf(add1(add1(a[0][e], a[2][e]), add1(a[4][e], a[6][e]))), u = rbf(add1(add1(a[1][e], a[3][e]), add1(a[5][e], a[7][e])));
          a2[e] = mul1(rbf(g / add1(1.f, __expf(-g))), u);
        }
        asm volatile("ds_write_b64 %0, %1\n\ts_nop 3" ::"v"(L_ACT + (4 * l + fq) * 128 + fr * 8), "v"((u32x2){pk2(a2[0], a2[1]), pk2(a2[2], a2[3])}) : "memory");
        lds_wr32(FL_TK + l * 4, 1u);
        lds_wait_ge(FL_TK + (l ^ 1) * 4, 1u);
      }
      const u32x4 af = lds_rd128(L_ACT + fa);
      STAMP(4);
      waitv<0>();
#pragma unroll
      for (int u = 0; u < 2; ++u)
        asm volatile("" : "+v"(w2[u][0]), "+v"(w2[u][1]), "+v"(w2[u][2]), "+v"(w2[u][3]), "+v"(w2[u][4]), "+v"(w2[u][5]), "+v"(w2[u][6]), "+v"(w2[u][7]));
#pragma unroll
      for (int u = 0; u < 2; ++u) {
#pragma unroll
        for (int q = 0; q < 8; ++q) d[q] = (f32x4){0.f, 0.f, 0.f, 0.f};
        unit_mma(w2[u], __builtin_bit_cast(bf16x8, af), d);
        fold(d, o);
        st2x128c(rws, W_YP + ((tk * 16 + kq * 4 + l * 2 + u) * 56 + sg) * 256 + lp, 16 * 56 * 256, o);
      }
      count1(rws, W_YF + (kq * 2 + l) * 128);
      STAMP(5);
    }
    u64 rows = __ballot(lane < 8 && (sel.x >> 7) == (unsigned)c);
    if (l) rows &= rows - 1;
    if (!NOFFN && rows) {
      lds_wait0(M_CONS + (16 + YG) * 16);
      __builtin_amdgcn_s_setprio(YPRIO);
      count_wait(rws, W_YF + (j >> 1) * 128, 56);
      STAMP(9);
#pragma unroll 1
      for (; rows; rows &= rows - 1, rows &= rows - 1) {
        const int s = __builtin_ctzll(rows);
        const int t = __builtin_amdgcn_readlane(sel.x, s);
        const float rs = __uint_as_float(__builtin_amdgcn_readlane(sel.y, s));
        f32x4 a[14];
#pragma unroll
        for (int k = 0; k < 14; ++k) a[k] = __builtin_bit_cast(f32x4, ld128c(rws, W_YP + (s * 16 + j) * (56 * 256) + lane * 16 + k * 1024));
        const float xf = bf2f(__builtin_amdgcn_raw_buffer_load_b16(rx, t * 2048 + j * 128 + fr * 8 + fq * 2, 0, 0));
        const float omg = rbf(1.f - __uint_as_float(lds_rd32(L_G + (t & 127) * 4)));
        float l0 = a[0][0], l1 = a[0][1], h0 = a[0][2], h1 = a[0][3];
#pragma unroll
        for (int k = 1; k < 14; ++k) { l0 = add1(l0, a[k][0]); l1 = add1(l1, a[k][1]); h0 = add1(h0, a[k][2]); h1 = add1(h1, a[k][3]); }
        asm volatile("s_nop 1" : "+v"(l0), "+v"(l1), "+v"(h0), "+v"(h1));
        auto p = __builtin_amdgcn_permlane16_swap(__float_as_uint(l0), __float_as_uint(l1), false, false);
        auto q = __builtin_amdgcn_permlane16_swap(__float_as_uint(h0), __float_as_uint(h1), false, false);
        const float u = __uint_as_float(p[0]) + __uint_as_float(p[1]), w = __uint_as_float(q[0]) + __uint_as_float(q[1]);
        auto r = __builtin_amdgcn_permlane32_swap(__float_as_uint(u), __float_as_uint(w), false, false);
        const float y = __uint_as_float(r[0]) + __uint_as_float(r[1]);
        lds_wr32(L_BROW + s * 256 + fr * 16 + fq * 4, __float_as_uint(mul1(omg, rbf(xf + rbf(mul1(rbf(y), rs))) - xf)));
      }
    }
    lds_wr32(FL_XD + l * 4, 1u);
    waitv<0>();
    STAMP(6);
    STAMPS();
    __builtin_amdgcn_endpgm();
  }

  __builtin_amdgcn_s_setprio(MPRIO);
  ARGREQ("\n\ts_waitcnt lgkmcnt(0)");
  ARGS();
  if (wid == 3) {
    const int sg = min(xcd * 7 + (loc >> 2), 55), kq = loc & 3;
    const unsigned wo = kq * (256 * F_) + sg * 64;
    const void* q[14] = {Bim + j * 65536, w_gate + wo, w_gate + wo + 255 * F_, w_up + wo, w_up + wo + 255 * F_, w_down + sg * 65536, Cre + j * 65536, Cim + j * 65536,
                         x + (1 << 20), x + (2 << 20) - 2, ws + W_H - 128, ws + W_H + (c * 64 + 4 * j) * 8192, out + c * 131072 + j * 64, Dskip};
    unsigned lo = (unsigned)(size_t)x, hi = (unsigned)((size_t)x >> 32);
#pragma unroll
    for (int i = 0; i < 14; ++i)
      asm("v_writelane_b32 %0, %2, %4\n\tv_writelane_b32 %1, %3, %4" : "+v"(lo), "+v"(hi) : "s"((unsigned)(size_t)q[i]), "s"((unsigned)((size_t)q[i] >> 32)), "n"(i));
    asm volatile("s_nop 1\n\ts_mov_b64 exec, 0x7fff\n\tglobal_load_dword v229, %0, off\n\ts_mov_b64 exec, -1" ::"v"((u32x2){lo, hi}) : "memory", "v229");
  }
  if (XPB) bar();
  if (XP >> (wid - (wid == 3)) & 1) {
    const unsigned sk0 = (((xcd >> 1) + 2 * (xcd & 1)) & 3) * 512;
    const rsrc_t rp = mk(wid < 2 ? x : wid == 2 ? Bre : Bim, wid < 2 ? 1u << 22 : 1u << 21);
    const unsigned o = wid < 2 ? (c * 128 + (j & 4) * 16 + lane) * 2048 + ((sk0 + 512 - wid * 512 + (j & 3) * 128) & 2047) : (j * 64 + lane) * 2048 + sk0 + (c & 3) * 128;
    if (XPD) __builtin_amdgcn_s_sleep(XPD);
    __builtin_amdgcn_raw_ptr_buffer_load_lds(rp, (LAS void*)(smem + L_JUNK), 4, o, 0, 0, 0);
  }
  if (!XPB) bar();
  unsigned tagA, tagB;
  int bud = SPIN_MAX * 4;
  const int tl0 = 32 * fq + 2 * wid;
  u32x2 xe[2][4], dv;
  {
    const rsrc_t rd = mk(Dskip, 2048);
    const unsigned dofs = (j * 64 + 4 * fr) * 2, xo = (32 * wid + 4 * fq) * 128 + ((fr >> 1) ^ (4 * (fq & 1))) * 16 + (fr & 1) * 8;
    const unsigned ks = (ROT1 ? -c : j - 4 * ((xcd >> 1) + 2 * (xcd & 1))) & 15;
    const int x0 = (fq ^ (fr & 7)) * 16, x1 = ((4 + fq) ^ (fr & 7)) * 16;
    const unsigned pa = 16384 + (wid * 16 + fr) * 128, pb = 131072 + fr * 128;
    unsigned pa0 = pa + x0, pa1 = pa + x1, pb0 = pb + x0, pb1 = pb + x1;
    const unsigned tq = L_TAB + (wid * 16 + fq * 4) * 4, tcl = L_TAB + 6 * 512 + (wid * 16 + fr) * 4, tcw = tcl + 512 + fq * 512;
    const unsigned l16 = lane * 16, pubo = (fq * 4 + (fr >> 2)) * 16, plo = fr * 16 + (3 - fq) * 1024;
    const unsigned sag = W_AGG + (j * 16 + c) * 1024 + wid * 256, sht = W_HT + (j * 4 + wid) * 128;
    unsigned sh = W_H + (c * 64 + 4 * j + wid) * 8192, fl = M_LAND, scn = M_CONS - 16 + wid * 4;
    unsigned sl[4];
    u64 em[4];
#pragma unroll
    for (int q = 0; q < 4; ++q) {
      sl[q] = sag - (4 + 4 * q) * 1024;
      const int n = min(max(c - 4 * q, 0), 4);
      em[q] = n == 4 ? ~0ull : (1ull << (16 * n)) - 1ull;
    }
    unsigned t_sk, t_s0, t_s1, t_s2, t_sx;
    asm volatile(ASM_MA : [pa0] "+&v"(pa0), [pa1] "+&v"(pa1), [pb0] "+&v"(pb0), [pb1] "+&v"(pb1), [fl] "+&v"(fl), [sk] "=&s"(t_sk), [s0] "=&s"(t_s0), [s1] "=&s"(t_s1), [s2] "=&s"(t_s2), [sx] "=&s"(t_sx), [bud] "+&s"(bud), [scn] "+&s"(scn), [sh] "+&s"(sh), [tgA] "=&s"(tagA), [tgB] "=&s"(tagB), [xe00] "=&v"(xe[0][0]), [xe01] "=&v"(xe[0][1]), [xe02] "=&v"(xe[0][2]), [xe03] "=&v"(xe[0][3]), [xe10] "=&v"(xe[1][0]), [xe11] "=&v"(xe[1][1]), [xe12] "=&v"(xe[1][2]), [xe13] "=&v"(xe[1][3]), [dv] "=&v"(dv) : [tq] "v"(tq), [tcl] "v"(tcl), [tcw] "v"(tcw), [pubo] "v"(pubo), [plo] "v"(plo), [l16] "v"(l16), [xo] "v"(xo), [dofs] "v"(dofs), [wrap] "s"(-98304), [wrap2] "s"(-131072), [srd] "s"(rws), [ks] "s"(ks), [srdd] "s"(rd), [srz] "s"(rz), [sag] "s"(sag), [mcar] "s"(M_CAR), [eh] "s"((1ull << c) - 1ull), [sht] "s"(sht), [shc] "s"(sht + c * 4), [sl0] "s"(sl[0]), [em0] "s"(em[0]), [sl1] "s"(sl[1]), [em1] "s"(em[1]), [sl2] "s"(sl[2]), [em2] "s"(em[2]), [sl3] "s"(sl[3]), [em3] "s"(em[3]) : "memory", "scc", "vcc", "m0", "v64", "v65", "v66", "v67", "v68", "v69", "v70", "v71", "v72", "v73", "v74", "v75", "v76", "v77", "v78", "v79", "v80", "v81", "v82", "v83", "v84", "v85", "v86", "v87", "v88", "v89", "v90", "v91", "v92", "v93", "v94", "v95", "v96", "v97", "v98", "v99", "v100", "v101", "v102", "v103", "v104", "v105", "v106", "v107", "v108", "v109", "v110", "v111", "v112", "v113", "v114", "v115", "v116", "v117", "v118", "v119", "v120", "v121", "v122", "v123", "v124", "v125", "v126", "v127", "v128", "v129", "v130", "v131", "v132", "v133", "v134", "v135", "v136", "v137", "v138", "v139", "v140", "v141", "v142", "v143", "v144", "v145", "v146", "v147", "v148", "v149", "v150", "v151", "v152", "v153", "v154", "v155", "v156", "v157", "v158", "v159", "v160", "v161", "v162", "v163", "v164", "v165", "v166", "v167", "v168", "v169", "v170", "v171", "v172", "v173", "v174", "v175", "v176", "v177", "v178", "v179", "v180", "v181", "v182", "v183", "v184", "v185", "v186", "v187", "v188", "v189", "v190", "v191", "v192", "v193", "v194", "v195", "v196", "v197", "v198", "v199", "v200", "v201", "v202", "v203", "v204", "v205", "v206", "v207", "v208", "v209", "v210", "v211", "v212", "v213", "v214", "v215", "v216", "v217", "v218", "v219", "v220", "v221", "v222", "v223", "v224", "v225", "v226", "v227", "v228", "v229", "v230", "v231", "v232", "v233", "v234", "v235", "v236", "v237", "v238", "v239", "v240", "v241", "v242", "v243", "v244", "v245", "v246", "v247", "v248", "v249", "v250", "v251", "v252", "v253", "v254", "v255");
  }
  f32x2 g2[4], base[4][4];
  unsigned mine, dp[8];
  {
    unsigned selu, m0_, m1_;
#pragma unroll 1
    for (int it = 0; it < SPIN_MAX * 4; ++it) {
      asm volatile("ds_read_b32 %5, %7\n\tds_read_b32 %6, %7 offset:%10\n\tds_read_b32 %4, %8\n\tds_read_b64 %0, %9\n\tds_read_b64 %1, %9 offset:32\n\t"
                   "ds_read_b64 %2, %9 offset:64\n\tds_read_b64 %3, %9 offset:96\n\ts_waitcnt lgkmcnt(0)"
                   : "=&v"(g2[0]), "=&v"(g2[1]), "=&v"(g2[2]), "=&v"(g2[3]), "=&v"(selu), "=&v"(m0_), "=&v"(m1_)
                   : "v"(M_GT), "v"(L_SEL + (lane & 7) * 8), "v"(L_G + tl0 * 4), "n"(M_XS - M_GT) : "memory");
      if (__builtin_amdgcn_readfirstlane(m0_ | m1_) == 0u) break;
      __builtin_amdgcn_s_sleep(1);
    }
    f32x2 dd[4];
#pragma unroll
    for (int n = 0; n < 4; ++n) { const unsigned w = dv[n >> 1]; const float f = (n & 1) ? bfhi(w) : bf2f(w & 0xffffu); dd[n] = (f32x2){f, f}; }
#pragma unroll
    for (int r = 0; r < 4; ++r) {
      const unsigned ob = pk2(1.f - g2[r].x, 1.f - g2[r].y);
      const f32x2 omg = {bf2f(ob & 0xffffu), bfhi(ob)};
#pragma unroll
      for (int n = 0; n < 4; ++n) {
        const unsigned w0 = xe[0][r][n >> 1], w1 = xe[1][r][n >> 1];
        const f32x2 xf = {(n & 1) ? bfhi(w0) : bf2f(w0 & 0xffffu), (n & 1) ? bfhi(w1) : bf2f(w1 & 0xffffu)};
        base[r][n] = pkmul(xf, pkfma(g2[r], dd[n], omg));
      }
    }
#pragma unroll
    for (int r = 0; r < 4; ++r) asm volatile("" : "+v"(base[r][0]), "+v"(base[r][1]), "+v"(base[r][2]), "+v"(base[r][3]));
    asm volatile("s_nop 4\n\ts_waitcnt vmcnt(0)\n\ts_mov_b64 exec, 1\n\tbuffer_atomic_add %0, off, %1, %2 sc1\n\tds_write_b32 %3, %4\n\ts_mov_b64 exec, -1"
                 ::"v"(1u), "s"(rws), "s"(W_HF + (c * 2 + (j >> 3)) * 128), "v"(M_OWN + wid * 4), "v"(0u) : "memory");
    STAMP(9);
    mine = (unsigned)__ballot(lane < 8 && (selu >> 7) == (unsigned)c && ((selu >> 1) & 3u) == (unsigned)wid);
#pragma unroll
    for (int q = 0; q < 8; ++q) dp[q] = L_ONE - 16;
#pragma unroll 1
    for (unsigned m = mine; m; m &= m - 1) {
      const int s = __builtin_ctz(m), t = __builtin_amdgcn_readlane((int)selu, s) & 127;
#pragma unroll
      for (int q = 0; q < 8; ++q) dp[q] = t == tl0 + 8 * (q >> 1) + (q & 1) ? L_BROW + s * 256 + fr * 16 : dp[q];
    }
  }
  f32x4 ac2[2][4];
  float bs[8][4];
#pragma unroll
  for (int q = 0; q < 8; ++q)
#pragma unroll
    for (int n = 0; n < 4; ++n) bs[q][n] = base[q >> 1][n][q & 1];
  {
#pragma unroll
    for (int m = 0; m < 2; ++m)
#pragma unroll
      for (int n = 0; n < 4; ++n) ac2[m][n] = (f32x4){0.f, 0.f, 0.f, 0.f};
    unsigned pc[4], sb = (j & (0 ? 7 : 8)) * 32768, fl2 = M_LAND + 17 * 8, scn2 = M_CONS + 256 + wid * 4;
    unsigned vo = W_H + c * 524288 + 0 * (j & 8) * 32768 + wid * 2048 + lane * 16;
    const unsigned hr = M_HR;
#pragma unroll
    for (int q = 0; q < 4; ++q) pc[q] = fr * 128 + (((2 * q + (fq >> 1)) ^ ((fr >> 1) & 7)) * 16) + 8192 * (fq & 1);
    const unsigned sf = W_HF + (c * 2 + (j >> 3)) * 128, sf1 = sf ^ 128;
    unsigned t_sk, t_sa, t_s0, t_s1;
    const unsigned hb_ = W_H + c * 524288, f9 = M_LAND + 17 * 8 + min(lane, 17) * 4;
    vo = (j & 8) * 32768 + wid * 2048 + lane * 16; fl2 = M_LAND + 26 * 8;
    asm volatile(ASM_MB : [c00] "+&v"(ac2[0][0]), [c01] "+&v"(ac2[0][1]), [c02] "+&v"(ac2[0][2]), [c03] "+&v"(ac2[0][3]), [c10] "+&v"(ac2[1][0]), [c11] "+&v"(ac2[1][1]), [c12] "+&v"(ac2[1][2]), [c13] "+&v"(ac2[1][3]), [pc0] "+&v"(pc[0]), [pc1] "+&v"(pc[1]), [pc2] "+&v"(pc[2]), [pc3] "+&v"(pc[3]), [b00] "+&v"(bs[0][0]), [b01] "+&v"(bs[0][1]), [b02] "+&v"(bs[0][2]), [b03] "+&v"(bs[0][3]), [b10] "+&v"(bs[1][0]), [b11] "+&v"(bs[1][1]), [b12] "+&v"(bs[1][2]), [b13] "+&v"(bs[1][3]), [b20] "+&v"(bs[2][0]), [b21] "+&v"(bs[2][1]), [b22] "+&v"(bs[2][2]), [b23] "+&v"(bs[2][3]), [b30] "+&v"(bs[3][0]), [b31] "+&v"(bs[3][1]), [b32] "+&v"(bs[3][2]), [b33] "+&v"(bs[3][3]), [b40] "+&v"(bs[4][0]), [b41] "+&v"(bs[4][1]), [b42] "+&v"(bs[4][2]), [b43] "+&v"(bs[4][3]), [b50] "+&v"(bs[5][0]), [b51] "+&v"(bs[5][1]), [b52] "+&v"(bs[5][2]), [b53] "+&v"(bs[5][3]), [b60] "+&v"(bs[6][0]), [b61] "+&v"(bs[6][1]), [b62] "+&v"(bs[6][2]), [b63] "+&v"(bs[6][3]), [b70] "+&v"(bs[7][0]), [b71] "+&v"(bs[7][1]), [b72] "+&v"(bs[7][2]), [b73] "+&v"(bs[7][3]), [vo] "+&v"(vo), [fl] "+&v"(fl2), [sg] "=&s"(t_sk), [s0] "=&s"(t_s0), [s1] "=&s"(t_s1), [bud] "+&s"(bud) : [d0] "v"(dp[0]), [d1] "v"(dp[1]), [d2] "v"(dp[2]), [d3] "v"(dp[3]), [d4] "v"(dp[4]), [d5] "v"(dp[5]), [d6] "v"(dp[6]), [d7] "v"(dp[7]), [xd] "v"(FL_XD), [mine] "s"(mine), [srd] "s"(rws), [srz] "s"(rz), [hr] "v"(hr), [f9] "v"(f9), [w] "s"(wid), [scn] "s"(scn2), [sf] "s"(sf), [sf1] "s"(sf1), [k2] "s"(0x4000), [h0] "s"(hb_ + 0), [h1] "s"(hb_ + 8192), [h2] "s"(hb_ + 16384), [h3] "s"(hb_ + 24576), [h4] "s"(hb_ + 32768), [h5] "s"(hb_ + 40960), [h6] "s"(hb_ + 49152), [h7] "s"(hb_ + 57344) : "memory", "scc", "vcc", "m0", "v64", "v65", "v66", "v67", "v68", "v69", "v70", "v71", "v72", "v73", "v74", "v75", "v76", "v77", "v78", "v79", "v80", "v81", "v82", "v83", "v84", "v85", "v86", "v87", "v88", "v89", "v90", "v91", "v92", "v93", "v94", "v95", "v96", "v97", "v98", "v99", "v100", "v101", "v102", "v103", "v104", "v105", "v106", "v107", "v108", "v109", "v110", "v111", "v112", "v113", "v114", "v115", "v116", "v117", "v118", "v119", "v120", "v121", "v122", "v123", "v124", "v125", "v126", "v127", "v128", "v129", "v130", "v131", "v132", "v133", "v134", "v135", "v136", "v137", "v138", "v139", "v140", "v141", "v142", "v143", "v144", "v145", "v146", "v147", "v148", "v149", "v150", "v151", "v152", "v153", "v154", "v155", "v156", "v157", "v158", "v159", "v160", "v161", "v162", "v163", "v164", "v165", "v166", "v167", "v168", "v169", "v170", "v171", "v172", "v173", "v174", "v175", "v176", "v177", "v178", "v179", "v180", "v181", "v182", "v183", "v184", "v185", "v186", "v187", "v188", "v189", "v190", "v191", "v192", "v193", "v194", "v195", "v196");
  }
  STAMP(6);
  asm volatile("" : "+v"(g2[0]), "+v"(g2[1]), "+v"(g2[2]), "+v"(g2[3]));
  int ln2 = lane;
  asm volatile("" : "+v"(ln2));
  const int fr2 = ln2 & 15, tl2 = 32 * (ln2 >> 4) + 2 * wid;
  f32x4 ov[2][4];
#pragma unroll
  for (int e = 0; e < 2; ++e)
#pragma unroll
    for (int r = 0; r < 4; ++r) {
      const float gg = 2.f * g2[r][e];
#pragma unroll
      for (int n = 0; n < 4; ++n) ov[e][r][n] = fma1(gg, ac2[e][n][r], bs[2 * r + e][n]);
    }
  {
    const rsrc_t ro = mk(out, 2048 * 1024 * 4);
    const unsigned oo = (c * 128 + tl2) * 4096 + (j * 64 + 4 * fr2) * 4, oo2 = oo + 4096;
    asm volatile("s_nop 4\n\t"
                 "buffer_store_dwordx4 %0, %8, %10, 0 offen" OST "\n\tbuffer_store_dwordx4 %4, %9, %10, 0 offen" OST "\n\t"
                 "buffer_store_dwordx4 %1, %8, %10, %11 offen" OST "\n\tbuffer_store_dwordx4 %5, %9, %10, %11 offen" OST "\n\t"
                 "buffer_store_dwordx4 %2, %8, %10, %12 offen" OST "\n\tbuffer_store_dwordx4 %6, %9, %10, %12 offen" OST "\n\t"
                 "buffer_store_dwordx4 %3, %8, %10, %13 offen" OST "\n\tbuffer_store_dwordx4 %7, %9, %10, %13 offen" OST "\n\t"
                 "s_nop 7\n\ts_nop 7"
                 ::"v"(ov[0][0]), "v"(ov[0][1]), "v"(ov[0][2]), "v"(ov[0][3]), "v"(ov[1][0]), "v"(ov[1][1]), "v"(ov[1][2]), "v"(ov[1][3]), "v"(oo), "v"(oo2), "s"(ro),
                 "s"(32768), "s"(65536), "s"(98304) : "memory");
  }
  if (c == 15 && j == 0 && wid == 0 && lane == 0) st64c(rws, W_EPOCH, (u32x2){tagA, tagB});
  STAMP(7);
  STAMPS();
#endif
}

extern "C" void kernel_launch(void* const* d_in, const int* in_sizes, int n_in, void* d_out, int out_size, void* d_ws, size_t ws_size,
                              hipStream_t stream) {
  (void)hipFuncSetAttribute((const void*)fused, hipFuncAttributeMaxDynamicSharedMemorySize, LDS_BYTES);
  fused<<<256, 512, LDS_BYTES, stream>>>((const u16*)d_in[0], (const u16*)d_in[1], (const u16*)d_in[2], (const u16*)d_in[3], (const u16*)d_in[4],
                                         (const u16*)d_in[5], (const u16*)d_in[6], (const u16*)d_in[7], (const u16*)d_in[8], (const u16*)d_in[10],
                                         (const u16*)d_in[11], (const u16*)d_in[12], (const u16*)d_in[13], (const u16*)d_in[14], (char*)d_ws + WS_BASE(ws_size),
                                         (float*)d_out);
}
```

```cpp
#include <hip/hip_runtime.h>
#include <hip/hip_bf16.h>
#include <stdint.h>

typedef __attribute__((ext_vector_type(8))) short bf16x8;
typedef __attribute__((ext_vector_type(4))) float f32x4;
typedef __attribute__((ext_vector_type(2))) float f32x2;
typedef __attribute__((ext_vector_type(4))) unsigned u32x4;
typedef __attribute__((ext_vector_type(2))) unsigned u32x2;
typedef unsigned short u16;
typedef unsigned long long u64;
#define LAS __attribute__((address_space(3)))
#define DEV __device__ __forceinline__

#define ASM_MA \
  "s_nop 4\n\t" \
  "buffer_load_dword v228, off, %[srd], 0 sc1\n\t" \
  "buffer_load_dwordx2 %[dv], %[dofs], %[srdd], 0 offen\n\t" \
  "s_mov_b32 %[sk], 0\n\t" \
  "s_mov_b32 %[sx], 0x20000\n\t" \
  "Lsp0_%=:\n\t" \
  "ds_read_b128 v[128:131], %[fl] offset:1056\n\t" \
  "s_sub_i32 %[bud], %[bud], 1\n\t" \
  "s_cmp_lt_i32 %[bud], 0\n\t" \
  "s_cbranch_scc1 Lb0_%=\n\t" \
  "s_waitcnt lgkmcnt(0)\n\t" \
  "v_or3_b32 v128, v128, v129, v130\n\t" \
  "v_or_b32 v128, v128, v131\n\t" \
  "s_nop 0\n\t" \
  "v_readfirstlane_b32 %[s0], v128\n\t" \
  "s_cmp_eq_u32 %[s0], 0\n\t" \
  "s_cbranch_scc0 Lsp0_%=\n\t" \
  "Lb0_%=:\n\t" \
  "s_cmp_eq_u32 %[sk], %[ks]\n\t" \
  "s_cbranch_scc1 Lx0_%=\n\t" \
  "Lxb0_%=:\n\t" \
  "ds_read_b128 v[128:131], %[pa0] offset:0\n\t" \
  "ds_read_b128 v[132:135], %[pa0] offset:8192\n\t" \
  "ds_read_b128 v[176:179], %[pa1] offset:0\n\t" \
  "ds_read_b128 v[180:183], %[pa1] offset:8192\n\t" \
  "ds_read_b128 v[144:147], %[pb0] offset:0\n\t" \
  "ds_read_b128 v[148:151], %[pb0] offset:2048\n\t" \
  "ds_read_b128 v[152:155], %[pb0] offset:4096\n\t" \
  "ds_read_b128 v[156:159], %[pb0] offset:6144\n\t" \
  "ds_read_b128 v[160:163], %[pb0] offset:8192\n\t" \
  "ds_read_b128 v[164:167], %[pb0] offset:10240\n\t" \
  "ds_read_b128 v[168:171], %[pb0] offset:12288\n\t" \
  "ds_read_b128 v[172:175], %[pb0] offset:14336\n\t" \
  "s_waitcnt lgkmcnt(4)\n\t" \
  "v_mfma_f32_16x16x32_bf16 v[64:67], v[128:131], v[144:147], 0\n\t" \
  "v_mfma_f32_16x16x32_bf16 v[96:99], v[132:135], v[144:147], 0\n\t" \
  "s_mov_b32 m0, %[scn]\n\t" \
  "s_mov_b64 exec, 1\n\t" \
  "buffer_load_dword %[l16], %[srz], 0 offen lds\n\t" \
  "s_mov_b64 exec, -1\n\t" \
  "s_add_u32 %[scn], %[scn], 16\n\t" \
  "ds_read_b128 v[144:147], %[pb1] offset:0\n\t" \
  "v_mov_b64 v[136:137], v[176:177]\n\t" \
  "v_mfma_f32_16x16x32_bf16 v[68:71], v[128:131], v[148:151], 0\n\t" \
  "v_mfma_f32_16x16x32_bf16 v[100:103], v[132:135], v[148:151], 0\n\t" \
  "ds_read_b128 v[148:151], %[pb1] offset:2048\n\t" \
  "v_mov_b64 v[138:139], v[178:179]\n\t" \
  "v_mfma_f32_16x16x32_bf16 v[72:75], v[128:131], v[152:155], 0\n\t" \
  "v_mfma_f32_16x16x32_bf16 v[104:107], v[132:135], v[152:155], 0\n\t" \
  "ds_read_b128 v[152:155], %[pb1] offset:4096\n\t" \
  "v_add_u32 %[fl], 8, %[fl]\n\t" \
  "v_mfma_f32_16x16x32_bf16 v[76:79], v[128:131], v[156:159], 0\n\t" \
  "v_mfma_f32_16x16x32_bf16 v[108:111], v[132:135], v[156:159], 0\n\t" \
  "ds_read_b128 v[156:159], %[pb1] offset:6144\n\t" \
  "ds_read_b64 v[224:225], %[fl]\n\t" \
  "s_waitcnt lgkmcnt(5)\n\t" \
  "v_mfma_f32_16x16x32_bf16 v[80:83], v[128:131], v[160:163], 0\n\t" \
  "v_mfma_f32_16x16x32_bf16 v[112:115], v[132:135], v[160:163], 0\n\t" \
  "ds_read_b128 v[160:163], %[pb1] offset:8192\n\t" \
  "v_mov_b64 v[140:141], v[180:181]\n\t" \
  "v_mfma_f32_16x16x32_bf16 v[84:87], v[128:131], v[164:167], 0\n\t" \
  "v_mfma_f32_16x16x32_bf16 v[116:119], v[132:135], v[164:167], 0\n\t" \
  "ds_read_b128 v[164:167], %[pb1] offset:10240\n\t" \
  "v_mov_b64 v[142:143], v[182:183]\n\t" \
  "v_mfma_f32_16x16x32_bf16 v[88:91], v[128:131], v[168:171], 0\n\t" \
  "v_mfma_f32_16x16x32_bf16 v[120:123], v[132:135], v[168:171], 0\n\t" \
  "ds_read_b128 v[168:171], %[pb1] offset:12288\n\t" \
  "v_mfma_f32_16x16x32_bf16 v[92:95], v[128:131], v[172:175], 0\n\t" \
  "v_mfma_f32_16x16x32_bf16 v[124:127], v[132:135], v[172:175], 0\n\t" \
  "ds_read_b128 v[172:175], %[pb1] offset:14336\n\t" \
  "Lloop_%=:\n\t" \
  "s_add_u32 %[sk], %[sk], 1\n\t" \
  "s_and_b32 %[s1], %[sk], 3\n\t" \
  "s_cselect_b32 %[s1], 0x8000, %[wrap]\n\t" \
  "s_add_u32 %[sx], %[sx], 0x8000\n\t" \
  "s_cmp_eq_u32 %[sx], 0x28000\n\t" \
  "s_cselect_b32 %[s2], %[wrap2], 0x8000\n\t" \
  "s_cselect_b32 %[sx], 0, %[sx]\n\t" \
  "s_waitcnt lgkmcnt(4)\n\t" \
  "v_or_b32 v224, v224, v225\n\t" \
  "v_mfma_f32_16x16x32_bf16 v[64:67], v[136:139], v[144:147], v[64:67]\n\t" \
  "v_readfirstlane_b32 %[s0], v224\n\t" \
  "v_mfma_f32_16x16x32_bf16 v[96:99], v[140:143], v[144:147], v[96:99]\n\t" \
  "v_add_u32 %[pa0], %[s1], %[pa0]\n\t" \
  "v_add_u32 %[pa1], %[s1], %[pa1]\n\t" \
  "v_add_u32 %[pb0], %[s2], %[pb0]\n\t" \
  "s_cmp_eq_u32 %[s0], 0\n\t" \
  "s_cbranch_scc0 Lsp1_%=\n\t" \
  "Lb1_%=:\n\t" \
  "ds_read_b128 v[128:131], %[pa0] offset:0\n\t" \
  "ds_read_b128 v[132:135], %[pa0] offset:8192\n\t" \
  "ds_read_b128 v[176:179], %[pa1] offset:0\n\t" \
  "ds_read_b128 v[180:183], %[pa1] offset:8192\n\t" \
  "ds_read_b128 v[144:147], %[pb0] offset:0\n\t" \
  "v_mfma_f32_16x16x32_bf16 v[68:71], v[136:139], v[148:151], v[68:71]\n\t" \
  "v_mfma_f32_16x16x32_bf16 v[100:103], v[140:143], v[148:151], v[100:103]\n\t" \
  "ds_read_b128 v[148:151], %[pb0] offset:2048\n\t" \
  "v_mfma_f32_16x16x32_bf16 v[72:75], v[136:139], v[152:155], v[72:75]\n\t" \
  "v_mfma_f32_16x16x32_bf16 v[104:107], v[140:143], v[152:155], v[104:107]\n\t" \
  "ds_read_b128 v[152:155], %[pb0] offset:4096\n\t" \
  "v_mfma_f32_16x16x32_bf16 v[76:79], v[136:139], v[156:159], v[76:79]\n\t" \
  "v_mfma_f32_16x16x32_bf16 v[108:111], v[140:143], v[156:159], v[108:111]\n\t" \
  "ds_read_b128 v[156:159], %[pb0] offset:6144\n\t" \
  "s_waitcnt lgkmcnt(4)\n\t" \
  "v_mfma_f32_16x16x32_bf16 v[80:83], v[136:139], v[160:163], v[80:83]\n\t" \
  "s_mov_b32 m0, %[scn]\n\t" \
  "s_mov_b64 exec, 1\n\t" \
  "buffer_load_dword %[l16], %[srz], 0 offen lds\n\t" \
  "s_mov_b64 exec, -1\n\t" \
  "v_mfma_f32_16x16x32_bf16 v[112:115], v[140:143], v[160:163], v[112:115]\n\t" \
  "ds_read_b128 v[160:163], %[pb0] offset:8192\n\t" \
  "v_mfma_f32_16x16x32_bf16 v[84:87], v[136:139], v[164:167], v[84:87]\n\t" \
  "v_mfma_f32_16x16x32_bf16 v[116:119], v[140:143], v[164:167], v[116:119]\n\t" \
  "ds_read_b128 v[164:167], %[pb0] offset:10240\n\t" \
  "v_add_u32 %[pb1], %[s2], %[pb1]\n\t" \
  "s_add_u32 %[scn], %[scn], 16\n\t" \
  "v_mfma_f32_16x16x32_bf16 v[88:91], v[136:139], v[168:171], v[88:91]\n\t" \
  "v_mfma_f32_16x16x32_bf16 v[120:123], v[140:143], v[168:171], v[120:123]\n\t" \
  "ds_read_b128 v[168:171], %[pb0] offset:12288\n\t" \
  "s_cmp_eq_u32 %[sk], %[ks]\n\t" \
  "s_cbranch_scc1 Lx1_%=\n\t" \
  "Lxb1_%=:\n\t" \
  "v_mfma_f32_16x16x32_bf16 v[92:95], v[136:139], v[172:175], v[92:95]\n\t" \
  "v_mfma_f32_16x16x32_bf16 v[124:127], v[140:143], v[172:175], v[124:127]\n\t" \
  "ds_read_b128 v[172:175], %[pb0] offset:14336\n\t" \
  "s_waitcnt lgkmcnt(4)\n\t" \
  "v_mfma_f32_16x16x32_bf16 v[64:67], v[128:131], v[144:147], v[64:67]\n\t" \
  "v_mfma_f32_16x16x32_bf16 v[96:99], v[132:135], v[144:147], v[96:99]\n\t" \
  "ds_read_b128 v[144:147], %[pb1] offset:0\n\t" \
  "v_mov_b64 v[136:137], v[176:177]\n\t" \
  "v_mfma_f32_16x16x32_bf16 v[68:71], v[128:131], v[148:151], v[68:71]\n\t" \
  "v_mfma_f32_16x16x32_bf16 v[100:103], v[132:135], v[148:151], v[100:103]\n\t" \
  "ds_read_b128 v[148:151], %[pb1] offset:2048\n\t" \
  "v_mov_b64 v[138:139], v[178:179]\n\t" \
  "v_mfma_f32_16x16x32_bf16 v[72:75], v[128:131], v[152:155], v[72:75]\n\t" \
  "v_mfma_f32_16x16x32_bf16 v[104:107], v[132:135], v[152:155], v[104:107]\n\t" \
  "ds_read_b128 v[152:155], %[pb1] offset:4096\n\t" \
  "v_add_u32 %[fl], 8, %[fl]\n\t" \
  "v_mfma_f32_16x16x32_bf16 v[76:79], v[128:131], v[156:159], v[76:79]\n\t" \
  "v_mfma_f32_16x16x32_bf16 v[108:111], v[132:135], v[156:159], v[108:111]\n\t" \
  "ds_read_b128 v[156:159], %[pb1] offset:6144\n\t" \
  "ds_read_b64 v[224:225], %[fl]\n\t" \
  "s_waitcnt lgkmcnt(5)\n\t" \
  "v_mfma_f32_16x16x32_bf16 v[80:83], v[128:131], v[160:163], v[80:83]\n\t" \
  "v_mfma_f32_16x16x32_bf16 v[112:115], v[132:135], v[160:163], v[112:115]\n\t" \
  "ds_read_b128 v[160:163], %[pb1] offset:8192\n\t" \
  "v_mov_b64 v[140:141], v[180:181]\n\t" \
  "v_mfma_f32_16x16x32_bf16 v[84:87], v[128:131], v[164:167], v[84:87]\n\t" \
  "v_mfma_f32_16x16x32_bf16 v[116:119], v[132:135], v[164:167], v[116:119]\n\t" \
  "ds_read_b128 v[164:167], %[pb1] offset:10240\n\t" \
  "v_mov_b64 v[142:143], v[182:183]\n\t" \
  "v_mfma_f32_16x16x32_bf16 v[88:91], v[128:131], v[168:171], v[88:91]\n\t" \
  "v_mfma_f32_16x16x32_bf16 v[120:123], v[132:135], v[168:171], v[120:123]\n\t" \
  "ds_read_b128 v[168:171], %[pb1] offset:12288\n\t" \
  "v_mfma_f32_16x16x32_bf16 v[92:95], v[128:131], v[172:175], v[92:95]\n\t" \
  "v_mfma_f32_16x16x32_bf16 v[124:127], v[132:135], v[172:175], v[124:127]\n\t" \
  "ds_read_b128 v[172:175], %[pb1] offset:14336\n\t" \
  "s_cmp_lt_u32 %[sk], 15\n\t" \
  "s_cbranch_scc1 Lloop_%=\n\t" \
  "s_waitcnt lgkmcnt(4)\n\t" \
  "v_or_b32 v224, v224, v225\n\t" \
  "v_mfma_f32_16x16x32_bf16 v[64:67], v[136:139], v[144:147], v[64:67]\n\t" \
  "v_readfirstlane_b32 %[s0], v224\n\t" \
  "v_mfma_f32_16x16x32_bf16 v[96:99], v[140:143], v[144:147], v[96:99]\n\t" \
  "s_cmp_eq_u32 %[s0], 0\n\t" \
  "s_cbranch_scc0 Lsp2_%=\n\t" \
  "Lb2_%=:\n\t" \
  "v_mfma_f32_16x16x32_bf16 v[68:71], v[136:139], v[148:151], v[68:71]\n\t" \
  "v_mfma_f32_16x16x32_bf16 v[100:103], v[140:143], v[148:151], v[100:103]\n\t" \
  "ds_read_b128 v[176:179], %[tq] offset:0\n\t" \
  "ds_read_b128 v[180:183], %[tq] offset:256\n\t" \
  "v_mfma_f32_16x16x32_bf16 v[72:75], v[136:139], v[152:155], v[72:75]\n\t" \
  "v_mfma_f32_16x16x32_bf16 v[104:107], v[140:143], v[152:155], v[104:107]\n\t" \
  "ds_read_b128 v[192:195], %[tq] offset:1024\n\t" \
  "ds_read_b128 v[196:199], %[tq] offset:1280\n\t" \
  "v_mfma_f32_16x16x32_bf16 v[76:79], v[136:139], v[156:159], v[76:79]\n\t" \
  "v_mfma_f32_16x16x32_bf16 v[108:111], v[140:143], v[156:159], v[108:111]\n\t" \
  "ds_read_b128 v[200:203], %[tq] offset:1536\n\t" \
  "ds_read_b128 v[204:207], %[tq] offset:1792\n\t" \
  "s_waitcnt lgkmcnt(6)\n\t" \
  "v_mfma_f32_16x16x32_bf16 v[80:83], v[136:139], v[160:163], v[80:83]\n\t" \
  "s_mov_b32 m0, %[scn]\n\t" \
  "s_mov_b64 exec, 1\n\t" \
  "buffer_load_dword %[l16], %[srz], 0 offen lds\n\t" \
  "s_mov_b64 exec, -1\n\t" \
  "v_mfma_f32_16x16x32_bf16 v[112:115], v[140:143], v[160:163], v[112:115]\n\t" \
  "ds_read_b128 v[208:211], %[tq] offset:2048\n\t" \
  "ds_read_b128 v[212:215], %[tq] offset:2304\n\t" \
  "v_mfma_f32_16x16x32_bf16 v[84:87], v[136:139], v[164:167], v[84:87]\n\t" \
  "v_mfma_f32_16x16x32_bf16 v[116:119], v[140:143], v[164:167], v[116:119]\n\t" \
  "ds_read_b128 v[216:219], %[tq] offset:2560\n\t" \
  "ds_read_b128 v[220:223], %[tq] offset:2816\n\t" \
  "v_mfma_f32_16x16x32_bf16 v[88:91], v[136:139], v[168:171], v[88:91]\n\t" \
  "v_mfma_f32_16x16x32_bf16 v[120:123], v[140:143], v[168:171], v[120:123]\n\t" \
  "ds_read_b128 v[184:187], %[tq] offset:512\n\t" \
  "ds_read_b128 v[188:191], %[tq] offset:768\n\t" \
  "v_mfma_f32_16x16x32_bf16 v[92:95], v[136:139], v[172:175], v[92:95]\n\t" \
  "v_mfma_f32_16x16x32_bf16 v[124:127], v[140:143], v[172:175], v[124:127]\n\t" \
  "ds_read2_b32 v[230:231], %[tcl] offset0:0 offset1:64\n\t" \
  "ds_read2_b32 v[232:233], %[tcw] offset0:0 offset1:64\n\t" \
  "s_setprio 3\n\t" \
  "s_waitcnt vmcnt(8) lgkmcnt(4)\n\t" \
  "v_readfirstlane_b32 %[tgB], v228\n\t" \
  "s_add_u32 %[tgA], %[tgB], 1\n\t" \
  "s_add_u32 %[tgB], %[tgB], 0x40000001\n\t" \
  "v_pk_fma_f32 v[244:245], v[180:181], v[96:97], v[68:69] neg_lo:[1,0,0] neg_hi:[1,0,0]\n\t" \
  "v_pk_fma_f32 v[246:247], v[182:183], v[98:99], v[70:71] neg_lo:[1,0,0] neg_hi:[1,0,0]\n\t" \
  "v_pk_fma_f32 v[248:249], v[180:181], v[64:65], v[100:101]\n\t" \
  "v_pk_fma_f32 v[250:251], v[182:183], v[66:67], v[102:103]\n\t" \
  "v_pk_fma_f32 v[236:237], v[176:177], v[64:65], v[244:245]\n\t" \
  "v_pk_fma_f32 v[238:239], v[178:179], v[66:67], v[246:247]\n\t" \
  "v_pk_fma_f32 v[240:241], v[176:177], v[96:97], v[248:249]\n\t" \
  "v_pk_fma_f32 v[242:243], v[178:179], v[98:99], v[250:251]\n\t" \
  "v_pk_fma_f32 v[244:245], v[180:181], v[240:241], v[72:73] neg_lo:[1,0,0] neg_hi:[1,0,0]\n\t" \
  "v_pk_fma_f32 v[246:247], v[182:183], v[242:243], v[74:75] neg_lo:[1,0,0] neg_hi:[1,0,0]\n\t" \
  "v_pk_fma_f32 v[248:249], v[180:181], v[236:237], v[104:105]\n\t" \
  "v_pk_fma_f32 v[250:251], v[182:183], v[238:239], v[106:107]\n\t" \
  "v_pk_fma_f32 v[236:237], v[176:177], v[236:237], v[244:245]\n\t" \
  "v_pk_fma_f32 v[238:239], v[178:179], v[238:239], v[246:247]\n\t" \
  "v_pk_fma_f32 v[240:241], v[176:177], v[240:241], v[248:249]\n\t" \
  "v_pk_fma_f32 v[242:243], v[178:179], v[242:243], v[250:251]\n\t" \
  "v_pk_fma_f32 v[244:245], v[180:181], v[240:241], v[76:77] neg_lo:[1,0,0] neg_hi:[1,0,0]\n\t" \
  "v_pk_fma_f32 v[246:247], v[182:183], v[242:243], v[78:79] neg_lo:[1,0,0] neg_hi:[1,0,0]\n\t" \
  "v_pk_fma_f32 v[248:249], v[180:181], v[236:237], v[108:109]\n\t" \
  "v_pk_fma_f32 v[250:251], v[182:183], v[238:239], v[110:111]\n\t" \
  "v_pk_fma_f32 v[236:237], v[176:177], v[236:237], v[244:245]\n\t" \
  "v_pk_fma_f32 v[238:239], v[178:179], v[238:239], v[246:247]\n\t" \
  "v_pk_fma_f32 v[240:241], v[176:177], v[240:241], v[248:249]\n\t" \
  "v_pk_fma_f32 v[242:243], v[178:179], v[242:243], v[250:251]\n\t" \
  "v_pk_fma_f32 v[244:245], v[180:181], v[240:241], v[80:81] neg_lo:[1,0,0] neg_hi:[1,0,0]\n\t" \
  "v_pk_fma_f32 v[246:247], v[182:183], v[242:243], v[82:83] neg_lo:[1,0,0] neg_hi:[1,0,0]\n\t" \
  "v_pk_fma_f32 v[248:249], v[180:181], v[236:237], v[112:113]\n\t" \
  "v_pk_fma_f32 v[250:251], v[182:183], v[238:239], v[114:115]\n\t" \
  "v_pk_fma_f32 v[236:237], v[176:177], v[236:237], v[244:245]\n\t" \
  "v_pk_fma_f32 v[238:239], v[178:179], v[238:239], v[246:247]\n\t" \
  "v_pk_fma_f32 v[240:241], v[176:177], v[240:241], v[248:249]\n\t" \
  "v_pk_fma_f32 v[242:243], v[178:179], v[242:243], v[250:251]\n\t" \
  "v_pk_fma_f32 v[244:245], v[180:181], v[240:241], v[84:85] neg_lo:[1,0,0] neg_hi:[1,0,0]\n\t" \
  "v_pk_fma_f32 v[246:247], v[182:183], v[242:243], v[86:87] neg_lo:[1,0,0] neg_hi:[1,0,0]\n\t" \
  "v_pk_fma_f32 v[248:249], v[180:181], v[236:237], v[116:117]\n\t" \
  "v_pk_fma_f32 v[250:251], v[182:183], v[238:239], v[118:119]\n\t" \
  "v_pk_fma_f32 v[236:237], v[176:177], v[236:237], v[244:245]\n\t" \
  "v_pk_fma_f32 v[238:239], v[178:179], v[238:239], v[246:247]\n\t" \
  "v_pk_fma_f32 v[240:241], v[176:177], v[240:241], v[248:249]\n\t" \
  "v_pk_fma_f32 v[242:243], v[178:179], v[242:243], v[250:251]\n\t" \
  "v_pk_fma_f32 v[244:245], v[180:181], v[240:241], v[88:89] neg_lo:[1,0,0] neg_hi:[1,0,0]\n\t" \
  "v_pk_fma_f32 v[246:247], v[182:183], v[242:243], v[90:91] neg_lo:[1,0,0] neg_hi:[1,0,0]\n\t" \
  "v_pk_fma_f32 v[248:249], v[180:181], v[236:237], v[120:121]\n\t" \
  "v_pk_fma_f32 v[250:251], v[182:183], v[238:239], v[122:123]\n\t" \
  "v_pk_fma_f32 v[236:237], v[176:177], v[236:237], v[244:245]\n\t" \
  "v_pk_fma_f32 v[238:239], v[178:179], v[238:239], v[246:247]\n\t" \
  "v_pk_fma_f32 v[240:241], v[176:177], v[240:241], v[248:249]\n\t" \
  "v_pk_fma_f32 v[242:243], v[178:179], v[242:243], v[250:251]\n\t" \
  "v_pk_fma_f32 v[244:245], v[180:181], v[240:241], v[92:93] neg_lo:[1,0,0] neg_hi:[1,0,0]\n\t" \
  "v_pk_fma_f32 v[246:247], v[182:183], v[242:243], v[94:95] neg_lo:[1,0,0] neg_hi:[1,0,0]\n\t" \
  "v_pk_fma_f32 v[248:249], v[180:181], v[236:237], v[124:125]\n\t" \
  "v_pk_fma_f32 v[250:251], v[182:183], v[238:239], v[126:127]\n\t" \
  "v_pk_fma_f32 v[236:237], v[176:177], v[236:237], v[244:245]\n\t" \
  "v_pk_fma_f32 v[238:239], v[178:179], v[238:239], v[246:247]\n\t" \
  "v_pk_fma_f32 v[240:241], v[176:177], v[240:241], v[248:249]\n\t" \
  "v_pk_fma_f32 v[242:243], v[178:179], v[242:243], v[250:251]\n\t" \
  "v_mov_b32 v253, %[tgA]\n\t" \
  "v_mov_b32 v255, %[tgB]\n\t" \
  "v_mov_b32_dpp v244, v236 row_shr:1 row_mask:0xf bank_mask:0xf bound_ctrl:0\n\t" \
  "v_mov_b32_dpp v245, v237 row_shr:1 row_mask:0xf bank_mask:0xf bound_ctrl:0\n\t" \
  "v_mov_b32_dpp v246, v238 row_shr:1 row_mask:0xf bank_mask:0xf bound_ctrl:0\n\t" \
  "v_mov_b32_dpp v247, v239 row_shr:1 row_mask:0xf bank_mask:0xf bound_ctrl:0\n\t" \
  "v_mov_b32_dpp v248, v240 row_shr:1 row_mask:0xf bank_mask:0xf bound_ctrl:0\n\t" \
  "v_mov_b32_dpp v249, v241 row_shr:1 row_mask:0xf bank_mask:0xf bound_ctrl:0\n\t" \
  "v_mov_b32_dpp v250, v242 row_shr:1 row_mask:0xf bank_mask:0xf bound_ctrl:0\n\t" \
  "v_mov_b32_dpp v251, v243 row_shr:1 row_mask:0xf bank_mask:0xf bound_ctrl:0\n\t" \
  "v_pk_fma_f32 v[236:237], v[192:193], v[244:245], v[236:237]\n\t" \
  "v_pk_fma_f32 v[238:239], v[194:195], v[246:247], v[238:239]\n\t" \
  "v_pk_fma_f32 v[240:241], v[192:193], v[248:249], v[240:241]\n\t" \
  "v_pk_fma_f32 v[242:243], v[194:195], v[250:251], v[242:243]\n\t" \
  "v_pk_fma_f32 v[236:237], v[196:197], v[248:249], v[236:237] neg_lo:[1,0,0] neg_hi:[1,0,0]\n\t" \
  "v_pk_fma_f32 v[238:239], v[198:199], v[250:251], v[238:239] neg_lo:[1,0,0] neg_hi:[1,0,0]\n\t" \
  "v_pk_fma_f32 v[240:241], v[196:197], v[244:245], v[240:241]\n\t" \
  "v_pk_fma_f32 v[242:243], v[198:199], v[246:247], v[242:243]\n\t" \
  "v_mov_b32_dpp v244, v236 row_shr:2 row_mask:0xf bank_mask:0xf bound_ctrl:0\n\t" \
  "v_mov_b32_dpp v245, v237 row_shr:2 row_mask:0xf bank_mask:0xf bound_ctrl:0\n\t" \
  "v_mov_b32_dpp v246, v238 row_shr:2 row_mask:0xf bank_mask:0xf bound_ctrl:0\n\t" \
  "v_mov_b32_dpp v247, v239 row_shr:2 row_mask:0xf bank_mask:0xf bound_ctrl:0\n\t" \
  "v_mov_b32_dpp v248, v240 row_shr:2 row_mask:0xf bank_mask:0xf bound_ctrl:0\n\t" \
  "v_mov_b32_dpp v249, v241 row_shr:2 row_mask:0xf bank_mask:0xf bound_ctrl:0\n\t" \
  "v_mov_b32_dpp v250, v242 row_shr:2 row_mask:0xf bank_mask:0xf bound_ctrl:0\n\t" \
  "v_mov_b32_dpp v251, v243 row_shr:2 row_mask:0xf bank_mask:0xf bound_ctrl:0\n\t" \
  "v_pk_fma_f32 v[236:237], v[200:201], v[244:245], v[236:237]\n\t" \
  "v_pk_fma_f32 v[238:239], v[202:203], v[246:247], v[238:239]\n\t" \
  "v_pk_fma_f32 v[240:241], v[200:201], v[248:249], v[240:241]\n\t" \
  "v_pk_fma_f32 v[242:243], v[202:203], v[250:251], v[242:243]\n\t" \
  "v_pk_fma_f32 v[236:237], v[204:205], v[248:249], v[236:237] neg_lo:[1,0,0] neg_hi:[1,0,0]\n\t" \
  "v_pk_fma_f32 v[238:239], v[206:207], v[250:251], v[238:239] neg_lo:[1,0,0] neg_hi:[1,0,0]\n\t" \
  "v_pk_fma_f32 v[240:241], v[204:205], v[244:245], v[240:241]\n\t" \
  "v_pk_fma_f32 v[242:243], v[206:207], v[246:247], v[242:243]\n\t" \
  "v_mov_b32_dpp v244, v236 row_shr:4 row_mask:0xf bank_mask:0xf bound_ctrl:0\n\t" \
  "v_mov_b32_dpp v245, v237 row_shr:4 row_mask:0xf bank_mask:0xf bound_ctrl:0\n\t" \
  "v_mov_b32_dpp v246, v238 row_shr:4 row_mask:0xf bank_mask:0xf bound_ctrl:0\n\t" \
  "v_mov_b32_dpp v247, v239 row_shr:4 row_mask:0xf bank_mask:0xf bound_ctrl:0\n\t" \
  "v_mov_b32_dpp v248, v240 row_shr:4 row_mask:0xf bank_mask:0xf bound_ctrl:0\n\t" \
  "v_mov_b32_dpp v249, v241 row_shr:4 row_mask:0xf bank_mask:0xf bound_ctrl:0\n\t" \
  "v_mov_b32_dpp v250, v242 row_shr:4 row_mask:0xf bank_mask:0xf bound_ctrl:0\n\t" \
  "v_mov_b32_dpp v251, v243 row_shr:4 row_mask:0xf bank_mask:0xf bound_ctrl:0\n\t" \
  "v_pk_fma_f32 v[236:237], v[208:209], v[244:245], v[236:237]\n\t" \
  "v_pk_fma_f32 v[238:239], v[210:211], v[246:247], v[238:239]\n\t" \
  "v_pk_fma_f32 v[240:241], v[208:209], v[248:249], v[240:241]\n\t" \
  "v_pk_fma_f32 v[242:243], v[210:211], v[250:251], v[242:243]\n\t" \
  "v_pk_fma_f32 v[236:237], v[212:213], v[248:249], v[236:237] neg_lo:[1,0,0] neg_hi:[1,0,0]\n\t" \
  "v_pk_fma_f32 v[238:239], v[214:215], v[250:251], v[238:239] neg_lo:[1,0,0] neg_hi:[1,0,0]\n\t" \
  "v_pk_fma_f32 v[240:241], v[212:213], v[244:245], v[240:241]\n\t" \
  "v_pk_fma_f32 v[242:243], v[214:215], v[246:247], v[242:243]\n\t" \
  "v_mov_b32_dpp v244, v236 row_shr:8 row_mask:0xf bank_mask:0xf bound_ctrl:0\n\t" \
  "v_mov_b32_dpp v245, v237 row_shr:8 row_mask:0xf bank_mask:0xf bound_ctrl:0\n\t" \
  "v_mov_b32_dpp v246, v238 row_shr:8 row_mask:0xf bank_mask:0xf bound_ctrl:0\n\t" \
  "v_mov_b32_dpp v247, v239 row_shr:8 row_mask:0xf bank_mask:0xf bound_ctrl:0\n\t" \
  "v_mov_b32_dpp v248, v240 row_shr:8 row_mask:0xf bank_mask:0xf bound_ctrl:0\n\t" \
  "v_mov_b32_dpp v249, v241 row_shr:8 row_mask:0xf bank_mask:0xf bound_ctrl:0\n\t" \
  "v_mov_b32_dpp v250, v242 row_shr:8 row_mask:0xf bank_mask:0xf bound_ctrl:0\n\t" \
  "v_mov_b32_dpp v251, v243 row_shr:8 row_mask:0xf bank_mask:0xf bound_ctrl:0\n\t" \
  "v_pk_fma_f32 v[236:237], v[216:217], v[244:245], v[236:237]\n\t" \
  "v_pk_fma_f32 v[238:239], v[218:219], v[246:247], v[238:239]\n\t" \
  "v_pk_fma_f32 v[240:241], v[216:217], v[248:249], v[240:241]\n\t" \
  "v_pk_fma_f32 v[242:243], v[218:219], v[250:251], v[242:243]\n\t" \
  "v_pk_fma_f32 v[236:237], v[220:221], v[248:249], v[236:237] neg_lo:[1,0,0] neg_hi:[1,0,0]\n\t" \
  "v_pk_fma_f32 v[238:239], v[222:223], v[250:251], v[238:239] neg_lo:[1,0,0] neg_hi:[1,0,0]\n\t" \
  "v_pk_fma_f32 v[240:241], v[220:221], v[244:245], v[240:241]\n\t" \
  "v_pk_fma_f32 v[242:243], v[222:223], v[246:247], v[242:243]\n\t" \
  "v_mov_b32_dpp v252, v236 row_newbcast:15 row_mask:0xf bank_mask:0x1\n\t" \
  "v_mov_b32_dpp v252, v237 row_newbcast:15 row_mask:0xf bank_mask:0x2\n\t" \
  "v_mov_b32_dpp v252, v238 row_newbcast:15 row_mask:0xf bank_mask:0x4\n\t" \
  "v_mov_b32_dpp v252, v239 row_newbcast:15 row_mask:0xf bank_mask:0x8\n\t" \
  "v_mov_b32_dpp v254, v240 row_newbcast:15 row_mask:0xf bank_mask:0x1\n\t" \
  "v_mov_b32_dpp v254, v241 row_newbcast:15 row_mask:0xf bank_mask:0x2\n\t" \
  "v_mov_b32_dpp v254, v242 row_newbcast:15 row_mask:0xf bank_mask:0x4\n\t" \
  "v_mov_b32_dpp v254, v243 row_newbcast:15 row_mask:0xf bank_mask:0x8\n\t" \
  "s_mov_b32 exec_lo, 0x11111111\n\t" \
  "s_mov_b32 exec_hi, 0x11111111\n\t" \
  "buffer_store_dwordx4 v[252:255], %[pubo], %[srd], %[sag] offen sc1\n\t" \
  "s_mov_b64 exec, 1\n\t" \
  "buffer_atomic_swap v253, off, %[srd], %[shc] sc1\n\t" \
  "s_mov_b64 exec, -1\n\t" \
  "s_mov_b64 exec, %[eh]\n\t" \
  "v_mov_b32 v226, 0\n\t" \
  "v_lshrrev_b32 v227, 2, %[l16]\n\t" \
  "s_cbranch_execz Lhza_%=\n\t" \
  "buffer_atomic_or v226, v227, %[srd], %[sht] offen sc0 sc1\n\t" \
  "s_branch Lhya_%=\n\t" \
  "Lhza_%=:\n\t" \
  "s_mov_b64 exec, 1\n\t" \
  "buffer_load_dword v226, %[l16], %[srz], 0 offen\n\t" \
  "Lhya_%=:\n\t" \
  "s_mov_b64 exec, -1\n\t" \
  "s_waitcnt lgkmcnt(0)\n\t" \
  "v_mov_b32_dpp v244, v236 row_shr:1 row_mask:0xf bank_mask:0xf bound_ctrl:0\n\t" \
  "v_mov_b32_dpp v245, v237 row_shr:1 row_mask:0xf bank_mask:0xf bound_ctrl:0\n\t" \
  "v_mov_b32_dpp v246, v238 row_shr:1 row_mask:0xf bank_mask:0xf bound_ctrl:0\n\t" \
  "v_mov_b32_dpp v247, v239 row_shr:1 row_mask:0xf bank_mask:0xf bound_ctrl:0\n\t" \
  "v_mov_b32_dpp v248, v240 row_shr:1 row_mask:0xf bank_mask:0xf bound_ctrl:0\n\t" \
  "v_mov_b32_dpp v249, v241 row_shr:1 row_mask:0xf bank_mask:0xf bound_ctrl:0\n\t" \
  "v_mov_b32_dpp v250, v242 row_shr:1 row_mask:0xf bank_mask:0xf bound_ctrl:0\n\t" \
  "v_mov_b32_dpp v251, v243 row_shr:1 row_mask:0xf bank_mask:0xf bound_ctrl:0\n\t" \
  "v_pk_mul_f32 v[236:237], v[184:185], v[244:245]\n\t" \
  "v_pk_mul_f32 v[238:239], v[186:187], v[246:247]\n\t" \
  "v_pk_mul_f32 v[240:241], v[184:185], v[248:249]\n\t" \
  "v_pk_mul_f32 v[242:243], v[186:187], v[250:251]\n\t" \
  "v_pk_fma_f32 v[236:237], v[188:189], v[248:249], v[236:237] neg_lo:[1,0,0] neg_hi:[1,0,0]\n\t" \
  "v_pk_fma_f32 v[238:239], v[190:191], v[250:251], v[238:239] neg_lo:[1,0,0] neg_hi:[1,0,0]\n\t" \
  "v_pk_fma_f32 v[240:241], v[188:189], v[244:245], v[240:241]\n\t" \
  "v_pk_fma_f32 v[242:243], v[190:191], v[246:247], v[242:243]\n\t" \
  "v_pk_mul_f32 v[128:129], v[184:185], v[64:65]\n\t" \
  "v_pk_mul_f32 v[130:131], v[186:187], v[66:67]\n\t" \
  "v_pk_fma_f32 v[128:129], v[188:189], v[96:97], v[128:129] neg_lo:[1,0,0] neg_hi:[1,0,0]\n\t" \
  "v_pk_fma_f32 v[130:131], v[190:191], v[98:99], v[130:131] neg_lo:[1,0,0] neg_hi:[1,0,0]\n\t" \
  "v_pk_mul_f32 v[96:97], v[184:185], v[96:97]\n\t" \
  "v_pk_mul_f32 v[98:99], v[186:187], v[98:99]\n\t" \
  "v_pk_fma_f32 v[96:97], v[188:189], v[64:65], v[96:97] neg_lo:[1,0,1] neg_hi:[1,0,1]\n\t" \
  "v_pk_fma_f32 v[98:99], v[190:191], v[66:67], v[98:99] neg_lo:[1,0,1] neg_hi:[1,0,1]\n\t" \
  "v_pk_mul_f32 v[132:133], v[184:185], v[68:69]\n\t" \
  "v_pk_mul_f32 v[134:135], v[186:187], v[70:71]\n\t" \
  "v_pk_fma_f32 v[132:133], v[188:189], v[100:101], v[132:133] neg_lo:[1,0,0] neg_hi:[1,0,0]\n\t" \
  "v_pk_fma_f32 v[134:135], v[190:191], v[102:103], v[134:135] neg_lo:[1,0,0] neg_hi:[1,0,0]\n\t" \
  "v_pk_mul_f32 v[100:101], v[184:185], v[100:101]\n\t" \
  "v_pk_mul_f32 v[102:103], v[186:187], v[102:103]\n\t" \
  "v_pk_fma_f32 v[100:101], v[188:189], v[68:69], v[100:101] neg_lo:[1,0,1] neg_hi:[1,0,1]\n\t" \
  "v_pk_fma_f32 v[102:103], v[190:191], v[70:71], v[102:103] neg_lo:[1,0,1] neg_hi:[1,0,1]\n\t" \
  "v_pk_mul_f32 v[136:137], v[184:185], v[72:73]\n\t" \
  "v_pk_mul_f32 v[138:139], v[186:187], v[74:75]\n\t" \
  "v_pk_fma_f32 v[136:137], v[188:189], v[104:105], v[136:137] neg_lo:[1,0,0] neg_hi:[1,0,0]\n\t" \
  "v_pk_fma_f32 v[138:139], v[190:191], v[106:107], v[138:139] neg_lo:[1,0,0] neg_hi:[1,0,0]\n\t" \
  "v_pk_mul_f32 v[104:105], v[184:185], v[104:105]\n\t" \
  "v_pk_mul_f32 v[106:107], v[186:187], v[106:107]\n\t" \
  "v_pk_fma_f32 v[104:105], v[188:189], v[72:73], v[104:105] neg_lo:[1,0,1] neg_hi:[1,0,1]\n\t" \
  "v_pk_fma_f32 v[106:107], v[190:191], v[74:75], v[106:107] neg_lo:[1,0,1] neg_hi:[1,0,1]\n\t" \
  "v_pk_mul_f32 v[140:141], v[184:185], v[76:77]\n\t" \
  "v_pk_mul_f32 v[142:143], v[186:187], v[78:79]\n\t" \
  "v_pk_fma_f32 v[140:141], v[188:189], v[108:109], v[140:141] neg_lo:[1,0,0] neg_hi:[1,0,0]\n\t" \
  "v_pk_fma_f32 v[142:143], v[190:191], v[110:111], v[142:143] neg_lo:[1,0,0] neg_hi:[1,0,0]\n\t" \
  "v_pk_mul_f32 v[108:109], v[184:185], v[108:109]\n\t" \
  "v_pk_mul_f32 v[110:111], v[186:187], v[110:111]\n\t" \
  "v_pk_fma_f32 v[108:109], v[188:189], v[76:77], v[108:109] neg_lo:[1,0,1] neg_hi:[1,0,1]\n\t" \
  "v_pk_fma_f32 v[110:111], v[190:191], v[78:79], v[110:111] neg_lo:[1,0,1] neg_hi:[1,0,1]\n\t" \
  "v_pk_mul_f32 v[144:145], v[184:185], v[80:81]\n\t" \
  "v_pk_mul_f32 v[146:147], v[186:187], v[82:83]\n\t" \
  "v_pk_fma_f32 v[144:145], v[188:189], v[112:113], v[144:145] neg_lo:[1,0,0] neg_hi:[1,0,0]\n\t" \
  "v_pk_fma_f32 v[146:147], v[190:191], v[114:115], v[146:147] neg_lo:[1,0,0] neg_hi:[1,0,0]\n\t" \
  "v_pk_mul_f32 v[112:113], v[184:185], v[112:113]\n\t" \
  "v_pk_mul_f32 v[114:115], v[186:187], v[114:115]\n\t" \
  "v_pk_fma_f32 v[112:113], v[188:189], v[80:81], v[112:113] neg_lo:[1,0,1] neg_hi:[1,0,1]\n\t" \
  "v_pk_fma_f32 v[114:115], v[190:191], v[82:83], v[114:115] neg_lo:[1,0,1] neg_hi:[1,0,1]\n\t" \
  "v_pk_mul_f32 v[148:149], v[184:185], v[84:85]\n\t" \
  "v_pk_mul_f32 v[150:151], v[186:187], v[86:87]\n\t" \
  "v_pk_fma_f32 v[148:149], v[188:189], v[116:117], v[148:149] neg_lo:[1,0,0] neg_hi:[1,0,0]\n\t" \
  "v_pk_fma_f32 v[150:151], v[190:191], v[118:119], v[150:151] neg_lo:[1,0,0] neg_hi:[1,0,0]\n\t" \
  "v_pk_mul_f32 v[116:117], v[184:185], v[116:117]\n\t" \
  "v_pk_mul_f32 v[118:119], v[186:187], v[118:119]\n\t" \
  "v_pk_fma_f32 v[116:117], v[188:189], v[84:85], v[116:117] neg_lo:[1,0,1] neg_hi:[1,0,1]\n\t" \
  "v_pk_fma_f32 v[118:119], v[190:191], v[86:87], v[118:119] neg_lo:[1,0,1] neg_hi:[1,0,1]\n\t" \
  "v_pk_mul_f32 v[152:153], v[184:185], v[88:89]\n\t" \
  "v_pk_mul_f32 v[154:155], v[186:187], v[90:91]\n\t" \
  "v_pk_fma_f32 v[152:153], v[188:189], v[120:121], v[152:153] neg_lo:[1,0,0] neg_hi:[1,0,0]\n\t" \
  "v_pk_fma_f32 v[154:155], v[190:191], v[122:123], v[154:155] neg_lo:[1,0,0] neg_hi:[1,0,0]\n\t" \
  "v_pk_mul_f32 v[120:121], v[184:185], v[120:121]\n\t" \
  "v_pk_mul_f32 v[122:123], v[186:187], v[122:123]\n\t" \
  "v_pk_fma_f32 v[120:121], v[188:189], v[88:89], v[120:121] neg_lo:[1,0,1] neg_hi:[1,0,1]\n\t" \
  "v_pk_fma_f32 v[122:123], v[190:191], v[90:91], v[122:123] neg_lo:[1,0,1] neg_hi:[1,0,1]\n\t" \
  "v_pk_mul_f32 v[156:157], v[184:185], v[92:93]\n\t" \
  "v_pk_mul_f32 v[158:159], v[186:187], v[94:95]\n\t" \
  "v_pk_fma_f32 v[156:157], v[188:189], v[124:125], v[156:157] neg_lo:[1,0,0] neg_hi:[1,0,0]\n\t" \
  "v_pk_fma_f32 v[158:159], v[190:191], v[126:127], v[158:159] neg_lo:[1,0,0] neg_hi:[1,0,0]\n\t" \
  "v_pk_mul_f32 v[124:125], v[184:185], v[124:125]\n\t" \
  "v_pk_mul_f32 v[126:127], v[186:187], v[126:127]\n\t" \
  "v_pk_fma_f32 v[124:125], v[188:189], v[92:93], v[124:125] neg_lo:[1,0,1] neg_hi:[1,0,1]\n\t" \
  "v_pk_fma_f32 v[126:127], v[190:191], v[94:95], v[126:127] neg_lo:[1,0,1] neg_hi:[1,0,1]\n\t" \
  "v_mov_b32 v160, 0\n\t" \
  "v_mov_b32 v161, %[tgA]\n\t" \
  "v_mov_b32 v162, 0\n\t" \
  "v_mov_b32 v163, %[tgB]\n\t" \
  "v_mov_b32 v164, 0\n\t" \
  "v_mov_b32 v165, %[tgA]\n\t" \
  "v_mov_b32 v166, 0\n\t" \
  "v_mov_b32 v167, %[tgB]\n\t" \
  "v_mov_b32 v168, 0\n\t" \
  "v_mov_b32 v169, %[tgA]\n\t" \
  "v_mov_b32 v170, 0\n\t" \
  "v_mov_b32 v171, %[tgB]\n\t" \
  "v_mov_b32 v172, 0\n\t" \
  "v_mov_b32 v173, %[tgA]\n\t" \
  "v_mov_b32 v174, 0\n\t" \
  "v_mov_b32 v175, %[tgB]\n\t" \
  "s_waitcnt vmcnt(0)\n\t" \
  "v_cmp_ne_u32 vcc, %[tgA], v226\n\t" \
  "s_and_b64 vcc, vcc, %[eh]\n\t" \
  "s_cselect_b32 %[s0], 0, 1\n\t" \
  "s_cbranch_scc0 Lh1_%=\n\t" \
  "s_mov_b64 exec, %[eh]\n\t" \
  "v_mov_b32 v226, 0\n\t" \
  "v_lshrrev_b32 v227, 2, %[l16]\n\t" \
  "s_cbranch_execz Lhzb_%=\n\t" \
  "buffer_atomic_or v226, v227, %[srd], %[sht] offen sc0 sc1\n\t" \
  "s_branch Lhyb_%=\n\t" \
  "Lhzb_%=:\n\t" \
  "s_mov_b64 exec, 1\n\t" \
  "buffer_load_dword v226, %[l16], %[srz], 0 offen\n\t" \
  "Lhyb_%=:\n\t" \
  "s_mov_b64 exec, -1\n\t" \
  "s_branch Lpa_%=\n\t" \
  "Lh1_%=:\n\t" \
  "s_mov_b64 exec, %[em0]\n\t" \
  "s_cbranch_execz Lpa_%=\n\t" \
  "buffer_load_dwordx4 v[160:163], %[plo], %[srd], %[sl0] offen sc1\n\t" \
  "s_mov_b64 exec, %[em1]\n\t" \
  "s_cbranch_execz Lpa_%=\n\t" \
  "buffer_load_dwordx4 v[164:167], %[plo], %[srd], %[sl1] offen sc1\n\t" \
  "s_mov_b64 exec, %[em2]\n\t" \
  "s_cbranch_execz Lpa_%=\n\t" \
  "buffer_load_dwordx4 v[168:171], %[plo], %[srd], %[sl2] offen sc1\n\t" \
  "s_mov_b64 exec, %[em3]\n\t" \
  "s_cbranch_execz Lpa_%=\n\t" \
  "buffer_load_dwordx4 v[172:175], %[plo], %[srd], %[sl3] offen sc1\n\t" \
  "Lpa_%=:\n\t" \
  "s_mov_b32 exec_lo, 0xaaaaaaaa\n\t" \
  "s_mov_b32 exec_hi, 0xaaaaaaaa\n\t" \
  "v_pk_mul_f32 v[244:245], v[196:197], v[188:189]\n\t" \
  "v_pk_mul_f32 v[246:247], v[198:199], v[190:191]\n\t" \
  "v_pk_mul_f32 v[248:249], v[196:197], v[184:185]\n\t" \
  "v_pk_mul_f32 v[250:251], v[198:199], v[186:187]\n\t" \
  "v_pk_fma_f32 v[184:185], v[192:193], v[184:185], v[244:245] neg_lo:[0,0,1] neg_hi:[0,0,1]\n\t" \
  "v_pk_fma_f32 v[186:187], v[194:195], v[186:187], v[246:247] neg_lo:[0,0,1] neg_hi:[0,0,1]\n\t" \
  "v_pk_fma_f32 v[188:189], v[192:193], v[188:189], v[248:249]\n\t" \
  "v_pk_fma_f32 v[190:191], v[194:195], v[190:191], v[250:251]\n\t" \
  "s_mov_b32 exec_lo, 0xcccccccc\n\t" \
  "s_mov_b32 exec_hi, 0xcccccccc\n\t" \
  "v_pk_mul_f32 v[244:245], v[204:205], v[188:189]\n\t" \
  "v_pk_mul_f32 v[246:247], v[206:207], v[190:191]\n\t" \
  "v_pk_mul_f32 v[248:249], v[204:205], v[184:185]\n\t" \
  "v_pk_mul_f32 v[250:251], v[206:207], v[186:187]\n\t" \
  "v_pk_fma_f32 v[184:185], v[200:201], v[184:185], v[244:245] neg_lo:[0,0,1] neg_hi:[0,0,1]\n\t" \
  "v_pk_fma_f32 v[186:187], v[202:203], v[186:187], v[246:247] neg_lo:[0,0,1] neg_hi:[0,0,1]\n\t" \
  "v_pk_fma_f32 v[188:189], v[200:201], v[188:189], v[248:249]\n\t" \
  "v_pk_fma_f32 v[190:191], v[202:203], v[190:191], v[250:251]\n\t" \
  "s_mov_b32 exec_lo, 0xf0f0f0f0\n\t" \
  "s_mov_b32 exec_hi, 0xf0f0f0f0\n\t" \
  "v_pk_mul_f32 v[244:245], v[212:213], v[188:189]\n\t" \
  "v_pk_mul_f32 v[246:247], v[214:215], v[190:191]\n\t" \
  "v_pk_mul_f32 v[248:249], v[212:213], v[184:185]\n\t" \
  "v_pk_mul_f32 v[250:251], v[214:215], v[186:187]\n\t" \
  "v_pk_fma_f32 v[184:185], v[208:209], v[184:185], v[244:245] neg_lo:[0,0,1] neg_hi:[0,0,1]\n\t" \
  "v_pk_fma_f32 v[186:187], v[210:211], v[186:187], v[246:247] neg_lo:[0,0,1] neg_hi:[0,0,1]\n\t" \
  "v_pk_fma_f32 v[188:189], v[208:209], v[188:189], v[248:249]\n\t" \
  "v_pk_fma_f32 v[190:191], v[210:211], v[190:191], v[250:251]\n\t" \
  "s_mov_b32 exec_lo, 0xff00ff00\n\t" \
  "s_mov_b32 exec_hi, 0xff00ff00\n\t" \
  "v_pk_mul_f32 v[244:245], v[220:221], v[188:189]\n\t" \
  "v_pk_mul_f32 v[246:247], v[222:223], v[190:191]\n\t" \
  "v_pk_mul_f32 v[248:249], v[220:221], v[184:185]\n\t" \
  "v_pk_mul_f32 v[250:251], v[222:223], v[186:187]\n\t" \
  "v_pk_fma_f32 v[184:185], v[216:217], v[184:185], v[244:245] neg_lo:[0,0,1] neg_hi:[0,0,1]\n\t" \
  "v_pk_fma_f32 v[186:187], v[218:219], v[186:187], v[246:247] neg_lo:[0,0,1] neg_hi:[0,0,1]\n\t" \
  "v_pk_fma_f32 v[188:189], v[216:217], v[188:189], v[248:249]\n\t" \
  "v_pk_fma_f32 v[190:191], v[218:219], v[190:191], v[250:251]\n\t" \
  "s_mov_b64 exec, -1\n\t" \
  "s_cmp_eq_u32 %[s0], 1\n\t" \
  "s_cbranch_scc1 Lq_%=\n\t" \
  "Lh2_%=:\n\t" \
  "s_waitcnt vmcnt(0)\n\t" \
  "v_cmp_ne_u32 vcc, %[tgA], v226\n\t" \
  "s_and_b64 vcc, vcc, %[eh]\n\t" \
  "s_cbranch_scc0 Lh3_%=\n\t" \
  "s_sub_i32 %[bud], %[bud], 16\n\t" \
  "s_cmp_lt_i32 %[bud], 0\n\t" \
  "s_cbranch_scc1 Lh3_%=\n\t" \
  "s_sleep 1\n\t" \
  "s_mov_b64 exec, %[eh]\n\t" \
  "v_mov_b32 v226, 0\n\t" \
  "v_lshrrev_b32 v227, 2, %[l16]\n\t" \
  "s_cbranch_execz Lhzc_%=\n\t" \
  "buffer_atomic_or v226, v227, %[srd], %[sht] offen sc0 sc1\n\t" \
  "s_branch Lhyc_%=\n\t" \
  "Lhzc_%=:\n\t" \
  "s_mov_b64 exec, 1\n\t" \
  "buffer_load_dword v226, %[l16], %[srz], 0 offen\n\t" \
  "Lhyc_%=:\n\t" \
  "s_mov_b64 exec, -1\n\t" \
  "s_branch Lh2_%=\n\t" \
  "Lh3_%=:\n\t" \
  "s_mov_b64 exec, %[em0]\n\t" \
  "s_cbranch_execz Lpc_%=\n\t" \
  "buffer_load_dwordx4 v[160:163], %[plo], %[srd], %[sl0] offen sc1\n\t" \
  "s_mov_b64 exec, %[em1]\n\t" \
  "s_cbranch_execz Lpc_%=\n\t" \
  "buffer_load_dwordx4 v[164:167], %[plo], %[srd], %[sl1] offen sc1\n\t" \
  "s_mov_b64 exec, %[em2]\n\t" \
  "s_cbranch_execz Lpc_%=\n\t" \
  "buffer_load_dwordx4 v[168:171], %[plo], %[srd], %[sl2] offen sc1\n\t" \
  "s_mov_b64 exec, %[em3]\n\t" \
  "s_cbranch_execz Lpc_%=\n\t" \
  "buffer_load_dwordx4 v[172:175], %[plo], %[srd], %[sl3] offen sc1\n\t" \
  "Lpc_%=:\n\t" \
  "Lq_%=:\n\t" \
  "s_mov_b64 exec, -1\n\t" \
  "s_waitcnt vmcnt(0)\n\t" \
  "v_cmpx_eq_u32 vcc, %[tgA], v161\n\t" \
  "v_cmpx_eq_u32 vcc, %[tgB], v163\n\t" \
  "v_cmpx_eq_u32 vcc, %[tgA], v165\n\t" \
  "v_cmpx_eq_u32 vcc, %[tgB], v167\n\t" \
  "v_cmpx_eq_u32 vcc, %[tgA], v169\n\t" \
  "v_cmpx_eq_u32 vcc, %[tgB], v171\n\t" \
  "v_cmpx_eq_u32 vcc, %[tgA], v173\n\t" \
  "v_cmpx_eq_u32 vcc, %[tgB], v175\n\t" \
  "s_cmp_eq_u64 exec, -1\n\t" \
  "s_mov_b64 exec, -1\n\t" \
  "s_cbranch_scc1 Lq2_%=\n\t" \
  "s_sub_i32 %[bud], %[bud], 16\n\t" \
  "s_cmp_lt_i32 %[bud], 0\n\t" \
  "s_cbranch_scc1 Lq2_%=\n\t" \
  "s_sleep 4\n\t" \
  "s_mov_b64 exec, %[em0]\n\t" \
  "s_cbranch_execz Lpb_%=\n\t" \
  "buffer_load_dwordx4 v[160:163], %[plo], %[srd], %[sl0] offen sc1\n\t" \
  "s_mov_b64 exec, %[em1]\n\t" \
  "s_cbranch_execz Lpb_%=\n\t" \
  "buffer_load_dwordx4 v[164:167], %[plo], %[srd], %[sl1] offen sc1\n\t" \
  "s_mov_b64 exec, %[em2]\n\t" \
  "s_cbranch_execz Lpb_%=\n\t" \
  "buffer_load_dwordx4 v[168:171], %[plo], %[srd], %[sl2] offen sc1\n\t" \
  "s_mov_b64 exec, %[em3]\n\t" \
  "s_cbranch_execz Lpb_%=\n\t" \
  "buffer_load_dwordx4 v[172:175], %[plo], %[srd], %[sl3] offen sc1\n\t" \
  "Lpb_%=:\n\t" \
  "s_branch Lq_%=\n\t" \
  "Lq2_%=:\n\t" \
  "s_mov_b32 %[scn], %[mcar]\n\t" \
  "s_mov_b32 m0, %[scn]\n\t" \
  "s_mov_b64 exec, 1\n\t" \
  "buffer_load_dword %[l16], %[srz], 0 offen lds\n\t" \
  "s_mov_b64 exec, -1\n\t" \
  "v_fma_f32 v244, -v231, v174, v168\n\t" \
  "v_fma_f32 v245, v231, v172, v170\n\t" \
  "v_fma_f32 v252, v230, v172, v244\n\t" \
  "v_fma_f32 v253, v230, v174, v245\n\t" \
  "v_fma_f32 v244, -v231, v253, v164\n\t" \
  "v_fma_f32 v245, v231, v252, v166\n\t" \
  "v_fma_f32 v252, v230, v252, v244\n\t" \
  "v_fma_f32 v253, v230, v253, v245\n\t" \
  "v_fma_f32 v244, -v231, v253, v160\n\t" \
  "v_fma_f32 v245, v231, v252, v162\n\t" \
  "v_fma_f32 v252, v230, v252, v244\n\t" \
  "v_fma_f32 v253, v230, v253, v245\n\t" \
  "v_mul_f32 v244, v233, v253\n\t" \
  "v_mul_f32 v245, v233, v252\n\t" \
  "v_fma_f32 v254, v232, v252, -v244\n\t" \
  "v_fma_f32 v255, v232, v253, v245\n\t" \
  "v_fma_f32 v252, v232, v252, -v244\n\t" \
  "v_fma_f32 v253, v232, v253, v245\n\t" \
  "s_nop 1\n\t" \
  "v_permlane16_swap_b32 v252, v254\n\t" \
  "v_permlane16_swap_b32 v253, v255\n\t" \
  "s_nop 0\n\t" \
  "v_add_f32 v244, v252, v254\n\t" \
  "v_add_f32 v245, v253, v255\n\t" \
  "v_add_f32 v252, v252, v254\n\t" \
  "v_add_f32 v253, v253, v255\n\t" \
  "v_mov_b32 v254, v244\n\t" \
  "v_mov_b32 v255, v245\n\t" \
  "s_nop 1\n\t" \
  "v_permlane32_swap_b32 v252, v254\n\t" \
  "v_permlane32_swap_b32 v253, v255\n\t" \
  "s_nop 0\n\t" \
  "v_add_f32 v244, v252, v254\n\t" \
  "v_add_f32 v245, v253, v255\n\t" \
  "v_add_f32 v252, v252, v254\n\t" \
  "v_add_f32 v253, v253, v255\n\t" \
  "v_mov_b32 v254, v244\n\t" \
  "v_mov_b32 v255, v245\n\t" \
  "v_mov_b32_dpp v254, v252 row_ror:12 row_mask:0x2 bank_mask:0xf\n\t" \
  "v_mov_b32_dpp v255, v253 row_ror:12 row_mask:0x2 bank_mask:0xf\n\t" \
  "v_mov_b32_dpp v254, v252 row_ror:8 row_mask:0x4 bank_mask:0xf\n\t" \
  "v_mov_b32_dpp v255, v253 row_ror:8 row_mask:0x4 bank_mask:0xf\n\t" \
  "v_mov_b32_dpp v254, v252 row_ror:4 row_mask:0x8 bank_mask:0xf\n\t" \
  "v_mov_b32_dpp v255, v253 row_ror:4 row_mask:0x8 bank_mask:0xf\n\t" \
  "s_nop 1\n\t" \
  "v_mov_b32_dpp v244, v254 row_newbcast:0 row_mask:0xf bank_mask:0xf\n\t" \
  "v_mov_b32_dpp v245, v254 row_newbcast:1 row_mask:0xf bank_mask:0xf\n\t" \
  "v_mov_b32_dpp v246, v254 row_newbcast:2 row_mask:0xf bank_mask:0xf\n\t" \
  "v_mov_b32_dpp v247, v254 row_newbcast:3 row_mask:0xf bank_mask:0xf\n\t" \
  "v_mov_b32_dpp v248, v255 row_newbcast:0 row_mask:0xf bank_mask:0xf\n\t" \
  "v_mov_b32_dpp v249, v255 row_newbcast:1 row_mask:0xf bank_mask:0xf\n\t" \
  "v_mov_b32_dpp v250, v255 row_newbcast:2 row_mask:0xf bank_mask:0xf\n\t" \
  "v_mov_b32_dpp v251, v255 row_newbcast:3 row_mask:0xf bank_mask:0xf\n\t" \
  "v_pk_fma_f32 v[236:237], v[184:185], v[244:245], v[236:237]\n\t" \
  "v_pk_fma_f32 v[238:239], v[186:187], v[246:247], v[238:239]\n\t" \
  "v_pk_fma_f32 v[240:241], v[184:185], v[248:249], v[240:241]\n\t" \
  "v_pk_fma_f32 v[242:243], v[186:187], v[250:251], v[242:243]\n\t" \
  "v_pk_fma_f32 v[236:237], v[188:189], v[248:249], v[236:237] neg_lo:[1,0,0] neg_hi:[1,0,0]\n\t" \
  "v_pk_fma_f32 v[238:239], v[190:191], v[250:251], v[238:239] neg_lo:[1,0,0] neg_hi:[1,0,0]\n\t" \
  "v_pk_fma_f32 v[240:241], v[188:189], v[244:245], v[240:241]\n\t" \
  "v_pk_fma_f32 v[242:243], v[190:191], v[246:247], v[242:243]\n\t" \
  "v_pk_fma_f32 v[128:129], v[180:181], v[240:241], v[128:129] neg_lo:[1,0,0] neg_hi:[1,0,0]\n\t" \
  "v_pk_fma_f32 v[130:131], v[182:183], v[242:243], v[130:131] neg_lo:[1,0,0] neg_hi:[1,0,0]\n\t" \
  "v_pk_fma_f32 v[96:97], v[180:181], v[236:237], v[96:97] neg_lo:[1,0,0] neg_hi:[1,0,0]\n\t" \
  "v_pk_fma_f32 v[98:99], v[182:183], v[238:239], v[98:99] neg_lo:[1,0,0] neg_hi:[1,0,0]\n\t" \
  "v_pk_fma_f32 v[128:129], v[176:177], v[236:237], v[128:129]\n\t" \
  "v_pk_fma_f32 v[130:131], v[178:179], v[238:239], v[130:131]\n\t" \
  "v_pk_fma_f32 v[96:97], v[176:177], v[240:241], v[96:97] neg_lo:[1,0,0] neg_hi:[1,0,0]\n\t" \
  "v_pk_fma_f32 v[98:99], v[178:179], v[242:243], v[98:99] neg_lo:[1,0,0] neg_hi:[1,0,0]\n\t" \
  "v_cvt_pk_bf16_f32 v160, v128, v129\n\t" \
  "v_cvt_pk_bf16_f32 v161, v130, v131\n\t" \
  "v_cvt_pk_bf16_f32 v162, v96, v97\n\t" \
  "v_cvt_pk_bf16_f32 v163, v98, v99\n\t" \
  "v_pk_fma_f32 v[132:133], v[180:181], v[96:97], v[132:133]\n\t" \
  "v_pk_fma_f32 v[134:135], v[182:183], v[98:99], v[134:135]\n\t" \
  "v_permlane16_swap_b32 v160, v162\n\t" \
  "v_permlane16_swap_b32 v161, v163\n\t" \
  "buffer_store_dwordx4 v[160:163], %[l16], %[srd], %[sh] offen offset:0 sc1\n\t" \
  "v_pk_fma_f32 v[100:101], v[180:181], v[128:129], v[100:101] neg_lo:[1,0,0] neg_hi:[1,0,0]\n\t" \
  "v_pk_fma_f32 v[102:103], v[182:183], v[130:131], v[102:103] neg_lo:[1,0,0] neg_hi:[1,0,0]\n\t" \
  "v_pk_fma_f32 v[132:133], v[176:177], v[128:129], v[132:133]\n\t" \
  "v_pk_fma_f32 v[134:135], v[178:179], v[130:131], v[134:135]\n\t" \
  "v_pk_fma_f32 v[100:101], v[176:177], v[96:97], v[100:101]\n\t" \
  "v_pk_fma_f32 v[102:103], v[178:179], v[98:99], v[102:103]\n\t" \
  "v_cvt_pk_bf16_f32 v164, v132, v133\n\t" \
  "v_cvt_pk_bf16_f32 v165, v134, v135\n\t" \
  "v_cvt_pk_bf16_f32 v166, v100, v101\n\t" \
  "v_cvt_pk_bf16_f32 v167, v102, v103\n\t" \
  "v_pk_fma_f32 v[136:137], v[180:181], v[100:101], v[136:137]\n\t" \
  "v_pk_fma_f32 v[138:139], v[182:183], v[102:103], v[138:139]\n\t" \
  "v_permlane16_swap_b32 v164, v166\n\t" \
  "v_permlane16_swap_b32 v165, v167\n\t" \
  "buffer_store_dwordx4 v[164:167], %[l16], %[srd], %[sh] offen offset:1024 sc1\n\t" \
  "v_pk_fma_f32 v[104:105], v[180:181], v[132:133], v[104:105] neg_lo:[1,0,0] neg_hi:[1,0,0]\n\t" \
  "v_pk_fma_f32 v[106:107], v[182:183], v[134:135], v[106:107] neg_lo:[1,0,0] neg_hi:[1,0,0]\n\t" \
  "v_pk_fma_f32 v[136:137], v[176:177], v[132:133], v[136:137]\n\t" \
  "v_pk_fma_f32 v[138:139], v[178:179], v[134:135], v[138:139]\n\t" \
  "v_pk_fma_f32 v[104:105], v[176:177], v[100:101], v[104:105]\n\t" \
  "v_pk_fma_f32 v[106:107], v[178:179], v[102:103], v[106:107]\n\t" \
  "v_cvt_pk_bf16_f32 v168, v136, v137\n\t" \
  "v_cvt_pk_bf16_f32 v169, v138, v139\n\t" \
  "v_cvt_pk_bf16_f32 v170, v104, v105\n\t" \
  "v_cvt_pk_bf16_f32 v171, v106, v107\n\t" \
  "v_pk_fma_f32 v[140:141], v[180:181], v[104:105], v[140:141]\n\t" \
  "v_pk_fma_f32 v[142:143], v[182:183], v[106:107], v[142:143]\n\t" \
  "v_permlane16_swap_b32 v168, v170\n\t" \
  "v_permlane16_swap_b32 v169, v171\n\t" \
  "buffer_store_dwordx4 v[168:171], %[l16], %[srd], %[sh] offen offset:2048 sc1\n\t" \
  "v_pk_fma_f32 v[108:109], v[180:181], v[136:137], v[108:109] neg_lo:[1,0,0] neg_hi:[1,0,0]\n\t" \
  "v_pk_fma_f32 v[110:111], v[182:183], v[138:139], v[110:111] neg_lo:[1,0,0] neg_hi:[1,0,0]\n\t" \
  "v_pk_fma_f32 v[140:141], v[176:177], v[136:137], v[140:141]\n\t" \
  "v_pk_fma_f32 v[142:143], v[178:179], v[138:139], v[142:143]\n\t" \
  "v_pk_fma_f32 v[108:109], v[176:177], v[104:105], v[108:109]\n\t" \
  "v_pk_fma_f32 v[110:111], v[178:179], v[106:107], v[110:111]\n\t" \
  "v_cvt_pk_bf16_f32 v172, v140, v141\n\t" \
  "v_cvt_pk_bf16_f32 v173, v142, v143\n\t" \
  "v_cvt_pk_bf16_f32 v174, v108, v109\n\t" \
  "v_cvt_pk_bf16_f32 v175, v110, v111\n\t" \
  "v_pk_fma_f32 v[144:145], v[180:181], v[108:109], v[144:145]\n\t" \
  "v_pk_fma_f32 v[146:147], v[182:183], v[110:111], v[146:147]\n\t" \
  "v_permlane16_swap_b32 v172, v174\n\t" \
  "v_permlane16_swap_b32 v173, v175\n\t" \
  "buffer_store_dwordx4 v[172:175], %[l16], %[srd], %[sh] offen offset:3072 sc1\n\t" \
  "s_add_u32 %[sh], %[sh], 0x1000\n\t" \
  "v_pk_fma_f32 v[112:113], v[180:181], v[140:141], v[112:113] neg_lo:[1,0,0] neg_hi:[1,0,0]\n\t" \
  "v_pk_fma_f32 v[114:115], v[182:183], v[142:143], v[114:115] neg_lo:[1,0,0] neg_hi:[1,0,0]\n\t" \
  "v_pk_fma_f32 v[144:145], v[176:177], v[140:141], v[144:145]\n\t" \
  "v_pk_fma_f32 v[146:147], v[178:179], v[142:143], v[146:147]\n\t" \
  "v_pk_fma_f32 v[112:113], v[176:177], v[108:109], v[112:113]\n\t" \
  "v_pk_fma_f32 v[114:115], v[178:179], v[110:111], v[114:115]\n\t" \
  "v_cvt_pk_bf16_f32 v192, v144, v145\n\t" \
  "v_cvt_pk_bf16_f32 v193, v146, v147\n\t" \
  "v_cvt_pk_bf16_f32 v194, v112, v113\n\t" \
  "v_cvt_pk_bf16_f32 v195, v114, v115\n\t" \
  "v_pk_fma_f32 v[148:149], v[180:181], v[112:113], v[148:149]\n\t" \
  "v_pk_fma_f32 v[150:151], v[182:183], v[114:115], v[150:151]\n\t" \
  "v_permlane16_swap_b32 v192, v194\n\t" \
  "v_permlane16_swap_b32 v193, v195\n\t" \
  "buffer_store_dwordx4 v[192:195], %[l16], %[srd], %[sh] offen offset:0 sc1\n\t" \
  "v_pk_fma_f32 v[116:117], v[180:181], v[144:145], v[116:117] neg_lo:[1,0,0] neg_hi:[1,0,0]\n\t" \
  "v_pk_fma_f32 v[118:119], v[182:183], v[146:147], v[118:119] neg_lo:[1,0,0] neg_hi:[1,0,0]\n\t" \
  "v_pk_fma_f32 v[148:149], v[176:177], v[144:145], v[148:149]\n\t" \
  "v_pk_fma_f32 v[150:151], v[178:179], v[146:147], v[150:151]\n\t" \
  "v_pk_fma_f32 v[116:117], v[176:177], v[112:113], v[116:117]\n\t" \
  "v_pk_fma_f32 v[118:119], v[178:179], v[114:115], v[118:119]\n\t" \
  "v_cvt_pk_bf16_f32 v196, v148, v149\n\t" \
  "v_cvt_pk_bf16_f32 v197, v150, v151\n\t" \
  "v_cvt_pk_bf16_f32 v198, v116, v117\n\t" \
  "v_cvt_pk_bf16_f32 v199, v118, v119\n\t" \
  "v_pk_fma_f32 v[152:153], v[180:181], v[116:117], v[152:153]\n\t" \
  "v_pk_fma_f32 v[154:155], v[182:183], v[118:119], v[154:155]\n\t" \
  "v_permlane16_swap_b32 v196, v198\n\t" \
  "v_permlane16_swap_b32 v197, v199\n\t" \
  "buffer_store_dwordx4 v[196:199], %[l16], %[srd], %[sh] offen offset:1024 sc1\n\t" \
  "v_pk_fma_f32 v[120:121], v[180:181], v[148:149], v[120:121] neg_lo:[1,0,0] neg_hi:[1,0,0]\n\t" \
  "v_pk_fma_f32 v[122:123], v[182:183], v[150:151], v[122:123] neg_lo:[1,0,0] neg_hi:[1,0,0]\n\t" \
  "v_pk_fma_f32 v[152:153], v[176:177], v[148:149], v[152:153]\n\t" \
  "v_pk_fma_f32 v[154:155], v[178:179], v[150:151], v[154:155]\n\t" \
  "v_pk_fma_f32 v[120:121], v[176:177], v[116:117], v[120:121]\n\t" \
  "v_pk_fma_f32 v[122:123], v[178:179], v[118:119], v[122:123]\n\t" \
  "v_cvt_pk_bf16_f32 v200, v152, v153\n\t" \
  "v_cvt_pk_bf16_f32 v201, v154, v155\n\t" \
  "v_cvt_pk_bf16_f32 v202, v120, v121\n\t" \
  "v_cvt_pk_bf16_f32 v203, v122, v123\n\t" \
  "v_pk_fma_f32 v[156:157], v[180:181], v[120:121], v[156:157]\n\t" \
  "v_pk_fma_f32 v[158:159], v[182:183], v[122:123], v[158:159]\n\t" \
  "v_permlane16_swap_b32 v200, v202\n\t" \
  "v_permlane16_swap_b32 v201, v203\n\t" \
  "buffer_store_dwordx4 v[200:203], %[l16], %[srd], %[sh] offen offset:2048 sc1\n\t" \
  "v_pk_fma_f32 v[124:125], v[180:181], v[152:153], v[124:125] neg_lo:[1,0,0] neg_hi:[1,0,0]\n\t" \
  "v_pk_fma_f32 v[126:127], v[182:183], v[154:155], v[126:127] neg_lo:[1,0,0] neg_hi:[1,0,0]\n\t" \
  "v_pk_fma_f32 v[156:157], v[176:177], v[152:153], v[156:157]\n\t" \
  "v_pk_fma_f32 v[158:159], v[178:179], v[154:155], v[158:159]\n\t" \
  "v_pk_fma_f32 v[124:125], v[176:177], v[120:121], v[124:125]\n\t" \
  "v_pk_fma_f32 v[126:127], v[178:179], v[122:123], v[126:127]\n\t" \
  "v_cvt_pk_bf16_f32 v204, v156, v157\n\t" \
  "v_cvt_pk_bf16_f32 v205, v158, v159\n\t" \
  "v_cvt_pk_bf16_f32 v206, v124, v125\n\t" \
  "v_cvt_pk_bf16_f32 v207, v126, v127\n\t" \
  "s_nop 1\n\t" \
  "v_permlane16_swap_b32 v204, v206\n\t" \
  "v_permlane16_swap_b32 v205, v207\n\t" \
  "buffer_store_dwordx4 v[204:207], %[l16], %[srd], %[sh] offen offset:3072 sc1\n\t" \
  "s_add_u32 %[scn], %[scn], 8\n\t" \
  "s_mov_b32 m0, %[scn]\n\t" \
  "s_mov_b64 exec, 1\n\t" \
  "buffer_load_dword %[l16], %[srz], 0 offen lds\n\t" \
  "s_mov_b64 exec, -1\n\t" \
  "s_branch Lend_%=\n\t" \
  "Lx0_%=:\n\t" \
  "v_add_u32 v244, %[sx], %[xo]\n\t" \
  "v_xor_b32 v245, 16, v244\n\t" \
  "v_xor_b32 v246, 32, v244\n\t" \
  "v_xor_b32 v247, 48, v244\n\t" \
  "ds_read_b64 %[xe00], v244 offset:0\n\t" \
  "ds_read_b64 %[xe10], v244 offset:2048\n\t" \
  "ds_read_b64 %[xe01], v245 offset:128\n\t" \
  "ds_read_b64 %[xe11], v245 offset:2176\n\t" \
  "ds_read_b64 %[xe02], v246 offset:256\n\t" \
  "ds_read_b64 %[xe12], v246 offset:2304\n\t" \
  "ds_read_b64 %[xe03], v247 offset:384\n\t" \
  "ds_read_b64 %[xe13], v247 offset:2432\n\t" \
  "s_branch Lxb0_%=\n\t" \
  "Lsp1_%=:\n\t" \
  "ds_read_b64 v[224:225], %[fl]\n\t" \
  "s_sub_i32 %[bud], %[bud], 1\n\t" \
  "s_cmp_lt_i32 %[bud], 0\n\t" \
  "s_cbranch_scc1 Lb1_%=\n\t" \
  "s_waitcnt lgkmcnt(0)\n\t" \
  "v_or_b32 v224, v224, v225\n\t" \
  "s_nop 0\n\t" \
  "v_readfirstlane_b32 %[s0], v224\n\t" \
  "s_cmp_eq_u32 %[s0], 0\n\t" \
  "s_cbranch_scc0 Lsp1_%=\n\t" \
  "s_branch Lb1_%=\n\t" \
  "Lx1_%=:\n\t" \
  "v_add_u32 v244, %[sx], %[xo]\n\t" \
  "v_xor_b32 v245, 16, v244\n\t" \
  "v_xor_b32 v246, 32, v244\n\t" \
  "v_xor_b32 v247, 48, v244\n\t" \
  "ds_read_b64 %[xe00], v244 offset:0\n\t" \
  "ds_read_b64 %[xe10], v244 offset:2048\n\t" \
  "ds_read_b64 %[xe01], v245 offset:128\n\t" \
  "ds_read_b64 %[xe11], v245 offset:2176\n\t" \
  "ds_read_b64 %[xe02], v246 offset:256\n\t" \
  "ds_read_b64 %[xe12], v246 offset:2304\n\t" \
  "ds_read_b64 %[xe03], v247 offset:384\n\t" \
  "ds_read_b64 %[xe13], v247 offset:2432\n\t" \
  "s_branch Lxb1_%=\n\t" \
  "Lsp2_%=:\n\t" \
  "ds_read_b64 v[224:225], %[fl]\n\t" \
  "s_sub_i32 %[bud], %[bud], 1\n\t" \
  "s_cmp_lt_i32 %[bud], 0\n\t" \
  "s_cbranch_scc1 Lb2_%=\n\t" \
  "s_waitcnt lgkmcnt(0)\n\t" \
  "v_or_b32 v224, v224, v225\n\t" \
  "s_nop 0\n\t" \
  "v_readfirstlane_b32 %[s0], v224\n\t" \
  "s_cmp_eq_u32 %[s0], 0\n\t" \
  "s_cbranch_scc0 Lsp2_%=\n\t" \
  "s_branch Lb2_%=\n\t" \
  "Lend_%=:\n\t" \
  "s_nop 7\n\t" \
  "s_nop 7\n\t"
#define ASM_MB \
  "s_nop 4\n\t" \
  "s_mov_b32 %[sg], 0\n\t" \
  "s_mov_b32 m0, %[scn]\n\t" \
  "Lc0_%=:\n\t" \
  "ds_read_b32 v192, %[f9]\n\t" \
  "s_sub_i32 %[bud], %[bud], 1\n\t" \
  "s_cmp_lt_i32 %[bud], 0\n\t" \
  "s_cbranch_scc1 Lc0b_%=\n\t" \
  "s_waitcnt lgkmcnt(0)\n\t" \
  "v_cmp_ne_u32 vcc, 0, v192\n\t" \
  "s_cbranch_vccnz Lc0_%=\n\t" \
  "Lc0b_%=:\n\t" \
  "ds_read_b128 v[128:131], %[pc0] offset:0\n\t" \
  "ds_read_b128 v[132:135], %[pc0] offset:2048\n\t" \
  "ds_read_b128 v[136:139], %[pc0] offset:4096\n\t" \
  "ds_read_b128 v[140:143], %[pc0] offset:6144\n\t" \
  "ds_read_b128 v[144:147], %[pc1] offset:0\n\t" \
  "ds_read_b128 v[148:151], %[pc1] offset:2048\n\t" \
  "ds_read_b128 v[152:155], %[pc1] offset:4096\n\t" \
  "ds_read_b128 v[156:159], %[pc1] offset:6144\n\t" \
  "s_cmp_eq_u32 %[w], 0\n\t" \
  "s_cbranch_scc0 Lw_%=\n\t" \
  "Lf_%=:\n\t" \
  "v_mov_b32 v196, 0\n\t" \
  "s_mov_b64 exec, 1\n\t" \
  "buffer_atomic_or v196, off, %[srd], %[sf] sc0 sc1\n\t" \
  "s_mov_b64 exec, -1\n\t" \
  "s_sub_i32 %[bud], %[bud], 16\n\t" \
  "s_cmp_lt_i32 %[bud], 0\n\t" \
  "s_cbranch_scc1 Lf2_%=\n\t" \
  "s_waitcnt vmcnt(0)\n\t" \
  "v_readfirstlane_b32 %[s0], v196\n\t" \
  "s_cmp_lg_u32 %[s0], 32\n\t" \
  "s_cbranch_scc0 Lf2_%=\n\t" \
  "s_sleep 1\n\t" \
  "s_branch Lf_%=\n\t" \
  "Lf2_%=:\n\t" \
  "v_mov_b32 v192, 0\n\t" \
  "v_mov_b32 v193, 0\n\t" \
  "ds_write_b64 %[hr], v[192:193]\n\t" \
  "Lf3_%=:\n\t" \
  "s_setprio 2\n\t" \
  "buffer_load_dwordx4 v[64:67], %[vo], %[srd], %[h0] offen offset:0 sc1\n\t" \
  "buffer_load_dwordx4 v[68:71], %[vo], %[srd], %[h0] offen offset:1024 sc1\n\t" \
  "buffer_load_dwordx4 v[72:75], %[vo], %[srd], %[h1] offen offset:0 sc1\n\t" \
  "buffer_load_dwordx4 v[76:79], %[vo], %[srd], %[h1] offen offset:1024 sc1\n\t" \
  "buffer_load_dwordx4 v[80:83], %[vo], %[srd], %[h2] offen offset:0 sc1\n\t" \
  "buffer_load_dword v194, off, %[srz], 0\n\t" \
  "buffer_load_dwordx4 v[84:87], %[vo], %[srd], %[h2] offen offset:1024 sc1\n\t" \
  "buffer_load_dwordx4 v[88:91], %[vo], %[srd], %[h3] offen offset:0 sc1\n\t" \
  "buffer_load_dwordx4 v[92:95], %[vo], %[srd], %[h3] offen offset:1024 sc1\n\t" \
  "buffer_load_dwordx4 v[96:99], %[vo], %[srd], %[h4] offen offset:0 sc1\n\t" \
  "buffer_load_dwordx4 v[100:103], %[vo], %[srd], %[h4] offen offset:1024 sc1\n\t" \
  "buffer_load_dwordx4 v[104:107], %[vo], %[srd], %[h5] offen offset:0 sc1\n\t" \
  "buffer_load_dwordx4 v[108:111], %[vo], %[srd], %[h5] offen offset:1024 sc1\n\t" \
  "buffer_load_dwordx4 v[112:115], %[vo], %[srd], %[h6] offen offset:0 sc1\n\t" \
  "buffer_load_dword v194, off, %[srz], 0\n\t" \
  "buffer_load_dwordx4 v[116:119], %[vo], %[srd], %[h6] offen offset:1024 sc1\n\t" \
  "s_waitcnt lgkmcnt(0)\n\t" \
  "Lloop_%=:\n\t" \
  "s_waitcnt vmcnt(14) lgkmcnt(4)\n\t" \
  "v_mfma_f32_16x16x32_bf16 %[c00], v[64:67], v[128:131], %[c00]\n\t" \
  "buffer_load_dwordx4 v[120:123], %[vo], %[srd], %[h7] offen offset:0 sc1\n\t" \
  "v_mfma_f32_16x16x32_bf16 %[c10], v[68:71], v[128:131], %[c10]\n\t" \
  "ds_read_b128 v[160:163], %[pc2] offset:0\n\t" \
  "v_add_u32 %[pc0], %[k2], %[pc0]\n\t" \
  "v_mfma_f32_16x16x32_bf16 %[c01], v[64:67], v[132:135], %[c01]\n\t" \
  "v_mfma_f32_16x16x32_bf16 %[c11], v[68:71], v[132:135], %[c11]\n\t" \
  "ds_read_b128 v[164:167], %[pc2] offset:2048\n\t" \
  "v_mfma_f32_16x16x32_bf16 %[c02], v[64:67], v[136:139], %[c02]\n\t" \
  "buffer_load_dwordx4 v[124:127], %[vo], %[srd], %[h7] offen offset:1024 sc1\n\t" \
  "v_mfma_f32_16x16x32_bf16 %[c12], v[68:71], v[136:139], %[c12]\n\t" \
  "ds_read_b128 v[168:171], %[pc2] offset:4096\n\t" \
  "v_mfma_f32_16x16x32_bf16 %[c03], v[64:67], v[140:143], %[c03]\n\t" \
  "v_add_u32 %[vo], 0x10000, %[vo]\n\t" \
  "v_and_b32 %[vo], 0x7ffff, %[vo]\n\t" \
  "v_mfma_f32_16x16x32_bf16 %[c13], v[68:71], v[140:143], %[c13]\n\t" \
  "ds_read_b128 v[172:175], %[pc2] offset:6144\n\t" \
  "s_waitcnt vmcnt(14) lgkmcnt(4)\n\t" \
  "v_mfma_f32_16x16x32_bf16 %[c00], v[72:75], v[144:147], %[c00]\n\t" \
  "buffer_load_dwordx4 v[64:67], %[vo], %[srd], %[h0] offen offset:0 sc1\n\t" \
  "v_mfma_f32_16x16x32_bf16 %[c10], v[76:79], v[144:147], %[c10]\n\t" \
  "ds_read_b128 v[176:179], %[pc3] offset:0\n\t" \
  "v_add_u32 %[pc1], %[k2], %[pc1]\n\t" \
  "v_mfma_f32_16x16x32_bf16 %[c01], v[72:75], v[148:151], %[c01]\n\t" \
  "s_cmp_eq_u32 %[sg], 2\n\t" \
  "s_cbranch_scc1 Lh_%=\n\t" \
  "Lhb_%=:\n\t" \
  "v_mfma_f32_16x16x32_bf16 %[c11], v[76:79], v[148:151], %[c11]\n\t" \
  "ds_read_b128 v[180:183], %[pc3] offset:2048\n\t" \
  "v_mfma_f32_16x16x32_bf16 %[c02], v[72:75], v[152:155], %[c02]\n\t" \
  "buffer_load_dwordx4 v[68:71], %[vo], %[srd], %[h0] offen offset:1024 sc1\n\t" \
  "v_mfma_f32_16x16x32_bf16 %[c12], v[76:79], v[152:155], %[c12]\n\t" \
  "ds_read_b128 v[184:187], %[pc3] offset:4096\n\t" \
  "v_mfma_f32_16x16x32_bf16 %[c03], v[72:75], v[156:159], %[c03]\n\t" \
  "v_mfma_f32_16x16x32_bf16 %[c13], v[76:79], v[156:159], %[c13]\n\t" \
  "ds_read_b128 v[188:191], %[pc3] offset:6144\n\t" \
  "s_waitcnt vmcnt(13) lgkmcnt(4)\n\t" \
  "v_mfma_f32_16x16x32_bf16 %[c00], v[80:83], v[160:163], %[c00]\n\t" \
  "buffer_load_dwordx4 v[72:75], %[vo], %[srd], %[h1] offen offset:0 sc1\n\t" \
  "v_mfma_f32_16x16x32_bf16 %[c10], v[84:87], v[160:163], %[c10]\n\t" \
  "ds_read_b128 v[128:131], %[pc0] offset:0\n\t" \
  "v_add_u32 %[pc2], %[k2], %[pc2]\n\t" \
  "v_mfma_f32_16x16x32_bf16 %[c01], v[80:83], v[164:167], %[c01]\n\t" \
  "v_mfma_f32_16x16x32_bf16 %[c11], v[84:87], v[164:167], %[c11]\n\t" \
  "ds_read_b128 v[132:135], %[pc0] offset:2048\n\t" \
  "v_mfma_f32_16x16x32_bf16 %[c02], v[80:83], v[168:171], %[c02]\n\t" \
  "buffer_load_dwordx4 v[76:79], %[vo], %[srd], %[h1] offen offset:1024 sc1\n\t" \
  "v_mfma_f32_16x16x32_bf16 %[c12], v[84:87], v[168:171], %[c12]\n\t" \
  "ds_read_b128 v[136:139], %[pc0] offset:4096\n\t" \
  "v_mfma_f32_16x16x32_bf16 %[c03], v[80:83], v[172:175], %[c03]\n\t" \
  "v_mfma_f32_16x16x32_bf16 %[c13], v[84:87], v[172:175], %[c13]\n\t" \
  "ds_read_b128 v[140:143], %[pc0] offset:6144\n\t" \
  "s_waitcnt vmcnt(13) lgkmcnt(4)\n\t" \
  "v_mfma_f32_16x16x32_bf16 %[c00], v[88:91], v[176:179], %[c00]\n\t" \
  "buffer_load_dwordx4 v[80:83], %[vo], %[srd], %[h2] offen offset:0 sc1\n\t" \
  "v_mfma_f32_16x16x32_bf16 %[c10], v[92:95], v[176:179], %[c10]\n\t" \
  "ds_read_b128 v[144:147], %[pc1] offset:0\n\t" \
  "v_add_u32 %[pc3], %[k2], %[pc3]\n\t" \
  "v_mfma_f32_16x16x32_bf16 %[c01], v[88:91], v[180:183], %[c01]\n\t" \
  "s_mov_b64 exec, 1\n\t" \
  "buffer_load_dword off, %[srz], 0 lds\n\t" \
  "s_mov_b64 exec, -1\n\t" \
  "v_mfma_f32_16x16x32_bf16 %[c11], v[92:95], v[180:183], %[c11]\n\t" \
  "ds_read_b128 v[148:151], %[pc1] offset:2048\n\t" \
  "v_mfma_f32_16x16x32_bf16 %[c02], v[88:91], v[184:187], %[c02]\n\t" \
  "buffer_load_dwordx4 v[84:87], %[vo], %[srd], %[h2] offen offset:1024 sc1\n\t" \
  "v_mfma_f32_16x16x32_bf16 %[c12], v[92:95], v[184:187], %[c12]\n\t" \
  "ds_read_b128 v[152:155], %[pc1] offset:4096\n\t" \
  "s_add_u32 m0, m0, 16\n\t" \
  "v_mfma_f32_16x16x32_bf16 %[c03], v[88:91], v[188:191], %[c03]\n\t" \
  "v_mfma_f32_16x16x32_bf16 %[c13], v[92:95], v[188:191], %[c13]\n\t" \
  "ds_read_b128 v[156:159], %[pc1] offset:6144\n\t" \
  "s_waitcnt vmcnt(14) lgkmcnt(4)\n\t" \
  "v_mfma_f32_16x16x32_bf16 %[c00], v[96:99], v[128:131], %[c00]\n\t" \
  "buffer_load_dwordx4 v[88:91], %[vo], %[srd], %[h3] offen offset:0 sc1\n\t" \
  "v_mfma_f32_16x16x32_bf16 %[c10], v[100:103], v[128:131], %[c10]\n\t" \
  "ds_read_b128 v[160:163], %[pc2] offset:0\n\t" \
  "v_add_u32 %[pc0], %[k2], %[pc0]\n\t" \
  "v_mfma_f32_16x16x32_bf16 %[c01], v[96:99], v[132:135], %[c01]\n\t" \
  "v_mfma_f32_16x16x32_bf16 %[c11], v[100:103], v[132:135], %[c11]\n\t" \
  "ds_read_b128 v[164:167], %[pc2] offset:2048\n\t" \
  "v_mfma_f32_16x16x32_bf16 %[c02], v[96:99], v[136:139], %[c02]\n\t" \
  "buffer_load_dwordx4 v[92:95], %[vo], %[srd], %[h3] offen offset:1024 sc1\n\t" \
  "v_mfma_f32_16x16x32_bf16 %[c12], v[100:103], v[136:139], %[c12]\n\t" \
  "ds_read_b128 v[168:171], %[pc2] offset:4096\n\t" \
  "v_mfma_f32_16x16x32_bf16 %[c03], v[96:99], v[140:143], %[c03]\n\t" \
  "v_mfma_f32_16x16x32_bf16 %[c13], v[100:103], v[140:143], %[c13]\n\t" \
  "ds_read_b128 v[172:175], %[pc2] offset:6144\n\t" \
  "s_waitcnt vmcnt(14) lgkmcnt(4)\n\t" \
  "v_mfma_f32_16x16x32_bf16 %[c00], v[104:107], v[144:147], %[c00]\n\t" \
  "buffer_load_dwordx4 v[96:99], %[vo], %[srd], %[h4] offen offset:0 sc1\n\t" \
  "v_mfma_f32_16x16x32_bf16 %[c10], v[108:111], v[144:147], %[c10]\n\t" \
  "ds_read_b128 v[176:179], %[pc3] offset:0\n\t" \
  "v_add_u32 %[pc1], %[k2], %[pc1]\n\t" \
  "v_mfma_f32_16x16x32_bf16 %[c01], v[104:107], v[148:151], %[c01]\n\t" \
  "v_mfma_f32_16x16x32_bf16 %[c11], v[108:111], v[148:151], %[c11]\n\t" \
  "ds_read_b128 v[180:183], %[pc3] offset:2048\n\t" \
  "v_mfma_f32_16x16x32_bf16 %[c02], v[104:107], v[152:155], %[c02]\n\t" \
  "buffer_load_dwordx4 v[100:103], %[vo], %[srd], %[h4] offen offset:1024 sc1\n\t" \
  "v_mfma_f32_16x16x32_bf16 %[c12], v[108:111], v[152:155], %[c12]\n\t" \
  "ds_read_b128 v[184:187], %[pc3] offset:4096\n\t" \
  "v_mfma_f32_16x16x32_bf16 %[c03], v[104:107], v[156:159], %[c03]\n\t" \
  "v_mfma_f32_16x16x32_bf16 %[c13], v[108:111], v[156:159], %[c13]\n\t" \
  "ds_read_b128 v[188:191], %[pc3] offset:6144\n\t" \
  "s_waitcnt vmcnt(13) lgkmcnt(4)\n\t" \
  "v_mfma_f32_16x16x32_bf16 %[c00], v[112:115], v[160:163], %[c00]\n\t" \
  "buffer_load_dwordx4 v[104:107], %[vo], %[srd], %[h5] offen offset:0 sc1\n\t" \
  "v_mfma_f32_16x16x32_bf16 %[c10], v[116:119], v[160:163], %[c10]\n\t" \
  "ds_read_b128 v[128:131], %[pc0] offset:0\n\t" \
  "v_add_u32 %[pc2], %[k2], %[pc2]\n\t" \
  "v_mfma_f32_16x16x32_bf16 %[c01], v[112:115], v[164:167], %[c01]\n\t" \
  "v_mfma_f32_16x16x32_bf16 %[c11], v[116:119], v[164:167], %[c11]\n\t" \
  "ds_read_b128 v[132:135], %[pc0] offset:2048\n\t" \
  "v_mfma_f32_16x16x32_bf16 %[c02], v[112:115], v[168:171], %[c02]\n\t" \
  "buffer_load_dwordx4 v[108:111], %[vo], %[srd], %[h5] offen offset:1024 sc1\n\t" \
  "v_mfma_f32_16x16x32_bf16 %[c12], v[116:119], v[168:171], %[c12]\n\t" \
  "ds_read_b128 v[136:139], %[pc0] offset:4096\n\t" \
  "v_mfma_f32_16x16x32_bf16 %[c03], v[112:115], v[172:175], %[c03]\n\t" \
  "v_mfma_f32_16x16x32_bf16 %[c13], v[116:119], v[172:175], %[c13]\n\t" \
  "ds_read_b128 v[140:143], %[pc0] offset:6144\n\t" \
  "s_waitcnt vmcnt(13) lgkmcnt(4)\n\t" \
  "v_mfma_f32_16x16x32_bf16 %[c00], v[120:123], v[176:179], %[c00]\n\t" \
  "buffer_load_dwordx4 v[112:115], %[vo], %[srd], %[h6] offen offset:0 sc1\n\t" \
  "v_mfma_f32_16x16x32_bf16 %[c10], v[124:127], v[176:179], %[c10]\n\t" \
  "ds_read_b128 v[144:147], %[pc1] offset:0\n\t" \
  "v_add_u32 %[pc3], %[k2], %[pc3]\n\t" \
  "v_mfma_f32_16x16x32_bf16 %[c01], v[120:123], v[180:183], %[c01]\n\t" \
  "s_mov_b64 exec, 1\n\t" \
  "buffer_load_dword off, %[srz], 0 lds\n\t" \
  "s_mov_b64 exec, -1\n\t" \
  "v_mfma_f32_16x16x32_bf16 %[c11], v[124:127], v[180:183], %[c11]\n\t" \
  "ds_read_b128 v[148:151], %[pc1] offset:2048\n\t" \
  "s_add_u32 %[sg], %[sg], 1\n\t" \
  "v_mfma_f32_16x16x32_bf16 %[c02], v[120:123], v[184:187], %[c02]\n\t" \
  "buffer_load_dwordx4 v[116:119], %[vo], %[srd], %[h6] offen offset:1024 sc1\n\t" \
  "v_mfma_f32_16x16x32_bf16 %[c12], v[124:127], v[184:187], %[c12]\n\t" \
  "ds_read_b128 v[152:155], %[pc1] offset:4096\n\t" \
  "s_add_u32 m0, m0, 16\n\t" \
  "v_mfma_f32_16x16x32_bf16 %[c03], v[120:123], v[188:191], %[c03]\n\t" \
  "s_cmp_eq_u32 %[sg], 3\n\t" \
  "s_cbranch_scc1 Lg_%=\n\t" \
  "Lgb_%=:\n\t" \
  "v_mfma_f32_16x16x32_bf16 %[c13], v[124:127], v[188:191], %[c13]\n\t" \
  "s_cmp_lt_u32 %[sg], 4\n\t" \
  "ds_read_b128 v[156:159], %[pc1] offset:6144\n\t" \
  "s_cbranch_scc1 Lloop_%=\n\t" \
  "s_mov_b32 %[s1], 0xfffe0000\n\t" \
  "Lloop2_%=:\n\t" \
  "s_waitcnt vmcnt(12) lgkmcnt(4)\n\t" \
  "v_mfma_f32_16x16x32_bf16 %[c00], v[64:67], v[128:131], %[c00]\n\t" \
  "ds_read_b64 v[192:193], %[fl] offset:0\n\t" \
  "buffer_load_dwordx4 v[120:123], %[vo], %[srd], %[h7] offen offset:0 sc1\n\t" \
  "v_mfma_f32_16x16x32_bf16 %[c10], v[68:71], v[128:131], %[c10]\n\t" \
  "ds_read_b128 v[160:163], %[pc2] offset:0\n\t" \
  "v_add_u32 %[pc0], %[s1], %[pc0]\n\t" \
  "v_mfma_f32_16x16x32_bf16 %[c01], v[64:67], v[132:135], %[c01]\n\t" \
  "v_mfma_f32_16x16x32_bf16 %[c11], v[68:71], v[132:135], %[c11]\n\t" \
  "ds_read_b128 v[164:167], %[pc2] offset:2048\n\t" \
  "v_mfma_f32_16x16x32_bf16 %[c02], v[64:67], v[136:139], %[c02]\n\t" \
  "buffer_load_dwordx4 v[124:127], %[vo], %[srd], %[h7] offen offset:1024 sc1\n\t" \
  "v_mfma_f32_16x16x32_bf16 %[c12], v[68:71], v[136:139], %[c12]\n\t" \
  "ds_read_b128 v[168:171], %[pc2] offset:4096\n\t" \
  "v_mfma_f32_16x16x32_bf16 %[c03], v[64:67], v[140:143], %[c03]\n\t" \
  "v_add_u32 %[vo], 0x10000, %[vo]\n\t" \
  "v_and_b32 %[vo], 0x7ffff, %[vo]\n\t" \
  "v_mfma_f32_16x16x32_bf16 %[c13], v[68:71], v[140:143], %[c13]\n\t" \
  "ds_read_b128 v[172:175], %[pc2] offset:6144\n\t" \
  "s_waitcnt vmcnt(12) lgkmcnt(4)\n\t" \
  "v_mfma_f32_16x16x32_bf16 %[c00], v[72:75], v[144:147], %[c00]\n\t" \
  "buffer_load_dwordx4 v[64:67], %[vo], %[srd], %[h0] offen offset:0 sc1\n\t" \
  "v_mfma_f32_16x16x32_bf16 %[c10], v[76:79], v[144:147], %[c10]\n\t" \
  "ds_read_b128 v[176:179], %[pc3] offset:0\n\t" \
  "v_add_u32 %[pc1], %[s1], %[pc1]\n\t" \
  "v_mfma_f32_16x16x32_bf16 %[c01], v[72:75], v[148:151], %[c01]\n\t" \
  "v_or_b32 v192, v192, v193\n\t" \
  "v_mfma_f32_16x16x32_bf16 %[c11], v[76:79], v[148:151], %[c11]\n\t" \
  "ds_read_b128 v[180:183], %[pc3] offset:2048\n\t" \
  "v_readfirstlane_b32 %[s0], v192\n\t" \
  "v_mfma_f32_16x16x32_bf16 %[c02], v[72:75], v[152:155], %[c02]\n\t" \
  "buffer_load_dwordx4 v[68:71], %[vo], %[srd], %[h0] offen offset:1024 sc1\n\t" \
  "v_mfma_f32_16x16x32_bf16 %[c12], v[76:79], v[152:155], %[c12]\n\t" \
  "ds_read_b128 v[184:187], %[pc3] offset:4096\n\t" \
  "v_mfma_f32_16x16x32_bf16 %[c03], v[72:75], v[156:159], %[c03]\n\t" \
  "s_cmp_eq_u32 %[s0], 0\n\t" \
  "s_cbranch_scc0 Lcb1_%=\n\t" \
  "Lcb1b_%=:\n\t" \
  "v_mfma_f32_16x16x32_bf16 %[c13], v[76:79], v[156:159], %[c13]\n\t" \
  "ds_read_b128 v[188:191], %[pc3] offset:6144\n\t" \
  "s_waitcnt vmcnt(12) lgkmcnt(4)\n\t" \
  "v_mfma_f32_16x16x32_bf16 %[c00], v[80:83], v[160:163], %[c00]\n\t" \
  "buffer_load_dwordx4 v[72:75], %[vo], %[srd], %[h1] offen offset:0 sc1\n\t" \
  "v_mfma_f32_16x16x32_bf16 %[c10], v[84:87], v[160:163], %[c10]\n\t" \
  "ds_read_b128 v[128:131], %[pc0] offset:0\n\t" \
  "v_add_u32 %[pc2], %[s1], %[pc2]\n\t" \
  "v_mfma_f32_16x16x32_bf16 %[c01], v[80:83], v[164:167], %[c01]\n\t" \
  "v_mfma_f32_16x16x32_bf16 %[c11], v[84:87], v[164:167], %[c11]\n\t" \
  "ds_read_b128 v[132:135], %[pc0] offset:2048\n\t" \
  "v_mfma_f32_16x16x32_bf16 %[c02], v[80:83], v[168:171], %[c02]\n\t" \
  "buffer_load_dwordx4 v[76:79], %[vo], %[srd], %[h1] offen offset:1024 sc1\n\t" \
  "v_mfma_f32_16x16x32_bf16 %[c12], v[84:87], v[168:171], %[c12]\n\t" \
  "ds_read_b128 v[136:139], %[pc0] offset:4096\n\t" \
  "v_mfma_f32_16x16x32_bf16 %[c03], v[80:83], v[172:175], %[c03]\n\t" \
  "v_mfma_f32_16x16x32_bf16 %[c13], v[84:87], v[172:175], %[c13]\n\t" \
  "ds_read_b128 v[140:143], %[pc0] offset:6144\n\t" \
  "s_waitcnt vmcnt(12) lgkmcnt(4)\n\t" \
  "v_mfma_f32_16x16x32_bf16 %[c00], v[88:91], v[176:179], %[c00]\n\t" \
  "buffer_load_dwordx4 v[80:83], %[vo], %[srd], %[h2] offen offset:0 sc1\n\t" \
  "v_mfma_f32_16x16x32_bf16 %[c10], v[92:95], v[176:179], %[c10]\n\t" \
  "ds_read_b128 v[144:147], %[pc1] offset:0\n\t" \
  "v_add_u32 %[pc3], %[s1], %[pc3]\n\t" \
  "v_mfma_f32_16x16x32_bf16 %[c01], v[88:91], v[180:183], %[c01]\n\t" \
  "v_mfma_f32_16x16x32_bf16 %[c11], v[92:95], v[180:183], %[c11]\n\t" \
  "ds_read_b128 v[148:151], %[pc1] offset:2048\n\t" \
  "v_mfma_f32_16x16x32_bf16 %[c02], v[88:91], v[184:187], %[c02]\n\t" \
  "buffer_load_dwordx4 v[84:87], %[vo], %[srd], %[h2] offen offset:1024 sc1\n\t" \
  "v_mfma_f32_16x16x32_bf16 %[c12], v[92:95], v[184:187], %[c12]\n\t" \
  "ds_read_b128 v[152:155], %[pc1] offset:4096\n\t" \
  "v_mfma_f32_16x16x32_bf16 %[c03], v[88:91], v[188:191], %[c03]\n\t" \
  "v_mfma_f32_16x16x32_bf16 %[c13], v[92:95], v[188:191], %[c13]\n\t" \
  "ds_read_b128 v[156:159], %[pc1] offset:6144\n\t" \
  "s_waitcnt vmcnt(12) lgkmcnt(4)\n\t" \
  "v_mfma_f32_16x16x32_bf16 %[c00], v[96:99], v[128:131], %[c00]\n\t" \
  "ds_read_b64 v[192:193], %[fl] offset:8\n\t" \
  "buffer_load_dwordx4 v[88:91], %[vo], %[srd], %[h3] offen offset:0 sc1\n\t" \
  "v_mfma_f32_16x16x32_bf16 %[c10], v[100:103], v[128:131], %[c10]\n\t" \
  "ds_read_b128 v[160:163], %[pc2] offset:0\n\t" \
  "v_add_u32 %[pc0], %[k2], %[pc0]\n\t" \
  "v_mfma_f32_16x16x32_bf16 %[c01], v[96:99], v[132:135], %[c01]\n\t" \
  "v_mfma_f32_16x16x32_bf16 %[c11], v[100:103], v[132:135], %[c11]\n\t" \
  "ds_read_b128 v[164:167], %[pc2] offset:2048\n\t" \
  "v_mfma_f32_16x16x32_bf16 %[c02], v[96:99], v[136:139], %[c02]\n\t" \
  "buffer_load_dwordx4 v[92:95], %[vo], %[srd], %[h3] offen offset:1024 sc1\n\t" \
  "v_mfma_f32_16x16x32_bf16 %[c12], v[100:103], v[136:139], %[c12]\n\t" \
  "ds_read_b128 v[168:171], %[pc2] offset:4096\n\t" \
  "v_mfma_f32_16x16x32_bf16 %[c03], v[96:99], v[140:143], %[c03]\n\t" \
  "v_mfma_f32_16x16x32_bf16 %[c13], v[100:103], v[140:143], %[c13]\n\t" \
  "ds_read_b128 v[172:175], %[pc2] offset:6144\n\t" \
  "s_waitcnt vmcnt(12) lgkmcnt(4)\n\t" \
  "v_mfma_f32_16x16x32_bf16 %[c00], v[104:107], v[144:147], %[c00]\n\t" \
  "buffer_load_dwordx4 v[96:99], %[vo], %[srd], %[h4] offen offset:0 sc1\n\t" \
  "v_mfma_f32_16x16x32_bf16 %[c10], v[108:111], v[144:147], %[c10]\n\t" \
  "ds_read_b128 v[176:179], %[pc3] offset:0\n\t" \
  "v_add_u32 %[pc1], %[k2], %[pc1]\n\t" \
  "v_mfma_f32_16x16x32_bf16 %[c01], v[104:107], v[148:151], %[c01]\n\t" \
  "v_or_b32 v192, v192, v193\n\t" \
  "v_mfma_f32_16x16x32_bf16 %[c11], v[108:111], v[148:151], %[c11]\n\t" \
  "ds_read_b128 v[180:183], %[pc3] offset:2048\n\t" \
  "v_readfirstlane_b32 %[s0], v192\n\t" \
  "v_mfma_f32_16x16x32_bf16 %[c02], v[104:107], v[152:155], %[c02]\n\t" \
  "buffer_load_dwordx4 v[100:103], %[vo], %[srd], %[h4] offen offset:1024 sc1\n\t" \
  "v_mfma_f32_16x16x32_bf16 %[c12], v[108:111], v[152:155], %[c12]\n\t" \
  "ds_read_b128 v[184:187], %[pc3] offset:4096\n\t" \
  "v_mfma_f32_16x16x32_bf16 %[c03], v[104:107], v[156:159], %[c03]\n\t" \
  "s_cmp_eq_u32 %[s0], 0\n\t" \
  "s_cbranch_scc0 Lcb5_%=\n\t" \
  "Lcb5b_%=:\n\t" \
  "v_mfma_f32_16x16x32_bf16 %[c13], v[108:111], v[156:159], %[c13]\n\t" \
  "ds_read_b128 v[188:191], %[pc3] offset:6144\n\t" \
  "s_waitcnt vmcnt(12) lgkmcnt(4)\n\t" \
  "v_mfma_f32_16x16x32_bf16 %[c00], v[112:115], v[160:163], %[c00]\n\t" \
  "buffer_load_dwordx4 v[104:107], %[vo], %[srd], %[h5] offen offset:0 sc1\n\t" \
  "v_mfma_f32_16x16x32_bf16 %[c10], v[116:119], v[160:163], %[c10]\n\t" \
  "ds_read_b128 v[128:131], %[pc0] offset:0\n\t" \
  "v_add_u32 %[pc2], %[k2], %[pc2]\n\t" \
  "v_mfma_f32_16x16x32_bf16 %[c01], v[112:115], v[164:167], %[c01]\n\t" \
  "v_add_u32 %[fl], 16, %[fl]\n\t" \
  "s_mov_b32 %[s1], %[k2]\n\t" \
  "v_mfma_f32_16x16x32_bf16 %[c11], v[116:119], v[164:167], %[c11]\n\t" \
  "ds_read_b128 v[132:135], %[pc0] offset:2048\n\t" \
  "v_mfma_f32_16x16x32_bf16 %[c02], v[112:115], v[168:171], %[c02]\n\t" \
  "buffer_load_dwordx4 v[108:111], %[vo], %[srd], %[h5] offen offset:1024 sc1\n\t" \
  "v_mfma_f32_16x16x32_bf16 %[c12], v[116:119], v[168:171], %[c12]\n\t" \
  "ds_read_b128 v[136:139], %[pc0] offset:4096\n\t" \
  "v_mfma_f32_16x16x32_bf16 %[c03], v[112:115], v[172:175], %[c03]\n\t" \
  "v_mfma_f32_16x16x32_bf16 %[c13], v[116:119], v[172:175], %[c13]\n\t" \
  "ds_read_b128 v[140:143], %[pc0] offset:6144\n\t" \
  "s_waitcnt vmcnt(12) lgkmcnt(4)\n\t" \
  "v_mfma_f32_16x16x32_bf16 %[c00], v[120:123], v[176:179], %[c00]\n\t" \
  "buffer_load_dwordx4 v[112:115], %[vo], %[srd], %[h6] offen offset:0 sc1\n\t" \
  "v_mfma_f32_16x16x32_bf16 %[c10], v[124:127], v[176:179], %[c10]\n\t" \
  "ds_read_b128 v[144:147], %[pc1] offset:0\n\t" \
  "v_add_u32 %[pc3], %[k2], %[pc3]\n\t" \
  "v_mfma_f32_16x16x32_bf16 %[c01], v[120:123], v[180:183], %[c01]\n\t" \
  "s_add_u32 %[sg], %[sg], 1\n\t" \
  "s_cmp_lt_u32 %[sg], 7\n\t" \
  "v_mfma_f32_16x16x32_bf16 %[c11], v[124:127], v[180:183], %[c11]\n\t" \
  "ds_read_b128 v[148:151], %[pc1] offset:2048\n\t" \
  "v_mfma_f32_16x16x32_bf16 %[c02], v[120:123], v[184:187], %[c02]\n\t" \
  "buffer_load_dwordx4 v[116:119], %[vo], %[srd], %[h6] offen offset:1024 sc1\n\t" \
  "v_mfma_f32_16x16x32_bf16 %[c12], v[124:127], v[184:187], %[c12]\n\t" \
  "ds_read_b128 v[152:155], %[pc1] offset:4096\n\t" \
  "v_mfma_f32_16x16x32_bf16 %[c03], v[120:123], v[188:191], %[c03]\n\t" \
  "v_mfma_f32_16x16x32_bf16 %[c13], v[124:127], v[188:191], %[c13]\n\t" \
  "ds_read_b128 v[156:159], %[pc1] offset:6144\n\t" \
  "s_cbranch_scc1 Lloop2_%=\n\t" \
  "s_waitcnt vmcnt(12) lgkmcnt(4)\n\t" \
  "v_mfma_f32_16x16x32_bf16 %[c00], v[64:67], v[128:131], %[c00]\n\t" \
  "ds_read_b64 v[192:193], %[fl] offset:0\n\t" \
  "buffer_load_dwordx4 v[120:123], %[vo], %[srd], %[h7] offen offset:0 sc1\n\t" \
  "v_mfma_f32_16x16x32_bf16 %[c10], v[68:71], v[128:131], %[c10]\n\t" \
  "ds_read_b128 v[160:163], %[pc2] offset:0\n\t" \
  "v_add_u32 %[pc0], %[k2], %[pc0]\n\t" \
  "v_mfma_f32_16x16x32_bf16 %[c01], v[64:67], v[132:135], %[c01]\n\t" \
  "v_mfma_f32_16x16x32_bf16 %[c11], v[68:71], v[132:135], %[c11]\n\t" \
  "ds_read_b128 v[164:167], %[pc2] offset:2048\n\t" \
  "v_mfma_f32_16x16x32_bf16 %[c02], v[64:67], v[136:139], %[c02]\n\t" \
  "buffer_load_dwordx4 v[124:127], %[vo], %[srd], %[h7] offen offset:1024 sc1\n\t" \
  "v_mfma_f32_16x16x32_bf16 %[c12], v[68:71], v[136:139], %[c12]\n\t" \
  "ds_read_b128 v[168:171], %[pc2] offset:4096\n\t" \
  "v_mfma_f32_16x16x32_bf16 %[c03], v[64:67], v[140:143], %[c03]\n\t" \
  "v_mfma_f32_16x16x32_bf16 %[c13], v[68:71], v[140:143], %[c13]\n\t" \
  "ds_read_b128 v[172:175], %[pc2] offset:6144\n\t" \
  "s_waitcnt vmcnt(12) lgkmcnt(4)\n\t" \
  "v_mfma_f32_16x16x32_bf16 %[c00], v[72:75], v[144:147], %[c00]\n\t" \
  "v_mfma_f32_16x16x32_bf16 %[c10], v[76:79], v[144:147], %[c10]\n\t" \
  "ds_read_b128 v[176:179], %[pc3] offset:0\n\t" \
  "v_add_u32 %[pc1], %[k2], %[pc1]\n\t" \
  "v_mfma_f32_16x16x32_bf16 %[c01], v[72:75], v[148:151], %[c01]\n\t" \
  "v_or_b32 v192, v192, v193\n\t" \
  "v_mfma_f32_16x16x32_bf16 %[c11], v[76:79], v[148:151], %[c11]\n\t" \
  "ds_read_b128 v[180:183], %[pc3] offset:2048\n\t" \
  "v_readfirstlane_b32 %[s0], v192\n\t" \
  "v_mfma_f32_16x16x32_bf16 %[c02], v[72:75], v[152:155], %[c02]\n\t" \
  "v_mfma_f32_16x16x32_bf16 %[c12], v[76:79], v[152:155], %[c12]\n\t" \
  "ds_read_b128 v[184:187], %[pc3] offset:4096\n\t" \
  "v_mfma_f32_16x16x32_bf16 %[c03], v[72:75], v[156:159], %[c03]\n\t" \
  "s_cmp_eq_u32 %[s0], 0\n\t" \
  "s_cbranch_scc0 Lcf1_%=\n\t" \
  "Lcf1b_%=:\n\t" \
  "v_mfma_f32_16x16x32_bf16 %[c13], v[76:79], v[156:159], %[c13]\n\t" \
  "ds_read_b128 v[188:191], %[pc3] offset:6144\n\t" \
  "s_waitcnt vmcnt(10) lgkmcnt(4)\n\t" \
  "v_mfma_f32_16x16x32_bf16 %[c00], v[80:83], v[160:163], %[c00]\n\t" \
  "v_mfma_f32_16x16x32_bf16 %[c10], v[84:87], v[160:163], %[c10]\n\t" \
  "ds_read_b128 v[128:131], %[pc0] offset:0\n\t" \
  "v_add_u32 %[pc2], %[k2], %[pc2]\n\t" \
  "v_mfma_f32_16x16x32_bf16 %[c01], v[80:83], v[164:167], %[c01]\n\t" \
  "v_mfma_f32_16x16x32_bf16 %[c11], v[84:87], v[164:167], %[c11]\n\t" \
  "ds_read_b128 v[132:135], %[pc0] offset:2048\n\t" \
  "v_mfma_f32_16x16x32_bf16 %[c02], v[80:83], v[168:171], %[c02]\n\t" \
  "v_mfma_f32_16x16x32_bf16 %[c12], v[84:87], v[168:171], %[c12]\n\t" \
  "ds_read_b128 v[136:139], %[pc0] offset:4096\n\t" \
  "v_mfma_f32_16x16x32_bf16 %[c03], v[80:83], v[172:175], %[c03]\n\t" \
  "v_mfma_f32_16x16x32_bf16 %[c13], v[84:87], v[172:175], %[c13]\n\t" \
  "ds_read_b128 v[140:143], %[pc0] offset:6144\n\t" \
  "s_waitcnt vmcnt(8) lgkmcnt(4)\n\t" \
  "v_mfma_f32_16x16x32_bf16 %[c00], v[88:91], v[176:179], %[c00]\n\t" \
  "v_mfma_f32_16x16x32_bf16 %[c10], v[92:95], v[176:179], %[c10]\n\t" \
  "ds_read_b128 v[144:147], %[pc1] offset:0\n\t" \
  "v_add_u32 %[pc3], %[k2], %[pc3]\n\t" \
  "v_mfma_f32_16x16x32_bf16 %[c01], v[88:91], v[180:183], %[c01]\n\t" \
  "v_mfma_f32_16x16x32_bf16 %[c11], v[92:95], v[180:183], %[c11]\n\t" \
  "ds_read_b128 v[148:151], %[pc1] offset:2048\n\t" \
  "v_mfma_f32_16x16x32_bf16 %[c02], v[88:91], v[184:187], %[c02]\n\t" \
  "v_mfma_f32_16x16x32_bf16 %[c12], v[92:95], v[184:187], %[c12]\n\t" \
  "ds_read_b128 v[152:155], %[pc1] offset:4096\n\t" \
  "v_mfma_f32_16x16x32_bf16 %[c03], v[88:91], v[188:191], %[c03]\n\t" \
  "v_mfma_f32_16x16x32_bf16 %[c13], v[92:95], v[188:191], %[c13]\n\t" \
  "ds_read_b128 v[156:159], %[pc1] offset:6144\n\t" \
  "s_waitcnt vmcnt(6) lgkmcnt(4)\n\t" \
  "v_mfma_f32_16x16x32_bf16 %[c00], v[96:99], v[128:131], %[c00]\n\t" \
  "v_mfma_f32_16x16x32_bf16 %[c10], v[100:103], v[128:131], %[c10]\n\t" \
  "ds_read_b128 v[160:163], %[pc2] offset:0\n\t" \
  "v_mfma_f32_16x16x32_bf16 %[c01], v[96:99], v[132:135], %[c01]\n\t" \
  "v_mfma_f32_16x16x32_bf16 %[c11], v[100:103], v[132:135], %[c11]\n\t" \
  "ds_read_b128 v[164:167], %[pc2] offset:2048\n\t" \
  "v_mfma_f32_16x16x32_bf16 %[c02], v[96:99], v[136:139], %[c02]\n\t" \
  "v_mfma_f32_16x16x32_bf16 %[c12], v[100:103], v[136:139], %[c12]\n\t" \
  "ds_read_b128 v[168:171], %[pc2] offset:4096\n\t" \
  "v_mfma_f32_16x16x32_bf16 %[c03], v[96:99], v[140:143], %[c03]\n\t" \
  "v_mfma_f32_16x16x32_bf16 %[c13], v[100:103], v[140:143], %[c13]\n\t" \
  "ds_read_b128 v[172:175], %[pc2] offset:6144\n\t" \
  "s_waitcnt vmcnt(4) lgkmcnt(4)\n\t" \
  "v_mfma_f32_16x16x32_bf16 %[c00], v[104:107], v[144:147], %[c00]\n\t" \
  "v_mfma_f32_16x16x32_bf16 %[c10], v[108:111], v[144:147], %[c10]\n\t" \
  "ds_read_b128 v[176:179], %[pc3] offset:0\n\t" \
  "v_mfma_f32_16x16x32_bf16 %[c01], v[104:107], v[148:151], %[c01]\n\t" \
  "v_mfma_f32_16x16x32_bf16 %[c11], v[108:111], v[148:151], %[c11]\n\t" \
  "ds_read_b128 v[180:183], %[pc3] offset:2048\n\t" \
  "v_mfma_f32_16x16x32_bf16 %[c02], v[104:107], v[152:155], %[c02]\n\t" \
  "v_mfma_f32_16x16x32_bf16 %[c12], v[108:111], v[152:155], %[c12]\n\t" \
  "ds_read_b128 v[184:187], %[pc3] offset:4096\n\t" \
  "v_mfma_f32_16x16x32_bf16 %[c03], v[104:107], v[156:159], %[c03]\n\t" \
  "v_mfma_f32_16x16x32_bf16 %[c13], v[108:111], v[156:159], %[c13]\n\t" \
  "ds_read_b128 v[188:191], %[pc3] offset:6144\n\t" \
  "s_waitcnt vmcnt(2) lgkmcnt(4)\n\t" \
  "v_mfma_f32_16x16x32_bf16 %[c00], v[112:115], v[160:163], %[c00]\n\t" \
  "ds_read_b64 v[192:193], %[xd]\n\t" \
  "v_mfma_f32_16x16x32_bf16 %[c10], v[116:119], v[160:163], %[c10]\n\t" \
  "ds_read_b128 v[128:131], %[d0]\n\t" \
  "v_mfma_f32_16x16x32_bf16 %[c01], v[112:115], v[164:167], %[c01]\n\t" \
  "ds_read_b128 v[132:135], %[d1]\n\t" \
  "v_mfma_f32_16x16x32_bf16 %[c11], v[116:119], v[164:167], %[c11]\n\t" \
  "ds_read_b128 v[136:139], %[d2]\n\t" \
  "v_mfma_f32_16x16x32_bf16 %[c02], v[112:115], v[168:171], %[c02]\n\t" \
  "ds_read_b128 v[140:143], %[d3]\n\t" \
  "v_mfma_f32_16x16x32_bf16 %[c12], v[116:119], v[168:171], %[c12]\n\t" \
  "ds_read_b128 v[144:147], %[d4]\n\t" \
  "v_mfma_f32_16x16x32_bf16 %[c03], v[112:115], v[172:175], %[c03]\n\t" \
  "ds_read_b128 v[148:151], %[d5]\n\t" \
  "v_mfma_f32_16x16x32_bf16 %[c13], v[116:119], v[172:175], %[c13]\n\t" \
  "ds_read_b128 v[152:155], %[d6]\n\t" \
  "ds_read_b128 v[156:159], %[d7]\n\t" \
  "s_waitcnt vmcnt(0) lgkmcnt(9)\n\t" \
  "v_mfma_f32_16x16x32_bf16 %[c00], v[120:123], v[176:179], %[c00]\n\t" \
  "v_mfma_f32_16x16x32_bf16 %[c10], v[124:127], v[176:179], %[c10]\n\t" \
  "v_mfma_f32_16x16x32_bf16 %[c01], v[120:123], v[180:183], %[c01]\n\t" \
  "v_mfma_f32_16x16x32_bf16 %[c11], v[124:127], v[180:183], %[c11]\n\t" \
  "v_mfma_f32_16x16x32_bf16 %[c02], v[120:123], v[184:187], %[c02]\n\t" \
  "v_mfma_f32_16x16x32_bf16 %[c12], v[124:127], v[184:187], %[c12]\n\t" \
  "v_mfma_f32_16x16x32_bf16 %[c03], v[120:123], v[188:191], %[c03]\n\t" \
  "v_mfma_f32_16x16x32_bf16 %[c13], v[124:127], v[188:191], %[c13]\n\t" \
  "s_cmp_eq_u32 %[mine], 0\n\t" \
  "s_cbranch_scc1 Lend_%=\n\t" \
  "Ld_%=:\n\t" \
  "s_waitcnt lgkmcnt(0)\n\t" \
  "v_and_b32 v192, v192, v193\n\t" \
  "s_nop 0\n\t" \
  "v_readfirstlane_b32 %[s0], v192\n\t" \
  "s_cmp_eq_u32 %[s0], 0\n\t" \
  "s_cbranch_scc1 Ld1_%=\n\t" \
  "Ld2_%=:\n\t" \
  "v_add_f32 %[b00], %[b00], v128\n\t" \
  "v_add_f32 %[b01], %[b01], v129\n\t" \
  "v_add_f32 %[b02], %[b02], v130\n\t" \
  "v_add_f32 %[b03], %[b03], v131\n\t" \
  "v_add_f32 %[b10], %[b10], v132\n\t" \
  "v_add_f32 %[b11], %[b11], v133\n\t" \
  "v_add_f32 %[b12], %[b12], v134\n\t" \
  "v_add_f32 %[b13], %[b13], v135\n\t" \
  "v_add_f32 %[b20], %[b20], v136\n\t" \
  "v_add_f32 %[b21], %[b21], v137\n\t" \
  "v_add_f32 %[b22], %[b22], v138\n\t" \
  "v_add_f32 %[b23], %[b23], v139\n\t" \
  "v_add_f32 %[b30], %[b30], v140\n\t" \
  "v_add_f32 %[b31], %[b31], v141\n\t" \
  "v_add_f32 %[b32], %[b32], v142\n\t" \
  "v_add_f32 %[b33], %[b33], v143\n\t" \
  "v_add_f32 %[b40], %[b40], v144\n\t" \
  "v_add_f32 %[b41], %[b41], v145\n\t" \
  "v_add_f32 %[b42], %[b42], v146\n\t" \
  "v_add_f32 %[b43], %[b43], v147\n\t" \
  "v_add_f32 %[b50], %[b50], v148\n\t" \
  "v_add_f32 %[b51], %[b51], v149\n\t" \
  "v_add_f32 %[b52], %[b52], v150\n\t" \
  "v_add_f32 %[b53], %[b53], v151\n\t" \
  "v_add_f32 %[b60], %[b60], v152\n\t" \
  "v_add_f32 %[b61], %[b61], v153\n\t" \
  "v_add_f32 %[b62], %[b62], v154\n\t" \
  "v_add_f32 %[b63], %[b63], v155\n\t" \
  "v_add_f32 %[b70], %[b70], v156\n\t" \
  "v_add_f32 %[b71], %[b71], v157\n\t" \
  "v_add_f32 %[b72], %[b72], v158\n\t" \
  "v_add_f32 %[b73], %[b73], v159\n\t" \
  "s_branch Lend_%=\n\t" \
  "Lw_%=:\n\t" \
  "ds_read_b64 v[192:193], %[hr]\n\t" \
  "s_sub_i32 %[bud], %[bud], 1\n\t" \
  "s_cmp_lt_i32 %[bud], 0\n\t" \
  "s_cbranch_scc1 Lf3_%=\n\t" \
  "s_waitcnt lgkmcnt(0)\n\t" \
  "v_or_b32 v192, v192, v193\n\t" \
  "s_nop 0\n\t" \
  "v_readfirstlane_b32 %[s0], v192\n\t" \
  "s_cmp_eq_u32 %[s0], 0\n\t" \
  "s_cbranch_scc0 Lw_%=\n\t" \
  "s_branch Lf3_%=\n\t" \
  "Lh_%=:\n\t" \
  "buffer_load_dword v196, off, %[srd], %[sf1] sc1\n\t" \
  "s_branch Lhb_%=\n\t" \
  "Lg1_%=:\n\t" \
  "v_mov_b32 v196, 0\n\t" \
  "s_mov_b64 exec, 1\n\t" \
  "buffer_atomic_or v196, off, %[srd], %[sf1] sc0 sc1\n\t" \
  "s_mov_b64 exec, -1\n\t" \
  "s_sub_i32 %[bud], %[bud], 16\n\t" \
  "s_cmp_lt_i32 %[bud], 0\n\t" \
  "s_cbranch_scc1 Lgb_%=\n\t" \
  "s_waitcnt vmcnt(0)\n\t" \
  "s_branch Lg2_%=\n\t" \
  "Lg_%=:\n\t" \
  "s_waitcnt vmcnt(12)\n\t" \
  "Lg2_%=:\n\t" \
  "v_readfirstlane_b32 %[s0], v196\n\t" \
  "s_cmp_lg_u32 %[s0], 32\n\t" \
  "s_cbranch_scc1 Lg1_%=\n\t" \
  "s_branch Lgb_%=\n\t" \
  "Lcb1_%=:\n\t" \
  "ds_read_b64 v[192:193], %[fl] offset:0\n\t" \
  "s_sub_i32 %[bud], %[bud], 1\n\t" \
  "s_cmp_lt_i32 %[bud], 0\n\t" \
  "s_cbranch_scc1 Lcb1b_%=\n\t" \
  "s_waitcnt lgkmcnt(0)\n\t" \
  "v_or_b32 v192, v192, v193\n\t" \
  "s_nop 0\n\t" \
  "v_readfirstlane_b32 %[s0], v192\n\t" \
  "s_cmp_eq_u32 %[s0], 0\n\t" \
  "s_cbranch_scc0 Lcb1_%=\n\t" \
  "s_branch Lcb1b_%=\n\t" \
  "Lcb5_%=:\n\t" \
  "ds_read_b64 v[192:193], %[fl] offset:8\n\t" \
  "s_sub_i32 %[bud], %[bud], 1\n\t" \
  "s_cmp_lt_i32 %[bud], 0\n\t" \
  "s_cbranch_scc1 Lcb5b_%=\n\t" \
  "s_waitcnt lgkmcnt(0)\n\t" \
  "v_or_b32 v192, v192, v193\n\t" \
  "s_nop 0\n\t" \
  "v_readfirstlane_b32 %[s0], v192\n\t" \
  "s_cmp_eq_u32 %[s0], 0\n\t" \
  "s_cbranch_scc0 Lcb5_%=\n\t" \
  "s_branch Lcb5b_%=\n\t" \
  "Lcf1_%=:\n\t" \
  "ds_read_b64 v[192:193], %[fl] offset:0\n\t" \
  "s_sub_i32 %[bud], %[bud], 1\n\t" \
  "s_cmp_lt_i32 %[bud], 0\n\t" \
  "s_cbranch_scc1 Lcf1b_%=\n\t" \
  "s_waitcnt lgkmcnt(0)\n\t" \
  "v_or_b32 v192, v192, v193\n\t" \
  "s_nop 0\n\t" \
  "v_readfirstlane_b32 %[s0], v192\n\t" \
  "s_cmp_eq_u32 %[s0], 0\n\t" \
  "s_cbranch_scc0 Lcf1_%=\n\t" \
  "s_branch Lcf1b_%=\n\t" \
  "Ld1_%=:\n\t" \
  "s_sub_i32 %[bud], %[bud], 1\n\t" \
  "s_cmp_lt_i32 %[bud], 0\n\t" \
  "s_cbranch_scc1 Ld2_%=\n\t" \
  "ds_read_b64 v[192:193], %[xd]\n\t" \
  "ds_read_b128 v[128:131], %[d0]\n\t" \
  "ds_read_b128 v[132:135], %[d1]\n\t" \
  "ds_read_b128 v[136:139], %[d2]\n\t" \
  "ds_read_b128 v[140:143], %[d3]\n\t" \
  "ds_read_b128 v[144:147], %[d4]\n\t" \
  "ds_read_b128 v[148:151], %[d5]\n\t" \
  "ds_read_b128 v[152:155], %[d6]\n\t" \
  "ds_read_b128 v[156:159], %[d7]\n\t" \
  "s_branch Ld_%=\n\t" \
  "Lend_%=:\n\t" \
  "s_waitcnt lgkmcnt(0)\n\t" \
  "s_nop 6\n\t"
#define ASM_EN \
  "s_bfe_u32 %[j], %[bid], 0x30003\n\t" \
  "v_readfirstlane_b32 %[w], %[tid]\n\t" \
  "s_lshr_b32 %[w], %[w], 6\n\t" \
  "s_cmp_lg_u32 %[j], %[w]\n\t" \
  "s_cbranch_scc1 Lnc_%=\n\t" \
  "s_getpc_b64 s[20:21]\n\t" \
  "v_lshlrev_b32 %[v0], 7, %[tid]\n\t" \
  "v_min_u32 %[v0], %[cpf], %[v0]\n\t" \
  "global_load_dword v255, %[v0], s[20:21]\n\t" \
  "Lnc_%=:\n\t" \
  "s_sub_u32 %[w], %[w], 4\n\t" \
  "s_cbranch_scc1 Lend_%=\n\t" \
  "s_lshl3_add_u32 %[t], %[w], 8\n\t" \
  "s_cmp_lt_u32 %[w], 2\n\t" \
  "s_cselect_b32 %[t], 0, %[t]\n\t" \
  "s_load_dwordx2 s[20:21], %[kp], %[t]\n\t" \
  "s_cselect_b32 %[sa], 14, 11\n\t" \
  "s_cselect_b32 %[sc], 11, 15\n\t" \
  "s_cselect_b32 %[m], 16, 0\n\t" \
  "s_bfe_u32 %[r], %[bid], 0x20001\n\t" \
  "s_lshr_b32 %[t], %[bid], 6\n\t" \
  "s_lshl2_add_u32 %[c], %[r], %[t]\n\t" \
  "s_and_b32 %[t], %[bid], 1\n\t" \
  "s_lshl3_add_u32 %[j], %[t], %[j]\n\t" \
  "s_lshl1_add_u32 %[r], %[t], %[r]\n\t" \
  "s_and_b32 %[r], %[r], 3\n\t" \
  "s_lshl_b32 %[c], %[c], 5\n\t" \
  "s_add_u32 %[c], %[c], %[w]\n\t" \
  "s_lshl_b32 %[j], %[j], 4\n\t" \
  "s_cmp_lt_u32 %[w], 2\n\t" \
  "s_cselect_b32 %[c], %[c], %[j]\n\t" \
  "v_bfe_u32 %[v1], %[tid], 3, 3\n\t" \
  "v_xor_b32 %[v0], %[v1], %[tid]\n\t" \
  "v_and_b32 %[v0], 7, %[v0]\n\t" \
  "v_lshlrev_b32 %[v0], 4, %[v0]\n\t" \
  "v_lshl_add_u32 %[v0], %[r], 9, %[v0]\n\t" \
  "v_lshl_add_u32 %[v0], %[v1], %[sa], %[v0]\n\t" \
  "v_lshl_add_u32 %[v0], %[c], 13, %[v0]\n\t" \
  "s_lshl_b32 %[sa], 8, %[sa]\n\t" \
  "s_lshl_b32 %[sc], 1, %[sc]\n\t" \
  "v_add_u32 %[v1], %[sa], %[v0]\n\t" \
  "s_add_u32 %[m], %[m], %[w]\n\t" \
  "s_lshl_b32 m0, %[m], 13\n\t" \
  "s_waitcnt lgkmcnt(0)\n\t" \
  "Ll_%=:\n\t" \
  "global_load_lds_dwordx4 %[v0], s[20:21]\n\t" \
  "s_addk_i32 m0, 0x400\n\t" \
  "v_add_u32 %[v0], %[sc], %[v0]\n\t" \
  "global_load_lds_dwordx4 %[v1], s[20:21]\n\t" \
  "s_addk_i32 m0, 0x400\n\t" \
  "v_add_u32 %[v1], %[sc], %[v1]\n\t" \
  "s_lshl_b32 %[t], m0, 19\n\t" \
  "s_cbranch_scc1 Ll_%=\n\t" \
  "s_lshl2_add_u32 m0, %[w], %[ms0]\n\t" \
  "s_and_b32 s21, s21, 0xffff\n\t" \
  "v_mov_b32 %[v0], m0\n\t" \
  "ds_write_b32 %[v0], %[v0]\n\t" \
  "s_mov_b32 s22, 0\n\t" \
  "s_mov_b32 s23, 0x20000\n\t" \
  "s_mov_b64 exec, 1\n\t" \
  "s_waitcnt lgkmcnt(0)\n\t" \
  "buffer_load_dword off, s[20:23], 0 lds\n\t" \
  "s_mov_b64 exec, -1\n\t" \
  "Lend_%=:\n\t"
#define ASM_LA \
  "s_nop 4\n\t" \
  "s_barrier\n\t" \
  "Ltop_%=:\n\t" \
  "s_mov_b32 m0, %[dst]\n\t" \
  "v_mov_b32 %[vc], %[ci]\n\t" \
  "buffer_load_dwordx4 %[v0], %[r0], %[so0] offen lds\n\t" \
  "s_addk_i32 m0, 0x400\n\t" \
  "s_add_u32 %[dst], %[dst], 0x8000\n\t" \
  "buffer_load_dwordx4 %[v0], %[r0], %[so1] offen lds\n\t" \
  "s_addk_i32 m0, 0x400\n\t" \
  "s_cmp_ge_u32 %[dst], 0x24000\n\t" \
  "s_cselect_b32 %[s0], %[wrap], 0\n\t" \
  "buffer_load_dwordx4 %[v0], %[r0], %[so2] offen lds\n\t" \
  "s_addk_i32 m0, 0x400\n\t" \
  "s_sub_u32 %[dst], %[dst], %[s0]\n\t" \
  "buffer_load_dwordx4 %[v0], %[r0], %[so3] offen lds\n\t" \
  "s_addk_i32 m0, 0x400\n\t" \
  "s_add_u32 %[sk], %[sk], 128\n\t" \
  "buffer_load_dwordx4 %[v0], %[r0], %[so4] offen lds\n\t" \
  "s_addk_i32 m0, 0x400\n\t" \
  "s_and_b32 %[sk], %[sk], 2047\n\t" \
  "buffer_load_dwordx4 %[v0], %[r0], %[so5] offen lds\n\t" \
  "s_addk_i32 m0, 0x400\n\t" \
  "s_nop 0\n\t" \
  "buffer_load_dwordx4 %[v0], %[r0], %[so6] offen lds\n\t" \
  "s_addk_i32 m0, 0x400\n\t" \
  "s_add_u32 %[ci], %[ci], 16\n\t" \
  "buffer_load_dwordx4 %[v0], %[r0], %[so7] offen lds\n\t" \
  "s_addk_i32 m0, 0x400\n\t" \
  "s_sub_u32 %[n], %[n], 1\n\t" \
  "buffer_load_dwordx4 %[v0], %[r1], %[so0] offen lds\n\t" \
  "s_addk_i32 m0, 0x400\n\t" \
  "s_nop 0\n\t" \
  "buffer_load_dwordx4 %[v0], %[r1], %[so1] offen lds\n\t" \
  "s_addk_i32 m0, 0x400\n\t" \
  "s_nop 0\n\t" \
  "buffer_load_dwordx4 %[v0], %[r1], %[so2] offen lds\n\t" \
  "s_addk_i32 m0, 0x400\n\t" \
  "s_nop 0\n\t" \
  "buffer_load_dwordx4 %[v0], %[r1], %[so3] offen lds\n\t" \
  "s_addk_i32 m0, 0x400\n\t" \
  "ds_read_b128 v[24:27], %[vc]\n\t" \
  "ds_read_b128 v[28:31], %[vw]\n\t" \
  "buffer_load_dwordx4 %[v0], %[r1], %[so4] offen lds\n\t" \
  "s_addk_i32 m0, 0x400\n\t" \
  "s_nop 0\n\t" \
  "buffer_load_dwordx4 %[v0], %[r1], %[so5] offen lds\n\t" \
  "s_addk_i32 m0, 0x400\n\t" \
  "s_nop 0\n\t" \
  "buffer_load_dwordx4 %[v0], %[r1], %[so6] offen lds\n\t" \
  "s_addk_i32 m0, 0x400\n\t" \
  "s_waitcnt lgkmcnt(0)\n\t" \
  "v_or3_b32 v24, v24, v25, v26\n\t" \
  "v_or_b32 v24, v24, v27\n\t" \
  "v_sub_u32 v28, v30, v28\n\t" \
  "v_sub_u32 v29, v31, v29\n\t" \
  "v_or3_b32 v24, v24, v28, v29\n\t" \
  "buffer_load_dwordx4 %[v0], %[r1], %[so7] offen lds\n\t" \
  "s_mov_b32 m0, %[mk]\n\t" \
  "s_mov_b64 exec, 1\n\t" \
  "buffer_load_dword %[v0], %[rz], 0 offen lds\n\t" \
  "s_mov_b64 exec, -1\n\t" \
  "v_readfirstlane_b32 %[s0], v24\n\t" \
  "v_add_u32 %[v0], %[sk], %[b0]\n\t" \
  "s_add_u32 %[mk], %[mk], 8\n\t" \
  "s_cmp_eq_u32 %[n], 0\n\t" \
  "s_cbranch_scc1 Lend_%=\n\t" \
  "s_cmp_eq_u32 %[s0], 0\n\t" \
  "s_cbranch_scc1 Ltop_%=\n\t" \
  "Lsp_%=:\n\t" \
  "ds_read_b128 v[24:27], %[vc]\n\t" \
  "ds_read_b128 v[28:31], %[vw]\n\t" \
  "s_sub_i32 %[bud], %[bud], 1\n\t" \
  "s_cmp_lt_i32 %[bud], 0\n\t" \
  "s_cbranch_scc1 Ltop_%=\n\t" \
  "s_waitcnt lgkmcnt(0)\n\t" \
  "v_or3_b32 v24, v24, v25, v26\n\t" \
  "v_or_b32 v24, v24, v27\n\t" \
  "v_sub_u32 v28, v30, v28\n\t" \
  "v_sub_u32 v29, v31, v29\n\t" \
  "v_or3_b32 v24, v24, v28, v29\n\t" \
  "s_nop 0\n\t" \
  "v_readfirstlane_b32 %[s0], v24\n\t" \
  "s_cmp_eq_u32 %[s0], 0\n\t" \
  "s_cbranch_scc1 Ltop_%=\n\t" \
  "s_branch Lsp_%=\n\t" \
  "Lend_%=:\n\t"
#define ASM_LB \
  "s_nop 4\n\t" \
  "s_branch Lsp_%=\n\t" \
  "Ltop_%=:\n\t" \
  "s_mov_b32 m0, %[dst]\n\t" \
  "v_mov_b32 %[vc], %[ci]\n\t" \
  "buffer_load_dwordx4 %[v0], %[r0], %[so0] offen lds\n\t" \
  "s_addk_i32 m0, 0x400\n\t" \
  "s_add_u32 %[dst], %[dst], 0x8000\n\t" \
  "buffer_load_dwordx4 %[v1], %[r0], %[so1] offen lds\n\t" \
  "s_addk_i32 m0, 0x400\n\t" \
  "s_cmp_ge_u32 %[dst], 0x24000\n\t" \
  "s_cselect_b32 %[s0], %[wrap], 0\n\t" \
  "buffer_load_dwordx4 %[v0], %[r0], %[so2] offen lds\n\t" \
  "s_addk_i32 m0, 0x400\n\t" \
  "s_sub_u32 %[dst], %[dst], %[s0]\n\t" \
  "buffer_load_dwordx4 %[v1], %[r0], %[so3] offen lds\n\t" \
  "s_addk_i32 m0, 0x400\n\t" \
  "s_add_u32 %[sk], %[sk], 256\n\t" \
  "buffer_load_dwordx4 %[v0], %[r0], %[so4] offen lds\n\t" \
  "s_addk_i32 m0, 0x400\n\t" \
  "s_and_b32 %[sk], %[sk], 1023\n\t" \
  "buffer_load_dwordx4 %[v1], %[r0], %[so5] offen lds\n\t" \
  "s_addk_i32 m0, 0x400\n\t" \
  "s_cmp_ge_u32 %[ci], %[cth]\n\t" \
  "s_cselect_b32 %[s0], 32, 16\n\t" \
  "buffer_load_dwordx4 %[v0], %[r0], %[so6] offen lds\n\t" \
  "s_addk_i32 m0, 0x400\n\t" \
  "s_add_u32 %[ci], %[ci], %[s0]\n\t" \
  "buffer_load_dwordx4 %[v1], %[r0], %[so7] offen lds\n\t" \
  "s_addk_i32 m0, 0x400\n\t" \
  "s_sub_u32 %[n], %[n], 1\n\t" \
  "buffer_load_dwordx4 %[v0], %[r1], %[so0] offen lds\n\t" \
  "s_addk_i32 m0, 0x400\n\t" \
  "s_cmp_eq_u32 %[n], 4\n\t" \
  "s_cselect_b32 %[s0], 0x400, 0\n\t" \
  "buffer_load_dwordx4 %[v1], %[r1], %[so1] offen lds\n\t" \
  "s_addk_i32 m0, 0x400\n\t" \
  "s_xor_b32 %[hi], %[hi], %[s0]\n\t" \
  "buffer_load_dwordx4 %[v0], %[r1], %[so2] offen lds\n\t" \
  "s_addk_i32 m0, 0x400\n\t" \
  "s_or_b32 %[s1], %[sk], %[hi]\n\t" \
  "buffer_load_dwordx4 %[v1], %[r1], %[so3] offen lds\n\t" \
  "s_addk_i32 m0, 0x400\n\t" \
  "ds_read_b128 v[24:27], %[vc]\n\t" \
  "buffer_load_dwordx4 %[v0], %[r1], %[so4] offen lds\n\t" \
  "s_addk_i32 m0, 0x400\n\t" \
  "s_nop 0\n\t" \
  "buffer_load_dwordx4 %[v1], %[r1], %[so5] offen lds\n\t" \
  "s_addk_i32 m0, 0x400\n\t" \
  "s_nop 0\n\t" \
  "buffer_load_dwordx4 %[v0], %[r1], %[so6] offen lds\n\t" \
  "s_addk_i32 m0, 0x400\n\t" \
  "s_waitcnt lgkmcnt(0)\n\t" \
  "v_or3_b32 v24, v24, v25, v26\n\t" \
  "v_or_b32 v24, v24, v27\n\t" \
  "buffer_load_dwordx4 %[v1], %[r1], %[so7] offen lds\n\t" \
  "s_mov_b32 m0, %[mk]\n\t" \
  "s_mov_b64 exec, 3\n\t" \
  "buffer_load_dword %[v0], %[rz], 0 offen lds\n\t" \
  "s_mov_b64 exec, -1\n\t" \
  "v_readfirstlane_b32 %[s0], v24\n\t" \
  "v_add_u32 %[v0], %[s1], %[b0]\n\t" \
  "v_add_u32 %[v1], %[s1], %[b1]\n\t" \
  "s_add_u32 %[mk], %[mk], 16\n\t" \
  "s_cmp_eq_u32 %[n], %[nend]\n\t" \
  "s_cbranch_scc1 Lend_%=\n\t" \
  "s_cmp_eq_u32 %[s0], 0\n\t" \
  "s_cbranch_scc1 Ltop_%=\n\t" \
  "Lsp_%=:\n\t" \
  "ds_read_b128 v[24:27], %[vc]\n\t" \
  "s_sub_i32 %[bud], %[bud], 1\n\t" \
  "s_cmp_lt_i32 %[bud], 0\n\t" \
  "s_cbranch_scc1 Ltop_%=\n\t" \
  "s_waitcnt lgkmcnt(0)\n\t" \
  "v_or3_b32 v24, v24, v25, v26\n\t" \
  "v_or_b32 v24, v24, v27\n\t" \
  "s_nop 0\n\t" \
  "v_readfirstlane_b32 %[s0], v24\n\t" \
  "s_cmp_eq_u32 %[s0], 0\n\t" \
  "s_cbranch_scc1 Ltop_%=\n\t" \
  "s_sleep 1\n\t" \
  "s_branch Lsp_%=\n\t" \
  "Lend_%=:\n\t" \
  "buffer_load_dwordx4 v[32:35], %[p0], %[rp], %[so0] offen offset:128\n\t" \
  "buffer_load_dwordx4 v[36:39], %[p1], %[rp], %[so1] offen offset:128\n\t" \
  "buffer_load_dwordx4 v[40:43], %[p0], %[rp], %[so2] offen offset:128\n\t" \
  "buffer_load_dwordx4 v[44:47], %[p1], %[rp], %[so3] offen offset:128\n\t" \
  "buffer_load_dwordx4 v[48:51], %[p0], %[rp], %[so4] offen offset:128\n\t" \
  "buffer_load_dwordx4 v[52:55], %[p1], %[rp], %[so5] offen offset:128\n\t" \
  "buffer_load_dwordx4 v[56:59], %[p0], %[rp], %[so6] offen offset:128\n\t" \
  "buffer_load_dwordx4 v[60:63], %[p1], %[rp], %[so7] offen offset:128\n\t" \
  "buffer_load_dwordx4 v[64:67], %[p0], %[rp], %[so0] offen offset:256\n\t" \
  "buffer_load_dwordx4 v[68:71], %[p1], %[rp], %[so1] offen offset:256\n\t" \
  "buffer_load_dwordx4 v[72:75], %[p0], %[rp], %[so2] offen offset:256\n\t" \
  "buffer_load_dwordx4 v[76:79], %[p1], %[rp], %[so3] offen offset:256\n\t" \
  "buffer_load_dwordx4 v[80:83], %[p0], %[rp], %[so4] offen offset:256\n\t" \
  "buffer_load_dwordx4 v[84:87], %[p1], %[rp], %[so5] offen offset:256\n\t" \
  "buffer_load_dwordx4 v[88:91], %[p0], %[rp], %[so6] offen offset:256\n\t" \
  "buffer_load_dwordx4 v[92:95], %[p1], %[rp], %[so7] offen offset:256\n\t" \
  "buffer_load_dwordx4 v[96:99], %[p0], %[rp], %[so0] offen offset:384\n\t" \
  "buffer_load_dwordx4 v[100:103], %[p1], %[rp], %[so1] offen offset:384\n\t" \
  "buffer_load_dwordx4 v[104:107], %[p0], %[rp], %[so2] offen offset:384\n\t" \
  "buffer_load_dwordx4 v[108:111], %[p1], %[rp], %[so3] offen offset:384\n\t" \
  "buffer_load_dwordx4 v[112:115], %[p0], %[rp], %[so4] offen offset:384\n\t" \
  "buffer_load_dwordx4 v[116:119], %[p1], %[rp], %[so5] offen offset:384\n\t" \
  "buffer_load_dwordx4 v[120:123], %[p0], %[rp], %[so6] offen offset:384\n\t" \
  "buffer_load_dwordx4 v[124:127], %[p1], %[rp], %[so7] offen offset:384\n\t" \
  "buffer_load_dwordx4 v[128:131], %[p0], %[rp], %[so0] offen offset:512\n\t" \
  "buffer_load_dwordx4 v[132:135], %[p1], %[rp], %[so1] offen offset:512\n\t" \
  "buffer_load_dwordx4 v[136:139], %[p0], %[rp], %[so2] offen offset:512\n\t" \
  "buffer_load_dwordx4 v[140:143], %[p1], %[rp], %[so3] offen offset:512\n\t" \
  "buffer_load_dwordx4 v[144:147], %[p0], %[rp], %[so4] offen offset:512\n\t" \
  "buffer_load_dwordx4 v[148:151], %[p1], %[rp], %[so5] offen offset:512\n\t" \
  "buffer_load_dwordx4 v[152:155], %[p0], %[rp], %[so6] offen offset:512\n\t" \
  "buffer_load_dwordx4 v[156:159], %[p1], %[rp], %[so7] offen offset:512\n\t" \
  "buffer_load_dwordx4 v[160:163], %[p0], %[rp], %[so0] offen offset:640\n\t" \
  "buffer_load_dwordx4 v[164:167], %[p1], %[rp], %[so1] offen offset:640\n\t" \
  "buffer_load_dwordx4 v[168:171], %[p0], %[rp], %[so2] offen offset:640\n\t" \
  "buffer_load_dwordx4 v[172:175], %[p1], %[rp], %[so3] offen offset:640\n\t" \
  "buffer_load_dwordx4 v[176:179], %[p0], %[rp], %[so4] offen offset:640\n\t" \
  "buffer_load_dwordx4 v[180:183], %[p1], %[rp], %[so5] offen offset:640\n\t" \
  "buffer_load_dwordx4 v[184:187], %[p0], %[rp], %[so6] offen offset:640\n\t" \
  "buffer_load_dwordx4 v[188:191], %[p1], %[rp], %[so7] offen offset:640\n\t" \
  "buffer_load_dwordx4 v[192:195], %[p0], %[rp], %[so0] offen offset:768\n\t" \
  "buffer_load_dwordx4 v[196:199], %[p1], %[rp], %[so1] offen offset:768\n\t" \
  "buffer_load_dwordx4 v[200:203], %[p0], %[rp], %[so2] offen offset:768\n\t" \
  "buffer_load_dwordx4 v[204:207], %[p1], %[rp], %[so3] offen offset:768\n\t" \
  "buffer_load_dwordx4 v[208:211], %[p0], %[rp], %[so4] offen offset:768\n\t" \
  "buffer_load_dwordx4 v[212:215], %[p1], %[rp], %[so5] offen offset:768\n\t" \
  "buffer_load_dwordx4 v[216:219], %[p0], %[rp], %[so6] offen offset:768\n\t" \
  "buffer_load_dwordx4 v[220:223], %[p1], %[rp], %[so7] offen offset:768\n\t" \
  "buffer_load_dwordx4 v[224:227], %[p0], %[rp], %[so0] offen offset:896\n\t" \
  "buffer_load_dwordx4 v[228:231], %[p1], %[rp], %[so1] offen offset:896\n\t" \
  "buffer_load_dwordx4 v[232:235], %[p0], %[rp], %[so2] offen offset:896\n\t" \
  "buffer_load_dwordx4 v[236:239], %[p1], %[rp], %[so3] offen offset:896\n\t" \
  "buffer_load_dwordx4 v[240:243], %[p0], %[rp], %[so4] offen offset:896\n\t" \
  "buffer_load_dwordx4 v[244:247], %[p1], %[rp], %[so5] offen offset:896\n\t" \
  "buffer_load_dwordx4 v[248:251], %[p0], %[rp], %[so6] offen offset:896\n\t" \
  "buffer_load_dwordx4 v[252:255], %[p1], %[rp], %[so7] offen offset:896\n\t" \
  "v_mov_b32 v28, 0\n\t" \
  "Lw0_%=:\n\t" \
  "ds_read_b128 v[24:27], %[vm] offset:0\n\t" \
  "s_sub_i32 %[bud], %[bud], 1\n\t" \
  "s_cmp_lt_i32 %[bud], 0\n\t" \
  "s_cbranch_scc1 Lx0_%=\n\t" \
  "s_waitcnt lgkmcnt(0)\n\t" \
  "v_or3_b32 v24, v24, v25, v26\n\t" \
  "v_or_b32 v24, v24, v27\n\t" \
  "s_nop 0\n\t" \
  "v_readfirstlane_b32 %[s0], v24\n\t" \
  "s_cmp_eq_u32 %[s0], 0\n\t" \
  "s_cbranch_scc1 Lx0_%=\n\t" \
  "s_sleep 2\n\t" \
  "s_branch Lw0_%=\n\t" \
  "Lx0_%=:\n\t" \
  "s_waitcnt vmcnt(0)\n\t" \
  "ds_write_b128 %[vd], v[32:35] offset:0\n\t" \
  "ds_write_b128 %[vd], v[36:39] offset:1024\n\t" \
  "ds_write_b128 %[vd], v[40:43] offset:2048\n\t" \
  "ds_write_b128 %[vd], v[44:47] offset:3072\n\t" \
  "ds_write_b128 %[vd], v[48:51] offset:4096\n\t" \
  "ds_write_b128 %[vd], v[52:55] offset:5120\n\t" \
  "ds_write_b128 %[vd], v[56:59] offset:6144\n\t" \
  "ds_write_b128 %[vd], v[60:63] offset:7168\n\t" \
  "s_waitcnt lgkmcnt(0)\n\t" \
  "ds_write_b32 %[vl], v28 offset:0\n\t" \
  "Lw1_%=:\n\t" \
  "ds_read_b128 v[24:27], %[vm] offset:16\n\t" \
  "s_sub_i32 %[bud], %[bud], 1\n\t" \
  "s_cmp_lt_i32 %[bud], 0\n\t" \
  "s_cbranch_scc1 Lx1_%=\n\t" \
  "s_waitcnt lgkmcnt(0)\n\t" \
  "v_or3_b32 v24, v24, v25, v26\n\t" \
  "v_or_b32 v24, v24, v27\n\t" \
  "s_nop 0\n\t" \
  "v_readfirstlane_b32 %[s0], v24\n\t" \
  "s_cmp_eq_u32 %[s0], 0\n\t" \
  "s_cbranch_scc1 Lx1_%=\n\t" \
  "s_sleep 2\n\t" \
  "s_branch Lw1_%=\n\t" \
  "Lx1_%=:\n\t" \
  "ds_write_b128 %[vd], v[64:67] offset:16384\n\t" \
  "ds_write_b128 %[vd], v[68:71] offset:17408\n\t" \
  "ds_write_b128 %[vd], v[72:75] offset:18432\n\t" \
  "ds_write_b128 %[vd], v[76:79] offset:19456\n\t" \
  "ds_write_b128 %[vd], v[80:83] offset:20480\n\t" \
  "ds_write_b128 %[vd], v[84:87] offset:21504\n\t" \
  "ds_write_b128 %[vd], v[88:91] offset:22528\n\t" \
  "ds_write_b128 %[vd], v[92:95] offset:23552\n\t" \
  "s_waitcnt lgkmcnt(0)\n\t" \
  "ds_write_b32 %[vl], v28 offset:8\n\t" \
  "Lw2_%=:\n\t" \
  "ds_read_b128 v[24:27], %[vm] offset:32\n\t" \
  "s_sub_i32 %[bud], %[bud], 1\n\t" \
  "s_cmp_lt_i32 %[bud], 0\n\t" \
  "s_cbranch_scc1 Lx2_%=\n\t" \
  "s_waitcnt lgkmcnt(0)\n\t" \
  "v_or3_b32 v24, v24, v25, v26\n\t" \
  "v_or_b32 v24, v24, v27\n\t" \
  "s_nop 0\n\t" \
  "v_readfirstlane_b32 %[s0], v24\n\t" \
  "s_cmp_eq_u32 %[s0], 0\n\t" \
  "s_cbranch_scc1 Lx2_%=\n\t" \
  "s_sleep 2\n\t" \
  "s_branch Lw2_%=\n\t" \
  "Lx2_%=:\n\t" \
  "ds_write_b128 %[vd], v[96:99] offset:32768\n\t" \
  "ds_write_b128 %[vd], v[100:103] offset:33792\n\t" \
  "ds_write_b128 %[vd], v[104:107] offset:34816\n\t" \
  "ds_write_b128 %[vd], v[108:111] offset:35840\n\t" \
  "ds_write_b128 %[vd], v[112:115] offset:36864\n\t" \
  "ds_write_b128 %[vd], v[116:119] offset:37888\n\t" \
  "ds_write_b128 %[vd], v[120:123] offset:38912\n\t" \
  "ds_write_b128 %[vd], v[124:127] offset:39936\n\t" \
  "s_waitcnt lgkmcnt(0)\n\t" \
  "ds_write_b32 %[vl], v28 offset:16\n\t" \
  "Lw3_%=:\n\t" \
  "ds_read_b128 v[24:27], %[vm] offset:48\n\t" \
  "s_sub_i32 %[bud], %[bud], 1\n\t" \
  "s_cmp_lt_i32 %[bud], 0\n\t" \
  "s_cbranch_scc1 Lx3_%=\n\t" \
  "s_waitcnt lgkmcnt(0)\n\t" \
  "v_or3_b32 v24, v24, v25, v26\n\t" \
  "v_or_b32 v24, v24, v27\n\t" \
  "s_nop 0\n\t" \
  "v_readfirstlane_b32 %[s0], v24\n\t" \
  "s_cmp_eq_u32 %[s0], 0\n\t" \
  "s_cbranch_scc1 Lx3_%=\n\t" \
  "s_sleep 2\n\t" \
  "s_branch Lw3_%=\n\t" \
  "Lx3_%=:\n\t" \
  "ds_write_b128 %[vd], v[128:131] offset:49152\n\t" \
  "ds_write_b128 %[vd], v[132:135] offset:50176\n\t" \
  "ds_write_b128 %[vd], v[136:139] offset:51200\n\t" \
  "ds_write_b128 %[vd], v[140:143] offset:52224\n\t" \
  "ds_write_b128 %[vd], v[144:147] offset:53248\n\t" \
  "ds_write_b128 %[vd], v[148:151] offset:54272\n\t" \
  "ds_write_b128 %[vd], v[152:155] offset:55296\n\t" \
  "ds_write_b128 %[vd], v[156:159] offset:56320\n\t" \
  "v_add_u32 %[vd], 0x10000, %[vd]\n\t" \
  "s_waitcnt lgkmcnt(0)\n\t" \
  "ds_write_b32 %[vl], v28 offset:24\n\t" \
  "Lw4_%=:\n\t" \
  "ds_read_b128 v[24:27], %[vm] offset:64\n\t" \
  "s_sub_i32 %[bud], %[bud], 1\n\t" \
  "s_cmp_lt_i32 %[bud], 0\n\t" \
  "s_cbranch_scc1 Lx4_%=\n\t" \
  "s_waitcnt lgkmcnt(0)\n\t" \
  "v_or3_b32 v24, v24, v25, v26\n\t" \
  "v_or_b32 v24, v24, v27\n\t" \
  "s_nop 0\n\t" \
  "v_readfirstlane_b32 %[s0], v24\n\t" \
  "s_cmp_eq_u32 %[s0], 0\n\t" \
  "s_cbranch_scc1 Lx4_%=\n\t" \
  "s_sleep 2\n\t" \
  "s_branch Lw4_%=\n\t" \
  "Lx4_%=:\n\t" \
  "ds_write_b128 %[vd], v[160:163] offset:0\n\t" \
  "ds_write_b128 %[vd], v[164:167] offset:1024\n\t" \
  "ds_write_b128 %[vd], v[168:171] offset:2048\n\t" \
  "ds_write_b128 %[vd], v[172:175] offset:3072\n\t" \
  "ds_write_b128 %[vd], v[176:179] offset:4096\n\t" \
  "ds_write_b128 %[vd], v[180:183] offset:5120\n\t" \
  "ds_write_b128 %[vd], v[184:187] offset:6144\n\t" \
  "ds_write_b128 %[vd], v[188:191] offset:7168\n\t" \
  "s_waitcnt lgkmcnt(0)\n\t" \
  "ds_write_b32 %[vl], v28 offset:32\n\t" \
  "Lw5_%=:\n\t" \
  "ds_read_b128 v[24:27], %[vm] offset:80\n\t" \
  "s_sub_i32 %[bud], %[bud], 1\n\t" \
  "s_cmp_lt_i32 %[bud], 0\n\t" \
  "s_cbranch_scc1 Lx5_%=\n\t" \
  "s_waitcnt lgkmcnt(0)\n\t" \
  "v_or3_b32 v24, v24, v25, v26\n\t" \
  "v_or_b32 v24, v24, v27\n\t" \
  "s_nop 0\n\t" \
  "v_readfirstlane_b32 %[s0], v24\n\t" \
  "s_cmp_eq_u32 %[s0], 0\n\t" \
  "s_cbranch_scc1 Lx5_%=\n\t" \
  "s_sleep 2\n\t" \
  "s_branch Lw5_%=\n\t" \
  "Lx5_%=:\n\t" \
  "ds_write_b128 %[vd], v[192:195] offset:16384\n\t" \
  "ds_write_b128 %[vd], v[196:199] offset:17408\n\t" \
  "ds_write_b128 %[vd], v[200:203] offset:18432\n\t" \
  "ds_write_b128 %[vd], v[204:207] offset:19456\n\t" \
  "ds_write_b128 %[vd], v[208:211] offset:20480\n\t" \
  "ds_write_b128 %[vd], v[212:215] offset:21504\n\t" \
  "ds_write_b128 %[vd], v[216:219] offset:22528\n\t" \
  "ds_write_b128 %[vd], v[220:223] offset:23552\n\t" \
  "s_waitcnt lgkmcnt(0)\n\t" \
  "ds_write_b32 %[vl], v28 offset:40\n\t" \
  "Lw6_%=:\n\t" \
  "ds_read_b128 v[24:27], %[vm] offset:96\n\t" \
  "s_sub_i32 %[bud], %[bud], 1\n\t" \
  "s_cmp_lt_i32 %[bud], 0\n\t" \
  "s_cbranch_scc1 Lx6_%=\n\t" \
  "s_waitcnt lgkmcnt(0)\n\t" \
  "v_or3_b32 v24, v24, v25, v26\n\t" \
  "v_or_b32 v24, v24, v27\n\t" \
  "s_nop 0\n\t" \
  "v_readfirstlane_b32 %[s0], v24\n\t" \
  "s_cmp_eq_u32 %[s0], 0\n\t" \
  "s_cbranch_scc1 Lx6_%=\n\t" \
  "s_sleep 2\n\t" \
  "s_branch Lw6_%=\n\t" \
  "Lx6_%=:\n\t" \
  "ds_write_b128 %[vd], v[224:227] offset:32768\n\t" \
  "ds_write_b128 %[vd], v[228:231] offset:33792\n\t" \
  "ds_write_b128 %[vd], v[232:235] offset:34816\n\t" \
  "ds_write_b128 %[vd], v[236:239] offset:35840\n\t" \
  "ds_write_b128 %[vd], v[240:243] offset:36864\n\t" \
  "ds_write_b128 %[vd], v[244:247] offset:37888\n\t" \
  "ds_write_b128 %[vd], v[248:251] offset:38912\n\t" \
  "ds_write_b128 %[vd], v[252:255] offset:39936\n\t" \
  "s_waitcnt lgkmcnt(0)\n\t" \
  "ds_write_b32 %[vl], v28 offset:48\n\t" \
  "s_waitcnt lgkmcnt(0)\n\t"

#define OST " sc1"
#define XPRIO0 3
#define XPRIO 1
#define CPRIO 2
#define LPRIO 2
#define FPRIO 1
#define MPRIO 2
#define WGRP (sg & 1)
#define WS0 1
#define WS1 5
#define WS2 9
#define WSLP 8
#define TKG0 3
#define GEXACT 0
#define GCG 0
#define YPRIO 3
#define GPRIO 1
#define W2G 14
#define W2P 1
#define CWS 16
#define GUG 25
#define TKG 4
#define YG 1
#define CGS 14
#define NOFFN 0
#define CODEPF 29184
#define CPULL 1
#define WNT ""
#define PRET 0
constexpr int D_ = 1024, F_ = 3584;
constexpr int MISC = 147456;
constexpr int L_TAB = MISC;
constexpr int L_FLG = L_TAB + 11 * 512;
constexpr int L_ONE = L_FLG + 256;
constexpr int L_G = L_ONE + 1024;
constexpr int L_SEL = L_G + 512;
constexpr int L_BROW = L_SEL + 128;
constexpr int L_ACT = L_BROW + 2048;
constexpr int L_XS = L_ACT + 1024;
constexpr int L_JUNK = L_XS + 4096;
constexpr int LDS_BYTES = 163840;
static_assert(L_JUNK + 768 <= LDS_BYTES, "lds");
constexpr int M_CONS = L_ONE + 16, M_LAND = L_ONE + 544, M_GT = M_LAND + 288, M_HR = M_LAND + 296, M_CAR = M_LAND + 304, M_XS = M_LAND + 320, M_WR = M_LAND + 336, M_OWN = M_LAND + 352;
constexpr int M_S0 = L_SEL + 64;
static_assert(M_S0 - M_LAND == 1056, "offset in ASM_MA");
constexpr int FL_XD = L_FLG + 16, FL_TK = L_FLG + 24;
constexpr unsigned W_EPOCH = 0;
constexpr unsigned W_RG = 256;
constexpr unsigned W_R2 = W_RG + 2048 * 8;
constexpr unsigned W_HF = 32768;
constexpr unsigned W_GF = W_HF + 32 * 128;
constexpr unsigned W_YF = W_GF + 56 * 128;
constexpr unsigned W_HT = 49152;
constexpr unsigned W_AGG = 65536;
constexpr unsigned W_GU = W_AGG + 262144;
constexpr unsigned W_YP = W_GU + 56 * 16384;
constexpr unsigned W_H = 4u << 20;
constexpr unsigned W_DBG = 24u << 20;
constexpr unsigned W_END = 25u << 20;
static_assert(W_R2 + 8192 <= W_HF && W_YF + 1024 <= W_HT && W_YP + 128 * 56 * 256 <= W_H, "ws");
#define WS_BASE(n) (((n) - W_END) & ~(size_t)0x1fffff)
constexpr int SPIN_MAX = 1 << 18;

DEV float bf2f(unsigned v) { return __uint_as_float(v << 16); }
DEV float bfhi(unsigned v) { return __uint_as_float(v & 0xffff0000u); }
DEV u16 f2bf(float f) { return __builtin_bit_cast(u16, (__bf16)f); }
DEV float rbf(float f) { return bf2f(f2bf(f)); }
typedef __bf16 bf16x2_t __attribute__((ext_vector_type(2)));
DEV unsigned pk2(float a, float b) { return __builtin_bit_cast(unsigned, __builtin_convertvector((f32x2){a, b}, bf16x2_t)); }
DEV float fma1(float a, float b, float c) { float d; asm("v_fma_f32 %0, %1, %2, %3" : "=v"(d) : "v"(a), "v"(b), "v"(c)); return d; }
DEV float fnma1(float a, float b, float c) { float d; asm("v_fma_f32 %0, -%1, %2, %3" : "=v"(d) : "v"(a), "v"(b), "v"(c)); return d; }
DEV float mul1(float a, float b) { float d; asm("v_mul_f32 %0, %1, %2" : "=v"(d) : "v"(a), "v"(b)); return d; }
DEV f32x2 pkfma(f32x2 a, f32x2 b, f32x2 c) { f32x2 d; asm("v_pk_fma_f32 %0, %1, %2, %3" : "=v"(d) : "v"(a), "v"(b), "v"(c)); return d; }
DEV float add1(float a, float b) { float d; asm("v_add_f32 %0, %1, %2" : "=v"(d) : "v"(a), "v"(b)); return d; }
DEV f32x2 pkmul(f32x2 a, f32x2 b) { f32x2 d; asm("v_pk_mul_f32 %0, %1, %2" : "=v"(d) : "v"(a), "v"(b)); return d; }
DEV f32x2 cmul(f32x2 a, f32x2 b) { return (f32x2){fnma1(a.y, b.y, mul1(a.x, b.x)), fma1(a.y, b.x, mul1(a.x, b.y))}; }
template <int N> DEV void waitv() { asm volatile("s_waitcnt vmcnt(%0)" ::"n"(N) : "memory"); }
DEV void bar() { asm volatile("s_barrier" ::: "memory"); }
DEV unsigned lds_rd32(unsigned a) { unsigned v; asm volatile("ds_read_b32 %0, %1\n\ts_waitcnt lgkmcnt(0)" : "=v"(v) : "v"(a) : "memory"); return v; }
DEV u32x2 lds_rd64(unsigned a) { u32x2 v; asm volatile("ds_read_b64 %0, %1\n\ts_waitcnt lgkmcnt(0)" : "=v"(v) : "v"(a) : "memory"); return v; }
DEV u32x4 lds_rd128(unsigned a) { u32x4 v; asm volatile("ds_read_b128 %0, %1\n\ts_waitcnt lgkmcnt(0)" : "=v"(v) : "v"(a) : "memory"); return v; }
DEV void lds_wr32(unsigned a, unsigned v) { asm volatile("ds_write_b32 %0, %1" ::"v"(a), "v"(v) : "memory"); }
DEV void lds_wait_ge(unsigned a, unsigned need) {
#pragma unroll 1
  for (int it = 0; it < SPIN_MAX * 4; ++it) { if (__builtin_amdgcn_readfirstlane(lds_rd32(a)) >= need) break; __builtin_amdgcn_s_sleep(1); }
}
DEV void lds_wait2(unsigned a) {
#pragma unroll 1
  for (int it = 0; it < SPIN_MAX * 4; ++it) { const u32x2 v = lds_rd64(a); if (__builtin_amdgcn_readfirstlane(v.x & v.y)) break; __builtin_amdgcn_s_sleep(1); }
}
DEV void lds_wait0(unsigned a) {
#pragma unroll 1
  for (int it = 0; it < SPIN_MAX * 4; ++it) { if (__builtin_amdgcn_readfirstlane(lds_rd32(a)) == 0u) break; __builtin_amdgcn_s_sleep(1); }
}
typedef __amdgpu_buffer_rsrc_t rsrc_t;
DEV void mark(rsrc_t rz, unsigned a) {
  if ((threadIdx.x & 63) == 0) __builtin_amdgcn_raw_ptr_buffer_load_lds(rz, (LAS void*)(size_t)a, 4, 0, 0, 0, 0);
}
DEV u32x4 ld128(rsrc_t r, unsigned vo, unsigned so = 0) { return __builtin_amdgcn_raw_buffer_load_b128(r, vo, so, 0); }
#define FRESH() asm volatile("" ::: "memory")
DEV u32x4 ld128c(rsrc_t r, unsigned vo) { return __builtin_amdgcn_raw_buffer_load_b128(r, vo, 0, 16); }
DEV void st128c(rsrc_t r, unsigned vo, u32x4 v) {
  asm volatile("s_nop 4\n\tbuffer_store_dwordx4 %0, %1, %2, 0 offen sc1\n\ts_nop 7\n\ts_nop 7" ::"v"(v), "v"(vo), "s"(r) : "memory");
}
DEV void st64c(rsrc_t r, unsigned vo, u32x2 v) { __builtin_amdgcn_raw_buffer_store_b64(v, r, vo, 0, 16); }
DEV void ld8(u32x4 (&w)[8], rsrc_t r, unsigned vo, unsigned so, unsigned step) {
  asm volatile("s_nop 4\n\tbuffer_load_dwordx4 %0, %9, %10, %8 offen\n\ts_add_u32 %8, %8, %11\n\tbuffer_load_dwordx4 %1, %9, %10, %8 offen\n\ts_add_u32 %8, %8, %11\n\t"
               "buffer_load_dwordx4 %2, %9, %10, %8 offen\n\ts_add_u32 %8, %8, %11\n\tbuffer_load_dwordx4 %3, %9, %10, %8 offen\n\ts_add_u32 %8, %8, %11\n\t"
               "buffer_load_dwordx4 %4, %9, %10, %8 offen\n\ts_add_u32 %8, %8, %11\n\tbuffer_load_dwordx4 %5, %9, %10, %8 offen\n\ts_add_u32 %8, %8, %11\n\t"
               "buffer_load_dwordx4 %6, %9, %10, %8 offen\n\ts_add_u32 %8, %8, %11\n\tbuffer_load_dwordx4 %7, %9, %10, %8 offen"
               : "=&v"(w[0]), "=&v"(w[1]), "=&v"(w[2]), "=&v"(w[3]), "=&v"(w[4]), "=&v"(w[5]), "=&v"(w[6]), "=&v"(w[7]), "+&s"(so)
               : "v"(vo), "s"(r), "s"(step) : "memory", "scc");
}
DEV void ld8c(u32x4 (&w)[8], rsrc_t r, unsigned vo, unsigned so, unsigned step, int a, int b) {
  asm volatile("s_nop 4\n\ts_cmp_eq_u32 %12, %13\n\ts_cbranch_scc0 Lskip_%=\n\t"
               "buffer_load_dwordx4 %0, %9, %10, %8 offen" WNT "\n\ts_add_u32 %8, %8, %11\n\tbuffer_load_dwordx4 %1, %9, %10, %8 offen" WNT "\n\ts_add_u32 %8, %8, %11\n\t"
               "buffer_load_dwordx4 %2, %9, %10, %8 offen" WNT "\n\ts_add_u32 %8, %8, %11\n\tbuffer_load_dwordx4 %3, %9, %10, %8 offen" WNT "\n\ts_add_u32 %8, %8, %11\n\t"
               "buffer_load_dwordx4 %4, %9, %10, %8 offen" WNT "\n\ts_add_u32 %8, %8, %11\n\tbuffer_load_dwordx4 %5, %9, %10, %8 offen" WNT "\n\ts_add_u32 %8, %8, %11\n\t"
               "buffer_load_dwordx4 %6, %9, %10, %8 offen" WNT "\n\ts_add_u32 %8, %8, %11\n\tbuffer_load_dwordx4 %7, %9, %10, %8 offen" WNT "\n\tLskip_%=:"
               : "+&v"(w[0]), "+&v"(w[1]), "+&v"(w[2]), "+&v"(w[3]), "+&v"(w[4]), "+&v"(w[5]), "+&v"(w[6]), "+&v"(w[7]), "+&s"(so)
               : "v"(vo), "s"(r), "s"(step), "s"(a), "n"(b) : "memory", "scc");
}
DEV rsrc_t mk(const void* p, unsigned bytes) { return __builtin_amdgcn_make_buffer_rsrc((void*)p, 0, bytes, 0x00020000); }

DEV unsigned wave_maxu(unsigned v) {
  unsigned r;
  asm("s_nop 1\n\tv_max_u32_dpp %1, %1, %1 row_shr:1 row_mask:0xf bank_mask:0xf\n\ts_nop 1\n\tv_max_u32_dpp %1, %1, %1 row_shr:2 row_mask:0xf bank_mask:0xf\n\t"
      "s_nop 1\n\tv_max_u32_dpp %1, %1, %1 row_shr:4 row_mask:0xf bank_mask:0xf\n\ts_nop 1\n\tv_max_u32_dpp %1, %1, %1 row_shr:8 row_mask:0xf bank_mask:0xf\n\t"
      "s_nop 1\n\tv_max_u32_dpp %1, %1, %1 row_bcast:15 row_mask:0xa bank_mask:0xf\n\ts_nop 1\n\tv_max_u32_dpp %1, %1, %1 row_bcast:31 row_mask:0xc bank_mask:0xf\n\t"
      "s_nop 0\n\tv_readlane_b32 %0, %1, 63\n\ts_nop 1" : "=s"(r), "+v"(v));
  return r;
}
DEV float sum4(const float (&a)[4]) {
  auto p = __builtin_amdgcn_permlane16_swap(__float_as_uint(a[0]), __float_as_uint(a[1]), false, false);
  auto q = __builtin_amdgcn_permlane16_swap(__float_as_uint(a[2]), __float_as_uint(a[3]), false, false);
  const float u = __uint_as_float(p[0]) + __uint_as_float(p[1]), w = __uint_as_float(q[0]) + __uint_as_float(q[1]);
  auto r = __builtin_amdgcn_permlane32_swap(__float_as_uint(u), __float_as_uint(w), false, false);
  float v = __uint_as_float(r[0]) + __uint_as_float(r[1]);
  v += __uint_as_float((unsigned)__builtin_amdgcn_update_dpp(0, (int)__float_as_uint(v), 0x128, 0xf, 0xf, false));
  v += __uint_as_float((unsigned)__builtin_amdgcn_update_dpp(0, (int)__float_as_uint(v), 0x124, 0xf, 0xf, false));
  v += __uint_as_float((unsigned)__builtin_amdgcn_update_dpp(0, (int)__float_as_uint(v), 0x122, 0xf, 0xf, false));
  v += __uint_as_float((unsigned)__builtin_amdgcn_update_dpp(0, (int)__float_as_uint(v), 0x121, 0xf, 0xf, false));
  return v;
}
DEV float dot2(unsigned a, unsigned b, float c) { return __builtin_amdgcn_fdot2_f32_bf16(__builtin_bit_cast(bf16x2_t, a), __builtin_bit_cast(bf16x2_t, b), c, false); }
DEV unsigned route_code(unsigned u) { return (u & 0x8000u) ? (~u & 0xffffu) : (u | 0x8000u); }
DEV unsigned med3u(unsigned a, unsigned b, unsigned c) { unsigned d; asm("v_med3_u32 %0, %1, %2, %3" : "=v"(d) : "v"(a), "v"(b), "v"(c)); return d; }
DEV float key_val(unsigned key) {
  unsigned u = key >> 16;
  u = (u & 0x8000u) ? (u & 0x7fffu) : (~u & 0xffffu);
  return __uint_as_float(u << 16);
}
#define STAMP(k)
#define STAMPS()

DEV void unit_mma(const u32x4 (&w)[8], bf16x8 a, f32x4 (&d)[8]) {
#pragma unroll
  for (int j = 0; j < 8; ++j) {
    u32x4 b;
#pragma unroll
    for (int t = 0; t < 4; ++t) b[t] = __builtin_amdgcn_perm(w[2 * t + 1][j >> 1], w[2 * t][j >> 1], (j & 1) ? 0x07060302u : 0x05040100u);
    d[j] = __builtin_amdgcn_mfma_f32_16x16x32_bf16(a, __builtin_bit_cast(bf16x8, b), d[j], 0, 0, 0);
  }
}

DEV void fold(const f32x4 (&d)[8], f32x4 (&o)[2]) {
#pragma unroll
  for (int r = 0; r < 2; ++r)
#pragma unroll
    for (int q = 0; q < 4; ++q) {
      const float a0 = d[q][r], a1 = d[q][r + 2], b0 = d[q + 4][r], b1 = d[q + 4][r + 2];
      auto a = __builtin_amdgcn_permlane32_swap(__float_as_uint(a0), __float_as_uint(a1), false, false);
      auto b = __builtin_amdgcn_permlane32_swap(__float_as_uint(b0), __float_as_uint(b1), false, false);
      float t;
      asm("s_nop 1\n\tv_add_f32_dpp %0, %1, %2 row_ror:8 row_mask:0xf bank_mask:0x3\n\tv_add_f32_dpp %0, %3, %4 row_ror:8 row_mask:0xf bank_mask:0xc"
          : "=&v"(t) : "v"(a[1]), "v"(a[0]), "v"(b[0]), "v"(b[1]));
      o[r][q] = t;
    }
}
DEV void st2x128c(rsrc_t r, unsigned vo, unsigned so, const f32x4 (&o)[2]) {
  asm volatile("s_nop 4\n\tbuffer_store_dwordx4 %0, %2, %3, 0 offen sc1\n\tbuffer_store_dwordx4 %1, %2, %3, %4 offen sc1\n\ts_nop 7\n\ts_nop 7"
               ::"v"(o[0]), "v"(o[1]), "v"(vo), "s"(r), "s"(so) : "memory");
}
DEV void poll32(rsrc_t r, unsigned vo, unsigned tg, bool on) {
#pragma unroll 1
  for (int it = 0; it < SPIN_MAX; ++it) {
    FRESH();
    const unsigned v = __builtin_amdgcn_raw_buffer_load_b32(r, vo, 0, 16);
    if (__all(!on || v == tg)) break;
    __builtin_amdgcn_s_sleep(16);
  }
}

DEV void count1(rsrc_t r, unsigned so) {
  asm volatile("s_nop 4\n\ts_waitcnt vmcnt(0)\n\ts_mov_b64 exec, 1\n\tbuffer_atomic_add %0, off, %1, %2 sc1\n\ts_mov_b64 exec, -1" ::"v"(1u), "s"(r), "s"(so) : "memory");
}
DEV void count_wait(rsrc_t r, unsigned so, unsigned n) {
#pragma unroll 1
  for (int it = 0; it < SPIN_MAX; ++it) {
    unsigned v = 0;
    asm volatile("s_nop 4\n\ts_mov_b64 exec, 1\n\tbuffer_atomic_or %0, off, %1, %2 sc0 sc1\n\ts_mov_b64 exec, -1\n\ts_waitcnt vmcnt(0)" : "+v"(v) : "s"(r), "s"(so) : "memory");
    if (__builtin_amdgcn_readfirstlane(v) == n) break;
    __builtin_amdgcn_s_sleep(CWS);
  }
}

typedef __attribute__((ext_vector_type(8))) u64 u64x8;
__global__ __launch_bounds__(512, 2) void fused(const u16*, const u16*, const u16*, const u16*, const u16*, const u16*, const u16*, const u16*, const u16*,
                                                const u16*, const u16*, const u16*, const u16*, const u16*, char*, float*) {
#if __HIP_DEVICE_COMPILE__
  extern __shared__ __attribute__((aligned(16))) char smem_[];
  LAS char* smem = (LAS char*)smem_;
#define ARGREQ(W) u64x8 ka, kb; \
  asm volatile("s_load_dwordx16 %0, %2, 0x0\n\ts_load_dwordx16 %1, %2, 0x40" W : "=&s"(ka), "=&s"(kb) : "s"(__builtin_amdgcn_kernarg_segment_ptr()) : "memory")
#define ARGS() \
  const u16 *x = (const u16*)ka[0], *Lre = (const u16*)ka[1], *Lim = (const u16*)ka[2], *Bre = (const u16*)ka[3], *Bim = (const u16*)ka[4], *Cre = (const u16*)ka[5], \
            *Cim = (const u16*)ka[6], *logdt = (const u16*)ka[7], *Dskip = (const u16*)kb[0], *w_r = (const u16*)kb[1], *w_gate = (const u16*)kb[2], \
            *w_up = (const u16*)kb[3], *w_down = (const u16*)kb[4], *gate_w = (const u16*)kb[5]; \
  char* ws = (char*)kb[6]; float* out = (float*)kb[7]; \
  const rsrc_t rws = mk(ws, W_END), rx = mk(x, 2048 * 1024 * 2), rz = mk(x, 0)
  STAMP(0);
  int tid = threadIdx.x, bid = blockIdx.x;
  {
    unsigned t_[8], u_[2];
    asm volatile(ASM_EN : [w] "=&s"(t_[0]), [j] "=&s"(t_[1]), [t] "=&s"(t_[2]), [sa] "=&s"(t_[3]), [sc] "=&s"(t_[4]), [m] "=&s"(t_[5]), [r] "=&s"(t_[6]), [c] "=&s"(t_[7]), [v0] "=&v"(u_[0]), [v1] "=&v"(u_[1]) : [kp] "s"(__builtin_amdgcn_kernarg_segment_ptr()), [tid] "v"(threadIdx.x), [bid] "s"(blockIdx.x), [cpf] "n"(CODEPF), [ms0] "n"(M_S0) : "memory", "scc", "m0", "s20", "s21", "s22", "s23", "v255");
    asm volatile("" : "+s"(bid)); asm volatile("" : "+v"(tid));
  }
  const int lane = tid & 63, wid = __builtin_amdgcn_readfirstlane(tid >> 6), xcd = bid & 7, loc = bid >> 3;
  const int c = (xcd >> 1) * 4 + (loc >> 3), j = (xcd & 1) * 8 + (loc & 7);
  const int fr = lane & 15, fq = lane >> 4;

  if (__builtin_expect(wid == 4 || wid == 5, 1)) {
    ARGREQ("");
    const int l = wid - 4;
    __builtin_amdgcn_s_setprio(LPRIO);
    const int l3 = lane >> 3;
    {
      const unsigned one = tid >= 272 ? 1u : 0u;
      asm volatile("ds_write_b128 %0, %1" ::"v"(L_FLG + min(tid - 256, 111) * 16), "v"((u32x4){one, one, one, one}) : "memory");
    }
    int bud = SPIN_MAX * 4;
    unsigned dst = 1 ? l * 49152 : 131072 - l * 114688, ci = M_CONS - 64 + 1 * 16, t_s0, vc = 0;
    unsigned b0 = (c * 128 + l * (j * 64 - c * 128) + l3 * (8 - 7 * l)) * 2048 + ((lane & 7) ^ l3) * 16, wrap = 163840 - l * 32768;
    unsigned so[8];
#pragma unroll
    for (int q = 0; q < 8; ++q) so[q] = (64 * (q & 1) + (q >> 1) + l * (8 * q - 64 * (q & 1) - (q >> 1))) * 2048;
    unsigned sk = (((xcd >> 1) + 2 * (xcd & 1)) & 3) * 512 + 1 * 128, n = 16 - 1, mka = M_LAND + 1 * 8 + l * 4, v0 = b0 + sk;
    asm volatile("" : "+s"(dst), "+v"(b0), "+s"(wrap), "+s"(so[1]), "+s"(so[2]), "+s"(so[3]), "+s"(so[4]), "+s"(so[5]), "+s"(so[6]), "+s"(so[7]),
                 "+s"(sk), "+s"(mka), "+v"(v0));
    asm volatile("s_waitcnt lgkmcnt(0)" : "+s"(ka), "+s"(kb)::"memory");
    ARGS();
    {
      const rsrc_t r0 = mk(l ? Bre : x, (1u << 22) >> l), r1 = mk(l ? Bim : x + 4096, l ? (1u << 21) : (1u << 22) - 8192);
      STAMP(8);
      asm volatile(ASM_LA : [dst] "+&s"(dst), [sk] "+&s"(sk), [ci] "+&s"(ci), [n] "+&s"(n), [mk] "+&s"(mka), [bud] "+&s"(bud), [s0] "=&s"(t_s0), [v0] "+&v"(v0), [vc] "+&v"(vc) : [r0] "s"(r0), [r1] "s"(r1), [rz] "s"(rz), [wrap] "s"(wrap), [b0] "v"(b0), [vw] "v"(M_WR), [so0] "s"(so[0]), [so1] "s"(so[1]), [so2] "s"(so[2]), [so3] "s"(so[3]), [so4] "s"(so[4]), [so5] "s"(so[5]), [so6] "s"(so[6]), [so7] "s"(so[7]) : "memory", "scc", "m0", "v24", "v25", "v26", "v27", "v28", "v29", "v30", "v31");
    }
    STAMP(1);
    {
      const rsrc_t r0 = mk(Cre, 1 << 21), r1 = mk(Cim, 1 << 21);
      const unsigned b0 = (j * 64 + 4 * l3) * 2048 + (((lane & 7) ^ (l3 >> 1)) * 16), b1 = b0 ^ 64, wrap = 147456, cth = M_CONS + (15 - l) * 16;
      unsigned so[8];
#pragma unroll
      for (int q = 0; q < 8; ++q) so[q] = (32 * (q & 1) + (q >> 1)) * 2048;
      unsigned sk = ((0 * j + l) & 7) * 128, hi = (j & 8) * 128, n = 8, mka = M_LAND + (17 + l) * 8, v0 = b0 + sk + hi, v1 = b1 + sk + hi, t_s1;
      const rsrc_t rp = mk(l ? Cim : Cre, 1 << 21);
      const unsigned p0 = b0 + (hi ^ 0x400);
      unsigned vd = l * 8192 + lane * 16;
      asm volatile(ASM_LB : [dst] "+&s"(dst), [sk] "+&s"(sk), [ci] "+&s"(ci), [n] "+&s"(n), [mk] "+&s"(mka), [bud] "+&s"(bud), [s0] "=&s"(t_s0), [v0] "+&v"(v0), [vc] "+&v"(vc), [v1] "+&v"(v1), [hi] "+&s"(hi), [s1] "=&s"(t_s1), [vd] "+&v"(vd) : [r0] "s"(r0), [r1] "s"(r1), [rz] "s"(rz), [wrap] "s"(wrap), [b0] "v"(b0), [sk2] "s"(l * 128), [cth] "s"(cth), [b1] "v"(b1), [nend] "s"(3 + l), [rp] "s"(rp), [p0] "v"(p0), [p1] "v"(p0 ^ 64), [vm] "v"(M_CONS + 256), [vl] "v"(M_LAND + 26 * 8 + 4 * l), [so0] "s"(so[0]), [so1] "s"(so[1]), [so2] "s"(so[2]), [so3] "s"(so[3]), [so4] "s"(so[4]), [so5] "s"(so[5]), [so6] "s"(so[6]), [so7] "s"(so[7]) : "memory", "scc", "m0", "v24", "v25", "v26", "v27", "v28", "v29", "v30", "v31", "v32", "v33", "v34", "v35", "v36", "v37", "v38", "v39", "v40", "v41", "v42", "v43", "v44", "v45", "v46", "v47", "v48", "v49", "v50", "v51", "v52", "v53", "v54", "v55", "v56", "v57", "v58", "v59", "v60", "v61", "v62", "v63", "v64", "v65", "v66", "v67", "v68", "v69", "v70", "v71", "v72", "v73", "v74", "v75", "v76", "v77", "v78", "v79", "v80", "v81", "v82", "v83", "v84", "v85", "v86", "v87", "v88", "v89", "v90", "v91", "v92", "v93", "v94", "v95", "v96", "v97", "v98", "v99", "v100", "v101", "v102", "v103", "v104", "v105", "v106", "v107", "v108", "v109", "v110", "v111", "v112", "v113", "v114", "v115", "v116", "v117", "v118", "v119", "v120", "v121", "v122", "v123", "v124", "v125", "v126", "v127", "v128", "v129", "v130", "v131", "v132", "v133", "v134", "v135", "v136", "v137", "v138", "v139", "v140", "v141", "v142", "v143", "v144", "v145", "v146", "v147", "v148", "v149", "v150", "v151", "v152", "v153", "v154", "v155", "v156", "v157", "v158", "v159", "v160", "v161", "v162", "v163", "v164", "v165", "v166", "v167", "v168", "v169", "v170", "v171", "v172", "v173", "v174", "v175", "v176", "v177", "v178", "v179", "v180", "v181", "v182", "v183", "v184", "v185", "v186", "v187", "v188", "v189", "v190", "v191", "v192", "v193", "v194", "v195", "v196", "v197", "v198", "v199", "v200", "v201", "v202", "v203", "v204", "v205", "v206", "v207", "v208", "v209", "v210", "v211", "v212", "v213", "v214", "v215", "v216", "v217", "v218", "v219", "v220", "v221", "v222", "v223", "v224", "v225", "v226", "v227", "v228", "v229", "v230", "v231", "v232", "v233", "v234", "v235", "v236", "v237", "v238", "v239", "v240", "v241", "v242", "v243", "v244", "v245", "v246", "v247", "v248", "v249", "v250", "v251", "v252", "v253", "v254", "v255");
    }
    waitv<0>();
    STAMP(3);
    STAMPS();
    __builtin_amdgcn_endpgm();
  }

  if (__builtin_expect(wid >= 6, 1)) {
    const int xw = wid - 6;
    __builtin_amdgcn_s_setprio(XPRIO0);
    ARGREQ("\n\ts_waitcnt lgkmcnt(0)");
    ARGS();
    const int row0 = c * 128 + j * 8 + xw * 4;
    const int l = xw;
    const bool ffn = !NOFFN && loc < 28;
    const int sg = xcd * 7 + (loc >> 2), kq = loc & 3;
    u32x4 w1[4][8], w2[2][8];
    unsigned tagA, tagB;
    const int grp = __builtin_amdgcn_readfirstlane(!ffn ? 9 : WGRP);
    auto wburst = [&](int p, unsigned gate) __attribute__((always_inline)) {
      const bool on = grp == p;
      const rsrc_t r1 = mk(xw ? w_up : w_gate, D_ * F_ * 2), r2 = mk(w_down, D_ * F_ * 2);
      const unsigned o1 = (kq * 256 + 32 * (fr >> 3) + 8 * fq) * (F_ * 2) + (sg * 64 + 8 * (fr & 7)) * 2;
      const unsigned o2 = (sg * 64 + 32 * (fr >> 3) + 8 * fq) * 2048 + (kq * 256 + xw * 128 + 8 * (fr & 7)) * 2;
      if (on) {
        lds_wait0(M_LAND + gate * 8);
        mark(rz, M_WR + xw * 4);
        __builtin_amdgcn_s_sleep(WSLP);
      }
#pragma unroll
      for (int u = 0; u < 4; ++u) {
        ld8c(w1[u], r1, o1, u * 64 * (F_ * 2), F_ * 2, grp, p);
        if (W2G >= 0 && u == 1 && on) mark(rz, M_WR + 8 + xw * 4);
      }
      if (on || (p == 0 && !ffn))
        __builtin_amdgcn_raw_ptr_buffer_load_lds(mk(xw ? Cim : Cre, 1 << 21), (LAS void*)(smem + L_JUNK + 256 + xw * 256), 4, (j * 64 + lane) * 2048 + c * 128, 0, 0, 0);
    };
    {
      unsigned ep;
      u32x4 xv[8], wv[2], gv[2];
      unsigned pl[3];
      const rsrc_t rw = mk(w_r, 2048), rg = mk(gate_w, 2048);
      const rsrc_t rp0 = mk(Lre, 2048), rp1 = mk(Lim, 2048), rp2 = mk(logdt, 2048);
      const unsigned zero = 0, l16 = lane * 16, xo = row0 * 2048 + lane * 16, po = (j * 64 + lane) * 2;
      asm volatile("" ::"s"(rg), "s"(rp0), "s"(rp1), "s"(rp2), "s"(rx), "v"(xo), "v"(po));
      asm volatile("s_nop 4\n\ts_barrier\n\tbuffer_load_dword %0, %3, %5, 0 offen sc1\n\tbuffer_load_dwordx4 %1, %4, %6, 0 offen\n\tbuffer_load_dwordx4 %2, %4, %6, 0 offen offset:1024"
                   : "=&v"(ep), "=&v"(wv[0]), "=&v"(wv[1]) : "v"(zero), "v"(l16), "s"(rws), "s"(rw) : "memory");
      ld8(xv, rx, xo, 0, 1024);
      asm volatile("s_nop 4\n\tbuffer_load_dwordx4 %3, %9, %10, 0 offen\n\tbuffer_load_dwordx4 %4, %9, %10, 0 offen offset:1024\n\t"
                   "buffer_load_ushort %0, %5, %6, 0 offen\n\tbuffer_load_ushort %1, %5, %7, 0 offen\n\tbuffer_load_ushort %2, %5, %8, 0 offen"
                   : "=&v"(pl[0]), "=&v"(pl[1]), "=&v"(pl[2]), "=&v"(gv[0]), "=&v"(gv[1]) : "v"(po), "s"(rp0), "s"(rp1), "s"(rp2), "v"(l16), "s"(rg) : "memory");
      asm volatile("s_nop 4\n\tbuffer_atomic_swap %0, %1, %2, 0 offen sc1" ::"v"(zero), "v"(tid == 384 && bid < 96 ? W_HF + bid * 128 : 0xf0000000u), "s"(rws) : "memory");
      asm volatile("s_waitcnt vmcnt(0)" : "+v"(ep), "+v"(xv[0]), "+v"(xv[1]), "+v"(xv[2]), "+v"(xv[3]), "+v"(xv[4]), "+v"(xv[5]), "+v"(xv[6]),
                   "+v"(xv[7]), "+v"(wv[0]), "+v"(wv[1]), "+v"(gv[0]), "+v"(gv[1]), "+v"(pl[0]), "+v"(pl[1]), "+v"(pl[2])::"memory");
      tagA = ep + 1u; tagB = ep + 0x40000001u;
      float sr[4];
#pragma unroll
      for (int r = 0; r < 4; ++r) {
        sr[r] = 0.f;
#pragma unroll
        for (int h = 0; h < 2; ++h)
#pragma unroll
          for (int e = 0; e < 4; ++e) { const unsigned xu = xv[2 * r + h][e], wu = wv[h][e]; sr[r] = dot2(xu, wu, sr[r]); }
      }
      {
        const unsigned rb = route_code(f2bf(sum4(sr)));
        const unsigned r01 = (unsigned)__builtin_amdgcn_readlane((int)rb, 0) | ((unsigned)__builtin_amdgcn_readlane((int)rb, 16) << 16);
        const unsigned r23 = (unsigned)__builtin_amdgcn_readlane((int)rb, 32) | ((unsigned)__builtin_amdgcn_readlane((int)rb, 48) << 16);
        st128c(rws, lane == 0 ? W_R2 + row0 * 4 : 0xf0000000u, (u32x4){r01, tagA, r23, tagB});
      }
#pragma unroll
      for (int r = 0; r < 4; ++r) {
        sr[r] = 0.f;
#pragma unroll
        for (int h = 0; h < 2; ++h)
#pragma unroll
          for (int e = 0; e < 4; ++e) {
            const unsigned xu = xv[2 * r + h][e], gu = gv[h][e];
            sr[r] = dot2(xu, gu, sr[r]);
          }
      }
      {
        const float z = rbf(sum4(sr));
        st64c(rws, fr == 0 ? W_RG + (row0 + fq) * 8 : 0xf0000000u, (u32x2){__float_as_uint(rbf(1.f / (1.f + __expf(-z)))), tagB});
      }
      STAMP(8);
      __builtin_amdgcn_s_setprio(XPRIO);
#pragma unroll
      for (int u = 0; u < 4; ++u) asm volatile("" : "=v"(w1[u][0]), "=v"(w1[u][1]), "=v"(w1[u][2]), "=v"(w1[u][3]), "=v"(w1[u][4]), "=v"(w1[u][5]), "=v"(w1[u][6]), "=v"(w1[u][7]));
#pragma unroll
      for (int u = 0; u < 2; ++u) asm volatile("" : "=v"(w2[u][0]), "=v"(w2[u][1]), "=v"(w2[u][2]), "=v"(w2[u][3]), "=v"(w2[u][4]), "=v"(w2[u][5]), "=v"(w2[u][6]), "=v"(w2[u][7]));
      wburst(0, WS0);
      if (xw == 1) {
        const float lre = bf2f(pl[0]), lim = bf2f(pl[1]);
        const float dt = rbf(__expf(bf2f(pl[2])));
        const float er = expf(dt * lre);
        float sn, cs;
        sincosf(dt * lim, &sn, &cs);
        const f32x2 l1 = {mul1(er, cs), mul1(er, sn)};
        const float nr = l1.x - 1.f, ni = l1.y, den = fma1(lre, lre, mul1(lim, lim));
        const f32x2 cf = {fma1(nr, lre, mul1(ni, lim)) / den, fnma1(nr, lim, mul1(ni, lre)) / den};
        const f32x2 l2 = cmul(l1, l1), l4 = cmul(l2, l2), l8 = cmul(l4, l4), l16_ = cmul(l8, l8), l32 = cmul(l16_, l16_), l64 = cmul(l32, l32),
                    l128 = cmul(l64, l64), l256 = cmul(l128, l128), l384 = cmul(l256, l128), l512 = cmul(l256, l256);
        auto put = [&](int e, f32x2 v) { const float re = v.x, im = v.y; asm volatile("ds_write2_b32 %0, %1, %2 offset0:0 offset1:64" ::"v"(L_TAB + e * 512 + lane * 4), "v"(re), "v"(im) : "memory"); };
        put(0, l1); put(1, cf); put(2, l8); put(3, l16_); put(4, l32); put(5, l64); put(6, l512); put(7, (f32x2){1.f, 0.f}); put(8, l128); put(9, l256); put(10, l384);
        asm volatile("ds_write_b64 %0, %1" ::"v"(M_LAND + 16 * 8), "v"((u32x2){0u, 0u}) : "memory");
      }
    }
    unsigned m = 0, m2 = 0, m3 = 0;
    auto gkey = [&](unsigned k) { return (k & 0xffff0000u) | ((k & 28u) << 6) | (k & 3u) | (unsigned)(252 - lane * 4); };
    lds_wait0(M_LAND + (grp == 0 ? TKG0 : TKG) * 8);
    if (xw == 1) {
#pragma unroll
      for (int h = 0; h < 2; ++h) {
        poll32(rws, W_RG + (c * 128 + h * 64 + lane) * 8 + 4, tagB, true);
        __builtin_amdgcn_raw_ptr_buffer_load_lds(rws, (LAS void*)(smem + L_G + h * 256), 4, W_RG + (c * 128 + h * 64 + lane) * 8, 0, 0, 16);
      }
      mark(rz, M_GT);
      STAMP(1);
    } else {
      {
        u32x4 g[8];
#pragma unroll 1
        for (int it = 0; it < SPIN_MAX; ++it) {
          FRESH();
#pragma unroll
          for (int q = 0; q < 8; ++q) g[q] = ld128c(rws, W_R2 + lane * 16 + q * 1024);
          bool ok = true;
#pragma unroll
          for (int q = 0; q < 8; ++q) ok = ok && g[q].y == tagA && g[q].w == tagB;
          if (__all(ok)) break;
          __builtin_amdgcn_s_sleep(16);
        }
        STAMP(1);
        auto lkey = [&](int i) { const unsigned w = g[i >> 2][i & 2]; return ((i & 1) ? (w & 0xffff0000u) : (w << 16)) | (unsigned)(31 - i); };
        m = lkey(0); m2 = 0; m3 = 0;
#pragma unroll
        for (int i = 1; i < 32; ++i) {
          const unsigned k = lkey(i);
          m3 = med3u(m2, m3, k); m2 = med3u(m, m2, k); m = max(m, k);
        }
      }
      m = gkey(m); m2 = gkey(m2); m3 = gkey(m3);
    }
    asm volatile("" : "+v"(m), "+v"(m2), "+v"(m3));
    wburst(1, WS1);
    if (xw == 0) {
      unsigned mine = 0;
#pragma unroll 1
      for (int s = 0; s < 8; ++s) {
        const unsigned mm = wave_maxu(m);
        mine = (lane >> 3) == s ? mm : mine;
        const bool hit = m == mm;
        m = hit ? m2 : m; m2 = hit ? m3 : m2; m3 = hit ? 0u : m3;
      }
      if (__ballot(m == 0u)) {
        unsigned lim = 0xffffffffu;
#pragma unroll 1
        for (int s = 0; s < 8; ++s) {
          unsigned mm = 0;
#pragma unroll 1
          for (int q = 0; q < 8; ++q) {
            const u32x4 w = ld128c(rws, W_R2 + lane * 16 + q * 1024);
#pragma unroll
            for (int e = 0; e < 4; ++e) {
              const unsigned k = gkey(((e & 1) ? (w[e & 2] & 0xffff0000u) : (w[e & 2] << 16)) | (unsigned)(31 - 4 * q - e));
              mm = max(mm, k < lim || s == 0 ? k : 0u);
            }
          }
          lim = wave_maxu(mm);
          mine = (lane >> 3) == s ? lim : mine;
        }
      }
      const unsigned myidx = 2047u - (mine & 0xffffu);
      if (loc < 28) {
#pragma unroll
        for (int u = 0; u < 4; ++u)
          __builtin_amdgcn_raw_ptr_buffer_load_lds(rx, (LAS void*)(smem + L_XS + u * 1024), 16, myidx * 2048 + (loc & 3) * 512 + (lane & 7) * 16, u * 128, 0, 0);
      }
      const float myrs = rbf(1.f / (1.f + __expf(-key_val(mine))));
      if ((lane & 7) == 0) asm volatile("ds_write_b64 %0, %1\n\ts_waitcnt lgkmcnt(0)" ::"v"(L_SEL + lane), "v"((u32x2){myidx, __float_as_uint(myrs)}) : "memory");
      mark(rz, M_XS);
    }
    STAMP(2);
    wburst(2, WS2);
    STAMP(7);
    __builtin_amdgcn_s_setprio(FPRIO);
    lds_wait0(M_XS);
    const u32x2 sel = lds_rd64(L_SEL + (lane & 7) * 8);
    const unsigned fa = ((fr & 7) * 8 + 4 * (fr >> 3) + fq) * 16, lp = (fr & 7) * 32 + (fr >> 3) * 16, tk = 4 * (fq & 1) + 2 * (fq >> 1);
    if (ffn) {
      f32x4 d[8], o[2];
      u32x4 xa[4];
#pragma unroll
      for (int u = 0; u < 4; ++u) xa[u] = *(const LAS u32x4*)(smem + L_XS + u * 1024 + fa);
#pragma unroll
      for (int q = 0; q < 8; ++q) d[q] = (f32x4){0.f, 0.f, 0.f, 0.f};
      waitv<0>();
      STAMP(10);
      {
        if (W2G) lds_wait0(M_LAND + W2G * 8);
        __builtin_amdgcn_s_setprio(3);
        const rsrc_t r2 = mk(w_down, D_ * F_ * 2);
        const unsigned o2 = (sg * 64 + 32 * (fr >> 3) + 8 * fq) * 2048 + (kq * 256 + xw * 128 + 8 * (fr & 7)) * 2;
#pragma unroll
        for (int u = 0; u < 2; ++u) ld8(w2[u], r2, o2, u * 128, 2048);
        __builtin_amdgcn_s_setprio(FPRIO);
      }
      if (GCG) lds_wait0(M_LAND + GCG * 8);
#pragma unroll
      for (int u = 0; u < 4; ++u) {
        asm volatile("" : "+v"(w1[u][0]), "+v"(w1[u][1]), "+v"(w1[u][2]), "+v"(w1[u][3]), "+v"(w1[u][4]), "+v"(w1[u][5]), "+v"(w1[u][6]), "+v"(w1[u][7]));
        unit_mma(w1[u], __builtin_bit_cast(bf16x8, xa[u]), d);
        asm volatile("" : "+v"(d[0]), "+v"(d[1]), "+v"(d[2]), "+v"(d[3]), "+v"(d[4]), "+v"(d[5]), "+v"(d[6]), "+v"(d[7]));
      }
      fold(d, o);
      STAMP(11);
      st2x128c(rws, W_GU + ((sg * 8 + tk) * 8 + kq * 2 + l) * 256 + lp, 2048, o);
      count1(rws, W_GF + sg * 128);
      STAMP(3);
      {
        if (GUG) lds_wait0(M_LAND + GUG * 8);
        __builtin_amdgcn_s_setprio(GPRIO);
        count_wait(rws, W_GF + sg * 128, 8);
        f32x4 a[8];
#pragma unroll
        for (int q = 0; q < 8; ++q) a[q] = __builtin_bit_cast(f32x4, ld128c(rws, W_GU + (sg * 8 + 4 * l + fq) * 2048 + fr * 16 + q * 256));
        float a2[4];
#pragma unroll
        for (int e = 0; e < 4; ++e) {
          const float g = rbf(add1(add1(a[0][e], a[2][e]), add1(a[4][e], a[6][e]))), u = rbf(add1(add1(a[1][e], a[3][e]), add1(a[5][e], a[7][e])));
          a2[e] = mul1(rbf(g / add1(1.f, __expf(-g))), u);
        }
        asm volatile("ds_write_b64 %0, %1\n\ts_nop 3" ::"v"(L_ACT + (4 * l + fq) * 128 + fr * 8), "v"((u32x2){pk2(a2[0], a2[1]), pk2(a2[2], a2[3])}) : "memory");
        lds_wr32(FL_TK + l * 4, 1u);
        lds_wait_ge(FL_TK + (l ^ 1) * 4, 1u);
      }
      const u32x4 af = lds_rd128(L_ACT + fa);
      STAMP(4);
      waitv<0>();
#pragma unroll
      for (int u = 0; u < 2; ++u)
        asm volatile("" : "+v"(w2[u][0]), "+v"(w2[u][1]), "+v"(w2[u][2]), "+v"(w2[u][3]), "+v"(w2[u][4]), "+v"(w2[u][5]), "+v"(w2[u][6]), "+v"(w2[u][7]));
#pragma unroll
      for (int u = 0; u < 2; ++u) {
#pragma unroll
        for (int q = 0; q < 8; ++q) d[q] = (f32x4){0.f, 0.f, 0.f, 0.f};
        unit_mma(w2[u], __builtin_bit_cast(bf16x8, af), d);
        fold(d, o);
        st2x128c(rws, W_YP + ((tk * 16 + kq * 4 + l * 2 + u) * 56 + sg) * 256 + lp, 16 * 56 * 256, o);
      }
      count1(rws, W_YF + (kq * 2 + l) * 128);
      STAMP(5);
    }
    u64 rows = __ballot(lane < 8 && (sel.x >> 7) == (unsigned)c);
    if (l) rows &= rows - 1;
    if (!NOFFN && rows) {
      lds_wait0(M_CONS + (16 + YG) * 16);
      __builtin_amdgcn_s_setprio(YPRIO);
      count_wait(rws, W_YF + (j >> 1) * 128, 56);
      STAMP(9);
#pragma unroll 1
      for (; rows; rows &= rows - 1, rows &= rows - 1) {
        const int s = __builtin_ctzll(rows);
        const int t = __builtin_amdgcn_readlane(sel.x, s);
        const float rs = __uint_as_float(__builtin_amdgcn_readlane(sel.y, s));
        f32x4 a[14];
#pragma unroll
        for (int k = 0; k < 14; ++k) a[k] = __builtin_bit_cast(f32x4, ld128c(rws, W_YP + (s * 16 + j) * (56 * 256) + lane * 16 + k * 1024));
        const float xf = bf2f(__builtin_amdgcn_raw_buffer_load_b16(rx, t * 2048 + j * 128 + fr * 8 + fq * 2, 0, 0));
        const float omg = rbf(1.f - __uint_as_float(lds_rd32(L_G + (t & 127) * 4)));
        float l0 = a[0][0], l1 = a[0][1], h0 = a[0][2], h1 = a[0][3];
#pragma unroll
        for (int k = 1; k < 14; ++k) { l0 = add1(l0, a[k][0]); l1 = add1(l1, a[k][1]); h0 = add1(h0, a[k][2]); h1 = add1(h1, a[k][3]); }
        asm volatile("s_nop 1" : "+v"(l0), "+v"(l1), "+v"(h0), "+v"(h1));
        auto p = __builtin_amdgcn_permlane16_swap(__float_as_uint(l0), __float_as_uint(l1), false, false);
        auto q = __builtin_amdgcn_permlane16_swap(__float_as_uint(h0), __float_as_uint(h1), false, false);
        const float u = __uint_as_float(p[0]) + __uint_as_float(p[1]), w = __uint_as_float(q[0]) + __uint_as_float(q[1]);
        auto r = __builtin_amdgcn_permlane32_swap(__float_as_uint(u), __float_as_uint(w), false, false);
        const float y = __uint_as_float(r[0]) + __uint_as_float(r[1]);
        lds_wr32(L_BROW + s * 256 + fr * 16 + fq * 4, __float_as_uint(mul1(omg, rbf(xf + rbf(mul1(rbf(y), rs))) - xf)));
      }
    }
    lds_wr32(FL_XD + l * 4, 1u);
    waitv<0>();
    STAMP(6);
    STAMPS();
    __builtin_amdgcn_endpgm();
  }

  __builtin_amdgcn_s_setprio(MPRIO);
  ARGREQ("\n\ts_waitcnt lgkmcnt(0)");
  ARGS();
  bar();
  unsigned tagA, tagB;
  int bud = SPIN_MAX * 4;
  const int tl0 = 32 * fq + 2 * wid;
  u32x2 xe[2][4], dv;
  {
    const rsrc_t rd = mk(Dskip, 2048);
    const unsigned dofs = (j * 64 + 4 * fr) * 2, xo = (32 * wid + 4 * fq) * 128 + ((fr >> 1) ^ (4 * (fq & 1))) * 16 + (fr & 1) * 8;
    const unsigned ks = (j - 4 * ((xcd >> 1) + 2 * (xcd & 1))) & 15;
    const int x0 = (fq ^ (fr & 7)) * 16, x1 = ((4 + fq) ^ (fr & 7)) * 16;
    const unsigned pa = 16384 + (wid * 16 + fr) * 128, pb = 131072 + fr * 128;
    unsigned pa0 = pa + x0, pa1 = pa + x1, pb0 = pb + x0, pb1 = pb + x1;
    const unsigned tq = L_TAB + (wid * 16 + fq * 4) * 4, tcl = L_TAB + 6 * 512 + (wid * 16 + fr) * 4, tcw = tcl + 512 + fq * 512;
    const unsigned l16 = lane * 16, pubo = (fq * 4 + (fr >> 2)) * 16, plo = fr * 16 + (3 - fq) * 1024;
    const unsigned sag = W_AGG + (j * 16 + c) * 1024 + wid * 256, sht = W_HT + (j * 4 + wid) * 128;
    unsigned sh = W_H + (c * 64 + 4 * j + wid) * 8192, fl = M_LAND, scn = M_CONS - 16 + wid * 4;
    unsigned sl[4];
    u64 em[4];
#pragma unroll
    for (int q = 0; q < 4; ++q) {
      sl[q] = sag - (4 + 4 * q) * 1024;
      const int n = min(max(c - 4 * q, 0), 4);
      em[q] = n == 4 ? ~0ull : (1ull << (16 * n)) - 1ull;
    }
    unsigned t_sk, t_s0, t_s1, t_s2, t_sx;
    asm volatile(ASM_MA : [pa0] "+&v"(pa0), [pa1] "+&v"(pa1), [pb0] "+&v"(pb0), [pb1] "+&v"(pb1), [fl] "+&v"(fl), [sk] "=&s"(t_sk), [s0] "=&s"(t_s0), [s1] "=&s"(t_s1), [s2] "=&s"(t_s2), [sx] "=&s"(t_sx), [bud] "+&s"(bud), [scn] "+&s"(scn), [sh] "+&s"(sh), [tgA] "=&s"(tagA), [tgB] "=&s"(tagB), [xe00] "=&v"(xe[0][0]), [xe01] "=&v"(xe[0][1]), [xe02] "=&v"(xe[0][2]), [xe03] "=&v"(xe[0][3]), [xe10] "=&v"(xe[1][0]), [xe11] "=&v"(xe[1][1]), [xe12] "=&v"(xe[1][2]), [xe13] "=&v"(xe[1][3]), [dv] "=&v"(dv) : [tq] "v"(tq), [tcl] "v"(tcl), [tcw] "v"(tcw), [pubo] "v"(pubo), [plo] "v"(plo), [l16] "v"(l16), [xo] "v"(xo), [dofs] "v"(dofs), [wrap] "s"(-98304), [wrap2] "s"(-131072), [srd] "s"(rws), [ks] "s"(ks), [srdd] "s"(rd), [srz] "s"(rz), [sag] "s"(sag), [mcar] "s"(M_CAR), [eh] "s"((1ull << c) - 1ull), [sht] "s"(sht), [shc] "s"(sht + c * 4), [sl0] "s"(sl[0]), [em0] "s"(em[0]), [sl1] "s"(sl[1]), [em1] "s"(em[1]), [sl2] "s"(sl[2]), [em2] "s"(em[2]), [sl3] "s"(sl[3]), [em3] "s"(em[3]) : "memory", "scc", "vcc", "m0", "v64", "v65", "v66", "v67", "v68", "v69", "v70", "v71", "v72", "v73", "v74", "v75", "v76", "v77", "v78", "v79", "v80", "v81", "v82", "v83", "v84", "v85", "v86", "v87", "v88", "v89", "v90", "v91", "v92", "v93", "v94", "v95", "v96", "v97", "v98", "v99", "v100", "v101", "v102", "v103", "v104", "v105", "v106", "v107", "v108", "v109", "v110", "v111", "v112", "v113", "v114", "v115", "v116", "v117", "v118", "v119", "v120", "v121", "v122", "v123", "v124", "v125", "v126", "v127", "v128", "v129", "v130", "v131", "v132", "v133", "v134", "v135", "v136", "v137", "v138", "v139", "v140", "v141", "v142", "v143", "v144", "v145", "v146", "v147", "v148", "v149", "v150", "v151", "v152", "v153", "v154", "v155", "v156", "v157", "v158", "v159", "v160", "v161", "v162", "v163", "v164", "v165", "v166", "v167", "v168", "v169", "v170", "v171", "v172", "v173", "v174", "v175", "v176", "v177", "v178", "v179", "v180", "v181", "v182", "v183", "v184", "v185", "v186", "v187", "v188", "v189", "v190", "v191", "v192", "v193", "v194", "v195", "v196", "v197", "v198", "v199", "v200", "v201", "v202", "v203", "v204", "v205", "v206", "v207", "v208", "v209", "v210", "v211", "v212", "v213", "v214", "v215", "v216", "v217", "v218", "v219", "v220", "v221", "v222", "v223", "v224", "v225", "v226", "v227", "v228", "v229", "v230", "v231", "v232", "v233", "v234", "v235", "v236", "v237", "v238", "v239", "v240", "v241", "v242", "v243", "v244", "v245", "v246", "v247", "v248", "v249", "v250", "v251", "v252", "v253", "v254", "v255");
  }
  f32x2 g2[4], base[4][4];
  unsigned mine, dp[8];
  {
    unsigned selu, m0_, m1_;
#pragma unroll 1
    for (int it = 0; it < SPIN_MAX * 4; ++it) {
      asm volatile("ds_read_b32 %5, %7\n\tds_read_b32 %6, %7 offset:%10\n\tds_read_b32 %4, %8\n\tds_read_b64 %0, %9\n\tds_read_b64 %1, %9 offset:32\n\t"
                   "ds_read_b64 %2, %9 offset:64\n\tds_read_b64 %3, %9 offset:96\n\ts_waitcnt lgkmcnt(0)"
                   : "=&v"(g2[0]), "=&v"(g2[1]), "=&v"(g2[2]), "=&v"(g2[3]), "=&v"(selu), "=&v"(m0_), "=&v"(m1_)
                   : "v"(M_GT), "v"(L_SEL + (lane & 7) * 8), "v"(L_G + tl0 * 4), "n"(M_XS - M_GT) : "memory");
      if (__builtin_amdgcn_readfirstlane(m0_ | m1_) == 0u) break;
      __builtin_amdgcn_s_sleep(1);
    }
    f32x2 dd[4];
#pragma unroll
    for (int n = 0; n < 4; ++n) { const unsigned w = dv[n >> 1]; const float f = (n & 1) ? bfhi(w) : bf2f(w & 0xffffu); dd[n] = (f32x2){f, f}; }
#pragma unroll
    for (int r = 0; r < 4; ++r) {
      const unsigned ob = pk2(1.f - g2[r].x, 1.f - g2[r].y);
      const f32x2 omg = {bf2f(ob & 0xffffu), bfhi(ob)};
#pragma unroll
      for (int n = 0; n < 4; ++n) {
        const unsigned w0 = xe[0][r][n >> 1], w1 = xe[1][r][n >> 1];
        const f32x2 xf = {(n & 1) ? bfhi(w0) : bf2f(w0 & 0xffffu), (n & 1) ? bfhi(w1) : bf2f(w1 & 0xffffu)};
        base[r][n] = pkmul(xf, pkfma(g2[r], dd[n], omg));
      }
    }
#pragma unroll
    for (int r = 0; r < 4; ++r) asm volatile("" : "+v"(base[r][0]), "+v"(base[r][1]), "+v"(base[r][2]), "+v"(base[r][3]));
    asm volatile("s_nop 4\n\ts_waitcnt vmcnt(0)\n\ts_mov_b64 exec, 1\n\tbuffer_atomic_add %0, off, %1, %2 sc1\n\tds_write_b32 %3, %4\n\ts_mov_b64 exec, -1"
                 ::"v"(1u), "s"(rws), "s"(W_HF + (c * 2 + (j >> 3)) * 128), "v"(M_OWN + wid * 4), "v"(0u) : "memory");
    STAMP(9);
    mine = (unsigned)__ballot(lane < 8 && (selu >> 7) == (unsigned)c && ((selu >> 1) & 3u) == (unsigned)wid);
#pragma unroll
    for (int q = 0; q < 8; ++q) dp[q] = L_ONE - 16;
#pragma unroll 1
    for (unsigned m = mine; m; m &= m - 1) {
      const int s = __builtin_ctz(m), t = __builtin_amdgcn_readlane((int)selu, s) & 127;
#pragma unroll
      for (int q = 0; q < 8; ++q) dp[q] = t == tl0 + 8 * (q >> 1) + (q & 1) ? L_BROW + s * 256 + fr * 16 : dp[q];
    }
  }
  f32x4 ac2[2][4];
  float bs[8][4];
#pragma unroll
  for (int q = 0; q < 8; ++q)
#pragma unroll
    for (int n = 0; n < 4; ++n) bs[q][n] = base[q >> 1][n][q & 1];
  {
#pragma unroll
    for (int m = 0; m < 2; ++m)
#pragma unroll
      for (int n = 0; n < 4; ++n) ac2[m][n] = (f32x4){0.f, 0.f, 0.f, 0.f};
    unsigned pc[4], sb = (j & (0 ? 7 : 8)) * 32768, fl2 = M_LAND + 17 * 8, scn2 = M_CONS + 256 + wid * 4;
    unsigned vo = W_H + c * 524288 + 0 * (j & 8) * 32768 + wid * 2048 + lane * 16;
    const unsigned hr = M_HR;
#pragma unroll
    for (int q = 0; q < 4; ++q) pc[q] = fr * 128 + (((2 * q + (fq >> 1)) ^ ((fr >> 1) & 7)) * 16) + 8192 * (fq & 1);
    const unsigned sf = W_HF + (c * 2 + (j >> 3)) * 128, sf1 = sf ^ 128;
    unsigned t_sk, t_sa, t_s0, t_s1;
    const unsigned hb_ = W_H + c * 524288, f9 = M_LAND + 17 * 8 + min(lane, 17) * 4;
    vo = (j & 8) * 32768 + wid * 2048 + lane * 16; fl2 = M_LAND + 26 * 8;
    asm volatile(ASM_MB : [c00] "+&v"(ac2[0][0]), [c01] "+&v"(ac2[0][1]), [c02] "+&v"(ac2[0][2]), [c03] "+&v"(ac2[0][3]), [c10] "+&v"(ac2[1][0]), [c11] "+&v"(ac2[1][1]), [c12] "+&v"(ac2[1][2]), [c13] "+&v"(ac2[1][3]), [pc0] "+&v"(pc[0]), [pc1] "+&v"(pc[1]), [pc2] "+&v"(pc[2]), [pc3] "+&v"(pc[3]), [b00] "+&v"(bs[0][0]), [b01] "+&v"(bs[0][1]), [b02] "+&v"(bs[0][2]), [b03] "+&v"(bs[0][3]), [b10] "+&v"(bs[1][0]), [b11] "+&v"(bs[1][1]), [b12] "+&v"(bs[1][2]), [b13] "+&v"(bs[1][3]), [b20] "+&v"(bs[2][0]), [b21] "+&v"(bs[2][1]), [b22] "+&v"(bs[2][2]), [b23] "+&v"(bs[2][3]), [b30] "+&v"(bs[3][0]), [b31] "+&v"(bs[3][1]), [b32] "+&v"(bs[3][2]), [b33] "+&v"(bs[3][3]), [b40] "+&v"(bs[4][0]), [b41] "+&v"(bs[4][1]), [b42] "+&v"(bs[4][2]), [b43] "+&v"(bs[4][3]), [b50] "+&v"(bs[5][0]), [b51] "+&v"(bs[5][1]), [b52] "+&v"(bs[5][2]), [b53] "+&v"(bs[5][3]), [b60] "+&v"(bs[6][0]), [b61] "+&v"(bs[6][1]), [b62] "+&v"(bs[6][2]), [b63] "+&v"(bs[6][3]), [b70] "+&v"(bs[7][0]), [b71] "+&v"(bs[7][1]), [b72] "+&v"(bs[7][2]), [b73] "+&v"(bs[7][3]), [vo] "+&v"(vo), [fl] "+&v"(fl2), [sg] "=&s"(t_sk), [s0] "=&s"(t_s0), [s1] "=&s"(t_s1), [bud] "+&s"(bud) : [d0] "v"(dp[0]), [d1] "v"(dp[1]), [d2] "v"(dp[2]), [d3] "v"(dp[3]), [d4] "v"(dp[4]), [d5] "v"(dp[5]), [d6] "v"(dp[6]), [d7] "v"(dp[7]), [xd] "v"(FL_XD), [mine] "s"(mine), [srd] "s"(rws), [srz] "s"(rz), [hr] "v"(hr), [f9] "v"(f9), [w] "s"(wid), [scn] "s"(scn2), [sf] "s"(sf), [sf1] "s"(sf1), [k2] "s"(0x4000), [h0] "s"(hb_ + 0), [h1] "s"(hb_ + 8192), [h2] "s"(hb_ + 16384), [h3] "s"(hb_ + 24576), [h4] "s"(hb_ + 32768), [h5] "s"(hb_ + 40960), [h6] "s"(hb_ + 49152), [h7] "s"(hb_ + 57344) : "memory", "scc", "vcc", "m0", "v64", "v65", "v66", "v67", "v68", "v69", "v70", "v71", "v72", "v73", "v74", "v75", "v76", "v77", "v78", "v79", "v80", "v81", "v82", "v83", "v84", "v85", "v86", "v87", "v88", "v89", "v90", "v91", "v92", "v93", "v94", "v95", "v96", "v97", "v98", "v99", "v100", "v101", "v102", "v103", "v104", "v105", "v106", "v107", "v108", "v109", "v110", "v111", "v112", "v113", "v114", "v115", "v116", "v117", "v118", "v119", "v120", "v121", "v122", "v123", "v124", "v125", "v126", "v127", "v128", "v129", "v130", "v131", "v132", "v133", "v134", "v135", "v136", "v137", "v138", "v139", "v140", "v141", "v142", "v143", "v144", "v145", "v146", "v147", "v148", "v149", "v150", "v151", "v152", "v153", "v154", "v155", "v156", "v157", "v158", "v159", "v160", "v161", "v162", "v163", "v164", "v165", "v166", "v167", "v168", "v169", "v170", "v171", "v172", "v173", "v174", "v175", "v176", "v177", "v178", "v179", "v180", "v181", "v182", "v183", "v184", "v185", "v186", "v187", "v188", "v189", "v190", "v191", "v192", "v193", "v194", "v195", "v196");
  }
  STAMP(6);
  asm volatile("" : "+v"(g2[0]), "+v"(g2[1]), "+v"(g2[2]), "+v"(g2[3]));
  int ln2 = lane;
  asm volatile("" : "+v"(ln2));
  const int fr2 = ln2 & 15, tl2 = 32 * (ln2 >> 4) + 2 * wid;
  f32x4 ov[2][4];
#pragma unroll
  for (int e = 0; e < 2; ++e)
#pragma unroll
    for (int r = 0; r < 4; ++r) {
      const float gg = 2.f * g2[r][e];
#pragma unroll
      for (int n = 0; n < 4; ++n) ov[e][r][n] = fma1(gg, ac2[e][n][r], bs[2 * r + e][n]);
    }
  {
    const rsrc_t ro = mk(out, 2048 * 1024 * 4);
    const unsigned oo = (c * 128 + tl2) * 4096 + (j * 64 + 4 * fr2) * 4, oo2 = oo + 4096;
    asm volatile("s_nop 4\n\t"
                 "buffer_store_dwordx4 %0, %8, %10, 0 offen" OST "\n\tbuffer_store_dwordx4 %4, %9, %10, 0 offen" OST "\n\t"
                 "buffer_store_dwordx4 %1, %8, %10, %11 offen" OST "\n\tbuffer_store_dwordx4 %5, %9, %10, %11 offen" OST "\n\t"
                 "buffer_store_dwordx4 %2, %8, %10, %12 offen" OST "\n\tbuffer_store_dwordx4 %6, %9, %10, %12 offen" OST "\n\t"
                 "buffer_store_dwordx4 %3, %8, %10, %13 offen" OST "\n\tbuffer_store_dwordx4 %7, %9, %10, %13 offen" OST "\n\t"
                 "s_nop 7\n\ts_nop 7"
                 ::"v"(ov[0][0]), "v"(ov[0][1]), "v"(ov[0][2]), "v"(ov[0][3]), "v"(ov[1][0]), "v"(ov[1][1]), "v"(ov[1][2]), "v"(ov[1][3]), "v"(oo), "v"(oo2), "s"(ro),
                 "s"(32768), "s"(65536), "s"(98304) : "memory");
  }
  if (c == 15 && j == 0 && wid == 0 && lane == 0) st64c(rws, W_EPOCH, (u32x2){tagA, tagB});
  STAMP(7);
  STAMPS();
#endif
}

extern "C" void kernel_launch(void* const* d_in, const int* in_sizes, int n_in, void* d_out, int out_size, void* d_ws, size_t ws_size,
                              hipStream_t stream) {
  (void)hipFuncSetAttribute((const void*)fused, hipFuncAttributeMaxDynamicSharedMemorySize, LDS_BYTES);
  fused<<<256, 512, LDS_BYTES, stream>>>((const u16*)d_in[0], (const u16*)d_in[1], (const u16*)d_in[2], (const u16*)d_in[3], (const u16*)d_in[4],
                                         (const u16*)d_in[5], (const u16*)d_in[6], (const u16*)d_in[7], (const u16*)d_in[8], (const u16*)d_in[10],
                                         (const u16*)d_in[11], (const u16*)d_in[12], (const u16*)d_in[13], (const u16*)d_in[14], (char*)d_ws + WS_BASE(ws_size),
                                         (float*)d_out);
}
```

```cpp
#include <hip/hip_runtime.h>
#include <hip/hip_bf16.h>
#include <stdint.h>

typedef __attribute__((ext_vector_type(8))) short bf16x8;
typedef __attribute__((ext_vector_type(4))) float f32x4;
typedef __attribute__((ext_vector_type(2))) float f32x2;
typedef __attribute__((ext_vector_type(4))) unsigned u32x4;
typedef __attribute__((ext_vector_type(2))) unsigned u32x2;
typedef unsigned short u16;
typedef unsigned long long u64;
#define LAS __attribute__((address_space(3)))
#define DEV __device__ __forceinline__

#define ASM_MA \
  "s_nop 4\n\t" \
  "buffer_load_dword v228, off, %[srd], 0 sc1\n\t" \
  "buffer_load_dwordx2 %[dv], %[dofs], %[srdd], 0 offen\n\t" \
  "s_mov_b32 %[sk], 0\n\t" \
  "s_mov_b32 %[sx], 0x20000\n\t" \
  "Lsp0_%=:\n\t" \
  "ds_read_b128 v[128:131], %[fl] offset:1056\n\t" \
  "s_sub_i32 %[bud], %[bud], 1\n\t" \
  "s_cmp_lt_i32 %[bud], 0\n\t" \
  "s_cbranch_scc1 Lb0_%=\n\t" \
  "s_waitcnt lgkmcnt(0)\n\t" \
  "v_or3_b32 v128, v128, v129, v130\n\t" \
  "v_or_b32 v128, v128, v131\n\t" \
  "s_nop 0\n\t" \
  "v_readfirstlane_b32 %[s0], v128\n\t" \
  "s_cmp_eq_u32 %[s0], 0\n\t" \
  "s_cbranch_scc0 Lsp0_%=\n\t" \
  "Lb0_%=:\n\t" \
  "s_cmp_eq_u32 %[sk], %[ks]\n\t" \
  "s_cbranch_scc1 Lx0_%=\n\t" \
  "Lxb0_%=:\n\t" \
  "s_mov_b32 m0, %[scn]\n\t" \
  "v_add_u32 %[fl], 8, %[fl]\n\t" \
  "ds_read_b128 v[208:211], %[pa0] offset:0\n\t" \
  "ds_read_b128 v[212:215], %[pa0] offset:8192\n\t" \
  "ds_read_b128 v[216:219], %[pa1] offset:0\n\t" \
  "ds_read_b128 v[220:223], %[pa1] offset:8192\n\t" \
  "ds_read_b128 v[176:179], %[pb0] offset:0\n\t" \
  "ds_read_b128 v[180:183], %[pb0] offset:2048\n\t" \
  "ds_read_b128 v[184:187], %[pb0] offset:4096\n\t" \
  "ds_read_b128 v[188:191], %[pb0] offset:6144\n\t" \
  "ds_read_b128 v[192:195], %[pb0] offset:8192\n\t" \
  "ds_read_b128 v[196:199], %[pb0] offset:10240\n\t" \
  "ds_read_b128 v[200:203], %[pb0] offset:12288\n\t" \
  "ds_read_b128 v[204:207], %[pb0] offset:14336\n\t" \
  "ds_read_b64 v[224:225], %[fl]\n\t" \
  "s_waitcnt lgkmcnt(7)\n\t" \
  "v_mfma_f32_16x16x32_bf16 v[64:67], v[208:211], v[176:179], 0\n\t" \
  "ds_read_b128 v[144:147], %[pb1] offset:0\n\t" \
  "s_add_u32 %[sk], %[sk], 1\n\t" \
  "v_mfma_f32_16x16x32_bf16 v[96:99], v[212:215], v[176:179], 0\n\t" \
  "s_and_b32 %[s1], %[sk], 3\n\t" \
  "s_cselect_b32 %[s1], 0x8000, %[wrap]\n\t" \
  "v_mfma_f32_16x16x32_bf16 v[68:71], v[208:211], v[180:183], 0\n\t" \
  "ds_read_b128 v[148:151], %[pb1] offset:2048\n\t" \
  "s_add_u32 %[sx], %[sx], 0x8000\n\t" \
  "v_mfma_f32_16x16x32_bf16 v[100:103], v[212:215], v[180:183], 0\n\t" \
  "s_cmp_eq_u32 %[sx], 0x28000\n\t" \
  "s_waitcnt lgkmcnt(7)\n\t" \
  "v_mfma_f32_16x16x32_bf16 v[72:75], v[208:211], v[184:187], 0\n\t" \
  "ds_read_b128 v[152:155], %[pb1] offset:4096\n\t" \
  "s_cselect_b32 %[s2], %[wrap2], 0x8000\n\t" \
  "v_mfma_f32_16x16x32_bf16 v[104:107], v[212:215], v[184:187], 0\n\t" \
  "s_cselect_b32 %[sx], 0, %[sx]\n\t" \
  "v_add_u32 %[pa0], %[s1], %[pa0]\n\t" \
  "v_mfma_f32_16x16x32_bf16 v[76:79], v[208:211], v[188:191], 0\n\t" \
  "ds_read_b128 v[156:159], %[pb1] offset:6144\n\t" \
  "v_add_u32 %[pb0], %[s2], %[pb0]\n\t" \
  "v_mfma_f32_16x16x32_bf16 v[108:111], v[212:215], v[188:191], 0\n\t" \
  "s_waitcnt lgkmcnt(5)\n\t" \
  "v_mfma_f32_16x16x32_bf16 v[80:83], v[208:211], v[192:195], 0\n\t" \
  "ds_read_b128 v[160:163], %[pb1] offset:8192\n\t" \
  "v_add_u32 %[pa1], %[s1], %[pa1]\n\t" \
  "v_mfma_f32_16x16x32_bf16 v[112:115], v[212:215], v[192:195], 0\n\t" \
  "s_mov_b64 exec, 1\n\t" \
  "buffer_load_dword %[l16], %[srz], 0 offen lds\n\t" \
  "s_mov_b64 exec, -1\n\t" \
  "v_mfma_f32_16x16x32_bf16 v[84:87], v[208:211], v[196:199], 0\n\t" \
  "ds_read_b128 v[164:167], %[pb1] offset:10240\n\t" \
  "s_add_u32 m0, m0, 16\n\t" \
  "v_mfma_f32_16x16x32_bf16 v[116:119], v[212:215], v[196:199], 0\n\t" \
  "s_waitcnt lgkmcnt(6)\n\t" \
  "v_or_b32 v224, v224, v225\n\t" \
  "v_mfma_f32_16x16x32_bf16 v[88:91], v[208:211], v[200:203], 0\n\t" \
  "ds_read_b128 v[168:171], %[pb1] offset:12288\n\t" \
  "v_readfirstlane_b32 %[s0], v224\n\t" \
  "v_mfma_f32_16x16x32_bf16 v[120:123], v[212:215], v[200:203], 0\n\t" \
  "s_cmp_eq_u32 %[s0], 0\n\t" \
  "s_cbranch_scc0 Lsp1_%=\n\t" \
  "Lb1_%=:\n\t" \
  "v_mfma_f32_16x16x32_bf16 v[92:95], v[208:211], v[204:207], 0\n\t" \
  "ds_read_b128 v[172:175], %[pb1] offset:14336\n\t" \
  "v_add_u32 %[fl], 8, %[fl]\n\t" \
  "v_mfma_f32_16x16x32_bf16 v[124:127], v[212:215], v[204:207], 0\n\t" \
  "s_branch Lmid_%=\n\t" \
  "Lloop_%=:\n\t" \
  "s_waitcnt lgkmcnt(6)\n\t" \
  "v_mfma_f32_16x16x32_bf16 v[64:67], v[136:139], v[144:147], v[64:67]\n\t" \
  "ds_read_b128 v[208:211], %[pa0] offset:0\n\t" \
  "v_mfma_f32_16x16x32_bf16 v[96:99], v[140:143], v[144:147], v[96:99]\n\t" \
  "ds_read_b128 v[212:215], %[pa0] offset:8192\n\t" \
  "v_mfma_f32_16x16x32_bf16 v[68:71], v[136:139], v[148:151], v[68:71]\n\t" \
  "ds_read_b128 v[216:219], %[pa1] offset:0\n\t" \
  "v_mfma_f32_16x16x32_bf16 v[100:103], v[140:143], v[148:151], v[100:103]\n\t" \
  "ds_read_b128 v[220:223], %[pa1] offset:8192\n\t" \
  "s_waitcnt lgkmcnt(8)\n\t" \
  "v_mfma_f32_16x16x32_bf16 v[72:75], v[136:139], v[152:155], v[72:75]\n\t" \
  "ds_read_b128 v[176:179], %[pb0] offset:0\n\t" \
  "v_mfma_f32_16x16x32_bf16 v[104:107], v[140:143], v[152:155], v[104:107]\n\t" \
  "ds_read_b128 v[180:183], %[pb0] offset:2048\n\t" \
  "v_mfma_f32_16x16x32_bf16 v[76:79], v[136:139], v[156:159], v[76:79]\n\t" \
  "v_add_u32 %[pb1], %[s2], %[pb1]\n\t" \
  "v_mfma_f32_16x16x32_bf16 v[108:111], v[140:143], v[156:159], v[108:111]\n\t" \
  "ds_read_b128 v[184:187], %[pb0] offset:4096\n\t" \
  "s_waitcnt lgkmcnt(7)\n\t" \
  "v_mfma_f32_16x16x32_bf16 v[80:83], v[136:139], v[160:163], v[80:83]\n\t" \
  "ds_read_b128 v[188:191], %[pb0] offset:6144\n\t" \
  "v_mfma_f32_16x16x32_bf16 v[112:115], v[140:143], v[160:163], v[112:115]\n\t" \
  "ds_read_b128 v[192:195], %[pb0] offset:8192\n\t" \
  "v_mfma_f32_16x16x32_bf16 v[84:87], v[136:139], v[164:167], v[84:87]\n\t" \
  "s_waitcnt lgkmcnt(5)\n\t" \
  "s_mov_b64 exec, 1\n\t" \
  "buffer_load_dword %[l16], %[srz], 0 offen lds\n\t" \
  "s_mov_b64 exec, -1\n\t" \
  "v_mfma_f32_16x16x32_bf16 v[116:119], v[140:143], v[164:167], v[116:119]\n\t" \
  "ds_read_b128 v[196:199], %[pb0] offset:10240\n\t" \
  "s_add_u32 m0, m0, 16\n\t" \
  "v_mfma_f32_16x16x32_bf16 v[88:91], v[136:139], v[168:171], v[88:91]\n\t" \
  "s_cmp_eq_u32 %[sk], %[ks]\n\t" \
  "s_cbranch_scc1 Lx2_%=\n\t" \
  "Lxb2_%=:\n\t" \
  "v_mfma_f32_16x16x32_bf16 v[120:123], v[140:143], v[168:171], v[120:123]\n\t" \
  "ds_read_b128 v[200:203], %[pb0] offset:12288\n\t" \
  "v_mfma_f32_16x16x32_bf16 v[92:95], v[136:139], v[172:175], v[92:95]\n\t" \
  "ds_read_b128 v[204:207], %[pb0] offset:14336\n\t" \
  "v_mfma_f32_16x16x32_bf16 v[124:127], v[140:143], v[172:175], v[124:127]\n\t" \
  "ds_read_b64 v[224:225], %[fl]\n\t" \
  "s_waitcnt lgkmcnt(7)\n\t" \
  "v_mfma_f32_16x16x32_bf16 v[64:67], v[208:211], v[176:179], v[64:67]\n\t" \
  "ds_read_b128 v[144:147], %[pb1] offset:0\n\t" \
  "s_add_u32 %[sk], %[sk], 1\n\t" \
  "v_mfma_f32_16x16x32_bf16 v[96:99], v[212:215], v[176:179], v[96:99]\n\t" \
  "s_and_b32 %[s1], %[sk], 3\n\t" \
  "s_cselect_b32 %[s1], 0x8000, %[wrap]\n\t" \
  "v_mfma_f32_16x16x32_bf16 v[68:71], v[208:211], v[180:183], v[68:71]\n\t" \
  "ds_read_b128 v[148:151], %[pb1] offset:2048\n\t" \
  "s_add_u32 %[sx], %[sx], 0x8000\n\t" \
  "v_mfma_f32_16x16x32_bf16 v[100:103], v[212:215], v[180:183], v[100:103]\n\t" \
  "s_cmp_eq_u32 %[sx], 0x28000\n\t" \
  "s_waitcnt lgkmcnt(7)\n\t" \
  "v_mfma_f32_16x16x32_bf16 v[72:75], v[208:211], v[184:187], v[72:75]\n\t" \
  "ds_read_b128 v[152:155], %[pb1] offset:4096\n\t" \
  "s_cselect_b32 %[s2], %[wrap2], 0x8000\n\t" \
  "v_mfma_f32_16x16x32_bf16 v[104:107], v[212:215], v[184:187], v[104:107]\n\t" \
  "s_cselect_b32 %[sx], 0, %[sx]\n\t" \
  "v_add_u32 %[pa0], %[s1], %[pa0]\n\t" \
  "v_mfma_f32_16x16x32_bf16 v[76:79], v[208:211], v[188:191], v[76:79]\n\t" \
  "ds_read_b128 v[156:159], %[pb1] offset:6144\n\t" \
  "v_add_u32 %[pb0], %[s2], %[pb0]\n\t" \
  "v_mfma_f32_16x16x32_bf16 v[108:111], v[212:215], v[188:191], v[108:111]\n\t" \
  "s_waitcnt lgkmcnt(5)\n\t" \
  "v_mfma_f32_16x16x32_bf16 v[80:83], v[208:211], v[192:195], v[80:83]\n\t" \
  "ds_read_b128 v[160:163], %[pb1] offset:8192\n\t" \
  "v_add_u32 %[pa1], %[s1], %[pa1]\n\t" \
  "v_mfma_f32_16x16x32_bf16 v[112:115], v[212:215], v[192:195], v[112:115]\n\t" \
  "v_mfma_f32_16x16x32_bf16 v[84:87], v[208:211], v[196:199], v[84:87]\n\t" \
  "ds_read_b128 v[164:167], %[pb1] offset:10240\n\t" \
  "v_mfma_f32_16x16x32_bf16 v[116:119], v[212:215], v[196:199], v[116:119]\n\t" \
  "s_waitcnt lgkmcnt(6)\n\t" \
  "v_or_b32 v224, v224, v225\n\t" \
  "v_mfma_f32_16x16x32_bf16 v[88:91], v[208:211], v[200:203], v[88:91]\n\t" \
  "ds_read_b128 v[168:171], %[pb1] offset:12288\n\t" \
  "v_readfirstlane_b32 %[s0], v224\n\t" \
  "v_mfma_f32_16x16x32_bf16 v[120:123], v[212:215], v[200:203], v[120:123]\n\t" \
  "s_cmp_eq_u32 %[s0], 0\n\t" \
  "s_cbranch_scc0 Lsp3_%=\n\t" \
  "Lb3_%=:\n\t" \
  "v_mfma_f32_16x16x32_bf16 v[92:95], v[208:211], v[204:207], v[92:95]\n\t" \
  "ds_read_b128 v[172:175], %[pb1] offset:14336\n\t" \
  "v_add_u32 %[fl], 8, %[fl]\n\t" \
  "v_mfma_f32_16x16x32_bf16 v[124:127], v[212:215], v[204:207], v[124:127]\n\t" \
  "Lmid_%=:\n\t" \
  "s_waitcnt lgkmcnt(6)\n\t" \
  "v_mfma_f32_16x16x32_bf16 v[64:67], v[216:219], v[144:147], v[64:67]\n\t" \
  "ds_read_b128 v[128:131], %[pa0] offset:0\n\t" \
  "v_mfma_f32_16x16x32_bf16 v[96:99], v[220:223], v[144:147], v[96:99]\n\t" \
  "ds_read_b128 v[132:135], %[pa0] offset:8192\n\t" \
  "v_mfma_f32_16x16x32_bf16 v[68:71], v[216:219], v[148:151], v[68:71]\n\t" \
  "ds_read_b128 v[136:139], %[pa1] offset:0\n\t" \
  "v_mfma_f32_16x16x32_bf16 v[100:103], v[220:223], v[148:151], v[100:103]\n\t" \
  "ds_read_b128 v[140:143], %[pa1] offset:8192\n\t" \
  "s_waitcnt lgkmcnt(8)\n\t" \
  "v_mfma_f32_16x16x32_bf16 v[72:75], v[216:219], v[152:155], v[72:75]\n\t" \
  "ds_read_b128 v[176:179], %[pb0] offset:0\n\t" \
  "v_mfma_f32_16x16x32_bf16 v[104:107], v[220:223], v[152:155], v[104:107]\n\t" \
  "ds_read_b128 v[180:183], %[pb0] offset:2048\n\t" \
  "v_mfma_f32_16x16x32_bf16 v[76:79], v[216:219], v[156:159], v[76:79]\n\t" \
  "v_add_u32 %[pb1], %[s2], %[pb1]\n\t" \
  "v_mfma_f32_16x16x32_bf16 v[108:111], v[220:223], v[156:159], v[108:111]\n\t" \
  "ds_read_b128 v[184:187], %[pb0] offset:4096\n\t" \
  "s_waitcnt lgkmcnt(7)\n\t" \
  "v_mfma_f32_16x16x32_bf16 v[80:83], v[216:219], v[160:163], v[80:83]\n\t" \
  "ds_read_b128 v[188:191], %[pb0] offset:6144\n\t" \
  "v_mfma_f32_16x16x32_bf16 v[112:115], v[220:223], v[160:163], v[112:115]\n\t" \
  "ds_read_b128 v[192:195], %[pb0] offset:8192\n\t" \
  "v_mfma_f32_16x16x32_bf16 v[84:87], v[216:219], v[164:167], v[84:87]\n\t" \
  "s_waitcnt lgkmcnt(5)\n\t" \
  "s_mov_b64 exec, 1\n\t" \
  "buffer_load_dword %[l16], %[srz], 0 offen lds\n\t" \
  "s_mov_b64 exec, -1\n\t" \
  "v_mfma_f32_16x16x32_bf16 v[116:119], v[220:223], v[164:167], v[116:119]\n\t" \
  "ds_read_b128 v[196:199], %[pb0] offset:10240\n\t" \
  "s_add_u32 m0, m0, 16\n\t" \
  "v_mfma_f32_16x16x32_bf16 v[88:91], v[216:219], v[168:171], v[88:91]\n\t" \
  "s_cmp_eq_u32 %[sk], %[ks]\n\t" \
  "s_cbranch_scc1 Lx4_%=\n\t" \
  "Lxb4_%=:\n\t" \
  "v_mfma_f32_16x16x32_bf16 v[120:123], v[220:223], v[168:171], v[120:123]\n\t" \
  "ds_read_b128 v[200:203], %[pb0] offset:12288\n\t" \
  "v_mfma_f32_16x16x32_bf16 v[92:95], v[216:219], v[172:175], v[92:95]\n\t" \
  "ds_read_b128 v[204:207], %[pb0] offset:14336\n\t" \
  "v_mfma_f32_16x16x32_bf16 v[124:127], v[220:223], v[172:175], v[124:127]\n\t" \
  "ds_read_b64 v[224:225], %[fl]\n\t" \
  "s_waitcnt lgkmcnt(7)\n\t" \
  "v_mfma_f32_16x16x32_bf16 v[64:67], v[128:131], v[176:179], v[64:67]\n\t" \
  "ds_read_b128 v[144:147], %[pb1] offset:0\n\t" \
  "s_add_u32 %[sk], %[sk], 1\n\t" \
  "v_mfma_f32_16x16x32_bf16 v[96:99], v[132:135], v[176:179], v[96:99]\n\t" \
  "s_and_b32 %[s1], %[sk], 3\n\t" \
  "s_cselect_b32 %[s1], 0x8000, %[wrap]\n\t" \
  "v_mfma_f32_16x16x32_bf16 v[68:71], v[128:131], v[180:183], v[68:71]\n\t" \
  "ds_read_b128 v[148:151], %[pb1] offset:2048\n\t" \
  "s_add_u32 %[sx], %[sx], 0x8000\n\t" \
  "v_mfma_f32_16x16x32_bf16 v[100:103], v[132:135], v[180:183], v[100:103]\n\t" \
  "s_cmp_eq_u32 %[sx], 0x28000\n\t" \
  "s_waitcnt lgkmcnt(7)\n\t" \
  "v_mfma_f32_16x16x32_bf16 v[72:75], v[128:131], v[184:187], v[72:75]\n\t" \
  "ds_read_b128 v[152:155], %[pb1] offset:4096\n\t" \
  "s_cselect_b32 %[s2], %[wrap2], 0x8000\n\t" \
  "v_mfma_f32_16x16x32_bf16 v[104:107], v[132:135], v[184:187], v[104:107]\n\t" \
  "s_cselect_b32 %[sx], 0, %[sx]\n\t" \
  "v_add_u32 %[pa0], %[s1], %[pa0]\n\t" \
  "v_mfma_f32_16x16x32_bf16 v[76:79], v[128:131], v[188:191], v[76:79]\n\t" \
  "ds_read_b128 v[156:159], %[pb1] offset:6144\n\t" \
  "v_add_u32 %[pb0], %[s2], %[pb0]\n\t" \
  "v_mfma_f32_16x16x32_bf16 v[108:111], v[132:135], v[188:191], v[108:111]\n\t" \
  "s_waitcnt lgkmcnt(5)\n\t" \
  "v_mfma_f32_16x16x32_bf16 v[80:83], v[128:131], v[192:195], v[80:83]\n\t" \
  "ds_read_b128 v[160:163], %[pb1] offset:8192\n\t" \
  "v_add_u32 %[pa1], %[s1], %[pa1]\n\t" \
  "v_mfma_f32_16x16x32_bf16 v[112:115], v[132:135], v[192:195], v[112:115]\n\t" \
  "v_mfma_f32_16x16x32_bf16 v[84:87], v[128:131], v[196:199], v[84:87]\n\t" \
  "ds_read_b128 v[164:167], %[pb1] offset:10240\n\t" \
  "v_mfma_f32_16x16x32_bf16 v[116:119], v[132:135], v[196:199], v[116:119]\n\t" \
  "s_waitcnt lgkmcnt(6)\n\t" \
  "v_or_b32 v224, v224, v225\n\t" \
  "v_mfma_f32_16x16x32_bf16 v[88:91], v[128:131], v[200:203], v[88:91]\n\t" \
  "ds_read_b128 v[168:171], %[pb1] offset:12288\n\t" \
  "v_readfirstlane_b32 %[s0], v224\n\t" \
  "v_mfma_f32_16x16x32_bf16 v[120:123], v[132:135], v[200:203], v[120:123]\n\t" \
  "s_cmp_eq_u32 %[s0], 0\n\t" \
  "s_cbranch_scc0 Lsp5_%=\n\t" \
  "Lb5_%=:\n\t" \
  "v_mfma_f32_16x16x32_bf16 v[92:95], v[128:131], v[204:207], v[92:95]\n\t" \
  "ds_read_b128 v[172:175], %[pb1] offset:14336\n\t" \
  "v_add_u32 %[fl], 8, %[fl]\n\t" \
  "v_mfma_f32_16x16x32_bf16 v[124:127], v[132:135], v[204:207], v[124:127]\n\t" \
  "s_cmp_lt_u32 %[sk], 16\n\t" \
  "s_cbranch_scc1 Lloop_%=\n\t" \
  "s_waitcnt lgkmcnt(6)\n\t" \
  "v_mfma_f32_16x16x32_bf16 v[64:67], v[136:139], v[144:147], v[64:67]\n\t" \
  "ds_read_b128 v[176:179], %[tq] offset:0\n\t" \
  "v_mfma_f32_16x16x32_bf16 v[96:99], v[140:143], v[144:147], v[96:99]\n\t" \
  "ds_read_b128 v[180:183], %[tq] offset:256\n\t" \
  "v_mfma_f32_16x16x32_bf16 v[68:71], v[136:139], v[148:151], v[68:71]\n\t" \
  "ds_read_b128 v[192:195], %[tq] offset:1024\n\t" \
  "v_mfma_f32_16x16x32_bf16 v[100:103], v[140:143], v[148:151], v[100:103]\n\t" \
  "ds_read_b128 v[196:199], %[tq] offset:1280\n\t" \
  "s_waitcnt lgkmcnt(8)\n\t" \
  "v_mfma_f32_16x16x32_bf16 v[72:75], v[136:139], v[152:155], v[72:75]\n\t" \
  "ds_read_b128 v[200:203], %[tq] offset:1536\n\t" \
  "v_mfma_f32_16x16x32_bf16 v[104:107], v[140:143], v[152:155], v[104:107]\n\t" \
  "ds_read_b128 v[204:207], %[tq] offset:1792\n\t" \
  "v_mfma_f32_16x16x32_bf16 v[76:79], v[136:139], v[156:159], v[76:79]\n\t" \
  "ds_read_b128 v[208:211], %[tq] offset:2048\n\t" \
  "v_mfma_f32_16x16x32_bf16 v[108:111], v[140:143], v[156:159], v[108:111]\n\t" \
  "ds_read_b128 v[212:215], %[tq] offset:2304\n\t" \
  "s_waitcnt lgkmcnt(8)\n\t" \
  "v_mfma_f32_16x16x32_bf16 v[80:83], v[136:139], v[160:163], v[80:83]\n\t" \
  "ds_read_b128 v[216:219], %[tq] offset:2560\n\t" \
  "v_mfma_f32_16x16x32_bf16 v[112:115], v[140:143], v[160:163], v[112:115]\n\t" \
  "ds_read_b128 v[220:223], %[tq] offset:2816\n\t" \
  "v_mfma_f32_16x16x32_bf16 v[84:87], v[136:139], v[164:167], v[84:87]\n\t" \
  "s_mov_b64 exec, 1\n\t" \
  "buffer_load_dword %[l16], %[srz], 0 offen lds\n\t" \
  "s_mov_b64 exec, -1\n\t" \
  "v_mfma_f32_16x16x32_bf16 v[116:119], v[140:143], v[164:167], v[116:119]\n\t" \
  "ds_read_b128 v[184:187], %[tq] offset:512\n\t" \
  "s_add_u32 m0, m0, 16\n\t" \
  "v_mfma_f32_16x16x32_bf16 v[88:91], v[136:139], v[168:171], v[88:91]\n\t" \
  "ds_read_b128 v[188:191], %[tq] offset:768\n\t" \
  "v_mfma_f32_16x16x32_bf16 v[120:123], v[140:143], v[168:171], v[120:123]\n\t" \
  "ds_read2_b32 v[230:231], %[tcl] offset0:0 offset1:64\n\t" \
  "v_mfma_f32_16x16x32_bf16 v[92:95], v[136:139], v[172:175], v[92:95]\n\t" \
  "ds_read2_b32 v[232:233], %[tcw] offset0:0 offset1:64\n\t" \
  "v_mfma_f32_16x16x32_bf16 v[124:127], v[140:143], v[172:175], v[124:127]\n\t" \
  "s_setprio 3\n\t" \
  "s_waitcnt vmcnt(8) lgkmcnt(4)\n\t" \
  "v_readfirstlane_b32 %[tgB], v228\n\t" \
  "s_add_u32 %[tgA], %[tgB], 1\n\t" \
  "s_add_u32 %[tgB], %[tgB], 0x40000001\n\t" \
  "v_pk_fma_f32 v[244:245], v[180:181], v[96:97], v[68:69] neg_lo:[1,0,0] neg_hi:[1,0,0]\n\t" \
  "v_pk_fma_f32 v[246:247], v[182:183], v[98:99], v[70:71] neg_lo:[1,0,0] neg_hi:[1,0,0]\n\t" \
  "v_pk_fma_f32 v[248:249], v[180:181], v[64:65], v[100:101]\n\t" \
  "v_pk_fma_f32 v[250:251], v[182:183], v[66:67], v[102:103]\n\t" \
  "v_pk_fma_f32 v[236:237], v[176:177], v[64:65], v[244:245]\n\t" \
  "v_pk_fma_f32 v[238:239], v[178:179], v[66:67], v[246:247]\n\t" \
  "v_pk_fma_f32 v[240:241], v[176:177], v[96:97], v[248:249]\n\t" \
  "v_pk_fma_f32 v[242:243], v[178:179], v[98:99], v[250:251]\n\t" \
  "v_pk_fma_f32 v[244:245], v[180:181], v[240:241], v[72:73] neg_lo:[1,0,0] neg_hi:[1,0,0]\n\t" \
  "v_pk_fma_f32 v[246:247], v[182:183], v[242:243], v[74:75] neg_lo:[1,0,0] neg_hi:[1,0,0]\n\t" \
  "v_pk_fma_f32 v[248:249], v[180:181], v[236:237], v[104:105]\n\t" \
  "v_pk_fma_f32 v[250:251], v[182:183], v[238:239], v[106:107]\n\t" \
  "v_pk_fma_f32 v[236:237], v[176:177], v[236:237], v[244:245]\n\t" \
  "v_pk_fma_f32 v[238:239], v[178:179], v[238:239], v[246:247]\n\t" \
  "v_pk_fma_f32 v[240:241], v[176:177], v[240:241], v[248:249]\n\t" \
  "v_pk_fma_f32 v[242:243], v[178:179], v[242:243], v[250:251]\n\t" \
  "v_pk_fma_f32 v[244:245], v[180:181], v[240:241], v[76:77] neg_lo:[1,0,0] neg_hi:[1,0,0]\n\t" \
  "v_pk_fma_f32 v[246:247], v[182:183], v[242:243], v[78:79] neg_lo:[1,0,0] neg_hi:[1,0,0]\n\t" \
  "v_pk_fma_f32 v[248:249], v[180:181], v[236:237], v[108:109]\n\t" \
  "v_pk_fma_f32 v[250:251], v[182:183], v[238:239], v[110:111]\n\t" \
  "v_pk_fma_f32 v[236:237], v[176:177], v[236:237], v[244:245]\n\t" \
  "v_pk_fma_f32 v[238:239], v[178:179], v[238:239], v[246:247]\n\t" \
  "v_pk_fma_f32 v[240:241], v[176:177], v[240:241], v[248:249]\n\t" \
  "v_pk_fma_f32 v[242:243], v[178:179], v[242:243], v[250:251]\n\t" \
  "v_pk_fma_f32 v[244:245], v[180:181], v[240:241], v[80:81] neg_lo:[1,0,0] neg_hi:[1,0,0]\n\t" \
  "v_pk_fma_f32 v[246:247], v[182:183], v[242:243], v[82:83] neg_lo:[1,0,0] neg_hi:[1,0,0]\n\t" \
  "v_pk_fma_f32 v[248:249], v[180:181], v[236:237], v[112:113]\n\t" \
  "v_pk_fma_f32 v[250:251], v[182:183], v[238:239], v[114:115]\n\t" \
  "v_pk_fma_f32 v[236:237], v[176:177], v[236:237], v[244:245]\n\t" \
  "v_pk_fma_f32 v[238:239], v[178:179], v[238:239], v[246:247]\n\t" \
  "v_pk_fma_f32 v[240:241], v[176:177], v[240:241], v[248:249]\n\t" \
  "v_pk_fma_f32 v[242:243], v[178:179], v[242:243], v[250:251]\n\t" \
  "v_pk_fma_f32 v[244:245], v[180:181], v[240:241], v[84:85] neg_lo:[1,0,0] neg_hi:[1,0,0]\n\t" \
  "v_pk_fma_f32 v[246:247], v[182:183], v[242:243], v[86:87] neg_lo:[1,0,0] neg_hi:[1,0,0]\n\t" \
  "v_pk_fma_f32 v[248:249], v[180:181], v[236:237], v[116:117]\n\t" \
  "v_pk_fma_f32 v[250:251], v[182:183], v[238:239], v[118:119]\n\t" \
  "v_pk_fma_f32 v[236:237], v[176:177], v[236:237], v[244:245]\n\t" \
  "v_pk_fma_f32 v[238:239], v[178:179], v[238:239], v[246:247]\n\t" \
  "v_pk_fma_f32 v[240:241], v[176:177], v[240:241], v[248:249]\n\t" \
  "v_pk_fma_f32 v[242:243], v[178:179], v[242:243], v[250:251]\n\t" \
  "v_pk_fma_f32 v[244:245], v[180:181], v[240:241], v[88:89] neg_lo:[1,0,0] neg_hi:[1,0,0]\n\t" \
  "v_pk_fma_f32 v[246:247], v[182:183], v[242:243], v[90:91] neg_lo:[1,0,0] neg_hi:[1,0,0]\n\t" \
  "v_pk_fma_f32 v[248:249], v[180:181], v[236:237], v[120:121]\n\t" \
  "v_pk_fma_f32 v[250:251], v[182:183], v[238:239], v[122:123]\n\t" \
  "v_pk_fma_f32 v[236:237], v[176:177], v[236:237], v[244:245]\n\t" \
  "v_pk_fma_f32 v[238:239], v[178:179], v[238:239], v[246:247]\n\t" \
  "v_pk_fma_f32 v[240:241], v[176:177], v[240:241], v[248:249]\n\t" \
  "v_pk_fma_f32 v[242:243], v[178:179], v[242:243], v[250:251]\n\t" \
  "v_pk_fma_f32 v[244:245], v[180:181], v[240:241], v[92:93] neg_lo:[1,0,0] neg_hi:[1,0,0]\n\t" \
  "v_pk_fma_f32 v[246:247], v[182:183], v[242:243], v[94:95] neg_lo:[1,0,0] neg_hi:[1,0,0]\n\t" \
  "v_pk_fma_f32 v[248:249], v[180:181], v[236:237], v[124:125]\n\t" \
  "v_pk_fma_f32 v[250:251], v[182:183], v[238:239], v[126:127]\n\t" \
  "v_pk_fma_f32 v[236:237], v[176:177], v[236:237], v[244:245]\n\t" \
  "v_pk_fma_f32 v[238:239], v[178:179], v[238:239], v[246:247]\n\t" \
  "v_pk_fma_f32 v[240:241], v[176:177], v[240:241], v[248:249]\n\t" \
  "v_pk_fma_f32 v[242:243], v[178:179], v[242:243], v[250:251]\n\t" \
  "s_add_u32 %[s0], %[eh], 1\n\t" \
  "v_mov_b32 v226, %[s0]\n\t" \
  "v_mov_b32 v253, %[tgA]\n\t" \
  "v_mov_b32 v255, %[tgB]\n\t" \
  "v_mov_b32_dpp v244, v236 row_shr:1 row_mask:0xf bank_mask:0xf bound_ctrl:0\n\t" \
  "v_mov_b32_dpp v245, v237 row_shr:1 row_mask:0xf bank_mask:0xf bound_ctrl:0\n\t" \
  "v_mov_b32_dpp v246, v238 row_shr:1 row_mask:0xf bank_mask:0xf bound_ctrl:0\n\t" \
  "v_mov_b32_dpp v247, v239 row_shr:1 row_mask:0xf bank_mask:0xf bound_ctrl:0\n\t" \
  "v_mov_b32_dpp v248, v240 row_shr:1 row_mask:0xf bank_mask:0xf bound_ctrl:0\n\t" \
  "v_mov_b32_dpp v249, v241 row_shr:1 row_mask:0xf bank_mask:0xf bound_ctrl:0\n\t" \
  "v_mov_b32_dpp v250, v242 row_shr:1 row_mask:0xf bank_mask:0xf bound_ctrl:0\n\t" \
  "v_mov_b32_dpp v251, v243 row_shr:1 row_mask:0xf bank_mask:0xf bound_ctrl:0\n\t" \
  "v_pk_fma_f32 v[236:237], v[192:193], v[244:245], v[236:237]\n\t" \
  "v_pk_fma_f32 v[238:239], v[194:195], v[246:247], v[238:239]\n\t" \
  "v_pk_fma_f32 v[240:241], v[192:193], v[248:249], v[240:241]\n\t" \
  "v_pk_fma_f32 v[242:243], v[194:195], v[250:251], v[242:243]\n\t" \
  "v_pk_fma_f32 v[236:237], v[196:197], v[248:249], v[236:237] neg_lo:[1,0,0] neg_hi:[1,0,0]\n\t" \
  "v_pk_fma_f32 v[238:239], v[198:199], v[250:251], v[238:239] neg_lo:[1,0,0] neg_hi:[1,0,0]\n\t" \
  "v_pk_fma_f32 v[240:241], v[196:197], v[244:245], v[240:241]\n\t" \
  "v_pk_fma_f32 v[242:243], v[198:199], v[246:247], v[242:243]\n\t" \
  "v_mov_b32_dpp v244, v236 row_shr:2 row_mask:0xf bank_mask:0xf bound_ctrl:0\n\t" \
  "v_mov_b32_dpp v245, v237 row_shr:2 row_mask:0xf bank_mask:0xf bound_ctrl:0\n\t" \
  "v_mov_b32_dpp v246, v238 row_shr:2 row_mask:0xf bank_mask:0xf bound_ctrl:0\n\t" \
  "v_mov_b32_dpp v247, v239 row_shr:2 row_mask:0xf bank_mask:0xf bound_ctrl:0\n\t" \
  "v_mov_b32_dpp v248, v240 row_shr:2 row_mask:0xf bank_mask:0xf bound_ctrl:0\n\t" \
  "v_mov_b32_dpp v249, v241 row_shr:2 row_mask:0xf bank_mask:0xf bound_ctrl:0\n\t" \
  "v_mov_b32_dpp v250, v242 row_shr:2 row_mask:0xf bank_mask:0xf bound_ctrl:0\n\t" \
  "v_mov_b32_dpp v251, v243 row_shr:2 row_mask:0xf bank_mask:0xf bound_ctrl:0\n\t" \
  "v_pk_fma_f32 v[236:237], v[200:201], v[244:245], v[236:237]\n\t" \
  "v_pk_fma_f32 v[238:239], v[202:203], v[246:247], v[238:239]\n\t" \
  "v_pk_fma_f32 v[240:241], v[200:201], v[248:249], v[240:241]\n\t" \
  "v_pk_fma_f32 v[242:243], v[202:203], v[250:251], v[242:243]\n\t" \
  "v_pk_fma_f32 v[236:237], v[204:205], v[248:249], v[236:237] neg_lo:[1,0,0] neg_hi:[1,0,0]\n\t" \
  "v_pk_fma_f32 v[238:239], v[206:207], v[250:251], v[238:239] neg_lo:[1,0,0] neg_hi:[1,0,0]\n\t" \
  "v_pk_fma_f32 v[240:241], v[204:205], v[244:245], v[240:241]\n\t" \
  "v_pk_fma_f32 v[242:243], v[206:207], v[246:247], v[242:243]\n\t" \
  "v_mov_b32_dpp v244, v236 row_shr:4 row_mask:0xf bank_mask:0xf bound_ctrl:0\n\t" \
  "v_mov_b32_dpp v245, v237 row_shr:4 row_mask:0xf bank_mask:0xf bound_ctrl:0\n\t" \
  "v_mov_b32_dpp v246, v238 row_shr:4 row_mask:0xf bank_mask:0xf bound_ctrl:0\n\t" \
  "v_mov_b32_dpp v247, v239 row_shr:4 row_mask:0xf bank_mask:0xf bound_ctrl:0\n\t" \
  "v_mov_b32_dpp v248, v240 row_shr:4 row_mask:0xf bank_mask:0xf bound_ctrl:0\n\t" \
  "v_mov_b32_dpp v249, v241 row_shr:4 row_mask:0xf bank_mask:0xf bound_ctrl:0\n\t" \
  "v_mov_b32_dpp v250, v242 row_shr:4 row_mask:0xf bank_mask:0xf bound_ctrl:0\n\t" \
  "v_mov_b32_dpp v251, v243 row_shr:4 row_mask:0xf bank_mask:0xf bound_ctrl:0\n\t" \
  "v_pk_fma_f32 v[236:237], v[208:209], v[244:245], v[236:237]\n\t" \
  "v_pk_fma_f32 v[238:239], v[210:211], v[246:247], v[238:239]\n\t" \
  "v_pk_fma_f32 v[240:241], v[208:209], v[248:249], v[240:241]\n\t" \
  "v_pk_fma_f32 v[242:243], v[210:211], v[250:251], v[242:243]\n\t" \
  "v_pk_fma_f32 v[236:237], v[212:213], v[248:249], v[236:237] neg_lo:[1,0,0] neg_hi:[1,0,0]\n\t" \
  "v_pk_fma_f32 v[238:239], v[214:215], v[250:251], v[238:239] neg_lo:[1,0,0] neg_hi:[1,0,0]\n\t" \
  "v_pk_fma_f32 v[240:241], v[212:213], v[244:245], v[240:241]\n\t" \
  "v_pk_fma_f32 v[242:243], v[214:215], v[246:247], v[242:243]\n\t" \
  "v_mov_b32_dpp v244, v236 row_shr:8 row_mask:0xf bank_mask:0xf bound_ctrl:0\n\t" \
  "v_mov_b32_dpp v245, v237 row_shr:8 row_mask:0xf bank_mask:0xf bound_ctrl:0\n\t" \
  "v_mov_b32_dpp v246, v238 row_shr:8 row_mask:0xf bank_mask:0xf bound_ctrl:0\n\t" \
  "v_mov_b32_dpp v247, v239 row_shr:8 row_mask:0xf bank_mask:0xf bound_ctrl:0\n\t" \
  "v_mov_b32_dpp v248, v240 row_shr:8 row_mask:0xf bank_mask:0xf bound_ctrl:0\n\t" \
  "v_mov_b32_dpp v249, v241 row_shr:8 row_mask:0xf bank_mask:0xf bound_ctrl:0\n\t" \
  "v_mov_b32_dpp v250, v242 row_shr:8 row_mask:0xf bank_mask:0xf bound_ctrl:0\n\t" \
  "v_mov_b32_dpp v251, v243 row_shr:8 row_mask:0xf bank_mask:0xf bound_ctrl:0\n\t" \
  "v_pk_fma_f32 v[236:237], v[216:217], v[244:245], v[236:237]\n\t" \
  "v_pk_fma_f32 v[238:239], v[218:219], v[246:247], v[238:239]\n\t" \
  "v_pk_fma_f32 v[240:241], v[216:217], v[248:249], v[240:241]\n\t" \
  "v_pk_fma_f32 v[242:243], v[218:219], v[250:251], v[242:243]\n\t" \
  "v_pk_fma_f32 v[236:237], v[220:221], v[248:249], v[236:237] neg_lo:[1,0,0] neg_hi:[1,0,0]\n\t" \
  "v_pk_fma_f32 v[238:239], v[222:223], v[250:251], v[238:239] neg_lo:[1,0,0] neg_hi:[1,0,0]\n\t" \
  "v_pk_fma_f32 v[240:241], v[220:221], v[244:245], v[240:241]\n\t" \
  "v_pk_fma_f32 v[242:243], v[222:223], v[246:247], v[242:243]\n\t" \
  "v_mov_b32_dpp v252, v236 row_newbcast:15 row_mask:0xf bank_mask:0x1\n\t" \
  "v_mov_b32_dpp v252, v237 row_newbcast:15 row_mask:0xf bank_mask:0x2\n\t" \
  "v_mov_b32_dpp v252, v238 row_newbcast:15 row_mask:0xf bank_mask:0x4\n\t" \
  "v_mov_b32_dpp v252, v239 row_newbcast:15 row_mask:0xf bank_mask:0x8\n\t" \
  "v_mov_b32_dpp v254, v240 row_newbcast:15 row_mask:0xf bank_mask:0x1\n\t" \
  "v_mov_b32_dpp v254, v241 row_newbcast:15 row_mask:0xf bank_mask:0x2\n\t" \
  "v_mov_b32_dpp v254, v242 row_newbcast:15 row_mask:0xf bank_mask:0x4\n\t" \
  "v_mov_b32_dpp v254, v243 row_newbcast:15 row_mask:0xf bank_mask:0x8\n\t" \
  "s_mov_b32 exec_lo, 0x11111111\n\t" \
  "s_mov_b32 exec_hi, 0x11111111\n\t" \
  "buffer_store_dwordx4 v[252:255], %[pubo], %[srd], %[sag] offen sc1\n\t" \
  "s_mov_b64 exec, 1\n\t" \
  "buffer_atomic_or v226, off, %[srd], %[sht] sc0 sc1\n\t" \
  "s_mov_b64 exec, -1\n\t" \
  "v_mov_b32 v160, 0\n\t" \
  "v_mov_b32 v161, %[tgA]\n\t" \
  "v_mov_b32 v162, 0\n\t" \
  "v_mov_b32 v163, %[tgB]\n\t" \
  "v_mov_b32 v164, 0\n\t" \
  "v_mov_b32 v165, %[tgA]\n\t" \
  "v_mov_b32 v166, 0\n\t" \
  "v_mov_b32 v167, %[tgB]\n\t" \
  "v_mov_b32 v168, 0\n\t" \
  "v_mov_b32 v169, %[tgA]\n\t" \
  "v_mov_b32 v170, 0\n\t" \
  "v_mov_b32 v171, %[tgB]\n\t" \
  "v_mov_b32 v172, 0\n\t" \
  "v_mov_b32 v173, %[tgA]\n\t" \
  "v_mov_b32 v174, 0\n\t" \
  "v_mov_b32 v175, %[tgB]\n\t" \
  "s_waitcnt lgkmcnt(0)\n\t" \
  "v_mov_b32_dpp v244, v236 row_shr:1 row_mask:0xf bank_mask:0xf bound_ctrl:0\n\t" \
  "v_mov_b32_dpp v245, v237 row_shr:1 row_mask:0xf bank_mask:0xf bound_ctrl:0\n\t" \
  "v_mov_b32_dpp v246, v238 row_shr:1 row_mask:0xf bank_mask:0xf bound_ctrl:0\n\t" \
  "v_mov_b32_dpp v247, v239 row_shr:1 row_mask:0xf bank_mask:0xf bound_ctrl:0\n\t" \
  "v_mov_b32_dpp v248, v240 row_shr:1 row_mask:0xf bank_mask:0xf bound_ctrl:0\n\t" \
  "v_mov_b32_dpp v249, v241 row_shr:1 row_mask:0xf bank_mask:0xf bound_ctrl:0\n\t" \
  "v_mov_b32_dpp v250, v242 row_shr:1 row_mask:0xf bank_mask:0xf bound_ctrl:0\n\t" \
  "v_mov_b32_dpp v251, v243 row_shr:1 row_mask:0xf bank_mask:0xf bound_ctrl:0\n\t" \
  "v_pk_mul_f32 v[236:237], v[184:185], v[244:245]\n\t" \
  "v_pk_mul_f32 v[238:239], v[186:187], v[246:247]\n\t" \
  "v_pk_mul_f32 v[240:241], v[184:185], v[248:249]\n\t" \
  "v_pk_mul_f32 v[242:243], v[186:187], v[250:251]\n\t" \
  "v_pk_fma_f32 v[236:237], v[188:189], v[248:249], v[236:237] neg_lo:[1,0,0] neg_hi:[1,0,0]\n\t" \
  "v_pk_fma_f32 v[238:239], v[190:191], v[250:251], v[238:239] neg_lo:[1,0,0] neg_hi:[1,0,0]\n\t" \
  "v_pk_fma_f32 v[240:241], v[188:189], v[244:245], v[240:241]\n\t" \
  "v_pk_fma_f32 v[242:243], v[190:191], v[246:247], v[242:243]\n\t" \
  "v_pk_mul_f32 v[128:129], v[184:185], v[64:65]\n\t" \
  "v_pk_mul_f32 v[130:131], v[186:187], v[66:67]\n\t" \
  "v_pk_fma_f32 v[128:129], v[188:189], v[96:97], v[128:129] neg_lo:[1,0,0] neg_hi:[1,0,0]\n\t" \
  "v_pk_fma_f32 v[130:131], v[190:191], v[98:99], v[130:131] neg_lo:[1,0,0] neg_hi:[1,0,0]\n\t" \
  "v_pk_mul_f32 v[96:97], v[184:185], v[96:97]\n\t" \
  "v_pk_mul_f32 v[98:99], v[186:187], v[98:99]\n\t" \
  "v_pk_fma_f32 v[96:97], v[188:189], v[64:65], v[96:97] neg_lo:[1,0,1] neg_hi:[1,0,1]\n\t" \
  "v_pk_fma_f32 v[98:99], v[190:191], v[66:67], v[98:99] neg_lo:[1,0,1] neg_hi:[1,0,1]\n\t" \
  "v_pk_mul_f32 v[132:133], v[184:185], v[68:69]\n\t" \
  "v_pk_mul_f32 v[134:135], v[186:187], v[70:71]\n\t" \
  "v_pk_fma_f32 v[132:133], v[188:189], v[100:101], v[132:133] neg_lo:[1,0,0] neg_hi:[1,0,0]\n\t" \
  "v_pk_fma_f32 v[134:135], v[190:191], v[102:103], v[134:135] neg_lo:[1,0,0] neg_hi:[1,0,0]\n\t" \
  "v_pk_mul_f32 v[100:101], v[184:185], v[100:101]\n\t" \
  "v_pk_mul_f32 v[102:103], v[186:187], v[102:103]\n\t" \
  "v_pk_fma_f32 v[100:101], v[188:189], v[68:69], v[100:101] neg_lo:[1,0,1] neg_hi:[1,0,1]\n\t" \
  "v_pk_fma_f32 v[102:103], v[190:191], v[70:71], v[102:103] neg_lo:[1,0,1] neg_hi:[1,0,1]\n\t" \
  "v_pk_mul_f32 v[136:137], v[184:185], v[72:73]\n\t" \
  "v_pk_mul_f32 v[138:139], v[186:187], v[74:75]\n\t" \
  "v_pk_fma_f32 v[136:137], v[188:189], v[104:105], v[136:137] neg_lo:[1,0,0] neg_hi:[1,0,0]\n\t" \
  "v_pk_fma_f32 v[138:139], v[190:191], v[106:107], v[138:139] neg_lo:[1,0,0] neg_hi:[1,0,0]\n\t" \
  "v_pk_mul_f32 v[104:105], v[184:185], v[104:105]\n\t" \
  "v_pk_mul_f32 v[106:107], v[186:187], v[106:107]\n\t" \
  "v_pk_fma_f32 v[104:105], v[188:189], v[72:73], v[104:105] neg_lo:[1,0,1] neg_hi:[1,0,1]\n\t" \
  "v_pk_fma_f32 v[106:107], v[190:191], v[74:75], v[106:107] neg_lo:[1,0,1] neg_hi:[1,0,1]\n\t" \
  "v_pk_mul_f32 v[140:141], v[184:185], v[76:77]\n\t" \
  "v_pk_mul_f32 v[142:143], v[186:187], v[78:79]\n\t" \
  "v_pk_fma_f32 v[140:141], v[188:189], v[108:109], v[140:141] neg_lo:[1,0,0] neg_hi:[1,0,0]\n\t" \
  "v_pk_fma_f32 v[142:143], v[190:191], v[110:111], v[142:143] neg_lo:[1,0,0] neg_hi:[1,0,0]\n\t" \
  "v_pk_mul_f32 v[108:109], v[184:185], v[108:109]\n\t" \
  "v_pk_mul_f32 v[110:111], v[186:187], v[110:111]\n\t" \
  "v_pk_fma_f32 v[108:109], v[188:189], v[76:77], v[108:109] neg_lo:[1,0,1] neg_hi:[1,0,1]\n\t" \
  "v_pk_fma_f32 v[110:111], v[190:191], v[78:79], v[110:111] neg_lo:[1,0,1] neg_hi:[1,0,1]\n\t" \
  "v_pk_mul_f32 v[144:145], v[184:185], v[80:81]\n\t" \
  "v_pk_mul_f32 v[146:147], v[186:187], v[82:83]\n\t" \
  "v_pk_fma_f32 v[144:145], v[188:189], v[112:113], v[144:145] neg_lo:[1,0,0] neg_hi:[1,0,0]\n\t" \
  "v_pk_fma_f32 v[146:147], v[190:191], v[114:115], v[146:147] neg_lo:[1,0,0] neg_hi:[1,0,0]\n\t" \
  "v_pk_mul_f32 v[112:113], v[184:185], v[112:113]\n\t" \
  "v_pk_mul_f32 v[114:115], v[186:187], v[114:115]\n\t" \
  "v_pk_fma_f32 v[112:113], v[188:189], v[80:81], v[112:113] neg_lo:[1,0,1] neg_hi:[1,0,1]\n\t" \
  "v_pk_fma_f32 v[114:115], v[190:191], v[82:83], v[114:115] neg_lo:[1,0,1] neg_hi:[1,0,1]\n\t" \
  "v_pk_mul_f32 v[148:149], v[184:185], v[84:85]\n\t" \
  "v_pk_mul_f32 v[150:151], v[186:187], v[86:87]\n\t" \
  "v_pk_fma_f32 v[148:149], v[188:189], v[116:117], v[148:149] neg_lo:[1,0,0] neg_hi:[1,0,0]\n\t" \
  "v_pk_fma_f32 v[150:151], v[190:191], v[118:119], v[150:151] neg_lo:[1,0,0] neg_hi:[1,0,0]\n\t" \
  "v_pk_mul_f32 v[116:117], v[184:185], v[116:117]\n\t" \
  "v_pk_mul_f32 v[118:119], v[186:187], v[118:119]\n\t" \
  "v_pk_fma_f32 v[116:117], v[188:189], v[84:85], v[116:117] neg_lo:[1,0,1] neg_hi:[1,0,1]\n\t" \
  "v_pk_fma_f32 v[118:119], v[190:191], v[86:87], v[118:119] neg_lo:[1,0,1] neg_hi:[1,0,1]\n\t" \
  "v_pk_mul_f32 v[152:153], v[184:185], v[88:89]\n\t" \
  "v_pk_mul_f32 v[154:155], v[186:187], v[90:91]\n\t" \
  "v_pk_fma_f32 v[152:153], v[188:189], v[120:121], v[152:153] neg_lo:[1,0,0] neg_hi:[1,0,0]\n\t" \
  "v_pk_fma_f32 v[154:155], v[190:191], v[122:123], v[154:155] neg_lo:[1,0,0] neg_hi:[1,0,0]\n\t" \
  "v_pk_mul_f32 v[120:121], v[184:185], v[120:121]\n\t" \
  "v_pk_mul_f32 v[122:123], v[186:187], v[122:123]\n\t" \
  "v_pk_fma_f32 v[120:121], v[188:189], v[88:89], v[120:121] neg_lo:[1,0,1] neg_hi:[1,0,1]\n\t" \
  "v_pk_fma_f32 v[122:123], v[190:191], v[90:91], v[122:123] neg_lo:[1,0,1] neg_hi:[1,0,1]\n\t" \
  "v_pk_mul_f32 v[156:157], v[184:185], v[92:93]\n\t" \
  "v_pk_mul_f32 v[158:159], v[186:187], v[94:95]\n\t" \
  "v_pk_fma_f32 v[156:157], v[188:189], v[124:125], v[156:157] neg_lo:[1,0,0] neg_hi:[1,0,0]\n\t" \
  "v_pk_fma_f32 v[158:159], v[190:191], v[126:127], v[158:159] neg_lo:[1,0,0] neg_hi:[1,0,0]\n\t" \
  "v_pk_mul_f32 v[124:125], v[184:185], v[124:125]\n\t" \
  "v_pk_mul_f32 v[126:127], v[186:187], v[126:127]\n\t" \
  "v_pk_fma_f32 v[124:125], v[188:189], v[92:93], v[124:125] neg_lo:[1,0,1] neg_hi:[1,0,1]\n\t" \
  "v_pk_fma_f32 v[126:127], v[190:191], v[94:95], v[126:127] neg_lo:[1,0,1] neg_hi:[1,0,1]\n\t" \
  "s_waitcnt vmcnt(0)\n\t" \
  "v_readfirstlane_b32 %[s0], v226\n\t" \
  "s_andn2_b32 %[s0], %[eh], %[s0]\n\t" \
  "s_cselect_b32 %[s1], 32, 0\n\t" \
  "s_cbranch_scc0 Lh1_%=\n\t" \
  "v_mov_b32 v226, 0\n\t" \
  "s_mov_b64 exec, 1\n\t" \
  "buffer_atomic_or v226, off, %[srd], %[sht] sc0 sc1\n\t" \
  "s_mov_b64 exec, -1\n\t" \
  "s_branch Lpa_%=\n\t" \
  "Lh1_%=:\n\t" \
  "s_mov_b64 exec, %[em0]\n\t" \
  "s_cbranch_execz Lpa_%=\n\t" \
  "buffer_load_dwordx4 v[160:163], %[plo], %[srd], %[sl0] offen sc1\n\t" \
  "s_mov_b64 exec, %[em1]\n\t" \
  "s_cbranch_execz Lpa_%=\n\t" \
  "buffer_load_dwordx4 v[164:167], %[plo], %[srd], %[sl1] offen sc1\n\t" \
  "s_mov_b64 exec, %[em2]\n\t" \
  "s_cbranch_execz Lpa_%=\n\t" \
  "buffer_load_dwordx4 v[168:171], %[plo], %[srd], %[sl2] offen sc1\n\t" \
  "s_mov_b64 exec, %[em3]\n\t" \
  "s_cbranch_execz Lpa_%=\n\t" \
  "buffer_load_dwordx4 v[172:175], %[plo], %[srd], %[sl3] offen sc1\n\t" \
  "Lpa_%=:\n\t" \
  "s_mov_b64 exec, -1\n\t" \
  "s_mov_b32 exec_lo, 0xaaaaaaaa\n\t" \
  "s_mov_b32 exec_hi, 0xaaaaaaaa\n\t" \
  "v_pk_mul_f32 v[244:245], v[196:197], v[188:189]\n\t" \
  "v_pk_mul_f32 v[246:247], v[198:199], v[190:191]\n\t" \
  "v_pk_mul_f32 v[248:249], v[196:197], v[184:185]\n\t" \
  "v_pk_mul_f32 v[250:251], v[198:199], v[186:187]\n\t" \
  "v_pk_fma_f32 v[184:185], v[192:193], v[184:185], v[244:245] neg_lo:[0,0,1] neg_hi:[0,0,1]\n\t" \
  "v_pk_fma_f32 v[186:187], v[194:195], v[186:187], v[246:247] neg_lo:[0,0,1] neg_hi:[0,0,1]\n\t" \
  "v_pk_fma_f32 v[188:189], v[192:193], v[188:189], v[248:249]\n\t" \
  "v_pk_fma_f32 v[190:191], v[194:195], v[190:191], v[250:251]\n\t" \
  "s_mov_b32 exec_lo, 0xcccccccc\n\t" \
  "s_mov_b32 exec_hi, 0xcccccccc\n\t" \
  "v_pk_mul_f32 v[244:245], v[204:205], v[188:189]\n\t" \
  "v_pk_mul_f32 v[246:247], v[206:207], v[190:191]\n\t" \
  "v_pk_mul_f32 v[248:249], v[204:205], v[184:185]\n\t" \
  "v_pk_mul_f32 v[250:251], v[206:207], v[186:187]\n\t" \
  "v_pk_fma_f32 v[184:185], v[200:201], v[184:185], v[244:245] neg_lo:[0,0,1] neg_hi:[0,0,1]\n\t" \
  "v_pk_fma_f32 v[186:187], v[202:203], v[186:187], v[246:247] neg_lo:[0,0,1] neg_hi:[0,0,1]\n\t" \
  "v_pk_fma_f32 v[188:189], v[200:201], v[188:189], v[248:249]\n\t" \
  "v_pk_fma_f32 v[190:191], v[202:203], v[190:191], v[250:251]\n\t" \
  "s_mov_b32 exec_lo, 0xf0f0f0f0\n\t" \
  "s_mov_b32 exec_hi, 0xf0f0f0f0\n\t" \
  "v_pk_mul_f32 v[244:245], v[212:213], v[188:189]\n\t" \
  "v_pk_mul_f32 v[246:247], v[214:215], v[190:191]\n\t" \
  "v_pk_mul_f32 v[248:249], v[212:213], v[184:185]\n\t" \
  "v_pk_mul_f32 v[250:251], v[214:215], v[186:187]\n\t" \
  "v_pk_fma_f32 v[184:185], v[208:209], v[184:185], v[244:245] neg_lo:[0,0,1] neg_hi:[0,0,1]\n\t" \
  "v_pk_fma_f32 v[186:187], v[210:211], v[186:187], v[246:247] neg_lo:[0,0,1] neg_hi:[0,0,1]\n\t" \
  "v_pk_fma_f32 v[188:189], v[208:209], v[188:189], v[248:249]\n\t" \
  "v_pk_fma_f32 v[190:191], v[210:211], v[190:191], v[250:251]\n\t" \
  "s_mov_b32 exec_lo, 0xff00ff00\n\t" \
  "s_mov_b32 exec_hi, 0xff00ff00\n\t" \
  "v_pk_mul_f32 v[244:245], v[220:221], v[188:189]\n\t" \
  "v_pk_mul_f32 v[246:247], v[222:223], v[190:191]\n\t" \
  "v_pk_mul_f32 v[248:249], v[220:221], v[184:185]\n\t" \
  "v_pk_mul_f32 v[250:251], v[222:223], v[186:187]\n\t" \
  "v_pk_fma_f32 v[184:185], v[216:217], v[184:185], v[244:245] neg_lo:[0,0,1] neg_hi:[0,0,1]\n\t" \
  "v_pk_fma_f32 v[186:187], v[218:219], v[186:187], v[246:247] neg_lo:[0,0,1] neg_hi:[0,0,1]\n\t" \
  "v_pk_fma_f32 v[188:189], v[216:217], v[188:189], v[248:249]\n\t" \
  "v_pk_fma_f32 v[190:191], v[218:219], v[190:191], v[250:251]\n\t" \
  "s_mov_b64 exec, -1\n\t" \
  "s_cmp_eq_u32 %[s1], 0\n\t" \
  "s_cbranch_scc1 Lq_%=\n\t" \
  "Lh2_%=:\n\t" \
  "s_waitcnt vmcnt(0)\n\t" \
  "v_readfirstlane_b32 %[s0], v226\n\t" \
  "s_andn2_b32 %[s0], %[eh], %[s0]\n\t" \
  "s_cbranch_scc0 Lh3_%=\n\t" \
  "s_sub_u32 %[s1], %[s1], 1\n\t" \
  "s_cbranch_scc1 Lh3_%=\n\t" \
  "s_sleep 1\n\t" \
  "v_mov_b32 v226, 0\n\t" \
  "s_mov_b64 exec, 1\n\t" \
  "buffer_atomic_or v226, off, %[srd], %[sht] sc0 sc1\n\t" \
  "s_mov_b64 exec, -1\n\t" \
  "s_branch Lh2_%=\n\t" \
  "Lh3_%=:\n\t" \
  "s_mov_b64 exec, %[em0]\n\t" \
  "s_cbranch_execz Lpc_%=\n\t" \
  "buffer_load_dwordx4 v[160:163], %[plo], %[srd], %[sl0] offen sc1\n\t" \
  "s_mov_b64 exec, %[em1]\n\t" \
  "s_cbranch_execz Lpc_%=\n\t" \
  "buffer_load_dwordx4 v[164:167], %[plo], %[srd], %[sl1] offen sc1\n\t" \
  "s_mov_b64 exec, %[em2]\n\t" \
  "s_cbranch_execz Lpc_%=\n\t" \
  "buffer_load_dwordx4 v[168:171], %[plo], %[srd], %[sl2] offen sc1\n\t" \
  "s_mov_b64 exec, %[em3]\n\t" \
  "s_cbranch_execz Lpc_%=\n\t" \
  "buffer_load_dwordx4 v[172:175], %[plo], %[srd], %[sl3] offen sc1\n\t" \
  "Lpc_%=:\n\t" \
  "s_mov_b64 exec, -1\n\t" \
  "Lq_%=:\n\t" \
  "s_waitcnt vmcnt(0)\n\t" \
  "v_cmpx_eq_u32 vcc, %[tgA], v161\n\t" \
  "v_cmpx_eq_u32 vcc, %[tgB], v163\n\t" \
  "v_cmpx_eq_u32 vcc, %[tgA], v165\n\t" \
  "v_cmpx_eq_u32 vcc, %[tgB], v167\n\t" \
  "v_cmpx_eq_u32 vcc, %[tgA], v169\n\t" \
  "v_cmpx_eq_u32 vcc, %[tgB], v171\n\t" \
  "v_cmpx_eq_u32 vcc, %[tgA], v173\n\t" \
  "v_cmpx_eq_u32 vcc, %[tgB], v175\n\t" \
  "s_cmp_eq_u64 exec, -1\n\t" \
  "s_mov_b64 exec, -1\n\t" \
  "s_cbranch_scc1 Lq2_%=\n\t" \
  "s_sub_i32 %[bud], %[bud], 16\n\t" \
  "s_cmp_lt_i32 %[bud], 0\n\t" \
  "s_cbranch_scc1 Lq2_%=\n\t" \
  "s_sleep 4\n\t" \
  "s_mov_b64 exec, %[em0]\n\t" \
  "s_cbranch_execz Lpb_%=\n\t" \
  "buffer_load_dwordx4 v[160:163], %[plo], %[srd], %[sl0] offen sc1\n\t" \
  "s_mov_b64 exec, %[em1]\n\t" \
  "s_cbranch_execz Lpb_%=\n\t" \
  "buffer_load_dwordx4 v[164:167], %[plo], %[srd], %[sl1] offen sc1\n\t" \
  "s_mov_b64 exec, %[em2]\n\t" \
  "s_cbranch_execz Lpb_%=\n\t" \
  "buffer_load_dwordx4 v[168:171], %[plo], %[srd], %[sl2] offen sc1\n\t" \
  "s_mov_b64 exec, %[em3]\n\t" \
  "s_cbranch_execz Lpb_%=\n\t" \
  "buffer_load_dwordx4 v[172:175], %[plo], %[srd], %[sl3] offen sc1\n\t" \
  "Lpb_%=:\n\t" \
  "s_mov_b64 exec, -1\n\t" \
  "s_branch Lq_%=\n\t" \
  "Lq2_%=:\n\t" \
  "s_mov_b32 %[scn], %[mcar]\n\t" \
  "s_mov_b32 m0, %[scn]\n\t" \
  "s_mov_b64 exec, 1\n\t" \
  "buffer_load_dword %[l16], %[srz], 0 offen lds\n\t" \
  "s_mov_b64 exec, -1\n\t" \
  "v_fma_f32 v244, -v231, v174, v168\n\t" \
  "v_fma_f32 v245, v231, v172, v170\n\t" \
  "v_fma_f32 v252, v230, v172, v244\n\t" \
  "v_fma_f32 v253, v230, v174, v245\n\t" \
  "v_fma_f32 v244, -v231, v253, v164\n\t" \
  "v_fma_f32 v245, v231, v252, v166\n\t" \
  "v_fma_f32 v252, v230, v252, v244\n\t" \
  "v_fma_f32 v253, v230, v253, v245\n\t" \
  "v_fma_f32 v244, -v231, v253, v160\n\t" \
  "v_fma_f32 v245, v231, v252, v162\n\t" \
  "v_fma_f32 v252, v230, v252, v244\n\t" \
  "v_fma_f32 v253, v230, v253, v245\n\t" \
  "v_mul_f32 v244, v233, v253\n\t" \
  "v_mul_f32 v245, v233, v252\n\t" \
  "v_fma_f32 v254, v232, v252, -v244\n\t" \
  "v_fma_f32 v255, v232, v253, v245\n\t" \
  "v_fma_f32 v252, v232, v252, -v244\n\t" \
  "v_fma_f32 v253, v232, v253, v245\n\t" \
  "s_nop 1\n\t" \
  "v_permlane16_swap_b32 v252, v254\n\t" \
  "v_permlane16_swap_b32 v253, v255\n\t" \
  "s_nop 0\n\t" \
  "v_add_f32 v244, v252, v254\n\t" \
  "v_add_f32 v245, v253, v255\n\t" \
  "v_add_f32 v252, v252, v254\n\t" \
  "v_add_f32 v253, v253, v255\n\t" \
  "v_mov_b32 v254, v244\n\t" \
  "v_mov_b32 v255, v245\n\t" \
  "s_nop 1\n\t" \
  "v_permlane32_swap_b32 v252, v254\n\t" \
  "v_permlane32_swap_b32 v253, v255\n\t" \
  "s_nop 0\n\t" \
  "v_add_f32 v244, v252, v254\n\t" \
  "v_add_f32 v245, v253, v255\n\t" \
  "v_add_f32 v252, v252, v254\n\t" \
  "v_add_f32 v253, v253, v255\n\t" \
  "v_mov_b32 v254, v244\n\t" \
  "v_mov_b32 v255, v245\n\t" \
  "v_mov_b32_dpp v254, v252 row_ror:12 row_mask:0x2 bank_mask:0xf\n\t" \
  "v_mov_b32_dpp v255, v253 row_ror:12 row_mask:0x2 bank_mask:0xf\n\t" \
  "v_mov_b32_dpp v254, v252 row_ror:8 row_mask:0x4 bank_mask:0xf\n\t" \
  "v_mov_b32_dpp v255, v253 row_ror:8 row_mask:0x4 bank_mask:0xf\n\t" \
  "v_mov_b32_dpp v254, v252 row_ror:4 row_mask:0x8 bank_mask:0xf\n\t" \
  "v_mov_b32_dpp v255, v253 row_ror:4 row_mask:0x8 bank_mask:0xf\n\t" \
  "s_nop 1\n\t" \
  "v_mov_b32_dpp v244, v254 row_newbcast:0 row_mask:0xf bank_mask:0xf\n\t" \
  "v_mov_b32_dpp v245, v254 row_newbcast:1 row_mask:0xf bank_mask:0xf\n\t" \
  "v_mov_b32_dpp v246, v254 row_newbcast:2 row_mask:0xf bank_mask:0xf\n\t" \
  "v_mov_b32_dpp v247, v254 row_newbcast:3 row_mask:0xf bank_mask:0xf\n\t" \
  "v_mov_b32_dpp v248, v255 row_newbcast:0 row_mask:0xf bank_mask:0xf\n\t" \
  "v_mov_b32_dpp v249, v255 row_newbcast:1 row_mask:0xf bank_mask:0xf\n\t" \
  "v_mov_b32_dpp v250, v255 row_newbcast:2 row_mask:0xf bank_mask:0xf\n\t" \
  "v_mov_b32_dpp v251, v255 row_newbcast:3 row_mask:0xf bank_mask:0xf\n\t" \
  "v_pk_fma_f32 v[236:237], v[184:185], v[244:245], v[236:237]\n\t" \
  "v_pk_fma_f32 v[238:239], v[186:187], v[246:247], v[238:239]\n\t" \
  "v_pk_fma_f32 v[240:241], v[184:185], v[248:249], v[240:241]\n\t" \
  "v_pk_fma_f32 v[242:243], v[186:187], v[250:251], v[242:243]\n\t" \
  "v_pk_fma_f32 v[236:237], v[188:189], v[248:249], v[236:237] neg_lo:[1,0,0] neg_hi:[1,0,0]\n\t" \
  "v_pk_fma_f32 v[238:239], v[190:191], v[250:251], v[238:239] neg_lo:[1,0,0] neg_hi:[1,0,0]\n\t" \
  "v_pk_fma_f32 v[240:241], v[188:189], v[244:245], v[240:241]\n\t" \
  "v_pk_fma_f32 v[242:243], v[190:191], v[246:247], v[242:243]\n\t" \
  "v_pk_fma_f32 v[128:129], v[180:181], v[240:241], v[128:129] neg_lo:[1,0,0] neg_hi:[1,0,0]\n\t" \
  "v_pk_fma_f32 v[130:131], v[182:183], v[242:243], v[130:131] neg_lo:[1,0,0] neg_hi:[1,0,0]\n\t" \
  "v_pk_fma_f32 v[96:97], v[180:181], v[236:237], v[96:97] neg_lo:[1,0,0] neg_hi:[1,0,0]\n\t" \
  "v_pk_fma_f32 v[98:99], v[182:183], v[238:239], v[98:99] neg_lo:[1,0,0] neg_hi:[1,0,0]\n\t" \
  "v_pk_fma_f32 v[128:129], v[176:177], v[236:237], v[128:129]\n\t" \
  "v_pk_fma_f32 v[130:131], v[178:179], v[238:239], v[130:131]\n\t" \
  "v_pk_fma_f32 v[96:97], v[176:177], v[240:241], v[96:97] neg_lo:[1,0,0] neg_hi:[1,0,0]\n\t" \
  "v_pk_fma_f32 v[98:99], v[178:179], v[242:243], v[98:99] neg_lo:[1,0,0] neg_hi:[1,0,0]\n\t" \
  "v_cvt_pk_bf16_f32 v160, v128, v129\n\t" \
  "v_cvt_pk_bf16_f32 v161, v130, v131\n\t" \
  "v_cvt_pk_bf16_f32 v162, v96, v97\n\t" \
  "v_cvt_pk_bf16_f32 v163, v98, v99\n\t" \
  "v_pk_fma_f32 v[132:133], v[180:181], v[96:97], v[132:133]\n\t" \
  "v_pk_fma_f32 v[134:135], v[182:183], v[98:99], v[134:135]\n\t" \
  "v_permlane16_swap_b32 v160, v162\n\t" \
  "v_permlane16_swap_b32 v161, v163\n\t" \
  "buffer_store_dwordx4 v[160:163], %[l16], %[srd], %[sh] offen offset:0 sc1\n\t" \
  "v_pk_fma_f32 v[100:101], v[180:181], v[128:129], v[100:101] neg_lo:[1,0,0] neg_hi:[1,0,0]\n\t" \
  "v_pk_fma_f32 v[102:103], v[182:183], v[130:131], v[102:103] neg_lo:[1,0,0] neg_hi:[1,0,0]\n\t" \
  "v_pk_fma_f32 v[132:133], v[176:177], v[128:129], v[132:133]\n\t" \
  "v_pk_fma_f32 v[134:135], v[178:179], v[130:131], v[134:135]\n\t" \
  "v_pk_fma_f32 v[100:101], v[176:177], v[96:97], v[100:101]\n\t" \
  "v_pk_fma_f32 v[102:103], v[178:179], v[98:99], v[102:103]\n\t" \
  "v_cvt_pk_bf16_f32 v164, v132, v133\n\t" \
  "v_cvt_pk_bf16_f32 v165, v134, v135\n\t" \
  "v_cvt_pk_bf16_f32 v166, v100, v101\n\t" \
  "v_cvt_pk_bf16_f32 v167, v102, v103\n\t" \
  "v_pk_fma_f32 v[136:137], v[180:181], v[100:101], v[136:137]\n\t" \
  "v_pk_fma_f32 v[138:139], v[182:183], v[102:103], v[138:139]\n\t" \
  "v_permlane16_swap_b32 v164, v166\n\t" \
  "v_permlane16_swap_b32 v165, v167\n\t" \
  "buffer_store_dwordx4 v[164:167], %[l16], %[srd], %[sh] offen offset:2048 sc1\n\t" \
  "s_add_u32 %[sh], %[sh], 0x1000\n\t" \
  "v_pk_fma_f32 v[104:105], v[180:181], v[132:133], v[104:105] neg_lo:[1,0,0] neg_hi:[1,0,0]\n\t" \
  "v_pk_fma_f32 v[106:107], v[182:183], v[134:135], v[106:107] neg_lo:[1,0,0] neg_hi:[1,0,0]\n\t" \
  "v_pk_fma_f32 v[136:137], v[176:177], v[132:133], v[136:137]\n\t" \
  "v_pk_fma_f32 v[138:139], v[178:179], v[134:135], v[138:139]\n\t" \
  "v_pk_fma_f32 v[104:105], v[176:177], v[100:101], v[104:105]\n\t" \
  "v_pk_fma_f32 v[106:107], v[178:179], v[102:103], v[106:107]\n\t" \
  "v_cvt_pk_bf16_f32 v168, v136, v137\n\t" \
  "v_cvt_pk_bf16_f32 v169, v138, v139\n\t" \
  "v_cvt_pk_bf16_f32 v170, v104, v105\n\t" \
  "v_cvt_pk_bf16_f32 v171, v106, v107\n\t" \
  "v_pk_fma_f32 v[140:141], v[180:181], v[104:105], v[140:141]\n\t" \
  "v_pk_fma_f32 v[142:143], v[182:183], v[106:107], v[142:143]\n\t" \
  "v_permlane16_swap_b32 v168, v170\n\t" \
  "v_permlane16_swap_b32 v169, v171\n\t" \
  "buffer_store_dwordx4 v[168:171], %[l16], %[srd], %[sh] offen offset:0 sc1\n\t" \
  "v_pk_fma_f32 v[108:109], v[180:181], v[136:137], v[108:109] neg_lo:[1,0,0] neg_hi:[1,0,0]\n\t" \
  "v_pk_fma_f32 v[110:111], v[182:183], v[138:139], v[110:111] neg_lo:[1,0,0] neg_hi:[1,0,0]\n\t" \
  "v_pk_fma_f32 v[140:141], v[176:177], v[136:137], v[140:141]\n\t" \
  "v_pk_fma_f32 v[142:143], v[178:179], v[138:139], v[142:143]\n\t" \
  "v_pk_fma_f32 v[108:109], v[176:177], v[104:105], v[108:109]\n\t" \
  "v_pk_fma_f32 v[110:111], v[178:179], v[106:107], v[110:111]\n\t" \
  "v_cvt_pk_bf16_f32 v172, v140, v141\n\t" \
  "v_cvt_pk_bf16_f32 v173, v142, v143\n\t" \
  "v_cvt_pk_bf16_f32 v174, v108, v109\n\t" \
  "v_cvt_pk_bf16_f32 v175, v110, v111\n\t" \
  "v_pk_fma_f32 v[144:145], v[180:181], v[108:109], v[144:145]\n\t" \
  "v_pk_fma_f32 v[146:147], v[182:183], v[110:111], v[146:147]\n\t" \
  "v_permlane16_swap_b32 v172, v174\n\t" \
  "v_permlane16_swap_b32 v173, v175\n\t" \
  "buffer_store_dwordx4 v[172:175], %[l16], %[srd], %[sh] offen offset:2048 sc1\n\t" \
  "s_add_u32 %[sh], %[sh], 0x1000\n\t" \
  "v_pk_fma_f32 v[112:113], v[180:181], v[140:141], v[112:113] neg_lo:[1,0,0] neg_hi:[1,0,0]\n\t" \
  "v_pk_fma_f32 v[114:115], v[182:183], v[142:143], v[114:115] neg_lo:[1,0,0] neg_hi:[1,0,0]\n\t" \
  "v_pk_fma_f32 v[144:145], v[176:177], v[140:141], v[144:145]\n\t" \
  "v_pk_fma_f32 v[146:147], v[178:179], v[142:143], v[146:147]\n\t" \
  "v_pk_fma_f32 v[112:113], v[176:177], v[108:109], v[112:113]\n\t" \
  "v_pk_fma_f32 v[114:115], v[178:179], v[110:111], v[114:115]\n\t" \
  "v_cvt_pk_bf16_f32 v192, v144, v145\n\t" \
  "v_cvt_pk_bf16_f32 v193, v146, v147\n\t" \
  "v_cvt_pk_bf16_f32 v194, v112, v113\n\t" \
  "v_cvt_pk_bf16_f32 v195, v114, v115\n\t" \
  "v_pk_fma_f32 v[148:149], v[180:181], v[112:113], v[148:149]\n\t" \
  "v_pk_fma_f32 v[150:151], v[182:183], v[114:115], v[150:151]\n\t" \
  "v_permlane16_swap_b32 v192, v194\n\t" \
  "v_permlane16_swap_b32 v193, v195\n\t" \
  "buffer_store_dwordx4 v[192:195], %[l16], %[srd], %[sh] offen offset:0 sc1\n\t" \
  "v_pk_fma_f32 v[116:117], v[180:181], v[144:145], v[116:117] neg_lo:[1,0,0] neg_hi:[1,0,0]\n\t" \
  "v_pk_fma_f32 v[118:119], v[182:183], v[146:147], v[118:119] neg_lo:[1,0,0] neg_hi:[1,0,0]\n\t" \
  "v_pk_fma_f32 v[148:149], v[176:177], v[144:145], v[148:149]\n\t" \
  "v_pk_fma_f32 v[150:151], v[178:179], v[146:147], v[150:151]\n\t" \
  "v_pk_fma_f32 v[116:117], v[176:177], v[112:113], v[116:117]\n\t" \
  "v_pk_fma_f32 v[118:119], v[178:179], v[114:115], v[118:119]\n\t" \
  "v_cvt_pk_bf16_f32 v196, v148, v149\n\t" \
  "v_cvt_pk_bf16_f32 v197, v150, v151\n\t" \
  "v_cvt_pk_bf16_f32 v198, v116, v117\n\t" \
  "v_cvt_pk_bf16_f32 v199, v118, v119\n\t" \
  "v_pk_fma_f32 v[152:153], v[180:181], v[116:117], v[152:153]\n\t" \
  "v_pk_fma_f32 v[154:155], v[182:183], v[118:119], v[154:155]\n\t" \
  "v_permlane16_swap_b32 v196, v198\n\t" \
  "v_permlane16_swap_b32 v197, v199\n\t" \
  "buffer_store_dwordx4 v[196:199], %[l16], %[srd], %[sh] offen offset:2048 sc1\n\t" \
  "s_add_u32 %[sh], %[sh], 0x1000\n\t" \
  "v_pk_fma_f32 v[120:121], v[180:181], v[148:149], v[120:121] neg_lo:[1,0,0] neg_hi:[1,0,0]\n\t" \
  "v_pk_fma_f32 v[122:123], v[182:183], v[150:151], v[122:123] neg_lo:[1,0,0] neg_hi:[1,0,0]\n\t" \
  "v_pk_fma_f32 v[152:153], v[176:177], v[148:149], v[152:153]\n\t" \
  "v_pk_fma_f32 v[154:155], v[178:179], v[150:151], v[154:155]\n\t" \
  "v_pk_fma_f32 v[120:121], v[176:177], v[116:117], v[120:121]\n\t" \
  "v_pk_fma_f32 v[122:123], v[178:179], v[118:119], v[122:123]\n\t" \
  "v_cvt_pk_bf16_f32 v200, v152, v153\n\t" \
  "v_cvt_pk_bf16_f32 v201, v154, v155\n\t" \
  "v_cvt_pk_bf16_f32 v202, v120, v121\n\t" \
  "v_cvt_pk_bf16_f32 v203, v122, v123\n\t" \
  "v_pk_fma_f32 v[156:157], v[180:181], v[120:121], v[156:157]\n\t" \
  "v_pk_fma_f32 v[158:159], v[182:183], v[122:123], v[158:159]\n\t" \
  "v_permlane16_swap_b32 v200, v202\n\t" \
  "v_permlane16_swap_b32 v201, v203\n\t" \
  "buffer_store_dwordx4 v[200:203], %[l16], %[srd], %[sh] offen offset:0 sc1\n\t" \
  "v_pk_fma_f32 v[124:125], v[180:181], v[152:153], v[124:125] neg_lo:[1,0,0] neg_hi:[1,0,0]\n\t" \
  "v_pk_fma_f32 v[126:127], v[182:183], v[154:155], v[126:127] neg_lo:[1,0,0] neg_hi:[1,0,0]\n\t" \
  "v_pk_fma_f32 v[156:157], v[176:177], v[152:153], v[156:157]\n\t" \
  "v_pk_fma_f32 v[158:159], v[178:179], v[154:155], v[158:159]\n\t" \
  "v_pk_fma_f32 v[124:125], v[176:177], v[120:121], v[124:125]\n\t" \
  "v_pk_fma_f32 v[126:127], v[178:179], v[122:123], v[126:127]\n\t" \
  "v_cvt_pk_bf16_f32 v204, v156, v157\n\t" \
  "v_cvt_pk_bf16_f32 v205, v158, v159\n\t" \
  "v_cvt_pk_bf16_f32 v206, v124, v125\n\t" \
  "v_cvt_pk_bf16_f32 v207, v126, v127\n\t" \
  "s_nop 1\n\t" \
  "v_permlane16_swap_b32 v204, v206\n\t" \
  "v_permlane16_swap_b32 v205, v207\n\t" \
  "buffer_store_dwordx4 v[204:207], %[l16], %[srd], %[sh] offen offset:2048 sc1\n\t" \
  "s_add_u32 %[scn], %[scn], 8\n\t" \
  "s_mov_b32 m0, %[scn]\n\t" \
  "s_mov_b64 exec, 1\n\t" \
  "buffer_load_dword %[l16], %[srz], 0 offen lds\n\t" \
  "s_mov_b64 exec, -1\n\t" \
  "s_branch Lend_%=\n\t" \
  "Lx0_%=:\n\t" \
  "v_add_u32 v244, %[sx], %[xo]\n\t" \
  "v_xor_b32 v245, 16, v244\n\t" \
  "v_xor_b32 v246, 32, v244\n\t" \
  "v_xor_b32 v247, 48, v244\n\t" \
  "ds_read_b64 %[xe00], v244 offset:0\n\t" \
  "ds_read_b64 %[xe10], v244 offset:2048\n\t" \
  "ds_read_b64 %[xe01], v245 offset:128\n\t" \
  "ds_read_b64 %[xe11], v245 offset:2176\n\t" \
  "ds_read_b64 %[xe02], v246 offset:256\n\t" \
  "ds_read_b64 %[xe12], v246 offset:2304\n\t" \
  "ds_read_b64 %[xe03], v247 offset:384\n\t" \
  "ds_read_b64 %[xe13], v247 offset:2432\n\t" \
  "s_branch Lxb0_%=\n\t" \
  "Lsp1_%=:\n\t" \
  "ds_read_b64 v[224:225], %[fl]\n\t" \
  "s_sub_i32 %[bud], %[bud], 1\n\t" \
  "s_cmp_lt_i32 %[bud], 0\n\t" \
  "s_cbranch_scc1 Lb1_%=\n\t" \
  "s_waitcnt lgkmcnt(0)\n\t" \
  "v_or_b32 v224, v224, v225\n\t" \
  "s_nop 0\n\t" \
  "v_readfirstlane_b32 %[s0], v224\n\t" \
  "s_cmp_eq_u32 %[s0], 0\n\t" \
  "s_cbranch_scc0 Lsp1_%=\n\t" \
  "s_branch Lb1_%=\n\t" \
  "Lx2_%=:\n\t" \
  "v_add_u32 v244, %[sx], %[xo]\n\t" \
  "v_xor_b32 v245, 16, v244\n\t" \
  "v_xor_b32 v246, 32, v244\n\t" \
  "v_xor_b32 v247, 48, v244\n\t" \
  "ds_read_b64 %[xe00], v244 offset:0\n\t" \
  "ds_read_b64 %[xe10], v244 offset:2048\n\t" \
  "ds_read_b64 %[xe01], v245 offset:128\n\t" \
  "ds_read_b64 %[xe11], v245 offset:2176\n\t" \
  "ds_read_b64 %[xe02], v246 offset:256\n\t" \
  "ds_read_b64 %[xe12], v246 offset:2304\n\t" \
  "ds_read_b64 %[xe03], v247 offset:384\n\t" \
  "ds_read_b64 %[xe13], v247 offset:2432\n\t" \
  "s_branch Lxb2_%=\n\t" \
  "Lsp3_%=:\n\t" \
  "ds_read_b64 v[224:225], %[fl]\n\t" \
  "s_sub_i32 %[bud], %[bud], 1\n\t" \
  "s_cmp_lt_i32 %[bud], 0\n\t" \
  "s_cbranch_scc1 Lb3_%=\n\t" \
  "s_waitcnt lgkmcnt(0)\n\t" \
  "v_or_b32 v224, v224, v225\n\t" \
  "s_nop 0\n\t" \
  "v_readfirstlane_b32 %[s0], v224\n\t" \
  "s_cmp_eq_u32 %[s0], 0\n\t" \
  "s_cbranch_scc0 Lsp3_%=\n\t" \
  "s_branch Lb3_%=\n\t" \
  "Lx4_%=:\n\t" \
  "v_add_u32 v244, %[sx], %[xo]\n\t" \
  "v_xor_b32 v245, 16, v244\n\t" \
  "v_xor_b32 v246, 32, v244\n\t" \
  "v_xor_b32 v247, 48, v244\n\t" \
  "ds_read_b64 %[xe00], v244 offset:0\n\t" \
  "ds_read_b64 %[xe10], v244 offset:2048\n\t" \
  "ds_read_b64 %[xe01], v245 offset:128\n\t" \
  "ds_read_b64 %[xe11], v245 offset:2176\n\t" \
  "ds_read_b64 %[xe02], v246 offset:256\n\t" \
  "ds_read_b64 %[xe12], v246 offset:2304\n\t" \
  "ds_read_b64 %[xe03], v247 offset:384\n\t" \
  "ds_read_b64 %[xe13], v247 offset:2432\n\t" \
  "s_branch Lxb4_%=\n\t" \
  "Lsp5_%=:\n\t" \
  "ds_read_b64 v[224:225], %[fl]\n\t" \
  "s_sub_i32 %[bud], %[bud], 1\n\t" \
  "s_cmp_lt_i32 %[bud], 0\n\t" \
  "s_cbranch_scc1 Lb5_%=\n\t" \
  "s_waitcnt lgkmcnt(0)\n\t" \
  "v_or_b32 v224, v224, v225\n\t" \
  "s_nop 0\n\t" \
  "v_readfirstlane_b32 %[s0], v224\n\t" \
  "s_cmp_eq_u32 %[s0], 0\n\t" \
  "s_cbranch_scc0 Lsp5_%=\n\t" \
  "s_branch Lb5_%=\n\t" \
  "Lend_%=:\n\t" \
  "s_nop 7\n\t" \
  "s_nop 7\n\t"
#define ASM_MB \
  "s_nop 4\n\t" \
  "s_mov_b32 %[sg], 0\n\t" \
  "s_mov_b32 m0, %[scn]\n\t" \
  "Lc0_%=:\n\t" \
  "ds_read_b32 v192, %[f9]\n\t" \
  "s_sub_i32 %[bud], %[bud], 1\n\t" \
  "s_cmp_lt_i32 %[bud], 0\n\t" \
  "s_cbranch_scc1 Lc0b_%=\n\t" \
  "s_waitcnt lgkmcnt(0)\n\t" \
  "v_cmp_ne_u32 vcc, 0, v192\n\t" \
  "s_cbranch_vccnz Lc0_%=\n\t" \
  "Lc0b_%=:\n\t" \
  "ds_read_b128 v[128:131], %[pc0] offset:0\n\t" \
  "ds_read_b128 v[132:135], %[pc0] offset:2048\n\t" \
  "ds_read_b128 v[136:139], %[pc0] offset:4096\n\t" \
  "ds_read_b128 v[140:143], %[pc0] offset:6144\n\t" \
  "ds_read_b128 v[144:147], %[pc1] offset:0\n\t" \
  "ds_read_b128 v[148:151], %[pc1] offset:2048\n\t" \
  "ds_read_b128 v[152:155], %[pc1] offset:4096\n\t" \
  "ds_read_b128 v[156:159], %[pc1] offset:6144\n\t" \
  "s_waitcnt vmcnt(0)\n\t" \
  "v_readfirstlane_b32 %[s0], %[cnt]\n\t" \
  "s_cmp_eq_u32 %[s0], 31\n\t" \
  "s_cbranch_scc1 Lf2_%=\n\t" \
  "s_cmp_eq_u32 %[w], 0\n\t" \
  "s_cbranch_scc0 Lw_%=\n\t" \
  "Lf_%=:\n\t" \
  "v_mov_b32 v196, 0\n\t" \
  "s_mov_b64 exec, 1\n\t" \
  "buffer_atomic_or v196, off, %[srd], %[sf] sc0 sc1\n\t" \
  "s_mov_b64 exec, -1\n\t" \
  "s_sub_i32 %[bud], %[bud], 16\n\t" \
  "s_cmp_lt_i32 %[bud], 0\n\t" \
  "s_cbranch_scc1 Lf2_%=\n\t" \
  "s_waitcnt vmcnt(0)\n\t" \
  "v_readfirstlane_b32 %[s0], v196\n\t" \
  "s_cmp_lg_u32 %[s0], 32\n\t" \
  "s_cbranch_scc0 Lf2_%=\n\t" \
  "s_sleep 1\n\t" \
  "s_branch Lf_%=\n\t" \
  "Lf2_%=:\n\t" \
  "v_mov_b32 v192, 0\n\t" \
  "v_mov_b32 v193, 0\n\t" \
  "ds_write_b64 %[hr], v[192:193]\n\t" \
  "Lf3_%=:\n\t" \
  "s_setprio 2\n\t" \
  "buffer_load_dwordx4 v[64:67], %[vo], %[srd], %[h0] offen offset:0 sc1\n\t" \
  "buffer_load_dwordx4 v[68:71], %[vo], %[srd], %[h0] offen offset:2048 sc1\n\t" \
  "buffer_load_dwordx4 v[72:75], %[vo], %[srd], %[h1] offen offset:0 sc1\n\t" \
  "buffer_load_dwordx4 v[76:79], %[vo], %[srd], %[h1] offen offset:2048 sc1\n\t" \
  "buffer_load_dwordx4 v[80:83], %[vo], %[srd], %[h2] offen offset:0 sc1\n\t" \
  "buffer_load_dword v194, off, %[srz], 0\n\t" \
  "buffer_load_dwordx4 v[84:87], %[vo], %[srd], %[h2] offen offset:2048 sc1\n\t" \
  "buffer_load_dwordx4 v[88:91], %[vo], %[srd], %[h3] offen offset:0 sc1\n\t" \
  "buffer_load_dwordx4 v[92:95], %[vo], %[srd], %[h3] offen offset:2048 sc1\n\t" \
  "buffer_load_dwordx4 v[96:99], %[vo], %[srd], %[h4] offen offset:0 sc1\n\t" \
  "buffer_load_dwordx4 v[100:103], %[vo], %[srd], %[h4] offen offset:2048 sc1\n\t" \
  "buffer_load_dwordx4 v[104:107], %[vo], %[srd], %[h5] offen offset:0 sc1\n\t" \
  "buffer_load_dwordx4 v[108:111], %[vo], %[srd], %[h5] offen offset:2048 sc1\n\t" \
  "buffer_load_dwordx4 v[112:115], %[vo], %[srd], %[h6] offen offset:0 sc1\n\t" \
  "buffer_load_dword v194, off, %[srz], 0\n\t" \
  "buffer_load_dwordx4 v[116:119], %[vo], %[srd], %[h6] offen offset:2048 sc1\n\t" \
  "s_waitcnt lgkmcnt(0)\n\t" \
  "Lloop_%=:\n\t" \
  "s_waitcnt vmcnt(14) lgkmcnt(4)\n\t" \
  "v_mfma_f32_16x16x32_bf16 %[c00], v[64:67], v[128:131], %[c00]\n\t" \
  "buffer_load_dwordx4 v[120:123], %[vo], %[srd], %[h7] offen offset:0 sc1\n\t" \
  "v_mfma_f32_16x16x32_bf16 %[c10], v[68:71], v[128:131], %[c10]\n\t" \
  "ds_read_b128 v[160:163], %[pc2] offset:0\n\t" \
  "v_add_u32 %[pc0], %[k2], %[pc0]\n\t" \
  "v_mfma_f32_16x16x32_bf16 %[c01], v[64:67], v[132:135], %[c01]\n\t" \
  "v_mfma_f32_16x16x32_bf16 %[c11], v[68:71], v[132:135], %[c11]\n\t" \
  "ds_read_b128 v[164:167], %[pc2] offset:2048\n\t" \
  "v_mfma_f32_16x16x32_bf16 %[c02], v[64:67], v[136:139], %[c02]\n\t" \
  "buffer_load_dwordx4 v[124:127], %[vo], %[srd], %[h7] offen offset:2048 sc1\n\t" \
  "v_mfma_f32_16x16x32_bf16 %[c12], v[68:71], v[136:139], %[c12]\n\t" \
  "ds_read_b128 v[168:171], %[pc2] offset:4096\n\t" \
  "v_mfma_f32_16x16x32_bf16 %[c03], v[64:67], v[140:143], %[c03]\n\t" \
  "v_add_u32 %[vo], 0x20000, %[vo]\n\t" \
  "v_and_b32 %[vo], 0xfffff, %[vo]\n\t" \
  "v_mfma_f32_16x16x32_bf16 %[c13], v[68:71], v[140:143], %[c13]\n\t" \
  "ds_read_b128 v[172:175], %[pc2] offset:6144\n\t" \
  "s_waitcnt vmcnt(14) lgkmcnt(4)\n\t" \
  "v_mfma_f32_16x16x32_bf16 %[c00], v[72:75], v[144:147], %[c00]\n\t" \
  "buffer_load_dwordx4 v[64:67], %[vo], %[srd], %[h0] offen offset:0 sc1\n\t" \
  "v_mfma_f32_16x16x32_bf16 %[c10], v[76:79], v[144:147], %[c10]\n\t" \
  "ds_read_b128 v[176:179], %[pc3] offset:0\n\t" \
  "v_add_u32 %[pc1], %[k2], %[pc1]\n\t" \
  "v_mfma_f32_16x16x32_bf16 %[c01], v[72:75], v[148:151], %[c01]\n\t" \
  "s_cmp_eq_u32 %[sg], 2\n\t" \
  "s_cbranch_scc1 Lh_%=\n\t" \
  "Lhb_%=:\n\t" \
  "v_mfma_f32_16x16x32_bf16 %[c11], v[76:79], v[148:151], %[c11]\n\t" \
  "ds_read_b128 v[180:183], %[pc3] offset:2048\n\t" \
  "v_mfma_f32_16x16x32_bf16 %[c02], v[72:75], v[152:155], %[c02]\n\t" \
  "buffer_load_dwordx4 v[68:71], %[vo], %[srd], %[h0] offen offset:2048 sc1\n\t" \
  "v_mfma_f32_16x16x32_bf16 %[c12], v[76:79], v[152:155], %[c12]\n\t" \
  "ds_read_b128 v[184:187], %[pc3] offset:4096\n\t" \
  "v_mfma_f32_16x16x32_bf16 %[c03], v[72:75], v[156:159], %[c03]\n\t" \
  "v_mfma_f32_16x16x32_bf16 %[c13], v[76:79], v[156:159], %[c13]\n\t" \
  "ds_read_b128 v[188:191], %[pc3] offset:6144\n\t" \
  "s_waitcnt vmcnt(13) lgkmcnt(4)\n\t" \
  "v_mfma_f32_16x16x32_bf16 %[c00], v[80:83], v[160:163], %[c00]\n\t" \
  "buffer_load_dwordx4 v[72:75], %[vo], %[srd], %[h1] offen offset:0 sc1\n\t" \
  "v_mfma_f32_16x16x32_bf16 %[c10], v[84:87], v[160:163], %[c10]\n\t" \
  "ds_read_b128 v[128:131], %[pc0] offset:0\n\t" \
  "v_add_u32 %[pc2], %[k2], %[pc2]\n\t" \
  "v_mfma_f32_16x16x32_bf16 %[c01], v[80:83], v[164:167], %[c01]\n\t" \
  "v_mfma_f32_16x16x32_bf16 %[c11], v[84:87], v[164:167], %[c11]\n\t" \
  "ds_read_b128 v[132:135], %[pc0] offset:2048\n\t" \
  "v_mfma_f32_16x16x32_bf16 %[c02], v[80:83], v[168:171], %[c02]\n\t" \
  "buffer_load_dwordx4 v[76:79], %[vo], %[srd], %[h1] offen offset:2048 sc1\n\t" \
  "v_mfma_f32_16x16x32_bf16 %[c12], v[84:87], v[168:171], %[c12]\n\t" \
  "ds_read_b128 v[136:139], %[pc0] offset:4096\n\t" \
  "v_mfma_f32_16x16x32_bf16 %[c03], v[80:83], v[172:175], %[c03]\n\t" \
  "v_mfma_f32_16x16x32_bf16 %[c13], v[84:87], v[172:175], %[c13]\n\t" \
  "ds_read_b128 v[140:143], %[pc0] offset:6144\n\t" \
  "s_waitcnt vmcnt(13) lgkmcnt(4)\n\t" \
  "v_mfma_f32_16x16x32_bf16 %[c00], v[88:91], v[176:179], %[c00]\n\t" \
  "buffer_load_dwordx4 v[80:83], %[vo], %[srd], %[h2] offen offset:0 sc1\n\t" \
  "v_mfma_f32_16x16x32_bf16 %[c10], v[92:95], v[176:179], %[c10]\n\t" \
  "ds_read_b128 v[144:147], %[pc1] offset:0\n\t" \
  "v_add_u32 %[pc3], %[k2], %[pc3]\n\t" \
  "v_mfma_f32_16x16x32_bf16 %[c01], v[88:91], v[180:183], %[c01]\n\t" \
  "s_mov_b64 exec, 1\n\t" \
  "buffer_load_dword off, %[srz], 0 lds\n\t" \
  "s_mov_b64 exec, -1\n\t" \
  "v_mfma_f32_16x16x32_bf16 %[c11], v[92:95], v[180:183], %[c11]\n\t" \
  "ds_read_b128 v[148:151], %[pc1] offset:2048\n\t" \
  "v_mfma_f32_16x16x32_bf16 %[c02], v[88:91], v[184:187], %[c02]\n\t" \
  "buffer_load_dwordx4 v[84:87], %[vo], %[srd], %[h2] offen offset:2048 sc1\n\t" \
  "v_mfma_f32_16x16x32_bf16 %[c12], v[92:95], v[184:187], %[c12]\n\t" \
  "ds_read_b128 v[152:155], %[pc1] offset:4096\n\t" \
  "s_add_u32 m0, m0, 16\n\t" \
  "v_mfma_f32_16x16x32_bf16 %[c03], v[88:91], v[188:191], %[c03]\n\t" \
  "v_mfma_f32_16x16x32_bf16 %[c13], v[92:95], v[188:191], %[c13]\n\t" \
  "ds_read_b128 v[156:159], %[pc1] offset:6144\n\t" \
  "s_waitcnt vmcnt(14) lgkmcnt(4)\n\t" \
  "v_mfma_f32_16x16x32_bf16 %[c00], v[96:99], v[128:131], %[c00]\n\t" \
  "buffer_load_dwordx4 v[88:91], %[vo], %[srd], %[h3] offen offset:0 sc1\n\t" \
  "v_mfma_f32_16x16x32_bf16 %[c10], v[100:103], v[128:131], %[c10]\n\t" \
  "ds_read_b128 v[160:163], %[pc2] offset:0\n\t" \
  "v_add_u32 %[pc0], %[k2], %[pc0]\n\t" \
  "v_mfma_f32_16x16x32_bf16 %[c01], v[96:99], v[132:135], %[c01]\n\t" \
  "v_mfma_f32_16x16x32_bf16 %[c11], v[100:103], v[132:135], %[c11]\n\t" \
  "ds_read_b128 v[164:167], %[pc2] offset:2048\n\t" \
  "v_mfma_f32_16x16x32_bf16 %[c02], v[96:99], v[136:139], %[c02]\n\t" \
  "buffer_load_dwordx4 v[92:95], %[vo], %[srd], %[h3] offen offset:2048 sc1\n\t" \
  "v_mfma_f32_16x16x32_bf16 %[c12], v[100:103], v[136:139], %[c12]\n\t" \
  "ds_read_b128 v[168:171], %[pc2] offset:4096\n\t" \
  "v_mfma_f32_16x16x32_bf16 %[c03], v[96:99], v[140:143], %[c03]\n\t" \
  "v_mfma_f32_16x16x32_bf16 %[c13], v[100:103], v[140:143], %[c13]\n\t" \
  "ds_read_b128 v[172:175], %[pc2] offset:6144\n\t" \
  "s_waitcnt vmcnt(14) lgkmcnt(4)\n\t" \
  "v_mfma_f32_16x16x32_bf16 %[c00], v[104:107], v[144:147], %[c00]\n\t" \
  "buffer_load_dwordx4 v[96:99], %[vo], %[srd], %[h4] offen offset:0 sc1\n\t" \
  "v_mfma_f32_16x16x32_bf16 %[c10], v[108:111], v[144:147], %[c10]\n\t" \
  "ds_read_b128 v[176:179], %[pc3] offset:0\n\t" \
  "v_add_u32 %[pc1], %[k2], %[pc1]\n\t" \
  "v_mfma_f32_16x16x32_bf16 %[c01], v[104:107], v[148:151], %[c01]\n\t" \
  "v_mfma_f32_16x16x32_bf16 %[c11], v[108:111], v[148:151], %[c11]\n\t" \
  "ds_read_b128 v[180:183], %[pc3] offset:2048\n\t" \
  "v_mfma_f32_16x16x32_bf16 %[c02], v[104:107], v[152:155], %[c02]\n\t" \
  "buffer_load_dwordx4 v[100:103], %[vo], %[srd], %[h4] offen offset:2048 sc1\n\t" \
  "v_mfma_f32_16x16x32_bf16 %[c12], v[108:111], v[152:155], %[c12]\n\t" \
  "ds_read_b128 v[184:187], %[pc3] offset:4096\n\t" \
  "v_mfma_f32_16x16x32_bf16 %[c03], v[104:107], v[156:159], %[c03]\n\t" \
  "v_mfma_f32_16x16x32_bf16 %[c13], v[108:111], v[156:159], %[c13]\n\t" \
  "ds_read_b128 v[188:191], %[pc3] offset:6144\n\t" \
  "s_waitcnt vmcnt(13) lgkmcnt(4)\n\t" \
  "v_mfma_f32_16x16x32_bf16 %[c00], v[112:115], v[160:163], %[c00]\n\t" \
  "buffer_load_dwordx4 v[104:107], %[vo], %[srd], %[h5] offen offset:0 sc1\n\t" \
  "v_mfma_f32_16x16x32_bf16 %[c10], v[116:119], v[160:163], %[c10]\n\t" \
  "ds_read_b128 v[128:131], %[pc0] offset:0\n\t" \
  "v_add_u32 %[pc2], %[k2], %[pc2]\n\t" \
  "v_mfma_f32_16x16x32_bf16 %[c01], v[112:115], v[164:167], %[c01]\n\t" \
  "v_mfma_f32_16x16x32_bf16 %[c11], v[116:119], v[164:167], %[c11]\n\t" \
  "ds_read_b128 v[132:135], %[pc0] offset:2048\n\t" \
  "v_mfma_f32_16x16x32_bf16 %[c02], v[112:115], v[168:171], %[c02]\n\t" \
  "buffer_load_dwordx4 v[108:111], %[vo], %[srd], %[h5] offen offset:2048 sc1\n\t" \
  "v_mfma_f32_16x16x32_bf16 %[c12], v[116:119], v[168:171], %[c12]\n\t" \
  "ds_read_b128 v[136:139], %[pc0] offset:4096\n\t" \
  "v_mfma_f32_16x16x32_bf16 %[c03], v[112:115], v[172:175], %[c03]\n\t" \
  "v_mfma_f32_16x16x32_bf16 %[c13], v[116:119], v[172:175], %[c13]\n\t" \
  "ds_read_b128 v[140:143], %[pc0] offset:6144\n\t" \
  "s_waitcnt vmcnt(13) lgkmcnt(4)\n\t" \
  "v_mfma_f32_16x16x32_bf16 %[c00], v[120:123], v[176:179], %[c00]\n\t" \
  "buffer_load_dwordx4 v[112:115], %[vo], %[srd], %[h6] offen offset:0 sc1\n\t" \
  "v_mfma_f32_16x16x32_bf16 %[c10], v[124:127], v[176:179], %[c10]\n\t" \
  "ds_read_b128 v[144:147], %[pc1] offset:0\n\t" \
  "v_add_u32 %[pc3], %[k2], %[pc3]\n\t" \
  "v_mfma_f32_16x16x32_bf16 %[c01], v[120:123], v[180:183], %[c01]\n\t" \
  "s_mov_b64 exec, 1\n\t" \
  "buffer_load_dword off, %[srz], 0 lds\n\t" \
  "s_mov_b64 exec, -1\n\t" \
  "v_mfma_f32_16x16x32_bf16 %[c11], v[124:127], v[180:183], %[c11]\n\t" \
  "ds_read_b128 v[148:151], %[pc1] offset:2048\n\t" \
  "s_add_u32 %[sg], %[sg], 1\n\t" \
  "v_mfma_f32_16x16x32_bf16 %[c02], v[120:123], v[184:187], %[c02]\n\t" \
  "buffer_load_dwordx4 v[116:119], %[vo], %[srd], %[h6] offen offset:2048 sc1\n\t" \
  "v_mfma_f32_16x16x32_bf16 %[c12], v[124:127], v[184:187], %[c12]\n\t" \
  "ds_read_b128 v[152:155], %[pc1] offset:4096\n\t" \
  "s_add_u32 m0, m0, 16\n\t" \
  "v_mfma_f32_16x16x32_bf16 %[c03], v[120:123], v[188:191], %[c03]\n\t" \
  "s_cmp_eq_u32 %[sg], 3\n\t" \
  "s_cbranch_scc1 Lg_%=\n\t" \
  "Lgb_%=:\n\t" \
  "v_mfma_f32_16x16x32_bf16 %[c13], v[124:127], v[188:191], %[c13]\n\t" \
  "s_cmp_lt_u32 %[sg], 4\n\t" \
  "ds_read_b128 v[156:159], %[pc1] offset:6144\n\t" \
  "s_cbranch_scc1 Lloop_%=\n\t" \
  "s_mov_b32 %[s1], 0xfffe0000\n\t" \
  "Lloop2_%=:\n\t" \
  "s_waitcnt vmcnt(12) lgkmcnt(4)\n\t" \
  "v_mfma_f32_16x16x32_bf16 %[c00], v[64:67], v[128:131], %[c00]\n\t" \
  "ds_read_b64 v[192:193], %[fl] offset:0\n\t" \
  "buffer_load_dwordx4 v[120:123], %[vo], %[srd], %[h7] offen offset:0 sc1\n\t" \
  "v_mfma_f32_16x16x32_bf16 %[c10], v[68:71], v[128:131], %[c10]\n\t" \
  "ds_read_b128 v[160:163], %[pc2] offset:0\n\t" \
  "v_add_u32 %[pc0], %[s1], %[pc0]\n\t" \
  "v_mfma_f32_16x16x32_bf16 %[c01], v[64:67], v[132:135], %[c01]\n\t" \
  "v_mfma_f32_16x16x32_bf16 %[c11], v[68:71], v[132:135], %[c11]\n\t" \
  "ds_read_b128 v[164:167], %[pc2] offset:2048\n\t" \
  "v_mfma_f32_16x16x32_bf16 %[c02], v[64:67], v[136:139], %[c02]\n\t" \
  "buffer_load_dwordx4 v[124:127], %[vo], %[srd], %[h7] offen offset:2048 sc1\n\t" \
  "v_mfma_f32_16x16x32_bf16 %[c12], v[68:71], v[136:139], %[c12]\n\t" \
  "ds_read_b128 v[168:171], %[pc2] offset:4096\n\t" \
  "v_mfma_f32_16x16x32_bf16 %[c03], v[64:67], v[140:143], %[c03]\n\t" \
  "v_add_u32 %[vo], 0x20000, %[vo]\n\t" \
  "v_and_b32 %[vo], 0xfffff, %[vo]\n\t" \
  "v_mfma_f32_16x16x32_bf16 %[c13], v[68:71], v[140:143], %[c13]\n\t" \
  "ds_read_b128 v[172:175], %[pc2] offset:6144\n\t" \
  "s_waitcnt vmcnt(12) lgkmcnt(4)\n\t" \
  "v_mfma_f32_16x16x32_bf16 %[c00], v[72:75], v[144:147], %[c00]\n\t" \
  "buffer_load_dwordx4 v[64:67], %[vo], %[srd], %[h0] offen offset:0 sc1\n\t" \
  "v_mfma_f32_16x16x32_bf16 %[c10], v[76:79], v[144:147], %[c10]\n\t" \
  "ds_read_b128 v[176:179], %[pc3] offset:0\n\t" \
  "v_add_u32 %[pc1], %[s1], %[pc1]\n\t" \
  "v_mfma_f32_16x16x32_bf16 %[c01], v[72:75], v[148:151], %[c01]\n\t" \
  "v_or_b32 v192, v192, v193\n\t" \
  "v_mfma_f32_16x16x32_bf16 %[c11], v[76:79], v[148:151], %[c11]\n\t" \
  "ds_read_b128 v[180:183], %[pc3] offset:2048\n\t" \
  "v_readfirstlane_b32 %[s0], v192\n\t" \
  "v_mfma_f32_16x16x32_bf16 %[c02], v[72:75], v[152:155], %[c02]\n\t" \
  "buffer_load_dwordx4 v[68:71], %[vo], %[srd], %[h0] offen offset:2048 sc1\n\t" \
  "v_mfma_f32_16x16x32_bf16 %[c12], v[76:79], v[152:155], %[c12]\n\t" \
  "ds_read_b128 v[184:187], %[pc3] offset:4096\n\t" \
  "v_mfma_f32_16x16x32_bf16 %[c03], v[72:75], v[156:159], %[c03]\n\t" \
  "s_cmp_eq_u32 %[s0], 0\n\t" \
  "s_cbranch_scc0 Lcb1_%=\n\t" \
  "Lcb1b_%=:\n\t" \
  "v_mfma_f32_16x16x32_bf16 %[c13], v[76:79], v[156:159], %[c13]\n\t" \
  "ds_read_b128 v[188:191], %[pc3] offset:6144\n\t" \
  "s_waitcnt vmcnt(12) lgkmcnt(4)\n\t" \
  "v_mfma_f32_16x16x32_bf16 %[c00], v[80:83], v[160:163], %[c00]\n\t" \
  "buffer_load_dwordx4 v[72:75], %[vo], %[srd], %[h1] offen offset:0 sc1\n\t" \
  "v_mfma_f32_16x16x32_bf16 %[c10], v[84:87], v[160:163], %[c10]\n\t" \
  "ds_read_b128 v[128:131], %[pc0] offset:0\n\t" \
  "v_add_u32 %[pc2], %[s1], %[pc2]\n\t" \
  "v_mfma_f32_16x16x32_bf16 %[c01], v[80:83], v[164:167], %[c01]\n\t" \
  "v_mfma_f32_16x16x32_bf16 %[c11], v[84:87], v[164:167], %[c11]\n\t" \
  "ds_read_b128 v[132:135], %[pc0] offset:2048\n\t" \
  "v_mfma_f32_16x16x32_bf16 %[c02], v[80:83], v[168:171], %[c02]\n\t" \
  "buffer_load_dwordx4 v[76:79], %[vo], %[srd], %[h1] offen offset:2048 sc1\n\t" \
  "v_mfma_f32_16x16x32_bf16 %[c12], v[84:87], v[168:171], %[c12]\n\t" \
  "ds_read_b128 v[136:139], %[pc0] offset:4096\n\t" \
  "v_mfma_f32_16x16x32_bf16 %[c03], v[80:83], v[172:175], %[c03]\n\t" \
  "v_mfma_f32_16x16x32_bf16 %[c13], v[84:87], v[172:175], %[c13]\n\t" \
  "ds_read_b128 v[140:143], %[pc0] offset:6144\n\t" \
  "s_waitcnt vmcnt(12) lgkmcnt(4)\n\t" \
  "v_mfma_f32_16x16x32_bf16 %[c00], v[88:91], v[176:179], %[c00]\n\t" \
  "buffer_load_dwordx4 v[80:83], %[vo], %[srd], %[h2] offen offset:0 sc1\n\t" \
  "v_mfma_f32_16x16x32_bf16 %[c10], v[92:95], v[176:179], %[c10]\n\t" \
  "ds_read_b128 v[144:147], %[pc1] offset:0\n\t" \
  "v_add_u32 %[pc3], %[s1], %[pc3]\n\t" \
  "v_mfma_f32_16x16x32_bf16 %[c01], v[88:91], v[180:183], %[c01]\n\t" \
  "v_mfma_f32_16x16x32_bf16 %[c11], v[92:95], v[180:183], %[c11]\n\t" \
  "ds_read_b128 v[148:151], %[pc1] offset:2048\n\t" \
  "v_mfma_f32_16x16x32_bf16 %[c02], v[88:91], v[184:187], %[c02]\n\t" \
  "buffer_load_dwordx4 v[84:87], %[vo], %[srd], %[h2] offen offset:2048 sc1\n\t" \
  "v_mfma_f32_16x16x32_bf16 %[c12], v[92:95], v[184:187], %[c12]\n\t" \
  "ds_read_b128 v[152:155], %[pc1] offset:4096\n\t" \
  "v_mfma_f32_16x16x32_bf16 %[c03], v[88:91], v[188:191], %[c03]\n\t" \
  "v_mfma_f32_16x16x32_bf16 %[c13], v[92:95], v[188:191], %[c13]\n\t" \
  "ds_read_b128 v[156:159], %[pc1] offset:6144\n\t" \
  "s_waitcnt vmcnt(12) lgkmcnt(4)\n\t" \
  "v_mfma_f32_16x16x32_bf16 %[c00], v[96:99], v[128:131], %[c00]\n\t" \
  "ds_read_b64 v[192:193], %[fl] offset:8\n\t" \
  "buffer_load_dwordx4 v[88:91], %[vo], %[srd], %[h3] offen offset:0 sc1\n\t" \
  "v_mfma_f32_16x16x32_bf16 %[c10], v[100:103], v[128:131], %[c10]\n\t" \
  "ds_read_b128 v[160:163], %[pc2] offset:0\n\t" \
  "v_add_u32 %[pc0], %[k2], %[pc0]\n\t" \
  "v_mfma_f32_16x16x32_bf16 %[c01], v[96:99], v[132:135], %[c01]\n\t" \
  "v_mfma_f32_16x16x32_bf16 %[c11], v[100:103], v[132:135], %[c11]\n\t" \
  "ds_read_b128 v[164:167], %[pc2] offset:2048\n\t" \
  "v_mfma_f32_16x16x32_bf16 %[c02], v[96:99], v[136:139], %[c02]\n\t" \
  "buffer_load_dwordx4 v[92:95], %[vo], %[srd], %[h3] offen offset:2048 sc1\n\t" \
  "v_mfma_f32_16x16x32_bf16 %[c12], v[100:103], v[136:139], %[c12]\n\t" \
  "ds_read_b128 v[168:171], %[pc2] offset:4096\n\t" \
  "v_mfma_f32_16x16x32_bf16 %[c03], v[96:99], v[140:143], %[c03]\n\t" \
  "v_mfma_f32_16x16x32_bf16 %[c13], v[100:103], v[140:143], %[c13]\n\t" \
  "ds_read_b128 v[172:175], %[pc2] offset:6144\n\t" \
  "s_waitcnt vmcnt(12) lgkmcnt(4)\n\t" \
  "v_mfma_f32_16x16x32_bf16 %[c00], v[104:107], v[144:147], %[c00]\n\t" \
  "buffer_load_dwordx4 v[96:99], %[vo], %[srd], %[h4] offen offset:0 sc1\n\t" \
  "v_mfma_f32_16x16x32_bf16 %[c10], v[108:111], v[144:147], %[c10]\n\t" \
  "ds_read_b128 v[176:179], %[pc3] offset:0\n\t" \
  "v_add_u32 %[pc1], %[k2], %[pc1]\n\t" \
  "v_mfma_f32_16x16x32_bf16 %[c01], v[104:107], v[148:151], %[c01]\n\t" \
  "v_or_b32 v192, v192, v193\n\t" \
  "v_mfma_f32_16x16x32_bf16 %[c11], v[108:111], v[148:151], %[c11]\n\t" \
  "ds_read_b128 v[180:183], %[pc3] offset:2048\n\t" \
  "v_readfirstlane_b32 %[s0], v192\n\t" \
  "v_mfma_f32_16x16x32_bf16 %[c02], v[104:107], v[152:155], %[c02]\n\t" \
  "buffer_load_dwordx4 v[100:103], %[vo], %[srd], %[h4] offen offset:2048 sc1\n\t" \
  "v_mfma_f32_16x16x32_bf16 %[c12], v[108:111], v[152:155], %[c12]\n\t" \
  "ds_read_b128 v[184:187], %[pc3] offset:4096\n\t" \
  "v_mfma_f32_16x16x32_bf16 %[c03], v[104:107], v[156:159], %[c03]\n\t" \
  "s_cmp_eq_u32 %[s0], 0\n\t" \
  "s_cbranch_scc0 Lcb5_%=\n\t" \
  "Lcb5b_%=:\n\t" \
  "v_mfma_f32_16x16x32_bf16 %[c13], v[108:111], v[156:159], %[c13]\n\t" \
  "ds_read_b128 v[188:191], %[pc3] offset:6144\n\t" \
  "s_waitcnt vmcnt(12) lgkmcnt(4)\n\t" \
  "v_mfma_f32_16x16x32_bf16 %[c00], v[112:115], v[160:163], %[c00]\n\t" \
  "buffer_load_dwordx4 v[104:107], %[vo], %[srd], %[h5] offen offset:0 sc1\n\t" \
  "v_mfma_f32_16x16x32_bf16 %[c10], v[116:119], v[160:163], %[c10]\n\t" \
  "ds_read_b128 v[128:131], %[pc0] offset:0\n\t" \
  "v_add_u32 %[pc2], %[k2], %[pc2]\n\t" \
  "v_mfma_f32_16x16x32_bf16 %[c01], v[112:115], v[164:167], %[c01]\n\t" \
  "v_add_u32 %[fl], 16, %[fl]\n\t" \
  "s_mov_b32 %[s1], %[k2]\n\t" \
  "v_mfma_f32_16x16x32_bf16 %[c11], v[116:119], v[164:167], %[c11]\n\t" \
  "ds_read_b128 v[132:135], %[pc0] offset:2048\n\t" \
  "v_mfma_f32_16x16x32_bf16 %[c02], v[112:115], v[168:171], %[c02]\n\t" \
  "buffer_load_dwordx4 v[108:111], %[vo], %[srd], %[h5] offen offset:2048 sc1\n\t" \
  "v_mfma_f32_16x16x32_bf16 %[c12], v[116:119], v[168:171], %[c12]\n\t" \
  "ds_read_b128 v[136:139], %[pc0] offset:4096\n\t" \
  "v_mfma_f32_16x16x32_bf16 %[c03], v[112:115], v[172:175], %[c03]\n\t" \
  "v_mfma_f32_16x16x32_bf16 %[c13], v[116:119], v[172:175], %[c13]\n\t" \
  "ds_read_b128 v[140:143], %[pc0] offset:6144\n\t" \
  "s_waitcnt vmcnt(12) lgkmcnt(4)\n\t" \
  "v_mfma_f32_16x16x32_bf16 %[c00], v[120:123], v[176:179], %[c00]\n\t" \
  "buffer_load_dwordx4 v[112:115], %[vo], %[srd], %[h6] offen offset:0 sc1\n\t" \
  "v_mfma_f32_16x16x32_bf16 %[c10], v[124:127], v[176:179], %[c10]\n\t" \
  "ds_read_b128 v[144:147], %[pc1] offset:0\n\t" \
  "v_add_u32 %[pc3], %[k2], %[pc3]\n\t" \
  "v_mfma_f32_16x16x32_bf16 %[c01], v[120:123], v[180:183], %[c01]\n\t" \
  "s_add_u32 %[sg], %[sg], 1\n\t" \
  "s_cmp_lt_u32 %[sg], 7\n\t" \
  "v_mfma_f32_16x16x32_bf16 %[c11], v[124:127], v[180:183], %[c11]\n\t" \
  "ds_read_b128 v[148:151], %[pc1] offset:2048\n\t" \
  "v_mfma_f32_16x16x32_bf16 %[c02], v[120:123], v[184:187], %[c02]\n\t" \
  "buffer_load_dwordx4 v[116:119], %[vo], %[srd], %[h6] offen offset:2048 sc1\n\t" \
  "v_mfma_f32_16x16x32_bf16 %[c12], v[124:127], v[184:187], %[c12]\n\t" \
  "ds_read_b128 v[152:155], %[pc1] offset:4096\n\t" \
  "v_mfma_f32_16x16x32_bf16 %[c03], v[120:123], v[188:191], %[c03]\n\t" \
  "v_mfma_f32_16x16x32_bf16 %[c13], v[124:127], v[188:191], %[c13]\n\t" \
  "ds_read_b128 v[156:159], %[pc1] offset:6144\n\t" \
  "s_cbranch_scc1 Lloop2_%=\n\t" \
  "s_waitcnt vmcnt(12) lgkmcnt(4)\n\t" \
  "v_mfma_f32_16x16x32_bf16 %[c00], v[64:67], v[128:131], %[c00]\n\t" \
  "ds_read_b64 v[192:193], %[fl] offset:0\n\t" \
  "buffer_load_dwordx4 v[120:123], %[vo], %[srd], %[h7] offen offset:0 sc1\n\t" \
  "v_mfma_f32_16x16x32_bf16 %[c10], v[68:71], v[128:131], %[c10]\n\t" \
  "ds_read_b128 v[160:163], %[pc2] offset:0\n\t" \
  "v_add_u32 %[pc0], %[k2], %[pc0]\n\t" \
  "v_mfma_f32_16x16x32_bf16 %[c01], v[64:67], v[132:135], %[c01]\n\t" \
  "v_mfma_f32_16x16x32_bf16 %[c11], v[68:71], v[132:135], %[c11]\n\t" \
  "ds_read_b128 v[164:167], %[pc2] offset:2048\n\t" \
  "v_mfma_f32_16x16x32_bf16 %[c02], v[64:67], v[136:139], %[c02]\n\t" \
  "buffer_load_dwordx4 v[124:127], %[vo], %[srd], %[h7] offen offset:2048 sc1\n\t" \
  "v_mfma_f32_16x16x32_bf16 %[c12], v[68:71], v[136:139], %[c12]\n\t" \
  "ds_read_b128 v[168:171], %[pc2] offset:4096\n\t" \
  "v_mfma_f32_16x16x32_bf16 %[c03], v[64:67], v[140:143], %[c03]\n\t" \
  "v_mfma_f32_16x16x32_bf16 %[c13], v[68:71], v[140:143], %[c13]\n\t" \
  "ds_read_b128 v[172:175], %[pc2] offset:6144\n\t" \
  "s_waitcnt vmcnt(12) lgkmcnt(4)\n\t" \
  "v_mfma_f32_16x16x32_bf16 %[c00], v[72:75], v[144:147], %[c00]\n\t" \
  "v_mfma_f32_16x16x32_bf16 %[c10], v[76:79], v[144:147], %[c10]\n\t" \
  "ds_read_b128 v[176:179], %[pc3] offset:0\n\t" \
  "v_add_u32 %[pc1], %[k2], %[pc1]\n\t" \
  "v_mfma_f32_16x16x32_bf16 %[c01], v[72:75], v[148:151], %[c01]\n\t" \
  "v_or_b32 v192, v192, v193\n\t" \
  "v_mfma_f32_16x16x32_bf16 %[c11], v[76:79], v[148:151], %[c11]\n\t" \
  "ds_read_b128 v[180:183], %[pc3] offset:2048\n\t" \
  "v_readfirstlane_b32 %[s0], v192\n\t" \
  "v_mfma_f32_16x16x32_bf16 %[c02], v[72:75], v[152:155], %[c02]\n\t" \
  "v_mfma_f32_16x16x32_bf16 %[c12], v[76:79], v[152:155], %[c12]\n\t" \
  "ds_read_b128 v[184:187], %[pc3] offset:4096\n\t" \
  "v_mfma_f32_16x16x32_bf16 %[c03], v[72:75], v[156:159], %[c03]\n\t" \
  "s_cmp_eq_u32 %[s0], 0\n\t" \
  "s_cbranch_scc0 Lcf1_%=\n\t" \
  "Lcf1b_%=:\n\t" \
  "v_mfma_f32_16x16x32_bf16 %[c13], v[76:79], v[156:159], %[c13]\n\t" \
  "ds_read_b128 v[188:191], %[pc3] offset:6144\n\t" \
  "s_waitcnt vmcnt(10) lgkmcnt(4)\n\t" \
  "v_mfma_f32_16x16x32_bf16 %[c00], v[80:83], v[160:163], %[c00]\n\t" \
  "v_mfma_f32_16x16x32_bf16 %[c10], v[84:87], v[160:163], %[c10]\n\t" \
  "ds_read_b128 v[128:131], %[pc0] offset:0\n\t" \
  "v_add_u32 %[pc2], %[k2], %[pc2]\n\t" \
  "v_mfma_f32_16x16x32_bf16 %[c01], v[80:83], v[164:167], %[c01]\n\t" \
  "v_mfma_f32_16x16x32_bf16 %[c11], v[84:87], v[164:167], %[c11]\n\t" \
  "ds_read_b128 v[132:135], %[pc0] offset:2048\n\t" \
  "v_mfma_f32_16x16x32_bf16 %[c02], v[80:83], v[168:171], %[c02]\n\t" \
  "v_mfma_f32_16x16x32_bf16 %[c12], v[84:87], v[168:171], %[c12]\n\t" \
  "ds_read_b128 v[136:139], %[pc0] offset:4096\n\t" \
  "v_mfma_f32_16x16x32_bf16 %[c03], v[80:83], v[172:175], %[c03]\n\t" \
  "v_mfma_f32_16x16x32_bf16 %[c13], v[84:87], v[172:175], %[c13]\n\t" \
  "ds_read_b128 v[140:143], %[pc0] offset:6144\n\t" \
  "s_waitcnt vmcnt(8) lgkmcnt(4)\n\t" \
  "v_mfma_f32_16x16x32_bf16 %[c00], v[88:91], v[176:179], %[c00]\n\t" \
  "v_mfma_f32_16x16x32_bf16 %[c10], v[92:95], v[176:179], %[c10]\n\t" \
  "ds_read_b128 v[144:147], %[pc1] offset:0\n\t" \
  "v_add_u32 %[pc3], %[k2], %[pc3]\n\t" \
  "v_mfma_f32_16x16x32_bf16 %[c01], v[88:91], v[180:183], %[c01]\n\t" \
  "v_mfma_f32_16x16x32_bf16 %[c11], v[92:95], v[180:183], %[c11]\n\t" \
  "ds_read_b128 v[148:151], %[pc1] offset:2048\n\t" \
  "v_mfma_f32_16x16x32_bf16 %[c02], v[88:91], v[184:187], %[c02]\n\t" \
  "v_mfma_f32_16x16x32_bf16 %[c12], v[92:95], v[184:187], %[c12]\n\t" \
  "ds_read_b128 v[152:155], %[pc1] offset:4096\n\t" \
  "v_mfma_f32_16x16x32_bf16 %[c03], v[88:91], v[188:191], %[c03]\n\t" \
  "v_mfma_f32_16x16x32_bf16 %[c13], v[92:95], v[188:191], %[c13]\n\t" \
  "ds_read_b128 v[156:159], %[pc1] offset:6144\n\t" \
  "s_waitcnt vmcnt(6) lgkmcnt(4)\n\t" \
  "v_mfma_f32_16x16x32_bf16 %[c00], v[96:99], v[128:131], %[c00]\n\t" \
  "v_mfma_f32_16x16x32_bf16 %[c10], v[100:103], v[128:131], %[c10]\n\t" \
  "ds_read_b128 v[160:163], %[pc2] offset:0\n\t" \
  "v_mfma_f32_16x16x32_bf16 %[c01], v[96:99], v[132:135], %[c01]\n\t" \
  "v_mfma_f32_16x16x32_bf16 %[c11], v[100:103], v[132:135], %[c11]\n\t" \
  "ds_read_b128 v[164:167], %[pc2] offset:2048\n\t" \
  "v_mfma_f32_16x16x32_bf16 %[c02], v[96:99], v[136:139], %[c02]\n\t" \
  "v_mfma_f32_16x16x32_bf16 %[c12], v[100:103], v[136:139], %[c12]\n\t" \
  "ds_read_b128 v[168:171], %[pc2] offset:4096\n\t" \
  "v_mfma_f32_16x16x32_bf16 %[c03], v[96:99], v[140:143], %[c03]\n\t" \
  "v_mfma_f32_16x16x32_bf16 %[c13], v[100:103], v[140:143], %[c13]\n\t" \
  "ds_read_b128 v[172:175], %[pc2] offset:6144\n\t" \
  "s_waitcnt vmcnt(4) lgkmcnt(4)\n\t" \
  "v_mfma_f32_16x16x32_bf16 %[c00], v[104:107], v[144:147], %[c00]\n\t" \
  "v_mfma_f32_16x16x32_bf16 %[c10], v[108:111], v[144:147], %[c10]\n\t" \
  "ds_read_b128 v[176:179], %[pc3] offset:0\n\t" \
  "v_mfma_f32_16x16x32_bf16 %[c01], v[104:107], v[148:151], %[c01]\n\t" \
  "v_mfma_f32_16x16x32_bf16 %[c11], v[108:111], v[148:151], %[c11]\n\t" \
  "ds_read_b128 v[180:183], %[pc3] offset:2048\n\t" \
  "v_mfma_f32_16x16x32_bf16 %[c02], v[104:107], v[152:155], %[c02]\n\t" \
  "v_mfma_f32_16x16x32_bf16 %[c12], v[108:111], v[152:155], %[c12]\n\t" \
  "ds_read_b128 v[184:187], %[pc3] offset:4096\n\t" \
  "v_mfma_f32_16x16x32_bf16 %[c03], v[104:107], v[156:159], %[c03]\n\t" \
  "v_mfma_f32_16x16x32_bf16 %[c13], v[108:111], v[156:159], %[c13]\n\t" \
  "ds_read_b128 v[188:191], %[pc3] offset:6144\n\t" \
  "s_waitcnt vmcnt(2) lgkmcnt(4)\n\t" \
  "v_mfma_f32_16x16x32_bf16 %[c00], v[112:115], v[160:163], %[c00]\n\t" \
  "ds_read_b64 v[192:193], %[xd]\n\t" \
  "v_mfma_f32_16x16x32_bf16 %[c10], v[116:119], v[160:163], %[c10]\n\t" \
  "ds_read_b128 v[128:131], %[d0]\n\t" \
  "v_mfma_f32_16x16x32_bf16 %[c01], v[112:115], v[164:167], %[c01]\n\t" \
  "ds_read_b128 v[132:135], %[d1]\n\t" \
  "v_mfma_f32_16x16x32_bf16 %[c11], v[116:119], v[164:167], %[c11]\n\t" \
  "ds_read_b128 v[136:139], %[d2]\n\t" \
  "v_mfma_f32_16x16x32_bf16 %[c02], v[112:115], v[168:171], %[c02]\n\t" \
  "ds_read_b128 v[140:143], %[d3]\n\t" \
  "v_mfma_f32_16x16x32_bf16 %[c12], v[116:119], v[168:171], %[c12]\n\t" \
  "ds_read_b128 v[144:147], %[d4]\n\t" \
  "v_mfma_f32_16x16x32_bf16 %[c03], v[112:115], v[172:175], %[c03]\n\t" \
  "ds_read_b128 v[148:151], %[d5]\n\t" \
  "v_mfma_f32_16x16x32_bf16 %[c13], v[116:119], v[172:175], %[c13]\n\t" \
  "ds_read_b128 v[152:155], %[d6]\n\t" \
  "ds_read_b128 v[156:159], %[d7]\n\t" \
  "s_waitcnt vmcnt(0) lgkmcnt(9)\n\t" \
  "v_mfma_f32_16x16x32_bf16 %[c00], v[120:123], v[176:179], %[c00]\n\t" \
  "v_mfma_f32_16x16x32_bf16 %[c10], v[124:127], v[176:179], %[c10]\n\t" \
  "v_mfma_f32_16x16x32_bf16 %[c01], v[120:123], v[180:183], %[c01]\n\t" \
  "v_mfma_f32_16x16x32_bf16 %[c11], v[124:127], v[180:183], %[c11]\n\t" \
  "v_mfma_f32_16x16x32_bf16 %[c02], v[120:123], v[184:187], %[c02]\n\t" \
  "v_mfma_f32_16x16x32_bf16 %[c12], v[124:127], v[184:187], %[c12]\n\t" \
  "v_mfma_f32_16x16x32_bf16 %[c03], v[120:123], v[188:191], %[c03]\n\t" \
  "v_mfma_f32_16x16x32_bf16 %[c13], v[124:127], v[188:191], %[c13]\n\t" \
  "s_cmp_eq_u32 %[mine], 0\n\t" \
  "s_cbranch_scc1 Lend_%=\n\t" \
  "Ld_%=:\n\t" \
  "s_waitcnt lgkmcnt(0)\n\t" \
  "v_and_b32 v192, v192, v193\n\t" \
  "s_nop 0\n\t" \
  "v_readfirstlane_b32 %[s0], v192\n\t" \
  "s_cmp_eq_u32 %[s0], 0\n\t" \
  "s_cbranch_scc1 Ld1_%=\n\t" \
  "Ld2_%=:\n\t" \
  "v_add_f32 %[b00], %[b00], v128\n\t" \
  "v_add_f32 %[b01], %[b01], v129\n\t" \
  "v_add_f32 %[b02], %[b02], v130\n\t" \
  "v_add_f32 %[b03], %[b03], v131\n\t" \
  "v_add_f32 %[b10], %[b10], v132\n\t" \
  "v_add_f32 %[b11], %[b11], v133\n\t" \
  "v_add_f32 %[b12], %[b12], v134\n\t" \
  "v_add_f32 %[b13], %[b13], v135\n\t" \
  "v_add_f32 %[b20], %[b20], v136\n\t" \
  "v_add_f32 %[b21], %[b21], v137\n\t" \
  "v_add_f32 %[b22], %[b22], v138\n\t" \
  "v_add_f32 %[b23], %[b23], v139\n\t" \
  "v_add_f32 %[b30], %[b30], v140\n\t" \
  "v_add_f32 %[b31], %[b31], v141\n\t" \
  "v_add_f32 %[b32], %[b32], v142\n\t" \
  "v_add_f32 %[b33], %[b33], v143\n\t" \
  "v_add_f32 %[b40], %[b40], v144\n\t" \
  "v_add_f32 %[b41], %[b41], v145\n\t" \
  "v_add_f32 %[b42], %[b42], v146\n\t" \
  "v_add_f32 %[b43], %[b43], v147\n\t" \
  "v_add_f32 %[b50], %[b50], v148\n\t" \
  "v_add_f32 %[b51], %[b51], v149\n\t" \
  "v_add_f32 %[b52], %[b52], v150\n\t" \
  "v_add_f32 %[b53], %[b53], v151\n\t" \
  "v_add_f32 %[b60], %[b60], v152\n\t" \
  "v_add_f32 %[b61], %[b61], v153\n\t" \
  "v_add_f32 %[b62], %[b62], v154\n\t" \
  "v_add_f32 %[b63], %[b63], v155\n\t" \
  "v_add_f32 %[b70], %[b70], v156\n\t" \
  "v_add_f32 %[b71], %[b71], v157\n\t" \
  "v_add_f32 %[b72], %[b72], v158\n\t" \
  "v_add_f32 %[b73], %[b73], v159\n\t" \
  "s_branch Lend_%=\n\t" \
  "Lw_%=:\n\t" \
  "ds_read_b64 v[192:193], %[hr]\n\t" \
  "s_sub_i32 %[bud], %[bud], 1\n\t" \
  "s_cmp_lt_i32 %[bud], 0\n\t" \
  "s_cbranch_scc1 Lf3_%=\n\t" \
  "s_waitcnt lgkmcnt(0)\n\t" \
  "v_or_b32 v192, v192, v193\n\t" \
  "s_nop 0\n\t" \
  "v_readfirstlane_b32 %[s0], v192\n\t" \
  "s_cmp_eq_u32 %[s0], 0\n\t" \
  "s_cbranch_scc0 Lw_%=\n\t" \
  "s_branch Lf3_%=\n\t" \
  "Lh_%=:\n\t" \
  "buffer_load_dword v196, off, %[srd], %[sf1] sc1\n\t" \
  "s_branch Lhb_%=\n\t" \
  "Lg1_%=:\n\t" \
  "v_mov_b32 v196, 0\n\t" \
  "s_mov_b64 exec, 1\n\t" \
  "buffer_atomic_or v196, off, %[srd], %[sf1] sc0 sc1\n\t" \
  "s_mov_b64 exec, -1\n\t" \
  "s_sub_i32 %[bud], %[bud], 16\n\t" \
  "s_cmp_lt_i32 %[bud], 0\n\t" \
  "s_cbranch_scc1 Lgb_%=\n\t" \
  "s_waitcnt vmcnt(0)\n\t" \
  "s_branch Lg2_%=\n\t" \
  "Lg_%=:\n\t" \
  "s_waitcnt vmcnt(12)\n\t" \
  "Lg2_%=:\n\t" \
  "v_readfirstlane_b32 %[s0], v196\n\t" \
  "s_cmp_lg_u32 %[s0], 32\n\t" \
  "s_cbranch_scc1 Lg1_%=\n\t" \
  "s_branch Lgb_%=\n\t" \
  "Lcb1_%=:\n\t" \
  "ds_read_b64 v[192:193], %[fl] offset:0\n\t" \
  "s_sub_i32 %[bud], %[bud], 1\n\t" \
  "s_cmp_lt_i32 %[bud], 0\n\t" \
  "s_cbranch_scc1 Lcb1b_%=\n\t" \
  "s_waitcnt lgkmcnt(0)\n\t" \
  "v_or_b32 v192, v192, v193\n\t" \
  "s_nop 0\n\t" \
  "v_readfirstlane_b32 %[s0], v192\n\t" \
  "s_cmp_eq_u32 %[s0], 0\n\t" \
  "s_cbranch_scc0 Lcb1_%=\n\t" \
  "s_branch Lcb1b_%=\n\t" \
  "Lcb5_%=:\n\t" \
  "ds_read_b64 v[192:193], %[fl] offset:8\n\t" \
  "s_sub_i32 %[bud], %[bud], 1\n\t" \
  "s_cmp_lt_i32 %[bud], 0\n\t" \
  "s_cbranch_scc1 Lcb5b_%=\n\t" \
  "s_waitcnt lgkmcnt(0)\n\t" \
  "v_or_b32 v192, v192, v193\n\t" \
  "s_nop 0\n\t" \
  "v_readfirstlane_b32 %[s0], v192\n\t" \
  "s_cmp_eq_u32 %[s0], 0\n\t" \
  "s_cbranch_scc0 Lcb5_%=\n\t" \
  "s_branch Lcb5b_%=\n\t" \
  "Lcf1_%=:\n\t" \
  "ds_read_b64 v[192:193], %[fl] offset:0\n\t" \
  "s_sub_i32 %[bud], %[bud], 1\n\t" \
  "s_cmp_lt_i32 %[bud], 0\n\t" \
  "s_cbranch_scc1 Lcf1b_%=\n\t" \
  "s_waitcnt lgkmcnt(0)\n\t" \
  "v_or_b32 v192, v192, v193\n\t" \
  "s_nop 0\n\t" \
  "v_readfirstlane_b32 %[s0], v192\n\t" \
  "s_cmp_eq_u32 %[s0], 0\n\t" \
  "s_cbranch_scc0 Lcf1_%=\n\t" \
  "s_branch Lcf1b_%=\n\t" \
  "Ld1_%=:\n\t" \
  "s_sub_i32 %[bud], %[bud], 1\n\t" \
  "s_cmp_lt_i32 %[bud], 0\n\t" \
  "s_cbranch_scc1 Ld2_%=\n\t" \
  "ds_read_b64 v[192:193], %[xd]\n\t" \
  "ds_read_b128 v[128:131], %[d0]\n\t" \
  "ds_read_b128 v[132:135], %[d1]\n\t" \
  "ds_read_b128 v[136:139], %[d2]\n\t" \
  "ds_read_b128 v[140:143], %[d3]\n\t" \
  "ds_read_b128 v[144:147], %[d4]\n\t" \
  "ds_read_b128 v[148:151], %[d5]\n\t" \
  "ds_read_b128 v[152:155], %[d6]\n\t" \
  "ds_read_b128 v[156:159], %[d7]\n\t" \
  "s_branch Ld_%=\n\t" \
  "Lend_%=:\n\t" \
  "s_waitcnt lgkmcnt(0)\n\t" \
  "s_nop 6\n\t"
#define ASM_EN \
  "s_bfe_u32 %[j], %[bid], 0x30003\n\t" \
  "v_readfirstlane_b32 %[w], %[tid]\n\t" \
  "s_lshr_b32 %[w], %[w], 6\n\t" \
  "s_cmp_lg_u32 %[j], %[w]\n\t" \
  "s_cbranch_scc1 Lnc_%=\n\t" \
  "s_getpc_b64 s[20:21]\n\t" \
  "v_lshlrev_b32 %[v0], 7, %[tid]\n\t" \
  "v_min_u32 %[v0], %[cpf], %[v0]\n\t" \
  "global_load_dword v255, %[v0], s[20:21]\n\t" \
  "Lnc_%=:\n\t" \
  "s_sub_u32 %[w], %[w], 4\n\t" \
  "s_cbranch_scc1 Lend_%=\n\t" \
  "s_lshl3_add_u32 %[t], %[w], 8\n\t" \
  "s_cmp_lt_u32 %[w], 2\n\t" \
  "s_cselect_b32 %[t], 0, %[t]\n\t" \
  "s_load_dwordx2 s[20:21], %[kp], %[t]\n\t" \
  "s_cselect_b32 %[sa], 14, 11\n\t" \
  "s_cselect_b32 %[sc], 11, 15\n\t" \
  "s_cselect_b32 %[m], 16, 0\n\t" \
  "s_bfe_u32 %[r], %[bid], 0x20001\n\t" \
  "s_lshr_b32 %[t], %[bid], 6\n\t" \
  "s_lshl2_add_u32 %[c], %[r], %[t]\n\t" \
  "s_and_b32 %[t], %[bid], 1\n\t" \
  "s_lshl3_add_u32 %[j], %[t], %[j]\n\t" \
  "s_lshl1_add_u32 %[r], %[t], %[r]\n\t" \
  "s_and_b32 %[r], %[r], 3\n\t" \
  "s_lshl_b32 %[c], %[c], 5\n\t" \
  "s_add_u32 %[c], %[c], %[w]\n\t" \
  "s_lshl_b32 %[j], %[j], 4\n\t" \
  "s_cmp_lt_u32 %[w], 2\n\t" \
  "s_cselect_b32 %[c], %[c], %[j]\n\t" \
  "v_bfe_u32 %[v1], %[tid], 3, 3\n\t" \
  "v_xor_b32 %[v0], %[v1], %[tid]\n\t" \
  "v_and_b32 %[v0], 7, %[v0]\n\t" \
  "v_lshlrev_b32 %[v0], 4, %[v0]\n\t" \
  "v_lshl_add_u32 %[v0], %[r], 9, %[v0]\n\t" \
  "v_lshl_add_u32 %[v0], %[v1], %[sa], %[v0]\n\t" \
  "v_lshl_add_u32 %[v0], %[c], 13, %[v0]\n\t" \
  "s_lshl_b32 %[sa], 8, %[sa]\n\t" \
  "s_lshl_b32 %[sc], 1, %[sc]\n\t" \
  "v_add_u32 %[v1], %[sa], %[v0]\n\t" \
  "s_add_u32 %[m], %[m], %[w]\n\t" \
  "s_lshl_b32 m0, %[m], 13\n\t" \
  "s_waitcnt lgkmcnt(0)\n\t" \
  "Ll_%=:\n\t" \
  "global_load_lds_dwordx4 %[v0], s[20:21]\n\t" \
  "s_addk_i32 m0, 0x400\n\t" \
  "v_add_u32 %[v0], %[sc], %[v0]\n\t" \
  "global_load_lds_dwordx4 %[v1], s[20:21]\n\t" \
  "s_addk_i32 m0, 0x400\n\t" \
  "v_add_u32 %[v1], %[sc], %[v1]\n\t" \
  "s_lshl_b32 %[t], m0, 19\n\t" \
  "s_cbranch_scc1 Ll_%=\n\t" \
  "s_lshl2_add_u32 m0, %[w], %[ms0]\n\t" \
  "s_and_b32 s21, s21, 0xffff\n\t" \
  "v_mov_b32 %[v0], m0\n\t" \
  "ds_write_b32 %[v0], %[v0]\n\t" \
  "s_mov_b32 s22, 0\n\t" \
  "s_mov_b32 s23, 0x20000\n\t" \
  "s_mov_b64 exec, 1\n\t" \
  "s_waitcnt lgkmcnt(0)\n\t" \
  "buffer_load_dword off, s[20:23], 0 lds\n\t" \
  "s_mov_b64 exec, -1\n\t" \
  "Lend_%=:\n\t"
#define ASM_LA \
  "s_nop 4\n\t" \
  "s_barrier\n\t" \
  "Ltop_%=:\n\t" \
  "s_mov_b32 m0, %[dst]\n\t" \
  "v_mov_b32 %[vc], %[ci]\n\t" \
  "buffer_load_dwordx4 %[v0], %[r0], %[so0] offen lds\n\t" \
  "s_addk_i32 m0, 0x400\n\t" \
  "s_add_u32 %[dst], %[dst], 0x8000\n\t" \
  "buffer_load_dwordx4 %[v0], %[r0], %[so1] offen lds\n\t" \
  "s_addk_i32 m0, 0x400\n\t" \
  "s_cmp_ge_u32 %[dst], 0x24000\n\t" \
  "s_cselect_b32 %[s0], %[wrap], 0\n\t" \
  "buffer_load_dwordx4 %[v0], %[r0], %[so2] offen lds\n\t" \
  "s_addk_i32 m0, 0x400\n\t" \
  "s_sub_u32 %[dst], %[dst], %[s0]\n\t" \
  "buffer_load_dwordx4 %[v0], %[r0], %[so3] offen lds\n\t" \
  "s_addk_i32 m0, 0x400\n\t" \
  "s_add_u32 %[sk], %[sk], 128\n\t" \
  "buffer_load_dwordx4 %[v0], %[r0], %[so4] offen lds\n\t" \
  "s_addk_i32 m0, 0x400\n\t" \
  "s_and_b32 %[sk], %[sk], 2047\n\t" \
  "buffer_load_dwordx4 %[v0], %[r0], %[so5] offen lds\n\t" \
  "s_addk_i32 m0, 0x400\n\t" \
  "s_nop 0\n\t" \
  "buffer_load_dwordx4 %[v0], %[r0], %[so6] offen lds\n\t" \
  "s_addk_i32 m0, 0x400\n\t" \
  "s_add_u32 %[ci], %[ci], 16\n\t" \
  "buffer_load_dwordx4 %[v0], %[r0], %[so7] offen lds\n\t" \
  "s_addk_i32 m0, 0x400\n\t" \
  "s_sub_u32 %[n], %[n], 1\n\t" \
  "buffer_load_dwordx4 %[v0], %[r1], %[so0] offen lds\n\t" \
  "s_addk_i32 m0, 0x400\n\t" \
  "s_nop 0\n\t" \
  "buffer_load_dwordx4 %[v0], %[r1], %[so1] offen lds\n\t" \
  "s_addk_i32 m0, 0x400\n\t" \
  "s_nop 0\n\t" \
  "buffer_load_dwordx4 %[v0], %[r1], %[so2] offen lds\n\t" \
  "s_addk_i32 m0, 0x400\n\t" \
  "s_nop 0\n\t" \
  "buffer_load_dwordx4 %[v0], %[r1], %[so3] offen lds\n\t" \
  "s_addk_i32 m0, 0x400\n\t" \
  "ds_read_b128 v[24:27], %[vc]\n\t" \
  "ds_read_b128 v[28:31], %[vw]\n\t" \
  "buffer_load_dwordx4 %[v0], %[r1], %[so4] offen lds\n\t" \
  "s_addk_i32 m0, 0x400\n\t" \
  "s_nop 0\n\t" \
  "buffer_load_dwordx4 %[v0], %[r1], %[so5] offen lds\n\t" \
  "s_addk_i32 m0, 0x400\n\t" \
  "s_nop 0\n\t" \
  "buffer_load_dwordx4 %[v0], %[r1], %[so6] offen lds\n\t" \
  "s_addk_i32 m0, 0x400\n\t" \
  "s_waitcnt lgkmcnt(0)\n\t" \
  "v_or3_b32 v24, v24, v25, v26\n\t" \
  "v_or_b32 v24, v24, v27\n\t" \
  "v_sub_u32 v28, v30, v28\n\t" \
  "v_sub_u32 v29, v31, v29\n\t" \
  "v_or3_b32 v24, v24, v28, v29\n\t" \
  "buffer_load_dwordx4 %[v0], %[r1], %[so7] offen lds\n\t" \
  "s_mov_b32 m0, %[mk]\n\t" \
  "s_mov_b64 exec, 1\n\t" \
  "buffer_load_dword %[v0], %[rz], 0 offen lds\n\t" \
  "s_mov_b64 exec, -1\n\t" \
  "v_readfirstlane_b32 %[s0], v24\n\t" \
  "v_add_u32 %[v0], %[sk], %[b0]\n\t" \
  "s_add_u32 %[mk], %[mk], 8\n\t" \
  "s_cmp_eq_u32 %[n], 0\n\t" \
  "s_cbranch_scc1 Lend_%=\n\t" \
  "s_cmp_eq_u32 %[s0], 0\n\t" \
  "s_cbranch_scc1 Ltop_%=\n\t" \
  "Lsp_%=:\n\t" \
  "ds_read_b128 v[24:27], %[vc]\n\t" \
  "ds_read_b128 v[28:31], %[vw]\n\t" \
  "s_sub_i32 %[bud], %[bud], 1\n\t" \
  "s_cmp_lt_i32 %[bud], 0\n\t" \
  "s_cbranch_scc1 Ltop_%=\n\t" \
  "s_waitcnt lgkmcnt(0)\n\t" \
  "v_or3_b32 v24, v24, v25, v26\n\t" \
  "v_or_b32 v24, v24, v27\n\t" \
  "v_sub_u32 v28, v30, v28\n\t" \
  "v_sub_u32 v29, v31, v29\n\t" \
  "v_or3_b32 v24, v24, v28, v29\n\t" \
  "s_nop 0\n\t" \
  "v_readfirstlane_b32 %[s0], v24\n\t" \
  "s_cmp_eq_u32 %[s0], 0\n\t" \
  "s_cbranch_scc1 Ltop_%=\n\t" \
  "s_branch Lsp_%=\n\t" \
  "Lend_%=:\n\t"
#define ASM_LB \
  "s_nop 4\n\t" \
  "s_branch Lsp_%=\n\t" \
  "Ltop_%=:\n\t" \
  "s_mov_b32 m0, %[dst]\n\t" \
  "v_mov_b32 %[vc], %[ci]\n\t" \
  "buffer_load_dwordx4 %[v0], %[r0], %[so0] offen lds\n\t" \
  "s_addk_i32 m0, 0x400\n\t" \
  "s_add_u32 %[dst], %[dst], 0x8000\n\t" \
  "buffer_load_dwordx4 %[v1], %[r0], %[so1] offen lds\n\t" \
  "s_addk_i32 m0, 0x400\n\t" \
  "s_cmp_ge_u32 %[dst], 0x24000\n\t" \
  "s_cselect_b32 %[s0], %[wrap], 0\n\t" \
  "buffer_load_dwordx4 %[v0], %[r0], %[so2] offen lds\n\t" \
  "s_addk_i32 m0, 0x400\n\t" \
  "s_sub_u32 %[dst], %[dst], %[s0]\n\t" \
  "buffer_load_dwordx4 %[v1], %[r0], %[so3] offen lds\n\t" \
  "s_addk_i32 m0, 0x400\n\t" \
  "s_add_u32 %[sk], %[sk], 256\n\t" \
  "buffer_load_dwordx4 %[v0], %[r0], %[so4] offen lds\n\t" \
  "s_addk_i32 m0, 0x400\n\t" \
  "s_and_b32 %[sk], %[sk], 1023\n\t" \
  "buffer_load_dwordx4 %[v1], %[r0], %[so5] offen lds\n\t" \
  "s_addk_i32 m0, 0x400\n\t" \
  "s_cmp_ge_u32 %[ci], %[cth]\n\t" \
  "s_cselect_b32 %[s0], 32, 16\n\t" \
  "buffer_load_dwordx4 %[v0], %[r0], %[so6] offen lds\n\t" \
  "s_addk_i32 m0, 0x400\n\t" \
  "s_add_u32 %[ci], %[ci], %[s0]\n\t" \
  "buffer_load_dwordx4 %[v1], %[r0], %[so7] offen lds\n\t" \
  "s_addk_i32 m0, 0x400\n\t" \
  "s_sub_u32 %[n], %[n], 1\n\t" \
  "buffer_load_dwordx4 %[v0], %[r1], %[so0] offen lds\n\t" \
  "s_addk_i32 m0, 0x400\n\t" \
  "s_cmp_eq_u32 %[n], 4\n\t" \
  "s_cselect_b32 %[s0], 0x400, 0\n\t" \
  "buffer_load_dwordx4 %[v1], %[r1], %[so1] offen lds\n\t" \
  "s_addk_i32 m0, 0x400\n\t" \
  "s_xor_b32 %[hi], %[hi], %[s0]\n\t" \
  "buffer_load_dwordx4 %[v0], %[r1], %[so2] offen lds\n\t" \
  "s_addk_i32 m0, 0x400\n\t" \
  "s_or_b32 %[s1], %[sk], %[hi]\n\t" \
  "buffer_load_dwordx4 %[v1], %[r1], %[so3] offen lds\n\t" \
  "s_addk_i32 m0, 0x400\n\t" \
  "ds_read_b128 v[24:27], %[vc]\n\t" \
  "buffer_load_dwordx4 %[v0], %[r1], %[so4] offen lds\n\t" \
  "s_addk_i32 m0, 0x400\n\t" \
  "s_nop 0\n\t" \
  "buffer_load_dwordx4 %[v1], %[r1], %[so5] offen lds\n\t" \
  "s_addk_i32 m0, 0x400\n\t" \
  "s_nop 0\n\t" \
  "buffer_load_dwordx4 %[v0], %[r1], %[so6] offen lds\n\t" \
  "s_addk_i32 m0, 0x400\n\t" \
  "s_waitcnt lgkmcnt(0)\n\t" \
  "v_or3_b32 v24, v24, v25, v26\n\t" \
  "v_or_b32 v24, v24, v27\n\t" \
  "buffer_load_dwordx4 %[v1], %[r1], %[so7] offen lds\n\t" \
  "s_mov_b32 m0, %[mk]\n\t" \
  "s_mov_b64 exec, 3\n\t" \
  "buffer_load_dword %[v0], %[rz], 0 offen lds\n\t" \
  "s_mov_b64 exec, -1\n\t" \
  "v_readfirstlane_b32 %[s0], v24\n\t" \
  "v_add_u32 %[v0], %[s1], %[b0]\n\t" \
  "v_add_u32 %[v1], %[s1], %[b1]\n\t" \
  "s_add_u32 %[mk], %[mk], 16\n\t" \
  "s_cmp_eq_u32 %[n], %[nend]\n\t" \
  "s_cbranch_scc1 Lend_%=\n\t" \
  "s_cmp_eq_u32 %[s0], 0\n\t" \
  "s_cbranch_scc1 Ltop_%=\n\t" \
  "Lsp_%=:\n\t" \
  "ds_read_b128 v[24:27], %[vc]\n\t" \
  "s_sub_i32 %[bud], %[bud], 1\n\t" \
  "s_cmp_lt_i32 %[bud], 0\n\t" \
  "s_cbranch_scc1 Ltop_%=\n\t" \
  "s_waitcnt lgkmcnt(0)\n\t" \
  "v_or3_b32 v24, v24, v25, v26\n\t" \
  "v_or_b32 v24, v24, v27\n\t" \
  "s_nop 0\n\t" \
  "v_readfirstlane_b32 %[s0], v24\n\t" \
  "s_cmp_eq_u32 %[s0], 0\n\t" \
  "s_cbranch_scc1 Ltop_%=\n\t" \
  "s_sleep 1\n\t" \
  "s_branch Lsp_%=\n\t" \
  "Lend_%=:\n\t" \
  "buffer_load_dwordx4 v[32:35], %[p0], %[rp], %[so0] offen offset:128\n\t" \
  "buffer_load_dwordx4 v[36:39], %[p1], %[rp], %[so1] offen offset:128\n\t" \
  "buffer_load_dwordx4 v[40:43], %[p0], %[rp], %[so2] offen offset:128\n\t" \
  "buffer_load_dwordx4 v[44:47], %[p1], %[rp], %[so3] offen offset:128\n\t" \
  "buffer_load_dwordx4 v[48:51], %[p0], %[rp], %[so4] offen offset:128\n\t" \
  "buffer_load_dwordx4 v[52:55], %[p1], %[rp], %[so5] offen offset:128\n\t" \
  "buffer_load_dwordx4 v[56:59], %[p0], %[rp], %[so6] offen offset:128\n\t" \
  "buffer_load_dwordx4 v[60:63], %[p1], %[rp], %[so7] offen offset:128\n\t" \
  "buffer_load_dwordx4 v[64:67], %[p0], %[rp], %[so0] offen offset:256\n\t" \
  "buffer_load_dwordx4 v[68:71], %[p1], %[rp], %[so1] offen offset:256\n\t" \
  "buffer_load_dwordx4 v[72:75], %[p0], %[rp], %[so2] offen offset:256\n\t" \
  "buffer_load_dwordx4 v[76:79], %[p1], %[rp], %[so3] offen offset:256\n\t" \
  "buffer_load_dwordx4 v[80:83], %[p0], %[rp], %[so4] offen offset:256\n\t" \
  "buffer_load_dwordx4 v[84:87], %[p1], %[rp], %[so5] offen offset:256\n\t" \
  "buffer_load_dwordx4 v[88:91], %[p0], %[rp], %[so6] offen offset:256\n\t" \
  "buffer_load_dwordx4 v[92:95], %[p1], %[rp], %[so7] offen offset:256\n\t" \
  "buffer_load_dwordx4 v[96:99], %[p0], %[rp], %[so0] offen offset:384\n\t" \
  "buffer_load_dwordx4 v[100:103], %[p1], %[rp], %[so1] offen offset:384\n\t" \
  "buffer_load_dwordx4 v[104:107], %[p0], %[rp], %[so2] offen offset:384\n\t" \
  "buffer_load_dwordx4 v[108:111], %[p1], %[rp], %[so3] offen offset:384\n\t" \
  "buffer_load_dwordx4 v[112:115], %[p0], %[rp], %[so4] offen offset:384\n\t" \
  "buffer_load_dwordx4 v[116:119], %[p1], %[rp], %[so5] offen offset:384\n\t" \
  "buffer_load_dwordx4 v[120:123], %[p0], %[rp], %[so6] offen offset:384\n\t" \
  "buffer_load_dwordx4 v[124:127], %[p1], %[rp], %[so7] offen offset:384\n\t" \
  "buffer_load_dwordx4 v[128:131], %[p0], %[rp], %[so0] offen offset:512\n\t" \
  "buffer_load_dwordx4 v[132:135], %[p1], %[rp], %[so1] offen offset:512\n\t" \
  "buffer_load_dwordx4 v[136:139], %[p0], %[rp], %[so2] offen offset:512\n\t" \
  "buffer_load_dwordx4 v[140:143], %[p1], %[rp], %[so3] offen offset:512\n\t" \
  "buffer_load_dwordx4 v[144:147], %[p0], %[rp], %[so4] offen offset:512\n\t" \
  "buffer_load_dwordx4 v[148:151], %[p1], %[rp], %[so5] offen offset:512\n\t" \
  "buffer_load_dwordx4 v[152:155], %[p0], %[rp], %[so6] offen offset:512\n\t" \
  "buffer_load_dwordx4 v[156:159], %[p1], %[rp], %[so7] offen offset:512\n\t" \
  "buffer_load_dwordx4 v[160:163], %[p0], %[rp], %[so0] offen offset:640\n\t" \
  "buffer_load_dwordx4 v[164:167], %[p1], %[rp], %[so1] offen offset:640\n\t" \
  "buffer_load_dwordx4 v[168:171], %[p0], %[rp], %[so2] offen offset:640\n\t" \
  "buffer_load_dwordx4 v[172:175], %[p1], %[rp], %[so3] offen offset:640\n\t" \
  "buffer_load_dwordx4 v[176:179], %[p0], %[rp], %[so4] offen offset:640\n\t" \
  "buffer_load_dwordx4 v[180:183], %[p1], %[rp], %[so5] offen offset:640\n\t" \
  "buffer_load_dwordx4 v[184:187], %[p0], %[rp], %[so6] offen offset:640\n\t" \
  "buffer_load_dwordx4 v[188:191], %[p1], %[rp], %[so7] offen offset:640\n\t" \
  "buffer_load_dwordx4 v[192:195], %[p0], %[rp], %[so0] offen offset:768\n\t" \
  "buffer_load_dwordx4 v[196:199], %[p1], %[rp], %[so1] offen offset:768\n\t" \
  "buffer_load_dwordx4 v[200:203], %[p0], %[rp], %[so2] offen offset:768\n\t" \
  "buffer_load_dwordx4 v[204:207], %[p1], %[rp], %[so3] offen offset:768\n\t" \
  "buffer_load_dwordx4 v[208:211], %[p0], %[rp], %[so4] offen offset:768\n\t" \
  "buffer_load_dwordx4 v[212:215], %[p1], %[rp], %[so5] offen offset:768\n\t" \
  "buffer_load_dwordx4 v[216:219], %[p0], %[rp], %[so6] offen offset:768\n\t" \
  "buffer_load_dwordx4 v[220:223], %[p1], %[rp], %[so7] offen offset:768\n\t" \
  "buffer_load_dwordx4 v[224:227], %[p0], %[rp], %[so0] offen offset:896\n\t" \
  "buffer_load_dwordx4 v[228:231], %[p1], %[rp], %[so1] offen offset:896\n\t" \
  "buffer_load_dwordx4 v[232:235], %[p0], %[rp], %[so2] offen offset:896\n\t" \
  "buffer_load_dwordx4 v[236:239], %[p1], %[rp], %[so3] offen offset:896\n\t" \
  "buffer_load_dwordx4 v[240:243], %[p0], %[rp], %[so4] offen offset:896\n\t" \
  "buffer_load_dwordx4 v[244:247], %[p1], %[rp], %[so5] offen offset:896\n\t" \
  "buffer_load_dwordx4 v[248:251], %[p0], %[rp], %[so6] offen offset:896\n\t" \
  "buffer_load_dwordx4 v[252:255], %[p1], %[rp], %[so7] offen offset:896\n\t" \
  "v_mov_b32 v28, 0\n\t" \
  "Lw0_%=:\n\t" \
  "ds_read_b128 v[24:27], %[vm] offset:0\n\t" \
  "s_sub_i32 %[bud], %[bud], 1\n\t" \
  "s_cmp_lt_i32 %[bud], 0\n\t" \
  "s_cbranch_scc1 Lx0_%=\n\t" \
  "s_waitcnt lgkmcnt(0)\n\t" \
  "v_or3_b32 v24, v24, v25, v26\n\t" \
  "v_or_b32 v24, v24, v27\n\t" \
  "s_nop 0\n\t" \
  "v_readfirstlane_b32 %[s0], v24\n\t" \
  "s_cmp_eq_u32 %[s0], 0\n\t" \
  "s_cbranch_scc1 Lx0_%=\n\t" \
  "s_sleep 2\n\t" \
  "s_branch Lw0_%=\n\t" \
  "Lx0_%=:\n\t" \
  "s_waitcnt vmcnt(0)\n\t" \
  "ds_write_b128 %[vd], v[32:35] offset:0\n\t" \
  "ds_write_b128 %[vd], v[36:39] offset:1024\n\t" \
  "ds_write_b128 %[vd], v[40:43] offset:2048\n\t" \
  "ds_write_b128 %[vd], v[44:47] offset:3072\n\t" \
  "ds_write_b128 %[vd], v[48:51] offset:4096\n\t" \
  "ds_write_b128 %[vd], v[52:55] offset:5120\n\t" \
  "ds_write_b128 %[vd], v[56:59] offset:6144\n\t" \
  "ds_write_b128 %[vd], v[60:63] offset:7168\n\t" \
  "s_waitcnt lgkmcnt(0)\n\t" \
  "ds_write_b32 %[vl], v28 offset:0\n\t" \
  "Lw1_%=:\n\t" \
  "ds_read_b128 v[24:27], %[vm] offset:16\n\t" \
  "s_sub_i32 %[bud], %[bud], 1\n\t" \
  "s_cmp_lt_i32 %[bud], 0\n\t" \
  "s_cbranch_scc1 Lx1_%=\n\t" \
  "s_waitcnt lgkmcnt(0)\n\t" \
  "v_or3_b32 v24, v24, v25, v26\n\t" \
  "v_or_b32 v24, v24, v27\n\t" \
  "s_nop 0\n\t" \
  "v_readfirstlane_b32 %[s0], v24\n\t" \
  "s_cmp_eq_u32 %[s0], 0\n\t" \
  "s_cbranch_scc1 Lx1_%=\n\t" \
  "s_sleep 2\n\t" \
  "s_branch Lw1_%=\n\t" \
  "Lx1_%=:\n\t" \
  "ds_write_b128 %[vd], v[64:67] offset:16384\n\t" \
  "ds_write_b128 %[vd], v[68:71] offset:17408\n\t" \
  "ds_write_b128 %[vd], v[72:75] offset:18432\n\t" \
  "ds_write_b128 %[vd], v[76:79] offset:19456\n\t" \
  "ds_write_b128 %[vd], v[80:83] offset:20480\n\t" \
  "ds_write_b128 %[vd], v[84:87] offset:21504\n\t" \
  "ds_write_b128 %[vd], v[88:91] offset:22528\n\t" \
  "ds_write_b128 %[vd], v[92:95] offset:23552\n\t" \
  "s_waitcnt lgkmcnt(0)\n\t" \
  "ds_write_b32 %[vl], v28 offset:8\n\t" \
  "Lw2_%=:\n\t" \
  "ds_read_b128 v[24:27], %[vm] offset:32\n\t" \
  "s_sub_i32 %[bud], %[bud], 1\n\t" \
  "s_cmp_lt_i32 %[bud], 0\n\t" \
  "s_cbranch_scc1 Lx2_%=\n\t" \
  "s_waitcnt lgkmcnt(0)\n\t" \
  "v_or3_b32 v24, v24, v25, v26\n\t" \
  "v_or_b32 v24, v24, v27\n\t" \
  "s_nop 0\n\t" \
  "v_readfirstlane_b32 %[s0], v24\n\t" \
  "s_cmp_eq_u32 %[s0], 0\n\t" \
  "s_cbranch_scc1 Lx2_%=\n\t" \
  "s_sleep 2\n\t" \
  "s_branch Lw2_%=\n\t" \
  "Lx2_%=:\n\t" \
  "ds_write_b128 %[vd], v[96:99] offset:32768\n\t" \
  "ds_write_b128 %[vd], v[100:103] offset:33792\n\t" \
  "ds_write_b128 %[vd], v[104:107] offset:34816\n\t" \
  "ds_write_b128 %[vd], v[108:111] offset:35840\n\t" \
  "ds_write_b128 %[vd], v[112:115] offset:36864\n\t" \
  "ds_write_b128 %[vd], v[116:119] offset:37888\n\t" \
  "ds_write_b128 %[vd], v[120:123] offset:38912\n\t" \
  "ds_write_b128 %[vd], v[124:127] offset:39936\n\t" \
  "s_waitcnt lgkmcnt(0)\n\t" \
  "ds_write_b32 %[vl], v28 offset:16\n\t" \
  "Lw3_%=:\n\t" \
  "ds_read_b128 v[24:27], %[vm] offset:48\n\t" \
  "s_sub_i32 %[bud], %[bud], 1\n\t" \
  "s_cmp_lt_i32 %[bud], 0\n\t" \
  "s_cbranch_scc1 Lx3_%=\n\t" \
  "s_waitcnt lgkmcnt(0)\n\t" \
  "v_or3_b32 v24, v24, v25, v26\n\t" \
  "v_or_b32 v24, v24, v27\n\t" \
  "s_nop 0\n\t" \
  "v_readfirstlane_b32 %[s0], v24\n\t" \
  "s_cmp_eq_u32 %[s0], 0\n\t" \
  "s_cbranch_scc1 Lx3_%=\n\t" \
  "s_sleep 2\n\t" \
  "s_branch Lw3_%=\n\t" \
  "Lx3_%=:\n\t" \
  "ds_write_b128 %[vd], v[128:131] offset:49152\n\t" \
  "ds_write_b128 %[vd], v[132:135] offset:50176\n\t" \
  "ds_write_b128 %[vd], v[136:139] offset:51200\n\t" \
  "ds_write_b128 %[vd], v[140:143] offset:52224\n\t" \
  "ds_write_b128 %[vd], v[144:147] offset:53248\n\t" \
  "ds_write_b128 %[vd], v[148:151] offset:54272\n\t" \
  "ds_write_b128 %[vd], v[152:155] offset:55296\n\t" \
  "ds_write_b128 %[vd], v[156:159] offset:56320\n\t" \
  "v_add_u32 %[vd], 0x10000, %[vd]\n\t" \
  "s_waitcnt lgkmcnt(0)\n\t" \
  "ds_write_b32 %[vl], v28 offset:24\n\t" \
  "Lw4_%=:\n\t" \
  "ds_read_b128 v[24:27], %[vm] offset:64\n\t" \
  "s_sub_i32 %[bud], %[bud], 1\n\t" \
  "s_cmp_lt_i32 %[bud], 0\n\t" \
  "s_cbranch_scc1 Lx4_%=\n\t" \
  "s_waitcnt lgkmcnt(0)\n\t" \
  "v_or3_b32 v24, v24, v25, v26\n\t" \
  "v_or_b32 v24, v24, v27\n\t" \
  "s_nop 0\n\t" \
  "v_readfirstlane_b32 %[s0], v24\n\t" \
  "s_cmp_eq_u32 %[s0], 0\n\t" \
  "s_cbranch_scc1 Lx4_%=\n\t" \
  "s_sleep 2\n\t" \
  "s_branch Lw4_%=\n\t" \
  "Lx4_%=:\n\t" \
  "ds_write_b128 %[vd], v[160:163] offset:0\n\t" \
  "ds_write_b128 %[vd], v[164:167] offset:1024\n\t" \
  "ds_write_b128 %[vd], v[168:171] offset:2048\n\t" \
  "ds_write_b128 %[vd], v[172:175] offset:3072\n\t" \
  "ds_write_b128 %[vd], v[176:179] offset:4096\n\t" \
  "ds_write_b128 %[vd], v[180:183] offset:5120\n\t" \
  "ds_write_b128 %[vd], v[184:187] offset:6144\n\t" \
  "ds_write_b128 %[vd], v[188:191] offset:7168\n\t" \
  "s_waitcnt lgkmcnt(0)\n\t" \
  "ds_write_b32 %[vl], v28 offset:32\n\t" \
  "Lw5_%=:\n\t" \
  "ds_read_b128 v[24:27], %[vm] offset:80\n\t" \
  "s_sub_i32 %[bud], %[bud], 1\n\t" \
  "s_cmp_lt_i32 %[bud], 0\n\t" \
  "s_cbranch_scc1 Lx5_%=\n\t" \
  "s_waitcnt lgkmcnt(0)\n\t" \
  "v_or3_b32 v24, v24, v25, v26\n\t" \
  "v_or_b32 v24, v24, v27\n\t" \
  "s_nop 0\n\t" \
  "v_readfirstlane_b32 %[s0], v24\n\t" \
  "s_cmp_eq_u32 %[s0], 0\n\t" \
  "s_cbranch_scc1 Lx5_%=\n\t" \
  "s_sleep 2\n\t" \
  "s_branch Lw5_%=\n\t" \
  "Lx5_%=:\n\t" \
  "ds_write_b128 %[vd], v[192:195] offset:16384\n\t" \
  "ds_write_b128 %[vd], v[196:199] offset:17408\n\t" \
  "ds_write_b128 %[vd], v[200:203] offset:18432\n\t" \
  "ds_write_b128 %[vd], v[204:207] offset:19456\n\t" \
  "ds_write_b128 %[vd], v[208:211] offset:20480\n\t" \
  "ds_write_b128 %[vd], v[212:215] offset:21504\n\t" \
  "ds_write_b128 %[vd], v[216:219] offset:22528\n\t" \
  "ds_write_b128 %[vd], v[220:223] offset:23552\n\t" \
  "s_waitcnt lgkmcnt(0)\n\t" \
  "ds_write_b32 %[vl], v28 offset:40\n\t" \
  "Lw6_%=:\n\t" \
  "ds_read_b128 v[24:27], %[vm] offset:96\n\t" \
  "s_sub_i32 %[bud], %[bud], 1\n\t" \
  "s_cmp_lt_i32 %[bud], 0\n\t" \
  "s_cbranch_scc1 Lx6_%=\n\t" \
  "s_waitcnt lgkmcnt(0)\n\t" \
  "v_or3_b32 v24, v24, v25, v26\n\t" \
  "v_or_b32 v24, v24, v27\n\t" \
  "s_nop 0\n\t" \
  "v_readfirstlane_b32 %[s0], v24\n\t" \
  "s_cmp_eq_u32 %[s0], 0\n\t" \
  "s_cbranch_scc1 Lx6_%=\n\t" \
  "s_sleep 2\n\t" \
  "s_branch Lw6_%=\n\t" \
  "Lx6_%=:\n\t" \
  "ds_write_b128 %[vd], v[224:227] offset:32768\n\t" \
  "ds_write_b128 %[vd], v[228:231] offset:33792\n\t" \
  "ds_write_b128 %[vd], v[232:235] offset:34816\n\t" \
  "ds_write_b128 %[vd], v[236:239] offset:35840\n\t" \
  "ds_write_b128 %[vd], v[240:243] offset:36864\n\t" \
  "ds_write_b128 %[vd], v[244:247] offset:37888\n\t" \
  "ds_write_b128 %[vd], v[248:251] offset:38912\n\t" \
  "ds_write_b128 %[vd], v[252:255] offset:39936\n\t" \
  "s_waitcnt lgkmcnt(0)\n\t" \
  "ds_write_b32 %[vl], v28 offset:48\n\t" \
  "s_waitcnt lgkmcnt(0)\n\t"

#define OST " sc1"
#define XPRIO0 3
#define XPRIO 1
#define LPRIO 2
#define FPRIO 1
#define MPRIO 2
#define WGRP (~sg & 1)
#define WS0 1
#define WS1 5
#define WSLP 8
#define TKG0 3
#define GEXACT 0
#define YPRIO 3
#define GPRIO 1
#define W2G 14
#define CWS 16
#define HELP 1
#define WRES 1
#define XG -1
#define GUG 25
#define TKG 4
#define YG 1
#define NOFFN 0
#define CODEPF 29312
#define WNT " nt"
constexpr int D_ = 1024, F_ = 3584;
constexpr int MISC = 147456;
constexpr int L_TAB = MISC;
constexpr int L_FLG = L_TAB + 11 * 512;
constexpr int L_ONE = L_FLG + 256;
constexpr int L_G = L_ONE + 1024;
constexpr int L_SEL = L_G + 512;
constexpr int L_BROW = L_SEL + 128;
constexpr int L_ACT = L_BROW + 2048;
constexpr int L_XS = L_ACT + 1024;
constexpr int L_JUNK = L_XS + 4096;
constexpr int LDS_BYTES = 163840;
static_assert(L_JUNK + 768 <= LDS_BYTES, "lds");
constexpr int M_CONS = L_ONE + 16, M_LAND = L_ONE + 544, M_GT = M_LAND + 288, M_HR = M_LAND + 296, M_CAR = M_LAND + 304, M_XS = M_LAND + 320, M_WR = M_LAND + 336;
constexpr int M_S0 = L_SEL + 64;
static_assert(M_S0 - M_LAND == 1056, "offset in ASM_MA");
constexpr int FL_XD = L_FLG + 16, FL_TK = L_FLG + 24;
constexpr unsigned W_EPOCH = 0;
constexpr unsigned W_RG = 256;
constexpr unsigned W_R2 = W_RG + 2048 * 8;
constexpr unsigned W_HF = 32768;
constexpr unsigned W_GF = W_HF + 32 * 128;
constexpr unsigned W_YF = W_GF + 56 * 128;
constexpr unsigned W_HT = W_YF + 8 * 128;
constexpr unsigned W_AGG = 65536;
constexpr unsigned W_GU = W_AGG + 262144;
constexpr unsigned W_YP = W_GU + 56 * 16384;
constexpr unsigned W_H = 4u << 20;
#define HSK 24
#define HOFS(l) ((l) * 16 + (2 > 1 && (l) >= HSK ? 128 : 0))
constexpr unsigned W_DBG = 24u << 20;
constexpr unsigned W_END = 25u << 20;
static_assert(W_R2 + 8192 <= W_HF && W_HT + 8192 <= W_AGG && W_YP + 128 * 56 * 256 <= W_H, "ws");
#define WS_BASE(n) (((n) - W_END) & ~(size_t)0x1fffff)
constexpr int SPIN_MAX = 1 << 18;

DEV float bf2f(unsigned v) { return __uint_as_float(v << 16); }
DEV float bfhi(unsigned v) { return __uint_as_float(v & 0xffff0000u); }
DEV u16 f2bf(float f) { return __builtin_bit_cast(u16, (__bf16)f); }
DEV float rbf(float f) { return bf2f(f2bf(f)); }
typedef __bf16 bf16x2_t __attribute__((ext_vector_type(2)));
DEV unsigned pk2(float a, float b) { return __builtin_bit_cast(unsigned, __builtin_convertvector((f32x2){a, b}, bf16x2_t)); }
DEV float fma1(float a, float b, float c) { float d; asm("v_fma_f32 %0, %1, %2, %3" : "=v"(d) : "v"(a), "v"(b), "v"(c)); return d; }
DEV float fnma1(float a, float b, float c) { float d; asm("v_fma_f32 %0, -%1, %2, %3" : "=v"(d) : "v"(a), "v"(b), "v"(c)); return d; }
DEV float mul1(float a, float b) { float d; asm("v_mul_f32 %0, %1, %2" : "=v"(d) : "v"(a), "v"(b)); return d; }
DEV f32x2 pkfma(f32x2 a, f32x2 b, f32x2 c) { f32x2 d; asm("v_pk_fma_f32 %0, %1, %2, %3" : "=v"(d) : "v"(a), "v"(b), "v"(c)); return d; }
DEV float add1(float a, float b) { float d; asm("v_add_f32 %0, %1, %2" : "=v"(d) : "v"(a), "v"(b)); return d; }
DEV f32x2 pkmul(f32x2 a, f32x2 b) { f32x2 d; asm("v_pk_mul_f32 %0, %1, %2" : "=v"(d) : "v"(a), "v"(b)); return d; }
DEV f32x2 cmul(f32x2 a, f32x2 b) { return (f32x2){fnma1(a.y, b.y, mul1(a.x, b.x)), fma1(a.y, b.x, mul1(a.x, b.y))}; }
template <int N> DEV void waitv() { asm volatile("s_waitcnt vmcnt(%0)" ::"n"(N) : "memory"); }
DEV void bar() { asm volatile("s_barrier" ::: "memory"); }
DEV unsigned lds_rd32(unsigned a) { unsigned v; asm volatile("ds_read_b32 %0, %1\n\ts_waitcnt lgkmcnt(0)" : "=v"(v) : "v"(a) : "memory"); return v; }
DEV u32x2 lds_rd64(unsigned a) { u32x2 v; asm volatile("ds_read_b64 %0, %1\n\ts_waitcnt lgkmcnt(0)" : "=v"(v) : "v"(a) : "memory"); return v; }
DEV u32x4 lds_rd128(unsigned a) { u32x4 v; asm volatile("ds_read_b128 %0, %1\n\ts_waitcnt lgkmcnt(0)" : "=v"(v) : "v"(a) : "memory"); return v; }
DEV void lds_wr32(unsigned a, unsigned v) { asm volatile("ds_write_b32 %0, %1" ::"v"(a), "v"(v) : "memory"); }
DEV void lds_wait_ge(unsigned a, unsigned need) {
#pragma unroll 1
  for (int it = 0; it < SPIN_MAX * 4; ++it) { if (__builtin_amdgcn_readfirstlane(lds_rd32(a)) >= need) break; __builtin_amdgcn_s_sleep(1); }
}
DEV void lds_wait2(unsigned a) {
#pragma unroll 1
  for (int it = 0; it < SPIN_MAX * 4; ++it) { const u32x2 v = lds_rd64(a); if (__builtin_amdgcn_readfirstlane(v.x & v.y)) break; __builtin_amdgcn_s_sleep(1); }
}
DEV void lds_wait0(unsigned a) {
#pragma unroll 1
  for (int it = 0; it < SPIN_MAX * 4; ++it) { if (__builtin_amdgcn_readfirstlane(lds_rd32(a)) == 0u) break; __builtin_amdgcn_s_sleep(1); }
}
typedef __amdgpu_buffer_rsrc_t rsrc_t;
DEV void mark(rsrc_t rz, unsigned a) {
  if ((threadIdx.x & 63) == 0) __builtin_amdgcn_raw_ptr_buffer_load_lds(rz, (LAS void*)(size_t)a, 4, 0, 0, 0, 0);
}
DEV u32x4 ld128(rsrc_t r, unsigned vo, unsigned so = 0) { return __builtin_amdgcn_raw_buffer_load_b128(r, vo, so, 0); }
#define FRESH() asm volatile("" ::: "memory")
DEV u32x4 ld128c(rsrc_t r, unsigned vo) { return __builtin_amdgcn_raw_buffer_load_b128(r, vo, 0, 16); }
DEV void st128c(rsrc_t r, unsigned vo, u32x4 v) {
  asm volatile("s_nop 4\n\tbuffer_store_dwordx4 %0, %1, %2, 0 offen sc1\n\ts_nop 7\n\ts_nop 7" ::"v"(v), "v"(vo), "s"(r) : "memory");
}
DEV void st64c(rsrc_t r, unsigned vo, u32x2 v) { __builtin_amdgcn_raw_buffer_store_b64(v, r, vo, 0, 16); }
DEV void ld8(u32x4 (&w)[8], rsrc_t r, unsigned vo, unsigned so, unsigned step) {
  asm volatile("s_nop 4\n\tbuffer_load_dwordx4 %0, %9, %10, %8 offen\n\ts_add_u32 %8, %8, %11\n\tbuffer_load_dwordx4 %1, %9, %10, %8 offen\n\ts_add_u32 %8, %8, %11\n\t"
               "buffer_load_dwordx4 %2, %9, %10, %8 offen\n\ts_add_u32 %8, %8, %11\n\tbuffer_load_dwordx4 %3, %9, %10, %8 offen\n\ts_add_u32 %8, %8, %11\n\t"
               "buffer_load_dwordx4 %4, %9, %10, %8 offen\n\ts_add_u32 %8, %8, %11\n\tbuffer_load_dwordx4 %5, %9, %10, %8 offen\n\ts_add_u32 %8, %8, %11\n\t"
               "buffer_load_dwordx4 %6, %9, %10, %8 offen\n\ts_add_u32 %8, %8, %11\n\tbuffer_load_dwordx4 %7, %9, %10, %8 offen"
               : "=&v"(w[0]), "=&v"(w[1]), "=&v"(w[2]), "=&v"(w[3]), "=&v"(w[4]), "=&v"(w[5]), "=&v"(w[6]), "=&v"(w[7]), "+&s"(so)
               : "v"(vo), "s"(r), "s"(step) : "memory", "scc");
}
DEV void ld8c(u32x4 (&w)[8], rsrc_t r, unsigned vo, unsigned so, unsigned step, int a, int b) {
  asm volatile("s_nop 4\n\ts_cmp_eq_u32 %12, %13\n\ts_cbranch_scc0 Lskip_%=\n\t"
               "buffer_load_dwordx4 %0, %9, %10, %8 offen" WNT "\n\ts_add_u32 %8, %8, %11\n\tbuffer_load_dwordx4 %1, %9, %10, %8 offen" WNT "\n\ts_add_u32 %8, %8, %11\n\t"
               "buffer_load_dwordx4 %2, %9, %10, %8 offen" WNT "\n\ts_add_u32 %8, %8, %11\n\tbuffer_load_dwordx4 %3, %9, %10, %8 offen" WNT "\n\ts_add_u32 %8, %8, %11\n\t"
               "buffer_load_dwordx4 %4, %9, %10, %8 offen" WNT "\n\ts_add_u32 %8, %8, %11\n\tbuffer_load_dwordx4 %5, %9, %10, %8 offen" WNT "\n\ts_add_u32 %8, %8, %11\n\t"
               "buffer_load_dwordx4 %6, %9, %10, %8 offen" WNT "\n\ts_add_u32 %8, %8, %11\n\tbuffer_load_dwordx4 %7, %9, %10, %8 offen" WNT "\n\tLskip_%=:"
               : "+&v"(w[0]), "+&v"(w[1]), "+&v"(w[2]), "+&v"(w[3]), "+&v"(w[4]), "+&v"(w[5]), "+&v"(w[6]), "+&v"(w[7]), "+&s"(so)
               : "v"(vo), "s"(r), "s"(step), "s"(a), "n"(b) : "memory", "scc");
}
DEV rsrc_t mk(const void* p, unsigned bytes) { return __builtin_amdgcn_make_buffer_rsrc((void*)p, 0, bytes, 0x00020000); }

DEV unsigned wave_maxu(unsigned v) {
  unsigned r;
  asm("s_nop 1\n\tv_max_u32_dpp %1, %1, %1 row_shr:1 row_mask:0xf bank_mask:0xf\n\ts_nop 1\n\tv_max_u32_dpp %1, %1, %1 row_shr:2 row_mask:0xf bank_mask:0xf\n\t"
      "s_nop 1\n\tv_max_u32_dpp %1, %1, %1 row_shr:4 row_mask:0xf bank_mask:0xf\n\ts_nop 1\n\tv_max_u32_dpp %1, %1, %1 row_shr:8 row_mask:0xf bank_mask:0xf\n\t"
      "s_nop 1\n\tv_max_u32_dpp %1, %1, %1 row_bcast:15 row_mask:0xa bank_mask:0xf\n\ts_nop 1\n\tv_max_u32_dpp %1, %1, %1 row_bcast:31 row_mask:0xc bank_mask:0xf\n\t"
      "s_nop 0\n\tv_readlane_b32 %0, %1, 63\n\ts_nop 1" : "=s"(r), "+v"(v));
  return r;
}
DEV float sum4(const float (&a)[4]) {
  auto p = __builtin_amdgcn_permlane16_swap(__float_as_uint(a[0]), __float_as_uint(a[1]), false, false);
  auto q = __builtin_amdgcn_permlane16_swap(__float_as_uint(a[2]), __float_as_uint(a[3]), false, false);
  const float u = __uint_as_float(p[0]) + __uint_as_float(p[1]), w = __uint_as_float(q[0]) + __uint_as_float(q[1]);
  auto r = __builtin_amdgcn_permlane32_swap(__float_as_uint(u), __float_as_uint(w), false, false);
  float v = __uint_as_float(r[0]) + __uint_as_float(r[1]);
  v += __uint_as_float((unsigned)__builtin_amdgcn_update_dpp(0, (int)__float_as_uint(v), 0x128, 0xf, 0xf, false));
  v += __uint_as_float((unsigned)__builtin_amdgcn_update_dpp(0, (int)__float_as_uint(v), 0x124, 0xf, 0xf, false));
  v += __uint_as_float((unsigned)__builtin_amdgcn_update_dpp(0, (int)__float_as_uint(v), 0x122, 0xf, 0xf, false));
  v += __uint_as_float((unsigned)__builtin_amdgcn_update_dpp(0, (int)__float_as_uint(v), 0x121, 0xf, 0xf, false));
  return v;
}
DEV float dot2(unsigned a, unsigned b, float c) { return __builtin_amdgcn_fdot2_f32_bf16(__builtin_bit_cast(bf16x2_t, a), __builtin_bit_cast(bf16x2_t, b), c, false); }
DEV unsigned route_code(unsigned u) { return (u & 0x8000u) ? (~u & 0xffffu) : (u | 0x8000u); }
DEV unsigned med3u(unsigned a, unsigned b, unsigned c) { unsigned d; asm("v_med3_u32 %0, %1, %2, %3" : "=v"(d) : "v"(a), "v"(b), "v"(c)); return d; }
DEV float key_val(unsigned key) {
  unsigned u = key >> 16;
  u = (u & 0x8000u) ? (u & 0x7fffu) : (~u & 0xffffu);
  return __uint_as_float(u << 16);
}
#define STAMP(k)
#define STAMPS()

DEV void unit_mma(const u32x4 (&w)[8], bf16x8 a, f32x4 (&d)[8]) {
#pragma unroll
  for (int j = 0; j < 8; ++j) {
    u32x4 b;
#pragma unroll
    for (int t = 0; t < 4; ++t) b[t] = __builtin_amdgcn_perm(w[2 * t + 1][j >> 1], w[2 * t][j >> 1], (j & 1) ? 0x07060302u : 0x05040100u);
    d[j] = __builtin_amdgcn_mfma_f32_16x16x32_bf16(a, __builtin_bit_cast(bf16x8, b), d[j], 0, 0, 0);
  }
}

DEV void fold(const f32x4 (&d)[8], f32x4 (&o)[2]) {
#pragma unroll
  for (int r = 0; r < 2; ++r)
#pragma unroll
    for (int q = 0; q < 4; ++q) {
      const float a0 = d[q][r], a1 = d[q][r + 2], b0 = d[q + 4][r], b1 = d[q + 4][r + 2];
      auto a = __builtin_amdgcn_permlane32_swap(__float_as_uint(a0), __float_as_uint(a1), false, false);
      auto b = __builtin_amdgcn_permlane32_swap(__float_as_uint(b0), __float_as_uint(b1), false, false);
      float t;
      asm("s_nop 1\n\tv_add_f32_dpp %0, %1, %2 row_ror:8 row_mask:0xf bank_mask:0x3\n\tv_add_f32_dpp %0, %3, %4 row_ror:8 row_mask:0xf bank_mask:0xc"
          : "=&v"(t) : "v"(a[1]), "v"(a[0]), "v"(b[0]), "v"(b[1]));
      o[r][q] = t;
    }
}
DEV void st2x128c(rsrc_t r, unsigned vo, unsigned so, const f32x4 (&o)[2]) {
  asm volatile("s_nop 4\n\tbuffer_store_dwordx4 %0, %2, %3, 0 offen sc1\n\tbuffer_store_dwordx4 %1, %2, %3, %4 offen sc1\n\ts_nop 7\n\ts_nop 7"
               ::"v"(o[0]), "v"(o[1]), "v"(vo), "s"(r), "s"(so) : "memory");
}
DEV void poll32(rsrc_t r, unsigned vo, unsigned tg, bool on) {
#pragma unroll 1
  for (int it = 0; it < SPIN_MAX; ++it) {
    FRESH();
    const unsigned v = __builtin_amdgcn_raw_buffer_load_b32(r, vo, 0, 16);
    if (__all(!on || v == tg)) break;
    __builtin_amdgcn_s_sleep(16);
  }
}

DEV void count1(rsrc_t r, unsigned so) {
  asm volatile("s_nop 4\n\ts_waitcnt vmcnt(0)\n\ts_mov_b64 exec, 1\n\tbuffer_atomic_add %0, off, %1, %2 sc1\n\ts_mov_b64 exec, -1" ::"v"(1u), "s"(r), "s"(so) : "memory");
}
DEV void count_wait(rsrc_t r, unsigned so, unsigned n) {
#pragma unroll 1
  for (int it = 0; it < SPIN_MAX; ++it) {
    unsigned v = 0;
    asm volatile("s_nop 4\n\ts_mov_b64 exec, 1\n\tbuffer_atomic_or %0, off, %1, %2 sc0 sc1\n\ts_mov_b64 exec, -1\n\ts_waitcnt vmcnt(0)" : "+v"(v) : "s"(r), "s"(so) : "memory");
    if (__builtin_amdgcn_readfirstlane(v) == n) break;
    __builtin_amdgcn_s_sleep(CWS);
  }
}

typedef __attribute__((ext_vector_type(8))) u64 u64x8;
__global__ __launch_bounds__(512, 2) void fused(const u16*, const u16*, const u16*, const u16*, const u16*, const u16*, const u16*, const u16*, const u16*,
                                                const u16*, const u16*, const u16*, const u16*, const u16*, char*, float*) {
#if __HIP_DEVICE_COMPILE__
  extern __shared__ __attribute__((aligned(16))) char smem_[];
  LAS char* smem = (LAS char*)smem_;
#define ARGREQ(W) u64x8 ka, kb; \
  asm volatile("s_load_dwordx16 %0, %2, 0x0\n\ts_load_dwordx16 %1, %2, 0x40" W : "=&s"(ka), "=&s"(kb) : "s"(__builtin_amdgcn_kernarg_segment_ptr()) : "memory")
#define ARGS() \
  const u16 *x = (const u16*)ka[0], *Lre = (const u16*)ka[1], *Lim = (const u16*)ka[2], *Bre = (const u16*)ka[3], *Bim = (const u16*)ka[4], *Cre = (const u16*)ka[5], \
            *Cim = (const u16*)ka[6], *logdt = (const u16*)ka[7], *Dskip = (const u16*)kb[0], *w_r = (const u16*)kb[1], *w_gate = (const u16*)kb[2], \
            *w_up = (const u16*)kb[3], *w_down = (const u16*)kb[4], *gate_w = (const u16*)kb[5]; \
  char* ws = (char*)kb[6]; float* out = (float*)kb[7]; \
  const rsrc_t rws = mk(ws, W_END), rx = mk(x, 2048 * 1024 * 2), rz = mk(x, 0)
  STAMP(0);
  int tid = threadIdx.x, bid = blockIdx.x;
  {
    unsigned t_[8], u_[2];
    asm volatile(ASM_EN : [w] "=&s"(t_[0]), [j] "=&s"(t_[1]), [t] "=&s"(t_[2]), [sa] "=&s"(t_[3]), [sc] "=&s"(t_[4]), [m] "=&s"(t_[5]), [r] "=&s"(t_[6]), [c] "=&s"(t_[7]), [v0] "=&v"(u_[0]), [v1] "=&v"(u_[1]) : [kp] "s"(__builtin_amdgcn_kernarg_segment_ptr()), [tid] "v"(threadIdx.x), [bid] "s"(blockIdx.x), [cpf] "n"(CODEPF), [ms0] "n"(M_S0) : "memory", "scc", "m0", "s20", "s21", "s22", "s23", "v255");
    asm volatile("" : "+s"(bid)); asm volatile("" : "+v"(tid));
  }
  const int lane = tid & 63, wid = __builtin_amdgcn_readfirstlane(tid >> 6), xcd = bid & 7, loc = bid >> 3;
  const int c = (xcd >> 1) * 4 + (loc >> 3), j = (xcd & 1) * 8 + (loc & 7);
  const int fr = lane & 15, fq = lane >> 4;

  if (__builtin_expect(wid == 4 || wid == 5, 1)) {
    ARGREQ("");
    const int l = wid - 4;
    __builtin_amdgcn_s_setprio(LPRIO);
    const int l3 = lane >> 3;
    {
      const unsigned one = tid >= 272 ? 1u : 0u;
      asm volatile("ds_write_b128 %0, %1" ::"v"(L_FLG + min(tid - 256, 111) * 16), "v"((u32x4){one, one, one, one}) : "memory");
    }
    int bud = SPIN_MAX * 4;
    unsigned dst = 1 ? l * 49152 : 131072 - l * 114688, ci = M_CONS - 64 + 1 * 16, t_s0, vc = 0;
    unsigned b0 = (c * 128 + l * (j * 64 - c * 128) + l3 * (8 - 7 * l)) * 2048 + ((lane & 7) ^ l3) * 16, wrap = 163840 - l * 32768;
    unsigned so[8];
#pragma unroll
    for (int q = 0; q < 8; ++q) so[q] = (64 * (q & 1) + (q >> 1) + l * (8 * q - 64 * (q & 1) - (q >> 1))) * 2048;
    unsigned sk = (((xcd >> 1) + 2 * (xcd & 1)) & 3) * 512 + 1 * 128, n = 16 - 1, mka = M_LAND + 1 * 8 + l * 4, v0 = b0 + sk;
    asm volatile("" : "+s"(dst), "+v"(b0), "+s"(wrap), "+s"(so[1]), "+s"(so[2]), "+s"(so[3]), "+s"(so[4]), "+s"(so[5]), "+s"(so[6]), "+s"(so[7]),
                 "+s"(sk), "+s"(mka), "+v"(v0));
    asm volatile("s_waitcnt lgkmcnt(0)" : "+s"(ka), "+s"(kb)::"memory");
    ARGS();
    {
      const rsrc_t r0 = mk(l ? Bre : x, (1u << 22) >> l), r1 = mk(l ? Bim : x + 4096, l ? (1u << 21) : (1u << 22) - 8192);
      STAMP(8);
      asm volatile(ASM_LA : [dst] "+&s"(dst), [sk] "+&s"(sk), [ci] "+&s"(ci), [n] "+&s"(n), [mk] "+&s"(mka), [bud] "+&s"(bud), [s0] "=&s"(t_s0), [v0] "+&v"(v0), [vc] "+&v"(vc) : [r0] "s"(r0), [r1] "s"(r1), [rz] "s"(rz), [wrap] "s"(wrap), [b0] "v"(b0), [vw] "v"(M_WR), [so0] "s"(so[0]), [so1] "s"(so[1]), [so2] "s"(so[2]), [so3] "s"(so[3]), [so4] "s"(so[4]), [so5] "s"(so[5]), [so6] "s"(so[6]), [so7] "s"(so[7]) : "memory", "scc", "m0", "v24", "v25", "v26", "v27", "v28", "v29", "v30", "v31");
    }
    STAMP(1);
    {
      const rsrc_t r0 = mk(Cre, 1 << 21), r1 = mk(Cim, 1 << 21);
      const unsigned b0 = (j * 64 + 4 * l3) * 2048 + (((lane & 7) ^ (l3 >> 1)) * 16), b1 = b0 ^ 64, wrap = 147456, cth = M_CONS + (15 - l) * 16;
      unsigned so[8];
#pragma unroll
      for (int q = 0; q < 8; ++q) so[q] = (32 * (q & 1) + (q >> 1)) * 2048;
      unsigned sk = l * 128, hi = (j & 8) * 128, n = 8, mka = M_LAND + (17 + l) * 8, v0 = b0 + sk + hi, v1 = b1 + sk + hi, t_s1;
      const rsrc_t rp = mk(l ? Cim : Cre, 1 << 21);
      const unsigned p0 = b0 + (hi ^ 0x400);
      unsigned vd = l * 8192 + lane * 16;
      asm volatile(ASM_LB : [dst] "+&s"(dst), [sk] "+&s"(sk), [ci] "+&s"(ci), [n] "+&s"(n), [mk] "+&s"(mka), [bud] "+&s"(bud), [s0] "=&s"(t_s0), [v0] "+&v"(v0), [vc] "+&v"(vc), [v1] "+&v"(v1), [hi] "+&s"(hi), [s1] "=&s"(t_s1), [vd] "+&v"(vd) : [r0] "s"(r0), [r1] "s"(r1), [rz] "s"(rz), [wrap] "s"(wrap), [b0] "v"(b0), [vw] "v"(M_WR + 16), [cth] "s"(cth), [b1] "v"(b1), [nend] "s"(3 + l), [rp] "s"(rp), [p0] "v"(p0), [p1] "v"(p0 ^ 64), [vm] "v"(M_CONS + 256), [vl] "v"(M_LAND + 26 * 8 + 4 * l), [so0] "s"(so[0]), [so1] "s"(so[1]), [so2] "s"(so[2]), [so3] "s"(so[3]), [so4] "s"(so[4]), [so5] "s"(so[5]), [so6] "s"(so[6]), [so7] "s"(so[7]) : "memory", "scc", "m0", "v24", "v25", "v26", "v27", "v28", "v29", "v30", "v31", "v32", "v33", "v34", "v35", "v36", "v37", "v38", "v39", "v40", "v41", "v42", "v43", "v44", "v45", "v46", "v47", "v48", "v49", "v50", "v51", "v52", "v53", "v54", "v55", "v56", "v57", "v58", "v59", "v60", "v61", "v62", "v63", "v64", "v65", "v66", "v67", "v68", "v69", "v70", "v71", "v72", "v73", "v74", "v75", "v76", "v77", "v78", "v79", "v80", "v81", "v82", "v83", "v84", "v85", "v86", "v87", "v88", "v89", "v90", "v91", "v92", "v93", "v94", "v95", "v96", "v97", "v98", "v99", "v100", "v101", "v102", "v103", "v104", "v105", "v106", "v107", "v108", "v109", "v110", "v111", "v112", "v113", "v114", "v115", "v116", "v117", "v118", "v119", "v120", "v121", "v122", "v123", "v124", "v125", "v126", "v127", "v128", "v129", "v130", "v131", "v132", "v133", "v134", "v135", "v136", "v137", "v138", "v139", "v140", "v141", "v142", "v143", "v144", "v145", "v146", "v147", "v148", "v149", "v150", "v151", "v152", "v153", "v154", "v155", "v156", "v157", "v158", "v159", "v160", "v161", "v162", "v163", "v164", "v165", "v166", "v167", "v168", "v169", "v170", "v171", "v172", "v173", "v174", "v175", "v176", "v177", "v178", "v179", "v180", "v181", "v182", "v183", "v184", "v185", "v186", "v187", "v188", "v189", "v190", "v191", "v192", "v193", "v194", "v195", "v196", "v197", "v198", "v199", "v200", "v201", "v202", "v203", "v204", "v205", "v206", "v207", "v208", "v209", "v210", "v211", "v212", "v213", "v214", "v215", "v216", "v217", "v218", "v219", "v220", "v221", "v222", "v223", "v224", "v225", "v226", "v227", "v228", "v229", "v230", "v231", "v232", "v233", "v234", "v235", "v236", "v237", "v238", "v239", "v240", "v241", "v242", "v243", "v244", "v245", "v246", "v247", "v248", "v249", "v250", "v251", "v252", "v253", "v254", "v255");
    }
    waitv<0>();
    STAMP(3);
    STAMPS();
    __builtin_amdgcn_endpgm();
  }

  if (__builtin_expect(wid >= 6, 1)) {
    const int xw = wid - 6;
    __builtin_amdgcn_s_setprio(XPRIO0);
    ARGREQ("\n\ts_waitcnt lgkmcnt(0)");
    ARGS();
    const int row0 = c * 128 + j * 8 + xw * 4;
    const int l = xw;
    const bool ffn = !NOFFN && loc < 28;
    const int sg = xcd * 7 + (loc >> 2), kq = loc & 3;
    u32x4 w1[4][8], w2[2][8];
    unsigned tagA, tagB;
    const int sb = HELP ? (3 - (int)((size_t)w_up >> 7)) & 7 : 9, s1 = ffn ? sg : 8 * xcd + sb;
    const bool gu = ffn ? !(xw && (sg & 7) == sb) : HELP && !NOFFN && xw && xcd < 7;
    const int pg = ffn ? WGRP : 0, grp = __builtin_amdgcn_readfirstlane(gu ? pg : 9);
    auto wburst = [&](int p, unsigned gate) __attribute__((always_inline)) {
      const bool on = grp == p;
      const rsrc_t r1 = mk(xw ? w_up : w_gate, D_ * F_ * 2);
      const unsigned o1 = (kq * 256 + 32 * (fr >> 3) + 8 * fq) * (F_ * 2) + (s1 * 64 + 8 * (fr & 7)) * 2;
      if (pg == p) {
        lds_wait0(M_LAND + gate * 8);
        if (on) mark(rz, M_WR + xw * 4);
        __builtin_amdgcn_s_sleep(WSLP);
      }
#pragma unroll
      for (int u = 0; u < 4; ++u) {
        ld8c(w1[u], r1, o1, u * 64 * (F_ * 2), F_ * 2, grp, p);
        if (u == WRES && on) mark(rz, M_WR + 8 + xw * 4);
      }
      if (pg == p)
        __builtin_amdgcn_raw_ptr_buffer_load_lds(mk(xw ? Cim : Cre, 1 << 21), (LAS void*)(smem + L_JUNK + 256 + xw * 256), 4, (j * 64 + lane) * 2048 + ((c + lane) & 15) * 128, 0, 0, 0);
    };
    {
      unsigned ep;
      u32x4 xv[8], wv[2], gv[2];
      unsigned pl[3];
      const rsrc_t rw = mk(w_r, 2048), rg = mk(gate_w, 2048);
      const rsrc_t rp0 = mk(Lre, 2048), rp1 = mk(Lim, 2048), rp2 = mk(logdt, 2048);
      const unsigned zero = 0, l16 = lane * 16, xo = row0 * 2048 + lane * 16, po = (j * 64 + lane) * 2;
      if (XG < -1) { asm volatile("" ::"s"(rg), "s"(rp0), "s"(rp1), "s"(rp2), "s"(rx), "v"(xo), "v"(po)); bar(); }
      asm volatile("s_nop 4\n\tbuffer_load_dword %0, %3, %5, 0 offen sc1\n\tbuffer_load_dwordx4 %1, %4, %6, 0 offen\n\tbuffer_load_dwordx4 %2, %4, %6, 0 offen offset:1024"
                   : "=&v"(ep), "=&v"(wv[0]), "=&v"(wv[1]) : "v"(zero), "v"(l16), "s"(rws), "s"(rw) : "memory");
      asm volatile("s_nop 4\n\tbuffer_load_dwordx4 %3, %9, %10, 0 offen\n\tbuffer_load_dwordx4 %4, %9, %10, 0 offen offset:1024\n\t"
                   "buffer_load_ushort %0, %5, %6, 0 offen\n\tbuffer_load_ushort %1, %5, %7, 0 offen\n\tbuffer_load_ushort %2, %5, %8, 0 offen"
                   : "=&v"(pl[0]), "=&v"(pl[1]), "=&v"(pl[2]), "=&v"(gv[0]), "=&v"(gv[1]) : "v"(po), "s"(rp0), "s"(rp1), "s"(rp2), "v"(l16), "s"(rg) : "memory");
      asm volatile("s_nop 4\n\tbuffer_atomic_swap %0, %1, %2, 0 offen sc1" ::"v"(zero), "v"(tid == 384 && bid < 160 ? W_HF + bid * 128 : 0xf0000000u), "s"(rws) : "memory");
      if (XG >= -1) bar();
      if (XG >= 0) lds_wait0(M_LAND + XG * 8);
      ld8(xv, rx, xo, 0, 1024);
      asm volatile("s_waitcnt vmcnt(0)" : "+v"(ep), "+v"(xv[0]), "+v"(xv[1]), "+v"(xv[2]), "+v"(xv[3]), "+v"(xv[4]), "+v"(xv[5]), "+v"(xv[6]),
                   "+v"(xv[7]), "+v"(wv[0]), "+v"(wv[1]), "+v"(gv[0]), "+v"(gv[1]), "+v"(pl[0]), "+v"(pl[1]), "+v"(pl[2])::"memory");
      tagA = ep + 1u; tagB = ep + 0x40000001u;
      float sr[4];
#pragma unroll
      for (int r = 0; r < 4; ++r) {
        sr[r] = 0.f;
#pragma unroll
        for (int h = 0; h < 2; ++h)
#pragma unroll
          for (int e = 0; e < 4; ++e) { const unsigned xu = xv[2 * r + h][e], wu = wv[h][e]; sr[r] = dot2(xu, wu, sr[r]); }
      }
      {
        const unsigned rb = route_code(f2bf(sum4(sr)));
        const unsigned r01 = (unsigned)__builtin_amdgcn_readlane((int)rb, 0) | ((unsigned)__builtin_amdgcn_readlane((int)rb, 16) << 16);
        const unsigned r23 = (unsigned)__builtin_amdgcn_readlane((int)rb, 32) | ((unsigned)__builtin_amdgcn_readlane((int)rb, 48) << 16);
        st128c(rws, lane == 0 ? W_R2 + row0 * 4 : 0xf0000000u, (u32x4){r01, tagA, r23, tagB});
      }
#pragma unroll
      for (int r = 0; r < 4; ++r) {
        sr[r] = 0.f;
#pragma unroll
        for (int h = 0; h < 2; ++h)
#pragma unroll
          for (int e = 0; e < 4; ++e) {
            const unsigned xu = xv[2 * r + h][e], gu = gv[h][e];
            sr[r] = dot2(xu, gu, sr[r]);
          }
      }
      {
        const float z = rbf(sum4(sr));
        st64c(rws, fr == 0 ? W_RG + (row0 + fq) * 8 : 0xf0000000u, (u32x2){__float_as_uint(rbf(1.f / (1.f + __expf(-z)))), tagB});
      }
      STAMP(8);
      __builtin_amdgcn_s_setprio(XPRIO);
#pragma unroll
      for (int u = 0; u < 4; ++u) asm volatile("" : "=v"(w1[u][0]), "=v"(w1[u][1]), "=v"(w1[u][2]), "=v"(w1[u][3]), "=v"(w1[u][4]), "=v"(w1[u][5]), "=v"(w1[u][6]), "=v"(w1[u][7]));
#pragma unroll
      for (int u = 0; u < 2; ++u) asm volatile("" : "=v"(w2[u][0]), "=v"(w2[u][1]), "=v"(w2[u][2]), "=v"(w2[u][3]), "=v"(w2[u][4]), "=v"(w2[u][5]), "=v"(w2[u][6]), "=v"(w2[u][7]));
      wburst(0, WS0);
      if (xw == 1) {
        const float lre = bf2f(pl[0]), lim = bf2f(pl[1]);
        const float dt = rbf(__expf(bf2f(pl[2])));
        const float er = expf(dt * lre);
        float sn, cs;
        sincosf(dt * lim, &sn, &cs);
        const f32x2 l1 = {mul1(er, cs), mul1(er, sn)};
        const float nr = l1.x - 1.f, ni = l1.y, den = fma1(lre, lre, mul1(lim, lim));
        const f32x2 cf = {fma1(nr, lre, mul1(ni, lim)) / den, fnma1(nr, lim, mul1(ni, lre)) / den};
        const f32x2 l2 = cmul(l1, l1), l4 = cmul(l2, l2), l8 = cmul(l4, l4), l16_ = cmul(l8, l8), l32 = cmul(l16_, l16_), l64 = cmul(l32, l32),
                    l128 = cmul(l64, l64), l256 = cmul(l128, l128), l384 = cmul(l256, l128), l512 = cmul(l256, l256);
        auto put = [&](int e, f32x2 v) { const float re = v.x, im = v.y; asm volatile("ds_write2_b32 %0, %1, %2 offset0:0 offset1:64" ::"v"(L_TAB + e * 512 + lane * 4), "v"(re), "v"(im) : "memory"); };
        put(0, l1); put(1, cf); put(2, l8); put(3, l16_); put(4, l32); put(5, l64); put(6, l512); put(7, (f32x2){1.f, 0.f}); put(8, l128); put(9, l256); put(10, l384);
        lds_wr32(M_LAND + 16 * 8, 0u);
      }
    }
    unsigned m = 0, m2 = 0, m3 = 0;
    auto gkey = [&](unsigned k) { return (k & 0xffff0000u) | ((k & 28u) << 6) | (k & 3u) | (unsigned)(252 - lane * 4); };
    lds_wait0(M_LAND + (pg == 0 ? TKG0 : TKG) * 8);
    if (xw == 1) {
#pragma unroll
      for (int h = 0; h < 2; ++h) {
        poll32(rws, W_RG + (c * 128 + h * 64 + lane) * 8 + 4, tagB, true);
        __builtin_amdgcn_raw_ptr_buffer_load_lds(rws, (LAS void*)(smem + L_G + h * 256), 4, W_RG + (c * 128 + h * 64 + lane) * 8, 0, 0, 16);
      }
      mark(rz, M_GT);
      STAMP(1);
    } else {
      {
        u32x4 g[8];
#pragma unroll 1
        for (int it = 0; it < SPIN_MAX; ++it) {
          FRESH();
#pragma unroll
          for (int q = 0; q < 8; ++q) g[q] = ld128c(rws, W_R2 + lane * 16 + q * 1024);
          bool ok = true;
#pragma unroll
          for (int q = 0; q < 8; ++q) ok = ok && g[q].y == tagA && g[q].w == tagB;
          if (__all(ok)) break;
          __builtin_amdgcn_s_sleep(16);
        }
        mark(rz, M_LAND + 16 * 8 + 4);
        STAMP(1);
        auto lkey = [&](int i) { const unsigned w = g[i >> 2][i & 2]; return ((i & 1) ? (w & 0xffff0000u) : (w << 16)) | (unsigned)(31 - i); };
        m = lkey(0); m2 = 0; m3 = 0;
#pragma unroll
        for (int i = 1; i < 32; ++i) {
          const unsigned k = lkey(i);
          m3 = med3u(m2, m3, k); m2 = med3u(m, m2, k); m = max(m, k);
        }
      }
      m = gkey(m); m2 = gkey(m2); m3 = gkey(m3);
    }
    asm volatile("" : "+v"(m), "+v"(m2), "+v"(m3));
    wburst(1, WS1);
    if (xw == 0) {
      unsigned mine = 0;
#pragma unroll 1
      for (int s = 0; s < 8; ++s) {
        const unsigned mm = wave_maxu(m);
        mine = (lane >> 3) == s ? mm : mine;
        const bool hit = m == mm;
        m = hit ? m2 : m; m2 = hit ? m3 : m2; m3 = hit ? 0u : m3;
      }
      if (__ballot(m == 0u)) {
        unsigned lim = 0xffffffffu;
#pragma unroll 1
        for (int s = 0; s < 8; ++s) {
          unsigned mm = 0;
#pragma unroll 1
          for (int q = 0; q < 8; ++q) {
            const u32x4 w = ld128c(rws, W_R2 + lane * 16 + q * 1024);
#pragma unroll
            for (int e = 0; e < 4; ++e) {
              const unsigned k = gkey(((e & 1) ? (w[e & 2] & 0xffff0000u) : (w[e & 2] << 16)) | (unsigned)(31 - 4 * q - e));
              mm = max(mm, k < lim || s == 0 ? k : 0u);
            }
          }
          lim = wave_maxu(mm);
          mine = (lane >> 3) == s ? lim : mine;
        }
      }
      const unsigned myidx = 2047u - (mine & 0xffffu);
      {
#pragma unroll
        for (int u = 0; u < 4; ++u)
          __builtin_amdgcn_raw_ptr_buffer_load_lds(rx, (LAS void*)(smem + L_XS + u * 1024), 16, myidx * 2048 + (loc & 3) * 512 + (lane & 7) * 16, u * 128, 0, 0);
      }
      const float myrs = rbf(1.f / (1.f + __expf(-key_val(mine))));
      if ((lane & 7) == 0) asm volatile("ds_write_b64 %0, %1\n\ts_waitcnt lgkmcnt(0)" ::"v"(L_SEL + lane), "v"((u32x2){myidx, __float_as_uint(myrs)}) : "memory");
      mark(rz, M_XS);
    }
    STAMP(2);
    __builtin_amdgcn_s_setprio(FPRIO);
    lds_wait0(M_XS);
    const u32x2 sel = lds_rd64(L_SEL + (lane & 7) * 8);
    const unsigned fa = ((fr & 7) * 8 + 4 * (fr >> 3) + fq) * 16, lp = (fr & 7) * 32 + (fr >> 3) * 16, tk = 4 * (fq & 1) + 2 * (fq >> 1);
    waitv<0>();
    STAMP(10);
    {
      if (0 && ffn) { lds_wait0(M_LAND + (W2G - 2) * 8); mark(rz, M_WR + 16 + xw * 4); }
      if (ffn) lds_wait0(M_LAND + W2G * 8);
      __builtin_amdgcn_s_setprio(3);
      const rsrc_t r2 = mk(w_down, D_ * F_ * 2);
      const unsigned o2 = (sg * 64 + 32 * (fr >> 3) + 8 * fq) * 2048 + (kq * 256 + xw * 128 + 8 * (fr & 7)) * 2;
      const int on = __builtin_amdgcn_readfirstlane(ffn ? 1 : 0);
#pragma unroll
      for (int u = 0; u < 2; ++u) {
        ld8c(w2[u], r2, o2, u * 128, 2048, on, 1);
        if (0 == u + 1 && ffn) mark(rz, M_WR + 24 + xw * 4);
      }
      __builtin_amdgcn_s_setprio(FPRIO);
    }
    f32x4 d[8], o[2];
    if (gu) {
      u32x4 xa[4];
      asm volatile("ds_read_b128 %0, %4\n\tds_read_b128 %1, %4 offset:1024\n\tds_read_b128 %2, %4 offset:2048\n\tds_read_b128 %3, %4 offset:3072\n\ts_waitcnt lgkmcnt(0)"
                   : "=&v"(xa[0]), "=&v"(xa[1]), "=&v"(xa[2]), "=&v"(xa[3]) : "v"(L_XS + fa) : "memory");
#pragma unroll
      for (int q = 0; q < 8; ++q) d[q] = (f32x4){0.f, 0.f, 0.f, 0.f};
#pragma unroll
      for (int u = 0; u < 4; ++u) {
        asm volatile("" : "+v"(w1[u][0]), "+v"(w1[u][1]), "+v"(w1[u][2]), "+v"(w1[u][3]), "+v"(w1[u][4]), "+v"(w1[u][5]), "+v"(w1[u][6]), "+v"(w1[u][7]));
        unit_mma(w1[u], __builtin_bit_cast(bf16x8, xa[u]), d);
        asm volatile("" : "+v"(d[0]), "+v"(d[1]), "+v"(d[2]), "+v"(d[3]), "+v"(d[4]), "+v"(d[5]), "+v"(d[6]), "+v"(d[7]));
      }
      fold(d, o);
      STAMP(11);
      st2x128c(rws, W_GU + ((s1 * 8 + tk) * 8 + kq * 2 + l) * 256 + lp, 2048, o);
      count1(rws, W_GF + s1 * 128);
    }
    STAMP(3);
    if (ffn) {
      {
        if (GUG) lds_wait0(M_LAND + GUG * 8);
        __builtin_amdgcn_s_setprio(GPRIO);
        count_wait(rws, W_GF + sg * 128, 8);
        f32x4 a[8];
#pragma unroll
        for (int q = 0; q < 8; ++q) a[q] = __builtin_bit_cast(f32x4, ld128c(rws, W_GU + (sg * 8 + 4 * l + fq) * 2048 + fr * 16 + q * 256));
        float a2[4];
#pragma unroll
        for (int e = 0; e < 4; ++e) {
          const float g = rbf(add1(add1(a[0][e], a[2][e]), add1(a[4][e], a[6][e]))), u = rbf(add1(add1(a[1][e], a[3][e]), add1(a[5][e], a[7][e])));
          a2[e] = mul1(rbf(g / add1(1.f, __expf(-g))), u);
        }
        asm volatile("ds_write_b64 %0, %1\n\ts_nop 3" ::"v"(L_ACT + (4 * l + fq) * 128 + fr * 8), "v"((u32x2){pk2(a2[0], a2[1]), pk2(a2[2], a2[3])}) : "memory");
        lds_wr32(FL_TK + l * 4, 1u);
        lds_wait_ge(FL_TK + (l ^ 1) * 4, 1u);
      }
      const u32x4 af = lds_rd128(L_ACT + fa);
      STAMP(4);
      waitv<0>();
#pragma unroll
      for (int u = 0; u < 2; ++u)
        asm volatile("" : "+v"(w2[u][0]), "+v"(w2[u][1]), "+v"(w2[u][2]), "+v"(w2[u][3]), "+v"(w2[u][4]), "+v"(w2[u][5]), "+v"(w2[u][6]), "+v"(w2[u][7]));
#pragma unroll
      for (int u = 0; u < 2; ++u) {
#pragma unroll
        for (int q = 0; q < 8; ++q) d[q] = (f32x4){0.f, 0.f, 0.f, 0.f};
        unit_mma(w2[u], __builtin_bit_cast(bf16x8, af), d);
        fold(d, o);
        st2x128c(rws, W_YP + ((tk * 16 + kq * 4 + l * 2 + u) * 56 + sg) * 256 + lp, 16 * 56 * 256, o);
      }
      count1(rws, W_YF + (kq * 2 + l) * 128);
      STAMP(5);
    }
    u64 rows = __ballot(lane < 8 && (sel.x >> 7) == (unsigned)c);
    if (l) rows &= rows - 1;
    if (!NOFFN && rows) {
      lds_wait0(M_CONS + (16 + YG) * 16);
      __builtin_amdgcn_s_setprio(YPRIO);
      count_wait(rws, W_YF + (j >> 1) * 128, 56);
      STAMP(9);
#pragma unroll 1
      for (; rows; rows &= rows - 1, rows &= rows - 1) {
        const int s = __builtin_ctzll(rows);
        const int t = __builtin_amdgcn_readlane(sel.x, s);
        const float rs = __uint_as_float(__builtin_amdgcn_readlane(sel.y, s));
        f32x4 a[14];
#pragma unroll
        for (int k = 0; k < 14; ++k) a[k] = __builtin_bit_cast(f32x4, ld128c(rws, W_YP + (s * 16 + j) * (56 * 256) + lane * 16 + k * 1024));
        const float xf = bf2f(__builtin_amdgcn_raw_buffer_load_b16(rx, t * 2048 + j * 128 + fr * 8 + fq * 2, 0, 0));
        const float omg = rbf(1.f - __uint_as_float(lds_rd32(L_G + (t & 127) * 4)));
        float l0 = a[0][0], l1 = a[0][1], h0 = a[0][2], h1 = a[0][3];
#pragma unroll
        for (int k = 1; k < 14; ++k) { l0 = add1(l0, a[k][0]); l1 = add1(l1, a[k][1]); h0 = add1(h0, a[k][2]); h1 = add1(h1, a[k][3]); }
        asm volatile("s_nop 1" : "+v"(l0), "+v"(l1), "+v"(h0), "+v"(h1));
        auto p = __builtin_amdgcn_permlane16_swap(__float_as_uint(l0), __float_as_uint(l1), false, false);
        auto q = __builtin_amdgcn_permlane16_swap(__float_as_uint(h0), __float_as_uint(h1), false, false);
        const float u = __uint_as_float(p[0]) + __uint_as_float(p[1]), w = __uint_as_float(q[0]) + __uint_as_float(q[1]);
        auto r = __builtin_amdgcn_permlane32_swap(__float_as_uint(u), __float_as_uint(w), false, false);
        const float y = __uint_as_float(r[0]) + __uint_as_float(r[1]);
        lds_wr32(L_BROW + s * 256 + fr * 16 + fq * 4, __float_as_uint(mul1(omg, rbf(xf + rbf(mul1(rbf(y), rs))) - xf)));
      }
    }
    lds_wr32(FL_XD + l * 4, 1u);
    waitv<0>();
    STAMP(6);
    STAMPS();
    __builtin_amdgcn_endpgm();
  }

  __builtin_amdgcn_s_setprio(MPRIO);
  ARGREQ("\n\ts_waitcnt lgkmcnt(0)\n\ts_barrier");
  ARGS();
  unsigned tagA, tagB;
  int bud = SPIN_MAX * 4;
  const int tl0 = 32 * fq + 2 * wid;
  u32x2 xe[2][4], dv;
  {
    const rsrc_t rd = mk(Dskip, 2048);
    const unsigned dofs = (j * 64 + 4 * fr) * 2, xo = (32 * wid + 4 * fq) * 128 + ((fr >> 1) ^ (4 * (fq & 1))) * 16 + (fr & 1) * 8;
    const unsigned ks = (j - 4 * ((xcd >> 1) + 2 * (xcd & 1))) & 15;
    const int x0 = (fq ^ (fr & 7)) * 16, x1 = ((4 + fq) ^ (fr & 7)) * 16;
    const unsigned pa = 16384 + (wid * 16 + fr) * 128, pb = 131072 + fr * 128;
    unsigned pa0 = pa + x0, pa1 = pa + x1, pb0 = pb + x0, pb1 = pb + x1;
    const unsigned tq = L_TAB + (wid * 16 + fq * 4) * 4, tcl = L_TAB + 6 * 512 + (wid * 16 + fr) * 4, tcw = tcl + 512 + fq * 512;
    const unsigned l16 = HOFS(lane), pubo = (fq * 4 + (fr >> 2)) * 16, plo = fr * 16 + (3 - fq) * 1024;
    const unsigned sag = W_AGG + (j * 16 + c) * 1024 + wid * 256, sht = W_HT + (j * 4 + wid) * 128;
    unsigned sh = W_H + (c * 64 + 4 * j + wid) * 8192 * 2, fl = M_LAND, scn = M_CONS - 16 + wid * 4;
    unsigned sl[4];
    u64 em[4];
#pragma unroll
    for (int q = 0; q < 4; ++q) {
      sl[q] = sag - (4 + 4 * q) * 1024;
      const int n = min(max(c - 4 * q, 0), 4);
      em[q] = n == 4 ? ~0ull : (1ull << (16 * n)) - 1ull;
    }
    unsigned t_sk, t_s0, t_s1, t_s2, t_sx;
    asm volatile(ASM_MA : [pa0] "+&v"(pa0), [pa1] "+&v"(pa1), [pb0] "+&v"(pb0), [pb1] "+&v"(pb1), [fl] "+&v"(fl), [sk] "=&s"(t_sk), [s0] "=&s"(t_s0), [s1] "=&s"(t_s1), [s2] "=&s"(t_s2), [sx] "=&s"(t_sx), [bud] "+&s"(bud), [scn] "+&s"(scn), [sh] "+&s"(sh), [tgA] "=&s"(tagA), [tgB] "=&s"(tagB), [xe00] "=&v"(xe[0][0]), [xe01] "=&v"(xe[0][1]), [xe02] "=&v"(xe[0][2]), [xe03] "=&v"(xe[0][3]), [xe10] "=&v"(xe[1][0]), [xe11] "=&v"(xe[1][1]), [xe12] "=&v"(xe[1][2]), [xe13] "=&v"(xe[1][3]), [dv] "=&v"(dv) : [tq] "v"(tq), [tcl] "v"(tcl), [tcw] "v"(tcw), [pubo] "v"(pubo), [plo] "v"(plo), [l16] "v"(l16), [xo] "v"(xo), [dofs] "v"(dofs), [wrap] "s"(-98304), [wrap2] "s"(-131072), [srd] "s"(rws), [ks] "s"(ks), [srdd] "s"(rd), [srz] "s"(rz), [sag] "s"(sag), [mcar] "s"(M_CAR), [eh] "s"((1u << c) - 1u), [sht] "s"(sht), [sl0] "s"(sl[0]), [em0] "s"(em[0]), [sl1] "s"(sl[1]), [em1] "s"(em[1]), [sl2] "s"(sl[2]), [em2] "s"(em[2]), [sl3] "s"(sl[3]), [em3] "s"(em[3]) : "memory", "scc", "vcc", "m0", "v64", "v65", "v66", "v67", "v68", "v69", "v70", "v71", "v72", "v73", "v74", "v75", "v76", "v77", "v78", "v79", "v80", "v81", "v82", "v83", "v84", "v85", "v86", "v87", "v88", "v89", "v90", "v91", "v92", "v93", "v94", "v95", "v96", "v97", "v98", "v99", "v100", "v101", "v102", "v103", "v104", "v105", "v106", "v107", "v108", "v109", "v110", "v111", "v112", "v113", "v114", "v115", "v116", "v117", "v118", "v119", "v120", "v121", "v122", "v123", "v124", "v125", "v126", "v127", "v128", "v129", "v130", "v131", "v132", "v133", "v134", "v135", "v136", "v137", "v138", "v139", "v140", "v141", "v142", "v143", "v144", "v145", "v146", "v147", "v148", "v149", "v150", "v151", "v152", "v153", "v154", "v155", "v156", "v157", "v158", "v159", "v160", "v161", "v162", "v163", "v164", "v165", "v166", "v167", "v168", "v169", "v170", "v171", "v172", "v173", "v174", "v175", "v176", "v177", "v178", "v179", "v180", "v181", "v182", "v183", "v184", "v185", "v186", "v187", "v188", "v189", "v190", "v191", "v192", "v193", "v194", "v195", "v196", "v197", "v198", "v199", "v200", "v201", "v202", "v203", "v204", "v205", "v206", "v207", "v208", "v209", "v210", "v211", "v212", "v213", "v214", "v215", "v216", "v217", "v218", "v219", "v220", "v221", "v222", "v223", "v224", "v225", "v226", "v227", "v228", "v229", "v230", "v231", "v232", "v233", "v234", "v235", "v236", "v237", "v238", "v239", "v240", "v241", "v242", "v243", "v244", "v245", "v246", "v247", "v248", "v249", "v250", "v251", "v252", "v253", "v254", "v255");
  }
  f32x2 g2[4], base[4][4];
  unsigned cnt = 1, mine, dp[8];
  {
    unsigned selu, m0_, m1_;
#pragma unroll 1
    for (int it = 0; it < SPIN_MAX * 4; ++it) {
      asm volatile("ds_read_b32 %5, %7\n\tds_read_b32 %6, %7 offset:%10\n\tds_read_b32 %4, %8\n\tds_read_b64 %0, %9\n\tds_read_b64 %1, %9 offset:32\n\t"
                   "ds_read_b64 %2, %9 offset:64\n\tds_read_b64 %3, %9 offset:96\n\ts_waitcnt lgkmcnt(0)"
                   : "=&v"(g2[0]), "=&v"(g2[1]), "=&v"(g2[2]), "=&v"(g2[3]), "=&v"(selu), "=&v"(m0_), "=&v"(m1_)
                   : "v"(M_GT), "v"(L_SEL + (lane & 7) * 8), "v"(L_G + tl0 * 4), "n"(M_XS - M_GT) : "memory");
      if (__builtin_amdgcn_readfirstlane(m0_ | m1_) == 0u) break;
      __builtin_amdgcn_s_sleep(1);
    }
    f32x2 dd[4];
#pragma unroll
    for (int n = 0; n < 4; ++n) { const unsigned w = dv[n >> 1]; const float f = (n & 1) ? bfhi(w) : bf2f(w & 0xffffu); dd[n] = (f32x2){f, f}; }
#pragma unroll
    for (int r = 0; r < 4; ++r) {
      const unsigned ob = pk2(1.f - g2[r].x, 1.f - g2[r].y);
      const f32x2 omg = {bf2f(ob & 0xffffu), bfhi(ob)};
#pragma unroll
      for (int n = 0; n < 4; ++n) {
        const unsigned w0 = xe[0][r][n >> 1], w1 = xe[1][r][n >> 1];
        const f32x2 xf = {(n & 1) ? bfhi(w0) : bf2f(w0 & 0xffffu), (n & 1) ? bfhi(w1) : bf2f(w1 & 0xffffu)};
        base[r][n] = pkmul(xf, pkfma(g2[r], dd[n], omg));
      }
    }
#pragma unroll
    for (int r = 0; r < 4; ++r) asm volatile("" : "+v"(base[r][0]), "+v"(base[r][1]), "+v"(base[r][2]), "+v"(base[r][3]));
    asm volatile("s_nop 4\n\ts_waitcnt vmcnt(0)\n\ts_mov_b64 exec, 1\n\tbuffer_atomic_add %0, off, %1, %2 sc0 sc1\n\ts_mov_b64 exec, -1"
                 : "+v"(cnt) : "s"(rws), "s"(W_HF + (c * 2 + (j >> 3)) * 128) : "memory");
    STAMP(9);
    mine = (unsigned)__ballot(lane < 8 && (selu >> 7) == (unsigned)c && ((selu >> 1) & 3u) == (unsigned)wid);
#pragma unroll
    for (int q = 0; q < 8; ++q) dp[q] = L_ONE - 16;
#pragma unroll 1
    for (unsigned m = mine; m; m &= m - 1) {
      const int s = __builtin_ctz(m), t = __builtin_amdgcn_readlane((int)selu, s) & 127;
#pragma unroll
      for (int q = 0; q < 8; ++q) dp[q] = t == tl0 + 8 * (q >> 1) + (q & 1) ? L_BROW + s * 256 + fr * 16 : dp[q];
    }
  }
  f32x4 ac2[2][4];
  float bs[8][4];
#pragma unroll
  for (int q = 0; q < 8; ++q)
#pragma unroll
    for (int n = 0; n < 4; ++n) bs[q][n] = base[q >> 1][n][q & 1];
  {
#pragma unroll
    for (int m = 0; m < 2; ++m)
#pragma unroll
      for (int n = 0; n < 4; ++n) ac2[m][n] = (f32x4){0.f, 0.f, 0.f, 0.f};
    unsigned pc[4], fl2 = M_LAND + 26 * 8, vo = ((j & 8) * 32768 + wid * 2048) * 2 + HOFS(lane);
    const unsigned hr = M_HR, scn2 = M_CONS + 256 + wid * 4, hb_ = W_H + c * 524288 * 2, f9 = M_LAND + 17 * 8 + min(lane, 17) * 4;
#pragma unroll
    for (int q = 0; q < 4; ++q) pc[q] = fr * 128 + (((2 * q + (fq >> 1)) ^ ((fr >> 1) & 7)) * 16) + 8192 * (fq & 1);
    const unsigned sf = W_HF + (c * 2 + (j >> 3)) * 128, sf1 = sf ^ 128;
    unsigned t_sk, t_s0, t_s1;
    asm volatile(ASM_MB : [c00] "+&v"(ac2[0][0]), [c01] "+&v"(ac2[0][1]), [c02] "+&v"(ac2[0][2]), [c03] "+&v"(ac2[0][3]), [c10] "+&v"(ac2[1][0]), [c11] "+&v"(ac2[1][1]), [c12] "+&v"(ac2[1][2]), [c13] "+&v"(ac2[1][3]), [pc0] "+&v"(pc[0]), [pc1] "+&v"(pc[1]), [pc2] "+&v"(pc[2]), [pc3] "+&v"(pc[3]), [b00] "+&v"(bs[0][0]), [b01] "+&v"(bs[0][1]), [b02] "+&v"(bs[0][2]), [b03] "+&v"(bs[0][3]), [b10] "+&v"(bs[1][0]), [b11] "+&v"(bs[1][1]), [b12] "+&v"(bs[1][2]), [b13] "+&v"(bs[1][3]), [b20] "+&v"(bs[2][0]), [b21] "+&v"(bs[2][1]), [b22] "+&v"(bs[2][2]), [b23] "+&v"(bs[2][3]), [b30] "+&v"(bs[3][0]), [b31] "+&v"(bs[3][1]), [b32] "+&v"(bs[3][2]), [b33] "+&v"(bs[3][3]), [b40] "+&v"(bs[4][0]), [b41] "+&v"(bs[4][1]), [b42] "+&v"(bs[4][2]), [b43] "+&v"(bs[4][3]), [b50] "+&v"(bs[5][0]), [b51] "+&v"(bs[5][1]), [b52] "+&v"(bs[5][2]), [b53] "+&v"(bs[5][3]), [b60] "+&v"(bs[6][0]), [b61] "+&v"(bs[6][1]), [b62] "+&v"(bs[6][2]), [b63] "+&v"(bs[6][3]), [b70] "+&v"(bs[7][0]), [b71] "+&v"(bs[7][1]), [b72] "+&v"(bs[7][2]), [b73] "+&v"(bs[7][3]), [vo] "+&v"(vo), [fl] "+&v"(fl2), [sg] "=&s"(t_sk), [s0] "=&s"(t_s0), [s1] "=&s"(t_s1), [bud] "+&s"(bud) : [d0] "v"(dp[0]), [d1] "v"(dp[1]), [d2] "v"(dp[2]), [d3] "v"(dp[3]), [d4] "v"(dp[4]), [d5] "v"(dp[5]), [d6] "v"(dp[6]), [d7] "v"(dp[7]), [cnt] "v"(cnt), [xd] "v"(FL_XD), [mine] "s"(mine), [srd] "s"(rws), [srz] "s"(rz), [hr] "v"(hr), [f9] "v"(f9), [w] "s"(wid), [scn] "s"(scn2), [sf] "s"(sf), [sf1] "s"(sf1), [k2] "s"(0x4000), [h0] "s"(hb_ + 0), [h1] "s"(hb_ + 16384), [h2] "s"(hb_ + 32768), [h3] "s"(hb_ + 49152), [h4] "s"(hb_ + 65536), [h5] "s"(hb_ + 81920), [h6] "s"(hb_ + 98304), [h7] "s"(hb_ + 114688) : "memory", "scc", "vcc", "m0", "v64", "v65", "v66", "v67", "v68", "v69", "v70", "v71", "v72", "v73", "v74", "v75", "v76", "v77", "v78", "v79", "v80", "v81", "v82", "v83", "v84", "v85", "v86", "v87", "v88", "v89", "v90", "v91", "v92", "v93", "v94", "v95", "v96", "v97", "v98", "v99", "v100", "v101", "v102", "v103", "v104", "v105", "v106", "v107", "v108", "v109", "v110", "v111", "v112", "v113", "v114", "v115", "v116", "v117", "v118", "v119", "v120", "v121", "v122", "v123", "v124", "v125", "v126", "v127", "v128", "v129", "v130", "v131", "v132", "v133", "v134", "v135", "v136", "v137", "v138", "v139", "v140", "v141", "v142", "v143", "v144", "v145", "v146", "v147", "v148", "v149", "v150", "v151", "v152", "v153", "v154", "v155", "v156", "v157", "v158", "v159", "v160", "v161", "v162", "v163", "v164", "v165", "v166", "v167", "v168", "v169", "v170", "v171", "v172", "v173", "v174", "v175", "v176", "v177", "v178", "v179", "v180", "v181", "v182", "v183", "v184", "v185", "v186", "v187", "v188", "v189", "v190", "v191", "v192", "v193", "v194", "v195", "v196");
  }
  STAMP(6);
  asm volatile("" : "+v"(g2[0]), "+v"(g2[1]), "+v"(g2[2]), "+v"(g2[3]));
  int ln2 = lane;
  asm volatile("" : "+v"(ln2));
  const int fr2 = ln2 & 15, tl2 = 32 * (ln2 >> 4) + 2 * wid;
  f32x4 ov[2][4];
#pragma unroll
  for (int e = 0; e < 2; ++e)
#pragma unroll
    for (int r = 0; r < 4; ++r) {
      const float gg = 2.f * g2[r][e];
#pragma unroll
      for (int n = 0; n < 4; ++n) ov[e][r][n] = fma1(gg, ac2[e][n][r], bs[2 * r + e][n]);
    }
  {
    const rsrc_t ro = mk(out, 2048 * 1024 * 4);
    const unsigned oo = (c * 128 + tl2) * 4096 + (j * 64 + 4 * fr2) * 4, oo2 = oo + 4096;
    asm volatile("s_nop 4\n\t"
                 "buffer_store_dwordx4 %0, %8, %10, 0 offen" OST "\n\tbuffer_store_dwordx4 %4, %9, %10, 0 offen" OST "\n\t"
                 "buffer_store_dwordx4 %1, %8, %10, %11 offen" OST "\n\tbuffer_store_dwordx4 %5, %9, %10, %11 offen" OST "\n\t"
                 "buffer_store_dwordx4 %2, %8, %10, %12 offen" OST "\n\tbuffer_store_dwordx4 %6, %9, %10, %12 offen" OST "\n\t"
                 "buffer_store_dwordx4 %3, %8, %10, %13 offen" OST "\n\tbuffer_store_dwordx4 %7, %9, %10, %13 offen" OST "\n\t"
                 "s_nop 7\n\ts_nop 7"
                 ::"v"(ov[0][0]), "v"(ov[0][1]), "v"(ov[0][2]), "v"(ov[0][3]), "v"(ov[1][0]), "v"(ov[1][1]), "v"(ov[1][2]), "v"(ov[1][3]), "v"(oo), "v"(oo2), "s"(ro),
                 "s"(32768), "s"(65536), "s"(98304) : "memory");
  }
  if (c == 15 && j == 0 && wid == 0 && lane == 0) st64c(rws, W_EPOCH, (u32x2){tagA, tagB});
  STAMP(7);
  STAMPS();
#endif
}

extern "C" void kernel_launch(void* const* d_in, const int* in_sizes, int n_in, void* d_out, int out_size, void* d_ws, size_t ws_size,
                              hipStream_t stream) {
  (void)hipFuncSetAttribute((const void*)fused, hipFuncAttributeMaxDynamicSharedMemorySize, LDS_BYTES);
  fused<<<256, 512, LDS_BYTES, stream>>>((const u16*)d_in[0], (const u16*)d_in[1], (const u16*)d_in[2], (const u16*)d_in[3], (const u16*)d_in[4],
                                         (const u16*)d_in[5], (const u16*)d_in[6], (const u16*)d_in[7], (const u16*)d_in[8], (const u16*)d_in[10],
                                         (const u16*)d_in[11], (const u16*)d_in[12], (const u16*)d_in[13], (const u16*)d_in[14], (char*)d_ws + WS_BASE(ws_size),
                                         (float*)d_out);
}
```

```cpp
#include <hip/hip_runtime.h>
#include <hip/hip_bf16.h>
#include <stdint.h>

typedef __attribute__((ext_vector_type(8))) short bf16x8;
typedef __attribute__((ext_vector_type(4))) float f32x4;
typedef __attribute__((ext_vector_type(2))) float f32x2;
typedef __attribute__((ext_vector_type(4))) unsigned u32x4;
typedef __attribute__((ext_vector_type(2))) unsigned u32x2;
typedef unsigned short u16;
typedef unsigned long long u64;
#define LAS __attribute__((address_space(3)))
#define DEV __device__ __forceinline__

#define ASM_MA \
  "s_nop 4\n\t" \
  "buffer_load_dword v228, off, %[srd], 0 sc1\n\t" \
  "buffer_load_dwordx2 %[dv], %[dofs], %[srdd], 0 offen\n\t" \
  "s_mov_b32 %[sk], 0\n\t" \
  "s_mov_b32 %[sx], 0x20000\n\t" \
  "Lsp0_%=:\n\t" \
  "ds_read_b128 v[128:131], %[fl] offset:1056\n\t" \
  "s_sub_i32 %[bud], %[bud], 1\n\t" \
  "s_cmp_lt_i32 %[bud], 0\n\t" \
  "s_cbranch_scc1 Lb0_%=\n\t" \
  "s_waitcnt lgkmcnt(0)\n\t" \
  "v_or3_b32 v128, v128, v129, v130\n\t" \
  "v_or_b32 v128, v128, v131\n\t" \
  "s_nop 0\n\t" \
  "v_readfirstlane_b32 %[s0], v128\n\t" \
  "s_cmp_eq_u32 %[s0], 0\n\t" \
  "s_cbranch_scc0 Lsp0_%=\n\t" \
  "Lb0_%=:\n\t" \
  "s_cmp_eq_u32 %[sk], %[ks]\n\t" \
  "s_cbranch_scc1 Lx0_%=\n\t" \
  "Lxb0_%=:\n\t" \
  "s_mov_b32 m0, %[scn]\n\t" \
  "v_add_u32 %[fl], 8, %[fl]\n\t" \
  "ds_read_b128 v[208:211], %[pa0] offset:0\n\t" \
  "ds_read_b128 v[212:215], %[pa0] offset:8192\n\t" \
  "ds_read_b128 v[216:219], %[pa1] offset:0\n\t" \
  "ds_read_b128 v[220:223], %[pa1] offset:8192\n\t" \
  "ds_read_b128 v[176:179], %[pb0] offset:0\n\t" \
  "ds_read_b128 v[180:183], %[pb0] offset:2048\n\t" \
  "ds_read_b128 v[184:187], %[pb0] offset:4096\n\t" \
  "ds_read_b128 v[188:191], %[pb0] offset:6144\n\t" \
  "ds_read_b128 v[192:195], %[pb0] offset:8192\n\t" \
  "ds_read_b128 v[196:199], %[pb0] offset:10240\n\t" \
  "ds_read_b128 v[200:203], %[pb0] offset:12288\n\t" \
  "ds_read_b128 v[204:207], %[pb0] offset:14336\n\t" \
  "ds_read_b64 v[224:225], %[fl]\n\t" \
  "s_waitcnt lgkmcnt(7)\n\t" \
  "v_mfma_f32_16x16x32_bf16 v[64:67], v[208:211], v[176:179], 0\n\t" \
  "ds_read_b128 v[144:147], %[pb1] offset:0\n\t" \
  "s_add_u32 %[sk], %[sk], 1\n\t" \
  "v_mfma_f32_16x16x32_bf16 v[96:99], v[212:215], v[176:179], 0\n\t" \
  "s_and_b32 %[s1], %[sk], 3\n\t" \
  "s_cselect_b32 %[s1], 0x8000, %[wrap]\n\t" \
  "v_mfma_f32_16x16x32_bf16 v[68:71], v[208:211], v[180:183], 0\n\t" \
  "ds_read_b128 v[148:151], %[pb1] offset:2048\n\t" \
  "s_add_u32 %[sx], %[sx], 0x8000\n\t" \
  "v_mfma_f32_16x16x32_bf16 v[100:103], v[212:215], v[180:183], 0\n\t" \
  "s_cmp_eq_u32 %[sx], 0x28000\n\t" \
  "s_waitcnt lgkmcnt(7)\n\t" \
  "v_mfma_f32_16x16x32_bf16 v[72:75], v[208:211], v[184:187], 0\n\t" \
  "ds_read_b128 v[152:155], %[pb1] offset:4096\n\t" \
  "s_cselect_b32 %[s2], %[wrap2], 0x8000\n\t" \
  "v_mfma_f32_16x16x32_bf16 v[104:107], v[212:215], v[184:187], 0\n\t" \
  "s_cselect_b32 %[sx], 0, %[sx]\n\t" \
  "v_add_u32 %[pa0], %[s1], %[pa0]\n\t" \
  "v_mfma_f32_16x16x32_bf16 v[76:79], v[208:211], v[188:191], 0\n\t" \
  "ds_read_b128 v[156:159], %[pb1] offset:6144\n\t" \
  "v_add_u32 %[pb0], %[s2], %[pb0]\n\t" \
  "v_mfma_f32_16x16x32_bf16 v[108:111], v[212:215], v[188:191], 0\n\t" \
  "s_waitcnt lgkmcnt(5)\n\t" \
  "v_mfma_f32_16x16x32_bf16 v[80:83], v[208:211], v[192:195], 0\n\t" \
  "ds_read_b128 v[160:163], %[pb1] offset:8192\n\t" \
  "v_add_u32 %[pa1], %[s1], %[pa1]\n\t" \
  "v_mfma_f32_16x16x32_bf16 v[112:115], v[212:215], v[192:195], 0\n\t" \
  "s_mov_b64 exec, 1\n\t" \
  "buffer_load_dword %[l16], %[srz], 0 offen lds\n\t" \
  "s_mov_b64 exec, -1\n\t" \
  "v_mfma_f32_16x16x32_bf16 v[84:87], v[208:211], v[196:199], 0\n\t" \
  "ds_read_b128 v[164:167], %[pb1] offset:10240\n\t" \
  "s_add_u32 m0, m0, 16\n\t" \
  "v_mfma_f32_16x16x32_bf16 v[116:119], v[212:215], v[196:199], 0\n\t" \
  "s_waitcnt lgkmcnt(6)\n\t" \
  "v_or_b32 v224, v224, v225\n\t" \
  "v_mfma_f32_16x16x32_bf16 v[88:91], v[208:211], v[200:203], 0\n\t" \
  "ds_read_b128 v[168:171], %[pb1] offset:12288\n\t" \
  "v_readfirstlane_b32 %[s0], v224\n\t" \
  "v_mfma_f32_16x16x32_bf16 v[120:123], v[212:215], v[200:203], 0\n\t" \
  "s_cmp_eq_u32 %[s0], 0\n\t" \
  "s_cbranch_scc0 Lsp1_%=\n\t" \
  "Lb1_%=:\n\t" \
  "v_mfma_f32_16x16x32_bf16 v[92:95], v[208:211], v[204:207], 0\n\t" \
  "ds_read_b128 v[172:175], %[pb1] offset:14336\n\t" \
  "v_add_u32 %[fl], 8, %[fl]\n\t" \
  "v_mfma_f32_16x16x32_bf16 v[124:127], v[212:215], v[204:207], 0\n\t" \
  "s_branch Lmid_%=\n\t" \
  "Lloop_%=:\n\t" \
  "s_waitcnt lgkmcnt(6)\n\t" \
  "v_mfma_f32_16x16x32_bf16 v[64:67], v[136:139], v[144:147], v[64:67]\n\t" \
  "ds_read_b128 v[208:211], %[pa0] offset:0\n\t" \
  "v_mfma_f32_16x16x32_bf16 v[96:99], v[140:143], v[144:147], v[96:99]\n\t" \
  "ds_read_b128 v[212:215], %[pa0] offset:8192\n\t" \
  "v_mfma_f32_16x16x32_bf16 v[68:71], v[136:139], v[148:151], v[68:71]\n\t" \
  "ds_read_b128 v[216:219], %[pa1] offset:0\n\t" \
  "v_mfma_f32_16x16x32_bf16 v[100:103], v[140:143], v[148:151], v[100:103]\n\t" \
  "ds_read_b128 v[220:223], %[pa1] offset:8192\n\t" \
  "s_waitcnt lgkmcnt(8)\n\t" \
  "v_mfma_f32_16x16x32_bf16 v[72:75], v[136:139], v[152:155], v[72:75]\n\t" \
  "ds_read_b128 v[176:179], %[pb0] offset:0\n\t" \
  "v_mfma_f32_16x16x32_bf16 v[104:107], v[140:143], v[152:155], v[104:107]\n\t" \
  "ds_read_b128 v[180:183], %[pb0] offset:2048\n\t" \
  "v_mfma_f32_16x16x32_bf16 v[76:79], v[136:139], v[156:159], v[76:79]\n\t" \
  "v_add_u32 %[pb1], %[s2], %[pb1]\n\t" \
  "v_mfma_f32_16x16x32_bf16 v[108:111], v[140:143], v[156:159], v[108:111]\n\t" \
  "ds_read_b128 v[184:187], %[pb0] offset:4096\n\t" \
  "s_waitcnt lgkmcnt(7)\n\t" \
  "v_mfma_f32_16x16x32_bf16 v[80:83], v[136:139], v[160:163], v[80:83]\n\t" \
  "ds_read_b128 v[188:191], %[pb0] offset:6144\n\t" \
  "v_mfma_f32_16x16x32_bf16 v[112:115], v[140:143], v[160:163], v[112:115]\n\t" \
  "ds_read_b128 v[192:195], %[pb0] offset:8192\n\t" \
  "v_mfma_f32_16x16x32_bf16 v[84:87], v[136:139], v[164:167], v[84:87]\n\t" \
  "s_waitcnt lgkmcnt(5)\n\t" \
  "s_mov_b64 exec, 1\n\t" \
  "buffer_load_dword %[l16], %[srz], 0 offen lds\n\t" \
  "s_mov_b64 exec, -1\n\t" \
  "v_mfma_f32_16x16x32_bf16 v[116:119], v[140:143], v[164:167], v[116:119]\n\t" \
  "ds_read_b128 v[196:199], %[pb0] offset:10240\n\t" \
  "s_add_u32 m0, m0, 16\n\t" \
  "v_mfma_f32_16x16x32_bf16 v[88:91], v[136:139], v[168:171], v[88:91]\n\t" \
  "s_cmp_eq_u32 %[sk], %[ks]\n\t" \
  "s_cbranch_scc1 Lx2_%=\n\t" \
  "Lxb2_%=:\n\t" \
  "v_mfma_f32_16x16x32_bf16 v[120:123], v[140:143], v[168:171], v[120:123]\n\t" \
  "ds_read_b128 v[200:203], %[pb0] offset:12288\n\t" \
  "v_mfma_f32_16x16x32_bf16 v[92:95], v[136:139], v[172:175], v[92:95]\n\t" \
  "ds_read_b128 v[204:207], %[pb0] offset:14336\n\t" \
  "v_mfma_f32_16x16x32_bf16 v[124:127], v[140:143], v[172:175], v[124:127]\n\t" \
  "Lnx5_%=:\n\t" \
  "ds_read_b64 v[224:225], %[fl]\n\t" \
  "s_waitcnt lgkmcnt(7)\n\t" \
  "v_mfma_f32_16x16x32_bf16 v[64:67], v[208:211], v[176:179], v[64:67]\n\t" \
  "ds_read_b128 v[144:147], %[pb1] offset:0\n\t" \
  "s_add_u32 %[sk], %[sk], 1\n\t" \
  "v_mfma_f32_16x16x32_bf16 v[96:99], v[212:215], v[176:179], v[96:99]\n\t" \
  "s_and_b32 %[s1], %[sk], 3\n\t" \
  "s_cselect_b32 %[s1], 0x8000, %[wrap]\n\t" \
  "v_mfma_f32_16x16x32_bf16 v[68:71], v[208:211], v[180:183], v[68:71]\n\t" \
  "ds_read_b128 v[148:151], %[pb1] offset:2048\n\t" \
  "s_add_u32 %[sx], %[sx], 0x8000\n\t" \
  "v_mfma_f32_16x16x32_bf16 v[100:103], v[212:215], v[180:183], v[100:103]\n\t" \
  "s_cmp_eq_u32 %[sx], 0x28000\n\t" \
  "s_waitcnt lgkmcnt(7)\n\t" \
  "v_mfma_f32_16x16x32_bf16 v[72:75], v[208:211], v[184:187], v[72:75]\n\t" \
  "ds_read_b128 v[152:155], %[pb1] offset:4096\n\t" \
  "s_cselect_b32 %[s2], %[wrap2], 0x8000\n\t" \
  "v_mfma_f32_16x16x32_bf16 v[104:107], v[212:215], v[184:187], v[104:107]\n\t" \
  "s_cselect_b32 %[sx], 0, %[sx]\n\t" \
  "v_add_u32 %[pa0], %[s1], %[pa0]\n\t" \
  "v_mfma_f32_16x16x32_bf16 v[76:79], v[208:211], v[188:191], v[76:79]\n\t" \
  "ds_read_b128 v[156:159], %[pb1] offset:6144\n\t" \
  "v_add_u32 %[pb0], %[s2], %[pb0]\n\t" \
  "v_mfma_f32_16x16x32_bf16 v[108:111], v[212:215], v[188:191], v[108:111]\n\t" \
  "s_waitcnt lgkmcnt(5)\n\t" \
  "v_mfma_f32_16x16x32_bf16 v[80:83], v[208:211], v[192:195], v[80:83]\n\t" \
  "ds_read_b128 v[160:163], %[pb1] offset:8192\n\t" \
  "v_add_u32 %[pa1], %[s1], %[pa1]\n\t" \
  "v_mfma_f32_16x16x32_bf16 v[112:115], v[212:215], v[192:195], v[112:115]\n\t" \
  "v_mfma_f32_16x16x32_bf16 v[84:87], v[208:211], v[196:199], v[84:87]\n\t" \
  "ds_read_b128 v[164:167], %[pb1] offset:10240\n\t" \
  "v_mfma_f32_16x16x32_bf16 v[116:119], v[212:215], v[196:199], v[116:119]\n\t" \
  "s_waitcnt lgkmcnt(6)\n\t" \
  "v_or_b32 v224, v224, v225\n\t" \
  "v_mfma_f32_16x16x32_bf16 v[88:91], v[208:211], v[200:203], v[88:91]\n\t" \
  "ds_read_b128 v[168:171], %[pb1] offset:12288\n\t" \
  "v_readfirstlane_b32 %[s0], v224\n\t" \
  "v_mfma_f32_16x16x32_bf16 v[120:123], v[212:215], v[200:203], v[120:123]\n\t" \
  "s_cmp_eq_u32 %[s0], 0\n\t" \
  "s_cbranch_scc0 Lsp3_%=\n\t" \
  "Lb3_%=:\n\t" \
  "v_mfma_f32_16x16x32_bf16 v[92:95], v[208:211], v[204:207], v[92:95]\n\t" \
  "ds_read_b128 v[172:175], %[pb1] offset:14336\n\t" \
  "v_add_u32 %[fl], 8, %[fl]\n\t" \
  "v_mfma_f32_16x16x32_bf16 v[124:127], v[212:215], v[204:207], v[124:127]\n\t" \
  "Lmid_%=:\n\t" \
  "s_waitcnt lgkmcnt(6)\n\t" \
  "v_mfma_f32_16x16x32_bf16 v[64:67], v[216:219], v[144:147], v[64:67]\n\t" \
  "ds_read_b128 v[128:131], %[pa0] offset:0\n\t" \
  "v_mfma_f32_16x16x32_bf16 v[96:99], v[220:223], v[144:147], v[96:99]\n\t" \
  "ds_read_b128 v[132:135], %[pa0] offset:8192\n\t" \
  "v_mfma_f32_16x16x32_bf16 v[68:71], v[216:219], v[148:151], v[68:71]\n\t" \
  "ds_read_b128 v[136:139], %[pa1] offset:0\n\t" \
  "v_mfma_f32_16x16x32_bf16 v[100:103], v[220:223], v[148:151], v[100:103]\n\t" \
  "ds_read_b128 v[140:143], %[pa1] offset:8192\n\t" \
  "s_waitcnt lgkmcnt(8)\n\t" \
  "v_mfma_f32_16x16x32_bf16 v[72:75], v[216:219], v[152:155], v[72:75]\n\t" \
  "ds_read_b128 v[176:179], %[pb0] offset:0\n\t" \
  "v_mfma_f32_16x16x32_bf16 v[104:107], v[220:223], v[152:155], v[104:107]\n\t" \
  "ds_read_b128 v[180:183], %[pb0] offset:2048\n\t" \
  "v_mfma_f32_16x16x32_bf16 v[76:79], v[216:219], v[156:159], v[76:79]\n\t" \
  "v_add_u32 %[pb1], %[s2], %[pb1]\n\t" \
  "v_mfma_f32_16x16x32_bf16 v[108:111], v[220:223], v[156:159], v[108:111]\n\t" \
  "ds_read_b128 v[184:187], %[pb0] offset:4096\n\t" \
  "s_waitcnt lgkmcnt(7)\n\t" \
  "v_mfma_f32_16x16x32_bf16 v[80:83], v[216:219], v[160:163], v[80:83]\n\t" \
  "ds_read_b128 v[188:191], %[pb0] offset:6144\n\t" \
  "v_mfma_f32_16x16x32_bf16 v[112:115], v[220:223], v[160:163], v[112:115]\n\t" \
  "ds_read_b128 v[192:195], %[pb0] offset:8192\n\t" \
  "v_mfma_f32_16x16x32_bf16 v[84:87], v[216:219], v[164:167], v[84:87]\n\t" \
  "s_waitcnt lgkmcnt(5)\n\t" \
  "s_mov_b64 exec, 1\n\t" \
  "buffer_load_dword %[l16], %[srz], 0 offen lds\n\t" \
  "s_mov_b64 exec, -1\n\t" \
  "v_mfma_f32_16x16x32_bf16 v[116:119], v[220:223], v[164:167], v[116:119]\n\t" \
  "ds_read_b128 v[196:199], %[pb0] offset:10240\n\t" \
  "s_add_u32 m0, m0, 16\n\t" \
  "v_mfma_f32_16x16x32_bf16 v[88:91], v[216:219], v[168:171], v[88:91]\n\t" \
  "s_cmp_eq_u32 %[sk], %[ks]\n\t" \
  "s_cbranch_scc1 Lx4_%=\n\t" \
  "Lxb4_%=:\n\t" \
  "v_mfma_f32_16x16x32_bf16 v[120:123], v[220:223], v[168:171], v[120:123]\n\t" \
  "ds_read_b128 v[200:203], %[pb0] offset:12288\n\t" \
  "v_mfma_f32_16x16x32_bf16 v[92:95], v[216:219], v[172:175], v[92:95]\n\t" \
  "ds_read_b128 v[204:207], %[pb0] offset:14336\n\t" \
  "v_mfma_f32_16x16x32_bf16 v[124:127], v[220:223], v[172:175], v[124:127]\n\t" \
  "Lnx3_%=:\n\t" \
  "ds_read_b64 v[224:225], %[fl]\n\t" \
  "s_waitcnt lgkmcnt(7)\n\t" \
  "v_mfma_f32_16x16x32_bf16 v[64:67], v[128:131], v[176:179], v[64:67]\n\t" \
  "ds_read_b128 v[144:147], %[pb1] offset:0\n\t" \
  "s_add_u32 %[sk], %[sk], 1\n\t" \
  "v_mfma_f32_16x16x32_bf16 v[96:99], v[132:135], v[176:179], v[96:99]\n\t" \
  "s_and_b32 %[s1], %[sk], 3\n\t" \
  "s_cselect_b32 %[s1], 0x8000, %[wrap]\n\t" \
  "v_mfma_f32_16x16x32_bf16 v[68:71], v[128:131], v[180:183], v[68:71]\n\t" \
  "ds_read_b128 v[148:151], %[pb1] offset:2048\n\t" \
  "s_add_u32 %[sx], %[sx], 0x8000\n\t" \
  "v_mfma_f32_16x16x32_bf16 v[100:103], v[132:135], v[180:183], v[100:103]\n\t" \
  "s_cmp_eq_u32 %[sx], 0x28000\n\t" \
  "s_waitcnt lgkmcnt(7)\n\t" \
  "v_mfma_f32_16x16x32_bf16 v[72:75], v[128:131], v[184:187], v[72:75]\n\t" \
  "ds_read_b128 v[152:155], %[pb1] offset:4096\n\t" \
  "s_cselect_b32 %[s2], %[wrap2], 0x8000\n\t" \
  "v_mfma_f32_16x16x32_bf16 v[104:107], v[132:135], v[184:187], v[104:107]\n\t" \
  "s_cselect_b32 %[sx], 0, %[sx]\n\t" \
  "v_add_u32 %[pa0], %[s1], %[pa0]\n\t" \
  "v_mfma_f32_16x16x32_bf16 v[76:79], v[128:131], v[188:191], v[76:79]\n\t" \
  "ds_read_b128 v[156:159], %[pb1] offset:6144\n\t" \
  "v_add_u32 %[pb0], %[s2], %[pb0]\n\t" \
  "v_mfma_f32_16x16x32_bf16 v[108:111], v[132:135], v[188:191], v[108:111]\n\t" \
  "s_waitcnt lgkmcnt(5)\n\t" \
  "v_mfma_f32_16x16x32_bf16 v[80:83], v[128:131], v[192:195], v[80:83]\n\t" \
  "ds_read_b128 v[160:163], %[pb1] offset:8192\n\t" \
  "v_add_u32 %[pa1], %[s1], %[pa1]\n\t" \
  "v_mfma_f32_16x16x32_bf16 v[112:115], v[132:135], v[192:195], v[112:115]\n\t" \
  "v_mfma_f32_16x16x32_bf16 v[84:87], v[128:131], v[196:199], v[84:87]\n\t" \
  "ds_read_b128 v[164:167], %[pb1] offset:10240\n\t" \
  "v_mfma_f32_16x16x32_bf16 v[116:119], v[132:135], v[196:199], v[116:119]\n\t" \
  "s_waitcnt lgkmcnt(6)\n\t" \
  "v_or_b32 v224, v224, v225\n\t" \
  "v_mfma_f32_16x16x32_bf16 v[88:91], v[128:131], v[200:203], v[88:91]\n\t" \
  "ds_read_b128 v[168:171], %[pb1] offset:12288\n\t" \
  "v_readfirstlane_b32 %[s0], v224\n\t" \
  "v_mfma_f32_16x16x32_bf16 v[120:123], v[132:135], v[200:203], v[120:123]\n\t" \
  "s_cmp_eq_u32 %[s0], 0\n\t" \
  "s_cbranch_scc0 Lsp5_%=\n\t" \
  "Lb5_%=:\n\t" \
  "v_mfma_f32_16x16x32_bf16 v[92:95], v[128:131], v[204:207], v[92:95]\n\t" \
  "ds_read_b128 v[172:175], %[pb1] offset:14336\n\t" \
  "v_add_u32 %[fl], 8, %[fl]\n\t" \
  "v_mfma_f32_16x16x32_bf16 v[124:127], v[132:135], v[204:207], v[124:127]\n\t" \
  "s_cmp_lt_u32 %[sk], 16\n\t" \
  "s_cbranch_scc1 Lloop_%=\n\t" \
  "s_waitcnt lgkmcnt(6)\n\t" \
  "v_mfma_f32_16x16x32_bf16 v[64:67], v[136:139], v[144:147], v[64:67]\n\t" \
  "ds_read_b128 v[176:179], %[tq] offset:0\n\t" \
  "v_mfma_f32_16x16x32_bf16 v[96:99], v[140:143], v[144:147], v[96:99]\n\t" \
  "ds_read_b128 v[180:183], %[tq] offset:256\n\t" \
  "v_mfma_f32_16x16x32_bf16 v[68:71], v[136:139], v[148:151], v[68:71]\n\t" \
  "ds_read_b128 v[192:195], %[tq] offset:1024\n\t" \
  "v_mfma_f32_16x16x32_bf16 v[100:103], v[140:143], v[148:151], v[100:103]\n\t" \
  "ds_read_b128 v[196:199], %[tq] offset:1280\n\t" \
  "s_waitcnt lgkmcnt(8)\n\t" \
  "v_mfma_f32_16x16x32_bf16 v[72:75], v[136:139], v[152:155], v[72:75]\n\t" \
  "ds_read_b128 v[200:203], %[tq] offset:1536\n\t" \
  "v_mfma_f32_16x16x32_bf16 v[104:107], v[140:143], v[152:155], v[104:107]\n\t" \
  "ds_read_b128 v[204:207], %[tq] offset:1792\n\t" \
  "v_mfma_f32_16x16x32_bf16 v[76:79], v[136:139], v[156:159], v[76:79]\n\t" \
  "ds_read_b128 v[208:211], %[tq] offset:2048\n\t" \
  "v_mfma_f32_16x16x32_bf16 v[108:111], v[140:143], v[156:159], v[108:111]\n\t" \
  "ds_read_b128 v[212:215], %[tq] offset:2304\n\t" \
  "s_waitcnt lgkmcnt(8)\n\t" \
  "v_mfma_f32_16x16x32_bf16 v[80:83], v[136:139], v[160:163], v[80:83]\n\t" \
  "ds_read_b128 v[216:219], %[tq] offset:2560\n\t" \
  "v_mfma_f32_16x16x32_bf16 v[112:115], v[140:143], v[160:163], v[112:115]\n\t" \
  "ds_read_b128 v[220:223], %[tq] offset:2816\n\t" \
  "v_mfma_f32_16x16x32_bf16 v[84:87], v[136:139], v[164:167], v[84:87]\n\t" \
  "s_mov_b64 exec, 1\n\t" \
  "buffer_load_dword %[l16], %[srz], 0 offen lds\n\t" \
  "s_mov_b64 exec, -1\n\t" \
  "v_mfma_f32_16x16x32_bf16 v[116:119], v[140:143], v[164:167], v[116:119]\n\t" \
  "ds_read_b128 v[184:187], %[tq] offset:512\n\t" \
  "s_add_u32 m0, m0, 16\n\t" \
  "v_mfma_f32_16x16x32_bf16 v[88:91], v[136:139], v[168:171], v[88:91]\n\t" \
  "ds_read_b128 v[188:191], %[tq] offset:768\n\t" \
  "v_mfma_f32_16x16x32_bf16 v[120:123], v[140:143], v[168:171], v[120:123]\n\t" \
  "ds_read2_b32 v[230:231], %[tcl] offset0:0 offset1:64\n\t" \
  "v_mfma_f32_16x16x32_bf16 v[92:95], v[136:139], v[172:175], v[92:95]\n\t" \
  "ds_read2_b32 v[232:233], %[tcw] offset0:0 offset1:64\n\t" \
  "v_mfma_f32_16x16x32_bf16 v[124:127], v[140:143], v[172:175], v[124:127]\n\t" \
  "s_setprio 3\n\t" \
  "s_waitcnt vmcnt(8) lgkmcnt(4)\n\t" \
  "v_readfirstlane_b32 %[tgB], v228\n\t" \
  "s_add_u32 %[tgA], %[tgB], 1\n\t" \
  "s_add_u32 %[tgB], %[tgB], 0x40000001\n\t" \
  "v_pk_fma_f32 v[244:245], v[180:181], v[96:97], v[68:69] neg_lo:[1,0,0] neg_hi:[1,0,0]\n\t" \
  "v_pk_fma_f32 v[246:247], v[182:183], v[98:99], v[70:71] neg_lo:[1,0,0] neg_hi:[1,0,0]\n\t" \
  "v_pk_fma_f32 v[248:249], v[180:181], v[64:65], v[100:101]\n\t" \
  "v_pk_fma_f32 v[250:251], v[182:183], v[66:67], v[102:103]\n\t" \
  "v_pk_fma_f32 v[236:237], v[176:177], v[64:65], v[244:245]\n\t" \
  "v_pk_fma_f32 v[238:239], v[178:179], v[66:67], v[246:247]\n\t" \
  "v_pk_fma_f32 v[240:241], v[176:177], v[96:97], v[248:249]\n\t" \
  "v_pk_fma_f32 v[242:243], v[178:179], v[98:99], v[250:251]\n\t" \
  "v_pk_fma_f32 v[244:245], v[180:181], v[240:241], v[72:73] neg_lo:[1,0,0] neg_hi:[1,0,0]\n\t" \
  "v_pk_fma_f32 v[246:247], v[182:183], v[242:243], v[74:75] neg_lo:[1,0,0] neg_hi:[1,0,0]\n\t" \
  "v_pk_fma_f32 v[248:249], v[180:181], v[236:237], v[104:105]\n\t" \
  "v_pk_fma_f32 v[250:251], v[182:183], v[238:239], v[106:107]\n\t" \
  "v_pk_fma_f32 v[236:237], v[176:177], v[236:237], v[244:245]\n\t" \
  "v_pk_fma_f32 v[238:239], v[178:179], v[238:239], v[246:247]\n\t" \
  "v_pk_fma_f32 v[240:241], v[176:177], v[240:241], v[248:249]\n\t" \
  "v_pk_fma_f32 v[242:243], v[178:179], v[242:243], v[250:251]\n\t" \
  "v_pk_fma_f32 v[244:245], v[180:181], v[240:241], v[76:77] neg_lo:[1,0,0] neg_hi:[1,0,0]\n\t" \
  "v_pk_fma_f32 v[246:247], v[182:183], v[242:243], v[78:79] neg_lo:[1,0,0] neg_hi:[1,0,0]\n\t" \
  "v_pk_fma_f32 v[248:249], v[180:181], v[236:237], v[108:109]\n\t" \
  "v_pk_fma_f32 v[250:251], v[182:183], v[238:239], v[110:111]\n\t" \
  "v_pk_fma_f32 v[236:237], v[176:177], v[236:237], v[244:245]\n\t" \
  "v_pk_fma_f32 v[238:239], v[178:179], v[238:239], v[246:247]\n\t" \
  "v_pk_fma_f32 v[240:241], v[176:177], v[240:241], v[248:249]\n\t" \
  "v_pk_fma_f32 v[242:243], v[178:179], v[242:243], v[250:251]\n\t" \
  "v_pk_fma_f32 v[244:245], v[180:181], v[240:241], v[80:81] neg_lo:[1,0,0] neg_hi:[1,0,0]\n\t" \
  "v_pk_fma_f32 v[246:247], v[182:183], v[242:243], v[82:83] neg_lo:[1,0,0] neg_hi:[1,0,0]\n\t" \
  "v_pk_fma_f32 v[248:249], v[180:181], v[236:237], v[112:113]\n\t" \
  "v_pk_fma_f32 v[250:251], v[182:183], v[238:239], v[114:115]\n\t" \
  "v_pk_fma_f32 v[236:237], v[176:177], v[236:237], v[244:245]\n\t" \
  "v_pk_fma_f32 v[238:239], v[178:179], v[238:239], v[246:247]\n\t" \
  "v_pk_fma_f32 v[240:241], v[176:177], v[240:241], v[248:249]\n\t" \
  "v_pk_fma_f32 v[242:243], v[178:179], v[242:243], v[250:251]\n\t" \
  "v_pk_fma_f32 v[244:245], v[180:181], v[240:241], v[84:85] neg_lo:[1,0,0] neg_hi:[1,0,0]\n\t" \
  "v_pk_fma_f32 v[246:247], v[182:183], v[242:243], v[86:87] neg_lo:[1,0,0] neg_hi:[1,0,0]\n\t" \
  "v_pk_fma_f32 v[248:249], v[180:181], v[236:237], v[116:117]\n\t" \
  "v_pk_fma_f32 v[250:251], v[182:183], v[238:239], v[118:119]\n\t" \
  "v_pk_fma_f32 v[236:237], v[176:177], v[236:237], v[244:245]\n\t" \
  "v_pk_fma_f32 v[238:239], v[178:179], v[238:239], v[246:247]\n\t" \
  "v_pk_fma_f32 v[240:241], v[176:177], v[240:241], v[248:249]\n\t" \
  "v_pk_fma_f32 v[242:243], v[178:179], v[242:243], v[250:251]\n\t" \
  "v_pk_fma_f32 v[244:245], v[180:181], v[240:241], v[88:89] neg_lo:[1,0,0] neg_hi:[1,0,0]\n\t" \
  "v_pk_fma_f32 v[246:247], v[182:183], v[242:243], v[90:91] neg_lo:[1,0,0] neg_hi:[1,0,0]\n\t" \
  "v_pk_fma_f32 v[248:249], v[180:181], v[236:237], v[120:121]\n\t" \
  "v_pk_fma_f32 v[250:251], v[182:183], v[238:239], v[122:123]\n\t" \
  "v_pk_fma_f32 v[236:237], v[176:177], v[236:237], v[244:245]\n\t" \
  "v_pk_fma_f32 v[238:239], v[178:179], v[238:239], v[246:247]\n\t" \
  "v_pk_fma_f32 v[240:241], v[176:177], v[240:241], v[248:249]\n\t" \
  "v_pk_fma_f32 v[242:243], v[178:179], v[242:243], v[250:251]\n\t" \
  "v_pk_fma_f32 v[244:245], v[180:181], v[240:241], v[92:93] neg_lo:[1,0,0] neg_hi:[1,0,0]\n\t" \
  "v_pk_fma_f32 v[246:247], v[182:183], v[242:243], v[94:95] neg_lo:[1,0,0] neg_hi:[1,0,0]\n\t" \
  "v_pk_fma_f32 v[248:249], v[180:181], v[236:237], v[124:125]\n\t" \
  "v_pk_fma_f32 v[250:251], v[182:183], v[238:239], v[126:127]\n\t" \
  "v_pk_fma_f32 v[236:237], v[176:177], v[236:237], v[244:245]\n\t" \
  "v_pk_fma_f32 v[238:239], v[178:179], v[238:239], v[246:247]\n\t" \
  "v_pk_fma_f32 v[240:241], v[176:177], v[240:241], v[248:249]\n\t" \
  "v_pk_fma_f32 v[242:243], v[178:179], v[242:243], v[250:251]\n\t" \
  "s_add_u32 %[s0], %[eh], 1\n\t" \
  "v_mov_b32 v226, %[s0]\n\t" \
  "v_mov_b32 v253, %[tgA]\n\t" \
  "v_mov_b32 v255, %[tgB]\n\t" \
  "v_mov_b32_dpp v244, v236 row_shr:1 row_mask:0xf bank_mask:0xf bound_ctrl:0\n\t" \
  "v_mov_b32_dpp v245, v237 row_shr:1 row_mask:0xf bank_mask:0xf bound_ctrl:0\n\t" \
  "v_mov_b32_dpp v246, v238 row_shr:1 row_mask:0xf bank_mask:0xf bound_ctrl:0\n\t" \
  "v_mov_b32_dpp v247, v239 row_shr:1 row_mask:0xf bank_mask:0xf bound_ctrl:0\n\t" \
  "v_mov_b32_dpp v248, v240 row_shr:1 row_mask:0xf bank_mask:0xf bound_ctrl:0\n\t" \
  "v_mov_b32_dpp v249, v241 row_shr:1 row_mask:0xf bank_mask:0xf bound_ctrl:0\n\t" \
  "v_mov_b32_dpp v250, v242 row_shr:1 row_mask:0xf bank_mask:0xf bound_ctrl:0\n\t" \
  "v_mov_b32_dpp v251, v243 row_shr:1 row_mask:0xf bank_mask:0xf bound_ctrl:0\n\t" \
  "v_pk_fma_f32 v[236:237], v[192:193], v[244:245], v[236:237]\n\t" \
  "v_pk_fma_f32 v[238:239], v[194:195], v[246:247], v[238:239]\n\t" \
  "v_pk_fma_f32 v[240:241], v[192:193], v[248:249], v[240:241]\n\t" \
  "v_pk_fma_f32 v[242:243], v[194:195], v[250:251], v[242:243]\n\t" \
  "v_pk_fma_f32 v[236:237], v[196:197], v[248:249], v[236:237] neg_lo:[1,0,0] neg_hi:[1,0,0]\n\t" \
  "v_pk_fma_f32 v[238:239], v[198:199], v[250:251], v[238:239] neg_lo:[1,0,0] neg_hi:[1,0,0]\n\t" \
  "v_pk_fma_f32 v[240:241], v[196:197], v[244:245], v[240:241]\n\t" \
  "v_pk_fma_f32 v[242:243], v[198:199], v[246:247], v[242:243]\n\t" \
  "v_mov_b32_dpp v244, v236 row_shr:2 row_mask:0xf bank_mask:0xf bound_ctrl:0\n\t" \
  "v_mov_b32_dpp v245, v237 row_shr:2 row_mask:0xf bank_mask:0xf bound_ctrl:0\n\t" \
  "v_mov_b32_dpp v246, v238 row_shr:2 row_mask:0xf bank_mask:0xf bound_ctrl:0\n\t" \
  "v_mov_b32_dpp v247, v239 row_shr:2 row_mask:0xf bank_mask:0xf bound_ctrl:0\n\t" \
  "v_mov_b32_dpp v248, v240 row_shr:2 row_mask:0xf bank_mask:0xf bound_ctrl:0\n\t" \
  "v_mov_b32_dpp v249, v241 row_shr:2 row_mask:0xf bank_mask:0xf bound_ctrl:0\n\t" \
  "v_mov_b32_dpp v250, v242 row_shr:2 row_mask:0xf bank_mask:0xf bound_ctrl:0\n\t" \
  "v_mov_b32_dpp v251, v243 row_shr:2 row_mask:0xf bank_mask:0xf bound_ctrl:0\n\t" \
  "v_pk_fma_f32 v[236:237], v[200:201], v[244:245], v[236:237]\n\t" \
  "v_pk_fma_f32 v[238:239], v[202:203], v[246:247], v[238:239]\n\t" \
  "v_pk_fma_f32 v[240:241], v[200:201], v[248:249], v[240:241]\n\t" \
  "v_pk_fma_f32 v[242:243], v[202:203], v[250:251], v[242:243]\n\t" \
  "v_pk_fma_f32 v[236:237], v[204:205], v[248:249], v[236:237] neg_lo:[1,0,0] neg_hi:[1,0,0]\n\t" \
  "v_pk_fma_f32 v[238:239], v[206:207], v[250:251], v[238:239] neg_lo:[1,0,0] neg_hi:[1,0,0]\n\t" \
  "v_pk_fma_f32 v[240:241], v[204:205], v[244:245], v[240:241]\n\t" \
  "v_pk_fma_f32 v[242:243], v[206:207], v[246:247], v[242:243]\n\t" \
  "v_mov_b32_dpp v244, v236 row_shr:4 row_mask:0xf bank_mask:0xf bound_ctrl:0\n\t" \
  "v_mov_b32_dpp v245, v237 row_shr:4 row_mask:0xf bank_mask:0xf bound_ctrl:0\n\t" \
  "v_mov_b32_dpp v246, v238 row_shr:4 row_mask:0xf bank_mask:0xf bound_ctrl:0\n\t" \
  "v_mov_b32_dpp v247, v239 row_shr:4 row_mask:0xf bank_mask:0xf bound_ctrl:0\n\t" \
  "v_mov_b32_dpp v248, v240 row_shr:4 row_mask:0xf bank_mask:0xf bound_ctrl:0\n\t" \
  "v_mov_b32_dpp v249, v241 row_shr:4 row_mask:0xf bank_mask:0xf bound_ctrl:0\n\t" \
  "v_mov_b32_dpp v250, v242 row_shr:4 row_mask:0xf bank_mask:0xf bound_ctrl:0\n\t" \
  "v_mov_b32_dpp v251, v243 row_shr:4 row_mask:0xf bank_mask:0xf bound_ctrl:0\n\t" \
  "v_pk_fma_f32 v[236:237], v[208:209], v[244:245], v[236:237]\n\t" \
  "v_pk_fma_f32 v[238:239], v[210:211], v[246:247], v[238:239]\n\t" \
  "v_pk_fma_f32 v[240:241], v[208:209], v[248:249], v[240:241]\n\t" \
  "v_pk_fma_f32 v[242:243], v[210:211], v[250:251], v[242:243]\n\t" \
  "v_pk_fma_f32 v[236:237], v[212:213], v[248:249], v[236:237] neg_lo:[1,0,0] neg_hi:[1,0,0]\n\t" \
  "v_pk_fma_f32 v[238:239], v[214:215], v[250:251], v[238:239] neg_lo:[1,0,0] neg_hi:[1,0,0]\n\t" \
  "v_pk_fma_f32 v[240:241], v[212:213], v[244:245], v[240:241]\n\t" \
  "v_pk_fma_f32 v[242:243], v[214:215], v[246:247], v[242:243]\n\t" \
  "v_mov_b32_dpp v244, v236 row_shr:8 row_mask:0xf bank_mask:0xf bound_ctrl:0\n\t" \
  "v_mov_b32_dpp v245, v237 row_shr:8 row_mask:0xf bank_mask:0xf bound_ctrl:0\n\t" \
  "v_mov_b32_dpp v246, v238 row_shr:8 row_mask:0xf bank_mask:0xf bound_ctrl:0\n\t" \
  "v_mov_b32_dpp v247, v239 row_shr:8 row_mask:0xf bank_mask:0xf bound_ctrl:0\n\t" \
  "v_mov_b32_dpp v248, v240 row_shr:8 row_mask:0xf bank_mask:0xf bound_ctrl:0\n\t" \
  "v_mov_b32_dpp v249, v241 row_shr:8 row_mask:0xf bank_mask:0xf bound_ctrl:0\n\t" \
  "v_mov_b32_dpp v250, v242 row_shr:8 row_mask:0xf bank_mask:0xf bound_ctrl:0\n\t" \
  "v_mov_b32_dpp v251, v243 row_shr:8 row_mask:0xf bank_mask:0xf bound_ctrl:0\n\t" \
  "v_pk_fma_f32 v[236:237], v[216:217], v[244:245], v[236:237]\n\t" \
  "v_pk_fma_f32 v[238:239], v[218:219], v[246:247], v[238:239]\n\t" \
  "v_pk_fma_f32 v[240:241], v[216:217], v[248:249], v[240:241]\n\t" \
  "v_pk_fma_f32 v[242:243], v[218:219], v[250:251], v[242:243]\n\t" \
  "v_pk_fma_f32 v[236:237], v[220:221], v[248:249], v[236:237] neg_lo:[1,0,0] neg_hi:[1,0,0]\n\t" \
  "v_pk_fma_f32 v[238:239], v[222:223], v[250:251], v[238:239] neg_lo:[1,0,0] neg_hi:[1,0,0]\n\t" \
  "v_pk_fma_f32 v[240:241], v[220:221], v[244:245], v[240:241]\n\t" \
  "v_pk_fma_f32 v[242:243], v[222:223], v[246:247], v[242:243]\n\t" \
  "v_mov_b32_dpp v252, v236 row_newbcast:15 row_mask:0xf bank_mask:0x1\n\t" \
  "v_mov_b32_dpp v252, v237 row_newbcast:15 row_mask:0xf bank_mask:0x2\n\t" \
  "v_mov_b32_dpp v252, v238 row_newbcast:15 row_mask:0xf bank_mask:0x4\n\t" \
  "v_mov_b32_dpp v252, v239 row_newbcast:15 row_mask:0xf bank_mask:0x8\n\t" \
  "v_mov_b32_dpp v254, v240 row_newbcast:15 row_mask:0xf bank_mask:0x1\n\t" \
  "v_mov_b32_dpp v254, v241 row_newbcast:15 row_mask:0xf bank_mask:0x2\n\t" \
  "v_mov_b32_dpp v254, v242 row_newbcast:15 row_mask:0xf bank_mask:0x4\n\t" \
  "v_mov_b32_dpp v254, v243 row_newbcast:15 row_mask:0xf bank_mask:0x8\n\t" \
  "s_mov_b32 exec_lo, 0x11111111\n\t" \
  "s_mov_b32 exec_hi, 0x11111111\n\t" \
  "buffer_store_dwordx4 v[252:255], %[pubo], %[srd], %[sag] offen sc1\n\t" \
  "s_mov_b64 exec, 1\n\t" \
  "buffer_atomic_or v226, off, %[srd], %[sht] sc0 sc1\n\t" \
  "s_mov_b64 exec, -1\n\t" \
  "v_mov_b32 v160, 0\n\t" \
  "v_mov_b32 v161, %[tgA]\n\t" \
  "v_mov_b32 v162, 0\n\t" \
  "v_mov_b32 v163, %[tgB]\n\t" \
  "v_mov_b32 v164, 0\n\t" \
  "v_mov_b32 v165, %[tgA]\n\t" \
  "v_mov_b32 v166, 0\n\t" \
  "v_mov_b32 v167, %[tgB]\n\t" \
  "v_mov_b32 v168, 0\n\t" \
  "v_mov_b32 v169, %[tgA]\n\t" \
  "v_mov_b32 v170, 0\n\t" \
  "v_mov_b32 v171, %[tgB]\n\t" \
  "v_mov_b32 v172, 0\n\t" \
  "v_mov_b32 v173, %[tgA]\n\t" \
  "v_mov_b32 v174, 0\n\t" \
  "v_mov_b32 v175, %[tgB]\n\t" \
  "s_waitcnt lgkmcnt(0)\n\t" \
  "v_mov_b32_dpp v244, v236 row_shr:1 row_mask:0xf bank_mask:0xf bound_ctrl:0\n\t" \
  "v_mov_b32_dpp v245, v237 row_shr:1 row_mask:0xf bank_mask:0xf bound_ctrl:0\n\t" \
  "v_mov_b32_dpp v246, v238 row_shr:1 row_mask:0xf bank_mask:0xf bound_ctrl:0\n\t" \
  "v_mov_b32_dpp v247, v239 row_shr:1 row_mask:0xf bank_mask:0xf bound_ctrl:0\n\t" \
  "v_mov_b32_dpp v248, v240 row_shr:1 row_mask:0xf bank_mask:0xf bound_ctrl:0\n\t" \
  "v_mov_b32_dpp v249, v241 row_shr:1 row_mask:0xf bank_mask:0xf bound_ctrl:0\n\t" \
  "v_mov_b32_dpp v250, v242 row_shr:1 row_mask:0xf bank_mask:0xf bound_ctrl:0\n\t" \
  "v_mov_b32_dpp v251, v243 row_shr:1 row_mask:0xf bank_mask:0xf bound_ctrl:0\n\t" \
  "v_pk_mul_f32 v[236:237], v[184:185], v[244:245]\n\t" \
  "v_pk_mul_f32 v[238:239], v[186:187], v[246:247]\n\t" \
  "v_pk_mul_f32 v[240:241], v[184:185], v[248:249]\n\t" \
  "v_pk_mul_f32 v[242:243], v[186:187], v[250:251]\n\t" \
  "v_pk_fma_f32 v[236:237], v[188:189], v[248:249], v[236:237] neg_lo:[1,0,0] neg_hi:[1,0,0]\n\t" \
  "v_pk_fma_f32 v[238:239], v[190:191], v[250:251], v[238:239] neg_lo:[1,0,0] neg_hi:[1,0,0]\n\t" \
  "v_pk_fma_f32 v[240:241], v[188:189], v[244:245], v[240:241]\n\t" \
  "v_pk_fma_f32 v[242:243], v[190:191], v[246:247], v[242:243]\n\t" \
  "v_pk_mul_f32 v[128:129], v[184:185], v[64:65]\n\t" \
  "v_pk_mul_f32 v[130:131], v[186:187], v[66:67]\n\t" \
  "v_pk_fma_f32 v[128:129], v[188:189], v[96:97], v[128:129] neg_lo:[1,0,0] neg_hi:[1,0,0]\n\t" \
  "v_pk_fma_f32 v[130:131], v[190:191], v[98:99], v[130:131] neg_lo:[1,0,0] neg_hi:[1,0,0]\n\t" \
  "v_pk_mul_f32 v[96:97], v[184:185], v[96:97]\n\t" \
  "v_pk_mul_f32 v[98:99], v[186:187], v[98:99]\n\t" \
  "v_pk_fma_f32 v[96:97], v[188:189], v[64:65], v[96:97] neg_lo:[1,0,1] neg_hi:[1,0,1]\n\t" \
  "v_pk_fma_f32 v[98:99], v[190:191], v[66:67], v[98:99] neg_lo:[1,0,1] neg_hi:[1,0,1]\n\t" \
  "v_pk_mul_f32 v[132:133], v[184:185], v[68:69]\n\t" \
  "v_pk_mul_f32 v[134:135], v[186:187], v[70:71]\n\t" \
  "v_pk_fma_f32 v[132:133], v[188:189], v[100:101], v[132:133] neg_lo:[1,0,0] neg_hi:[1,0,0]\n\t" \
  "v_pk_fma_f32 v[134:135], v[190:191], v[102:103], v[134:135] neg_lo:[1,0,0] neg_hi:[1,0,0]\n\t" \
  "v_pk_mul_f32 v[100:101], v[184:185], v[100:101]\n\t" \
  "v_pk_mul_f32 v[102:103], v[186:187], v[102:103]\n\t" \
  "v_pk_fma_f32 v[100:101], v[188:189], v[68:69], v[100:101] neg_lo:[1,0,1] neg_hi:[1,0,1]\n\t" \
  "v_pk_fma_f32 v[102:103], v[190:191], v[70:71], v[102:103] neg_lo:[1,0,1] neg_hi:[1,0,1]\n\t" \
  "v_pk_mul_f32 v[136:137], v[184:185], v[72:73]\n\t" \
  "v_pk_mul_f32 v[138:139], v[186:187], v[74:75]\n\t" \
  "v_pk_fma_f32 v[136:137], v[188:189], v[104:105], v[136:137] neg_lo:[1,0,0] neg_hi:[1,0,0]\n\t" \
  "v_pk_fma_f32 v[138:139], v[190:191], v[106:107], v[138:139] neg_lo:[1,0,0] neg_hi:[1,0,0]\n\t" \
  "v_pk_mul_f32 v[104:105], v[184:185], v[104:105]\n\t" \
  "v_pk_mul_f32 v[106:107], v[186:187], v[106:107]\n\t" \
  "v_pk_fma_f32 v[104:105], v[188:189], v[72:73], v[104:105] neg_lo:[1,0,1] neg_hi:[1,0,1]\n\t" \
  "v_pk_fma_f32 v[106:107], v[190:191], v[74:75], v[106:107] neg_lo:[1,0,1] neg_hi:[1,0,1]\n\t" \
  "v_pk_mul_f32 v[140:141], v[184:185], v[76:77]\n\t" \
  "v_pk_mul_f32 v[142:143], v[186:187], v[78:79]\n\t" \
  "v_pk_fma_f32 v[140:141], v[188:189], v[108:109], v[140:141] neg_lo:[1,0,0] neg_hi:[1,0,0]\n\t" \
  "v_pk_fma_f32 v[142:143], v[190:191], v[110:111], v[142:143] neg_lo:[1,0,0] neg_hi:[1,0,0]\n\t" \
  "v_pk_mul_f32 v[108:109], v[184:185], v[108:109]\n\t" \
  "v_pk_mul_f32 v[110:111], v[186:187], v[110:111]\n\t" \
  "v_pk_fma_f32 v[108:109], v[188:189], v[76:77], v[108:109] neg_lo:[1,0,1] neg_hi:[1,0,1]\n\t" \
  "v_pk_fma_f32 v[110:111], v[190:191], v[78:79], v[110:111] neg_lo:[1,0,1] neg_hi:[1,0,1]\n\t" \
  "v_pk_mul_f32 v[144:145], v[184:185], v[80:81]\n\t" \
  "v_pk_mul_f32 v[146:147], v[186:187], v[82:83]\n\t" \
  "v_pk_fma_f32 v[144:145], v[188:189], v[112:113], v[144:145] neg_lo:[1,0,0] neg_hi:[1,0,0]\n\t" \
  "v_pk_fma_f32 v[146:147], v[190:191], v[114:115], v[146:147] neg_lo:[1,0,0] neg_hi:[1,0,0]\n\t" \
  "v_pk_mul_f32 v[112:113], v[184:185], v[112:113]\n\t" \
  "v_pk_mul_f32 v[114:115], v[186:187], v[114:115]\n\t" \
  "v_pk_fma_f32 v[112:113], v[188:189], v[80:81], v[112:113] neg_lo:[1,0,1] neg_hi:[1,0,1]\n\t" \
  "v_pk_fma_f32 v[114:115], v[190:191], v[82:83], v[114:115] neg_lo:[1,0,1] neg_hi:[1,0,1]\n\t" \
  "v_pk_mul_f32 v[148:149], v[184:185], v[84:85]\n\t" \
  "v_pk_mul_f32 v[150:151], v[186:187], v[86:87]\n\t" \
  "v_pk_fma_f32 v[148:149], v[188:189], v[116:117], v[148:149] neg_lo:[1,0,0] neg_hi:[1,0,0]\n\t" \
  "v_pk_fma_f32 v[150:151], v[190:191], v[118:119], v[150:151] neg_lo:[1,0,0] neg_hi:[1,0,0]\n\t" \
  "v_pk_mul_f32 v[116:117], v[184:185], v[116:117]\n\t" \
  "v_pk_mul_f32 v[118:119], v[186:187], v[118:119]\n\t" \
  "v_pk_fma_f32 v[116:117], v[188:189], v[84:85], v[116:117] neg_lo:[1,0,1] neg_hi:[1,0,1]\n\t" \
  "v_pk_fma_f32 v[118:119], v[190:191], v[86:87], v[118:119] neg_lo:[1,0,1] neg_hi:[1,0,1]\n\t" \
  "v_pk_mul_f32 v[152:153], v[184:185], v[88:89]\n\t" \
  "v_pk_mul_f32 v[154:155], v[186:187], v[90:91]\n\t" \
  "v_pk_fma_f32 v[152:153], v[188:189], v[120:121], v[152:153] neg_lo:[1,0,0] neg_hi:[1,0,0]\n\t" \
  "v_pk_fma_f32 v[154:155], v[190:191], v[122:123], v[154:155] neg_lo:[1,0,0] neg_hi:[1,0,0]\n\t" \
  "v_pk_mul_f32 v[120:121], v[184:185], v[120:121]\n\t" \
  "v_pk_mul_f32 v[122:123], v[186:187], v[122:123]\n\t" \
  "v_pk_fma_f32 v[120:121], v[188:189], v[88:89], v[120:121] neg_lo:[1,0,1] neg_hi:[1,0,1]\n\t" \
  "v_pk_fma_f32 v[122:123], v[190:191], v[90:91], v[122:123] neg_lo:[1,0,1] neg_hi:[1,0,1]\n\t" \
  "v_pk_mul_f32 v[156:157], v[184:185], v[92:93]\n\t" \
  "v_pk_mul_f32 v[158:159], v[186:187], v[94:95]\n\t" \
  "v_pk_fma_f32 v[156:157], v[188:189], v[124:125], v[156:157] neg_lo:[1,0,0] neg_hi:[1,0,0]\n\t" \
  "v_pk_fma_f32 v[158:159], v[190:191], v[126:127], v[158:159] neg_lo:[1,0,0] neg_hi:[1,0,0]\n\t" \
  "v_pk_mul_f32 v[124:125], v[184:185], v[124:125]\n\t" \
  "v_pk_mul_f32 v[126:127], v[186:187], v[126:127]\n\t" \
  "v_pk_fma_f32 v[124:125], v[188:189], v[92:93], v[124:125] neg_lo:[1,0,1] neg_hi:[1,0,1]\n\t" \
  "v_pk_fma_f32 v[126:127], v[190:191], v[94:95], v[126:127] neg_lo:[1,0,1] neg_hi:[1,0,1]\n\t" \
  "s_waitcnt vmcnt(0)\n\t" \
  "v_readfirstlane_b32 %[s0], v226\n\t" \
  "s_andn2_b32 %[s0], %[eh], %[s0]\n\t" \
  "s_cselect_b32 %[s1], 32, 0\n\t" \
  "s_cbranch_scc0 Lh1_%=\n\t" \
  "v_mov_b32 v226, 0\n\t" \
  "s_mov_b64 exec, 1\n\t" \
  "buffer_atomic_or v226, off, %[srd], %[sht] sc0 sc1\n\t" \
  "s_mov_b64 exec, -1\n\t" \
  "s_branch Lpa_%=\n\t" \
  "Lh1_%=:\n\t" \
  "s_mov_b64 exec, %[em0]\n\t" \
  "s_cbranch_execz Lpa_%=\n\t" \
  "buffer_load_dwordx4 v[160:163], %[plo], %[srd], %[sl0] offen sc1\n\t" \
  "s_mov_b64 exec, %[em1]\n\t" \
  "s_cbranch_execz Lpa_%=\n\t" \
  "buffer_load_dwordx4 v[164:167], %[plo], %[srd], %[sl1] offen sc1\n\t" \
  "s_mov_b64 exec, %[em2]\n\t" \
  "s_cbranch_execz Lpa_%=\n\t" \
  "buffer_load_dwordx4 v[168:171], %[plo], %[srd], %[sl2] offen sc1\n\t" \
  "s_mov_b64 exec, %[em3]\n\t" \
  "s_cbranch_execz Lpa_%=\n\t" \
  "buffer_load_dwordx4 v[172:175], %[plo], %[srd], %[sl3] offen sc1\n\t" \
  "Lpa_%=:\n\t" \
  "s_mov_b64 exec, -1\n\t" \
  "s_mov_b32 exec_lo, 0xaaaaaaaa\n\t" \
  "s_mov_b32 exec_hi, 0xaaaaaaaa\n\t" \
  "v_pk_mul_f32 v[244:245], v[196:197], v[188:189]\n\t" \
  "v_pk_mul_f32 v[246:247], v[198:199], v[190:191]\n\t" \
  "v_pk_mul_f32 v[248:249], v[196:197], v[184:185]\n\t" \
  "v_pk_mul_f32 v[250:251], v[198:199], v[186:187]\n\t" \
  "v_pk_fma_f32 v[184:185], v[192:193], v[184:185], v[244:245] neg_lo:[0,0,1] neg_hi:[0,0,1]\n\t" \
  "v_pk_fma_f32 v[186:187], v[194:195], v[186:187], v[246:247] neg_lo:[0,0,1] neg_hi:[0,0,1]\n\t" \
  "v_pk_fma_f32 v[188:189], v[192:193], v[188:189], v[248:249]\n\t" \
  "v_pk_fma_f32 v[190:191], v[194:195], v[190:191], v[250:251]\n\t" \
  "s_mov_b32 exec_lo, 0xcccccccc\n\t" \
  "s_mov_b32 exec_hi, 0xcccccccc\n\t" \
  "v_pk_mul_f32 v[244:245], v[204:205], v[188:189]\n\t" \
  "v_pk_mul_f32 v[246:247], v[206:207], v[190:191]\n\t" \
  "v_pk_mul_f32 v[248:249], v[204:205], v[184:185]\n\t" \
  "v_pk_mul_f32 v[250:251], v[206:207], v[186:187]\n\t" \
  "v_pk_fma_f32 v[184:185], v[200:201], v[184:185], v[244:245] neg_lo:[0,0,1] neg_hi:[0,0,1]\n\t" \
  "v_pk_fma_f32 v[186:187], v[202:203], v[186:187], v[246:247] neg_lo:[0,0,1] neg_hi:[0,0,1]\n\t" \
  "v_pk_fma_f32 v[188:189], v[200:201], v[188:189], v[248:249]\n\t" \
  "v_pk_fma_f32 v[190:191], v[202:203], v[190:191], v[250:251]\n\t" \
  "s_mov_b32 exec_lo, 0xf0f0f0f0\n\t" \
  "s_mov_b32 exec_hi, 0xf0f0f0f0\n\t" \
  "v_pk_mul_f32 v[244:245], v[212:213], v[188:189]\n\t" \
  "v_pk_mul_f32 v[246:247], v[214:215], v[190:191]\n\t" \
  "v_pk_mul_f32 v[248:249], v[212:213], v[184:185]\n\t" \
  "v_pk_mul_f32 v[250:251], v[214:215], v[186:187]\n\t" \
  "v_pk_fma_f32 v[184:185], v[208:209], v[184:185], v[244:245] neg_lo:[0,0,1] neg_hi:[0,0,1]\n\t" \
  "v_pk_fma_f32 v[186:187], v[210:211], v[186:187], v[246:247] neg_lo:[0,0,1] neg_hi:[0,0,1]\n\t" \
  "v_pk_fma_f32 v[188:189], v[208:209], v[188:189], v[248:249]\n\t" \
  "v_pk_fma_f32 v[190:191], v[210:211], v[190:191], v[250:251]\n\t" \
  "s_mov_b32 exec_lo, 0xff00ff00\n\t" \
  "s_mov_b32 exec_hi, 0xff00ff00\n\t" \
  "v_pk_mul_f32 v[244:245], v[220:221], v[188:189]\n\t" \
  "v_pk_mul_f32 v[246:247], v[222:223], v[190:191]\n\t" \
  "v_pk_mul_f32 v[248:249], v[220:221], v[184:185]\n\t" \
  "v_pk_mul_f32 v[250:251], v[222:223], v[186:187]\n\t" \
  "v_pk_fma_f32 v[184:185], v[216:217], v[184:185], v[244:245] neg_lo:[0,0,1] neg_hi:[0,0,1]\n\t" \
  "v_pk_fma_f32 v[186:187], v[218:219], v[186:187], v[246:247] neg_lo:[0,0,1] neg_hi:[0,0,1]\n\t" \
  "v_pk_fma_f32 v[188:189], v[216:217], v[188:189], v[248:249]\n\t" \
  "v_pk_fma_f32 v[190:191], v[218:219], v[190:191], v[250:251]\n\t" \
  "s_mov_b64 exec, -1\n\t" \
  "s_cmp_eq_u32 %[s1], 0\n\t" \
  "s_cbranch_scc1 Lq_%=\n\t" \
  "Lh2_%=:\n\t" \
  "s_waitcnt vmcnt(0)\n\t" \
  "v_readfirstlane_b32 %[s0], v226\n\t" \
  "s_andn2_b32 %[s0], %[eh], %[s0]\n\t" \
  "s_cbranch_scc0 Lh3_%=\n\t" \
  "s_sub_u32 %[s1], %[s1], 1\n\t" \
  "s_cbranch_scc1 Lh3_%=\n\t" \
  "s_sleep 1\n\t" \
  "v_mov_b32 v226, 0\n\t" \
  "s_mov_b64 exec, 1\n\t" \
  "buffer_atomic_or v226, off, %[srd], %[sht] sc0 sc1\n\t" \
  "s_mov_b64 exec, -1\n\t" \
  "s_branch Lh2_%=\n\t" \
  "Lh3_%=:\n\t" \
  "s_mov_b64 exec, %[em0]\n\t" \
  "s_cbranch_execz Lpc_%=\n\t" \
  "buffer_load_dwordx4 v[160:163], %[plo], %[srd], %[sl0] offen sc1\n\t" \
  "s_mov_b64 exec, %[em1]\n\t" \
  "s_cbranch_execz Lpc_%=\n\t" \
  "buffer_load_dwordx4 v[164:167], %[plo], %[srd], %[sl1] offen sc1\n\t" \
  "s_mov_b64 exec, %[em2]\n\t" \
  "s_cbranch_execz Lpc_%=\n\t" \
  "buffer_load_dwordx4 v[168:171], %[plo], %[srd], %[sl2] offen sc1\n\t" \
  "s_mov_b64 exec, %[em3]\n\t" \
  "s_cbranch_execz Lpc_%=\n\t" \
  "buffer_load_dwordx4 v[172:175], %[plo], %[srd], %[sl3] offen sc1\n\t" \
  "Lpc_%=:\n\t" \
  "s_mov_b64 exec, -1\n\t" \
  "Lq_%=:\n\t" \
  "s_waitcnt vmcnt(0)\n\t" \
  "v_cmpx_eq_u32 vcc, %[tgA], v161\n\t" \
  "v_cmpx_eq_u32 vcc, %[tgB], v163\n\t" \
  "v_cmpx_eq_u32 vcc, %[tgA], v165\n\t" \
  "v_cmpx_eq_u32 vcc, %[tgB], v167\n\t" \
  "v_cmpx_eq_u32 vcc, %[tgA], v169\n\t" \
  "v_cmpx_eq_u32 vcc, %[tgB], v171\n\t" \
  "v_cmpx_eq_u32 vcc, %[tgA], v173\n\t" \
  "v_cmpx_eq_u32 vcc, %[tgB], v175\n\t" \
  "s_cmp_eq_u64 exec, -1\n\t" \
  "s_mov_b64 exec, -1\n\t" \
  "s_cbranch_scc1 Lq2_%=\n\t" \
  "s_sub_i32 %[bud], %[bud], 16\n\t" \
  "s_cmp_lt_i32 %[bud], 0\n\t" \
  "s_cbranch_scc1 Lq2_%=\n\t" \
  "s_sleep 4\n\t" \
  "s_mov_b64 exec, %[em0]\n\t" \
  "s_cbranch_execz Lpb_%=\n\t" \
  "buffer_load_dwordx4 v[160:163], %[plo], %[srd], %[sl0] offen sc1\n\t" \
  "s_mov_b64 exec, %[em1]\n\t" \
  "s_cbranch_execz Lpb_%=\n\t" \
  "buffer_load_dwordx4 v[164:167], %[plo], %[srd], %[sl1] offen sc1\n\t" \
  "s_mov_b64 exec, %[em2]\n\t" \
  "s_cbranch_execz Lpb_%=\n\t" \
  "buffer_load_dwordx4 v[168:171], %[plo], %[srd], %[sl2] offen sc1\n\t" \
  "s_mov_b64 exec, %[em3]\n\t" \
  "s_cbranch_execz Lpb_%=\n\t" \
  "buffer_load_dwordx4 v[172:175], %[plo], %[srd], %[sl3] offen sc1\n\t" \
  "Lpb_%=:\n\t" \
  "s_mov_b64 exec, -1\n\t" \
  "s_branch Lq_%=\n\t" \
  "Lq2_%=:\n\t" \
  "s_mov_b32 %[scn], %[mcar]\n\t" \
  "s_mov_b32 m0, %[scn]\n\t" \
  "s_mov_b64 exec, 1\n\t" \
  "buffer_load_dword %[l16], %[srz], 0 offen lds\n\t" \
  "s_mov_b64 exec, -1\n\t" \
  "v_fma_f32 v244, -v231, v174, v168\n\t" \
  "v_fma_f32 v245, v231, v172, v170\n\t" \
  "v_fma_f32 v252, v230, v172, v244\n\t" \
  "v_fma_f32 v253, v230, v174, v245\n\t" \
  "v_fma_f32 v244, -v231, v253, v164\n\t" \
  "v_fma_f32 v245, v231, v252, v166\n\t" \
  "v_fma_f32 v252, v230, v252, v244\n\t" \
  "v_fma_f32 v253, v230, v253, v245\n\t" \
  "v_fma_f32 v244, -v231, v253, v160\n\t" \
  "v_fma_f32 v245, v231, v252, v162\n\t" \
  "v_fma_f32 v252, v230, v252, v244\n\t" \
  "v_fma_f32 v253, v230, v253, v245\n\t" \
  "v_mul_f32 v244, v233, v253\n\t" \
  "v_mul_f32 v245, v233, v252\n\t" \
  "v_fma_f32 v254, v232, v252, -v244\n\t" \
  "v_fma_f32 v255, v232, v253, v245\n\t" \
  "v_fma_f32 v252, v232, v252, -v244\n\t" \
  "v_fma_f32 v253, v232, v253, v245\n\t" \
  "s_nop 1\n\t" \
  "v_permlane16_swap_b32 v252, v254\n\t" \
  "v_permlane16_swap_b32 v253, v255\n\t" \
  "s_nop 0\n\t" \
  "v_add_f32 v244, v252, v254\n\t" \
  "v_add_f32 v245, v253, v255\n\t" \
  "v_add_f32 v252, v252, v254\n\t" \
  "v_add_f32 v253, v253, v255\n\t" \
  "v_mov_b32 v254, v244\n\t" \
  "v_mov_b32 v255, v245\n\t" \
  "s_nop 1\n\t" \
  "v_permlane32_swap_b32 v252, v254\n\t" \
  "v_permlane32_swap_b32 v253, v255\n\t" \
  "s_nop 0\n\t" \
  "v_add_f32 v244, v252, v254\n\t" \
  "v_add_f32 v245, v253, v255\n\t" \
  "v_add_f32 v252, v252, v254\n\t" \
  "v_add_f32 v253, v253, v255\n\t" \
  "v_mov_b32 v254, v244\n\t" \
  "v_mov_b32 v255, v245\n\t" \
  "v_mov_b32_dpp v254, v252 row_ror:12 row_mask:0x2 bank_mask:0xf\n\t" \
  "v_mov_b32_dpp v255, v253 row_ror:12 row_mask:0x2 bank_mask:0xf\n\t" \
  "v_mov_b32_dpp v254, v252 row_ror:8 row_mask:0x4 bank_mask:0xf\n\t" \
  "v_mov_b32_dpp v255, v253 row_ror:8 row_mask:0x4 bank_mask:0xf\n\t" \
  "v_mov_b32_dpp v254, v252 row_ror:4 row_mask:0x8 bank_mask:0xf\n\t" \
  "v_mov_b32_dpp v255, v253 row_ror:4 row_mask:0x8 bank_mask:0xf\n\t" \
  "s_nop 1\n\t" \
  "v_mov_b32_dpp v244, v254 row_newbcast:0 row_mask:0xf bank_mask:0xf\n\t" \
  "v_mov_b32_dpp v245, v254 row_newbcast:1 row_mask:0xf bank_mask:0xf\n\t" \
  "v_mov_b32_dpp v246, v254 row_newbcast:2 row_mask:0xf bank_mask:0xf\n\t" \
  "v_mov_b32_dpp v247, v254 row_newbcast:3 row_mask:0xf bank_mask:0xf\n\t" \
  "v_mov_b32_dpp v248, v255 row_newbcast:0 row_mask:0xf bank_mask:0xf\n\t" \
  "v_mov_b32_dpp v249, v255 row_newbcast:1 row_mask:0xf bank_mask:0xf\n\t" \
  "v_mov_b32_dpp v250, v255 row_newbcast:2 row_mask:0xf bank_mask:0xf\n\t" \
  "v_mov_b32_dpp v251, v255 row_newbcast:3 row_mask:0xf bank_mask:0xf\n\t" \
  "v_pk_fma_f32 v[236:237], v[184:185], v[244:245], v[236:237]\n\t" \
  "v_pk_fma_f32 v[238:239], v[186:187], v[246:247], v[238:239]\n\t" \
  "v_pk_fma_f32 v[240:241], v[184:185], v[248:249], v[240:241]\n\t" \
  "v_pk_fma_f32 v[242:243], v[186:187], v[250:251], v[242:243]\n\t" \
  "v_pk_fma_f32 v[236:237], v[188:189], v[248:249], v[236:237] neg_lo:[1,0,0] neg_hi:[1,0,0]\n\t" \
  "v_pk_fma_f32 v[238:239], v[190:191], v[250:251], v[238:239] neg_lo:[1,0,0] neg_hi:[1,0,0]\n\t" \
  "v_pk_fma_f32 v[240:241], v[188:189], v[244:245], v[240:241]\n\t" \
  "v_pk_fma_f32 v[242:243], v[190:191], v[246:247], v[242:243]\n\t" \
  "v_pk_fma_f32 v[128:129], v[180:181], v[240:241], v[128:129] neg_lo:[1,0,0] neg_hi:[1,0,0]\n\t" \
  "v_pk_fma_f32 v[130:131], v[182:183], v[242:243], v[130:131] neg_lo:[1,0,0] neg_hi:[1,0,0]\n\t" \
  "v_pk_fma_f32 v[96:97], v[180:181], v[236:237], v[96:97] neg_lo:[1,0,0] neg_hi:[1,0,0]\n\t" \
  "v_pk_fma_f32 v[98:99], v[182:183], v[238:239], v[98:99] neg_lo:[1,0,0] neg_hi:[1,0,0]\n\t" \
  "v_pk_fma_f32 v[128:129], v[176:177], v[236:237], v[128:129]\n\t" \
  "v_pk_fma_f32 v[130:131], v[178:179], v[238:239], v[130:131]\n\t" \
  "v_pk_fma_f32 v[96:97], v[176:177], v[240:241], v[96:97] neg_lo:[1,0,0] neg_hi:[1,0,0]\n\t" \
  "v_pk_fma_f32 v[98:99], v[178:179], v[242:243], v[98:99] neg_lo:[1,0,0] neg_hi:[1,0,0]\n\t" \
  "v_cvt_pk_bf16_f32 v160, v128, v129\n\t" \
  "v_cvt_pk_bf16_f32 v161, v130, v131\n\t" \
  "v_cvt_pk_bf16_f32 v162, v96, v97\n\t" \
  "v_cvt_pk_bf16_f32 v163, v98, v99\n\t" \
  "v_pk_fma_f32 v[132:133], v[180:181], v[96:97], v[132:133]\n\t" \
  "v_pk_fma_f32 v[134:135], v[182:183], v[98:99], v[134:135]\n\t" \
  "v_permlane16_swap_b32 v160, v162\n\t" \
  "v_permlane16_swap_b32 v161, v163\n\t" \
  "buffer_store_dwordx4 v[160:163], %[l16], %[srd], %[sh] offen offset:0 sc1\n\t" \
  "v_pk_fma_f32 v[100:101], v[180:181], v[128:129], v[100:101] neg_lo:[1,0,0] neg_hi:[1,0,0]\n\t" \
  "v_pk_fma_f32 v[102:103], v[182:183], v[130:131], v[102:103] neg_lo:[1,0,0] neg_hi:[1,0,0]\n\t" \
  "v_pk_fma_f32 v[132:133], v[176:177], v[128:129], v[132:133]\n\t" \
  "v_pk_fma_f32 v[134:135], v[178:179], v[130:131], v[134:135]\n\t" \
  "v_pk_fma_f32 v[100:101], v[176:177], v[96:97], v[100:101]\n\t" \
  "v_pk_fma_f32 v[102:103], v[178:179], v[98:99], v[102:103]\n\t" \
  "v_cvt_pk_bf16_f32 v164, v132, v133\n\t" \
  "v_cvt_pk_bf16_f32 v165, v134, v135\n\t" \
  "v_cvt_pk_bf16_f32 v166, v100, v101\n\t" \
  "v_cvt_pk_bf16_f32 v167, v102, v103\n\t" \
  "v_pk_fma_f32 v[136:137], v[180:181], v[100:101], v[136:137]\n\t" \
  "v_pk_fma_f32 v[138:139], v[182:183], v[102:103], v[138:139]\n\t" \
  "v_permlane16_swap_b32 v164, v166\n\t" \
  "v_permlane16_swap_b32 v165, v167\n\t" \
  "buffer_store_dwordx4 v[164:167], %[l16], %[srd], %[sh] offen offset:2048 sc1\n\t" \
  "s_add_u32 %[sh], %[sh], 0x1000\n\t" \
  "v_pk_fma_f32 v[104:105], v[180:181], v[132:133], v[104:105] neg_lo:[1,0,0] neg_hi:[1,0,0]\n\t" \
  "v_pk_fma_f32 v[106:107], v[182:183], v[134:135], v[106:107] neg_lo:[1,0,0] neg_hi:[1,0,0]\n\t" \
  "v_pk_fma_f32 v[136:137], v[176:177], v[132:133], v[136:137]\n\t" \
  "v_pk_fma_f32 v[138:139], v[178:179], v[134:135], v[138:139]\n\t" \
  "v_pk_fma_f32 v[104:105], v[176:177], v[100:101], v[104:105]\n\t" \
  "v_pk_fma_f32 v[106:107], v[178:179], v[102:103], v[106:107]\n\t" \
  "v_cvt_pk_bf16_f32 v168, v136, v137\n\t" \
  "v_cvt_pk_bf16_f32 v169, v138, v139\n\t" \
  "v_cvt_pk_bf16_f32 v170, v104, v105\n\t" \
  "v_cvt_pk_bf16_f32 v171, v106, v107\n\t" \
  "v_pk_fma_f32 v[140:141], v[180:181], v[104:105], v[140:141]\n\t" \
  "v_pk_fma_f32 v[142:143], v[182:183], v[106:107], v[142:143]\n\t" \
  "v_permlane16_swap_b32 v168, v170\n\t" \
  "v_permlane16_swap_b32 v169, v171\n\t" \
  "buffer_store_dwordx4 v[168:171], %[l16], %[srd], %[sh] offen offset:0 sc1\n\t" \
  "v_pk_fma_f32 v[108:109], v[180:181], v[136:137], v[108:109] neg_lo:[1,0,0] neg_hi:[1,0,0]\n\t" \
  "v_pk_fma_f32 v[110:111], v[182:183], v[138:139], v[110:111] neg_lo:[1,0,0] neg_hi:[1,0,0]\n\t" \
  "v_pk_fma_f32 v[140:141], v[176:177], v[136:137], v[140:141]\n\t" \
  "v_pk_fma_f32 v[142:143], v[178:179], v[138:139], v[142:143]\n\t" \
  "v_pk_fma_f32 v[108:109], v[176:177], v[104:105], v[108:109]\n\t" \
  "v_pk_fma_f32 v[110:111], v[178:179], v[106:107], v[110:111]\n\t" \
  "v_cvt_pk_bf16_f32 v172, v140, v141\n\t" \
  "v_cvt_pk_bf16_f32 v173, v142, v143\n\t" \
  "v_cvt_pk_bf16_f32 v174, v108, v109\n\t" \
  "v_cvt_pk_bf16_f32 v175, v110, v111\n\t" \
  "v_pk_fma_f32 v[144:145], v[180:181], v[108:109], v[144:145]\n\t" \
  "v_pk_fma_f32 v[146:147], v[182:183], v[110:111], v[146:147]\n\t" \
  "v_permlane16_swap_b32 v172, v174\n\t" \
  "v_permlane16_swap_b32 v173, v175\n\t" \
  "buffer_store_dwordx4 v[172:175], %[l16], %[srd], %[sh] offen offset:2048 sc1\n\t" \
  "s_add_u32 %[sh], %[sh], 0x1000\n\t" \
  "v_pk_fma_f32 v[112:113], v[180:181], v[140:141], v[112:113] neg_lo:[1,0,0] neg_hi:[1,0,0]\n\t" \
  "v_pk_fma_f32 v[114:115], v[182:183], v[142:143], v[114:115] neg_lo:[1,0,0] neg_hi:[1,0,0]\n\t" \
  "v_pk_fma_f32 v[144:145], v[176:177], v[140:141], v[144:145]\n\t" \
  "v_pk_fma_f32 v[146:147], v[178:179], v[142:143], v[146:147]\n\t" \
  "v_pk_fma_f32 v[112:113], v[176:177], v[108:109], v[112:113]\n\t" \
  "v_pk_fma_f32 v[114:115], v[178:179], v[110:111], v[114:115]\n\t" \
  "v_cvt_pk_bf16_f32 v192, v144, v145\n\t" \
  "v_cvt_pk_bf16_f32 v193, v146, v147\n\t" \
  "v_cvt_pk_bf16_f32 v194, v112, v113\n\t" \
  "v_cvt_pk_bf16_f32 v195, v114, v115\n\t" \
  "v_pk_fma_f32 v[148:149], v[180:181], v[112:113], v[148:149]\n\t" \
  "v_pk_fma_f32 v[150:151], v[182:183], v[114:115], v[150:151]\n\t" \
  "v_permlane16_swap_b32 v192, v194\n\t" \
  "v_permlane16_swap_b32 v193, v195\n\t" \
  "buffer_store_dwordx4 v[192:195], %[l16], %[srd], %[sh] offen offset:0 sc1\n\t" \
  "v_pk_fma_f32 v[116:117], v[180:181], v[144:145], v[116:117] neg_lo:[1,0,0] neg_hi:[1,0,0]\n\t" \
  "v_pk_fma_f32 v[118:119], v[182:183], v[146:147], v[118:119] neg_lo:[1,0,0] neg_hi:[1,0,0]\n\t" \
  "v_pk_fma_f32 v[148:149], v[176:177], v[144:145], v[148:149]\n\t" \
  "v_pk_fma_f32 v[150:151], v[178:179], v[146:147], v[150:151]\n\t" \
  "v_pk_fma_f32 v[116:117], v[176:177], v[112:113], v[116:117]\n\t" \
  "v_pk_fma_f32 v[118:119], v[178:179], v[114:115], v[118:119]\n\t" \
  "v_cvt_pk_bf16_f32 v196, v148, v149\n\t" \
  "v_cvt_pk_bf16_f32 v197, v150, v151\n\t" \
  "v_cvt_pk_bf16_f32 v198, v116, v117\n\t" \
  "v_cvt_pk_bf16_f32 v199, v118, v119\n\t" \
  "v_pk_fma_f32 v[152:153], v[180:181], v[116:117], v[152:153]\n\t" \
  "v_pk_fma_f32 v[154:155], v[182:183], v[118:119], v[154:155]\n\t" \
  "v_permlane16_swap_b32 v196, v198\n\t" \
  "v_permlane16_swap_b32 v197, v199\n\t" \
  "buffer_store_dwordx4 v[196:199], %[l16], %[srd], %[sh] offen offset:2048 sc1\n\t" \
  "s_add_u32 %[sh], %[sh], 0x1000\n\t" \
  "v_pk_fma_f32 v[120:121], v[180:181], v[148:149], v[120:121] neg_lo:[1,0,0] neg_hi:[1,0,0]\n\t" \
  "v_pk_fma_f32 v[122:123], v[182:183], v[150:151], v[122:123] neg_lo:[1,0,0] neg_hi:[1,0,0]\n\t" \
  "v_pk_fma_f32 v[152:153], v[176:177], v[148:149], v[152:153]\n\t" \
  "v_pk_fma_f32 v[154:155], v[178:179], v[150:151], v[154:155]\n\t" \
  "v_pk_fma_f32 v[120:121], v[176:177], v[116:117], v[120:121]\n\t" \
  "v_pk_fma_f32 v[122:123], v[178:179], v[118:119], v[122:123]\n\t" \
  "v_cvt_pk_bf16_f32 v200, v152, v153\n\t" \
  "v_cvt_pk_bf16_f32 v201, v154, v155\n\t" \
  "v_cvt_pk_bf16_f32 v202, v120, v121\n\t" \
  "v_cvt_pk_bf16_f32 v203, v122, v123\n\t" \
  "v_pk_fma_f32 v[156:157], v[180:181], v[120:121], v[156:157]\n\t" \
  "v_pk_fma_f32 v[158:159], v[182:183], v[122:123], v[158:159]\n\t" \
  "v_permlane16_swap_b32 v200, v202\n\t" \
  "v_permlane16_swap_b32 v201, v203\n\t" \
  "buffer_store_dwordx4 v[200:203], %[l16], %[srd], %[sh] offen offset:0 sc1\n\t" \
  "v_pk_fma_f32 v[124:125], v[180:181], v[152:153], v[124:125] neg_lo:[1,0,0] neg_hi:[1,0,0]\n\t" \
  "v_pk_fma_f32 v[126:127], v[182:183], v[154:155], v[126:127] neg_lo:[1,0,0] neg_hi:[1,0,0]\n\t" \
  "v_pk_fma_f32 v[156:157], v[176:177], v[152:153], v[156:157]\n\t" \
  "v_pk_fma_f32 v[158:159], v[178:179], v[154:155], v[158:159]\n\t" \
  "v_pk_fma_f32 v[124:125], v[176:177], v[120:121], v[124:125]\n\t" \
  "v_pk_fma_f32 v[126:127], v[178:179], v[122:123], v[126:127]\n\t" \
  "v_cvt_pk_bf16_f32 v204, v156, v157\n\t" \
  "v_cvt_pk_bf16_f32 v205, v158, v159\n\t" \
  "v_cvt_pk_bf16_f32 v206, v124, v125\n\t" \
  "v_cvt_pk_bf16_f32 v207, v126, v127\n\t" \
  "s_nop 1\n\t" \
  "v_permlane16_swap_b32 v204, v206\n\t" \
  "v_permlane16_swap_b32 v205, v207\n\t" \
  "buffer_store_dwordx4 v[204:207], %[l16], %[srd], %[sh] offen offset:2048 sc1\n\t" \
  "s_add_u32 %[scn], %[scn], 8\n\t" \
  "s_mov_b32 m0, %[scn]\n\t" \
  "s_mov_b64 exec, 1\n\t" \
  "buffer_load_dword %[l16], %[srz], 0 offen lds\n\t" \
  "s_mov_b64 exec, -1\n\t" \
  "s_branch Lend_%=\n\t" \
  "Lx0_%=:\n\t" \
  "v_add_u32 v244, %[sx], %[xo]\n\t" \
  "v_xor_b32 v245, 16, v244\n\t" \
  "v_xor_b32 v246, 32, v244\n\t" \
  "v_xor_b32 v247, 48, v244\n\t" \
  "ds_read_b64 %[xe00], v244 offset:0\n\t" \
  "ds_read_b64 %[xe10], v244 offset:2048\n\t" \
  "ds_read_b64 %[xe01], v245 offset:128\n\t" \
  "ds_read_b64 %[xe11], v245 offset:2176\n\t" \
  "ds_read_b64 %[xe02], v246 offset:256\n\t" \
  "ds_read_b64 %[xe12], v246 offset:2304\n\t" \
  "ds_read_b64 %[xe03], v247 offset:384\n\t" \
  "ds_read_b64 %[xe13], v247 offset:2432\n\t" \
  "s_branch Lxb0_%=\n\t" \
  "Lsp1_%=:\n\t" \
  "ds_read_b64 v[224:225], %[fl]\n\t" \
  "s_sub_i32 %[bud], %[bud], 1\n\t" \
  "s_cmp_lt_i32 %[bud], 0\n\t" \
  "s_cbranch_scc1 Lb1_%=\n\t" \
  "s_waitcnt lgkmcnt(0)\n\t" \
  "v_or_b32 v224, v224, v225\n\t" \
  "s_nop 0\n\t" \
  "v_readfirstlane_b32 %[s0], v224\n\t" \
  "s_cmp_eq_u32 %[s0], 0\n\t" \
  "s_cbranch_scc1 Lb1_%=\n\t" \
  "s_branch Lsp1_%=\n\t" \
  "Lx2_%=:\n\t" \
  "v_add_u32 v244, %[sx], %[xo]\n\t" \
  "v_xor_b32 v245, 16, v244\n\t" \
  "v_xor_b32 v246, 32, v244\n\t" \
  "v_xor_b32 v247, 48, v244\n\t" \
  "ds_read_b64 %[xe00], v244 offset:0\n\t" \
  "ds_read_b64 %[xe10], v244 offset:2048\n\t" \
  "ds_read_b64 %[xe01], v245 offset:128\n\t" \
  "ds_read_b64 %[xe11], v245 offset:2176\n\t" \
  "ds_read_b64 %[xe02], v246 offset:256\n\t" \
  "ds_read_b64 %[xe12], v246 offset:2304\n\t" \
  "ds_read_b64 %[xe03], v247 offset:384\n\t" \
  "ds_read_b64 %[xe13], v247 offset:2432\n\t" \
  "s_branch Lxb2_%=\n\t" \
  "Lsp3_%=:\n\t" \
  "v_mfma_f32_16x16x32_bf16 v[92:95], v[208:211], v[204:207], v[92:95]\n\t" \
  "ds_read_b128 v[172:175], %[pb1] offset:14336\n\t" \
  "v_mfma_f32_16x16x32_bf16 v[124:127], v[212:215], v[204:207], v[124:127]\n\t" \
  "s_waitcnt lgkmcnt(0)\n\t" \
  "v_mfma_f32_16x16x32_bf16 v[64:67], v[216:219], v[144:147], v[64:67]\n\t" \
  "v_mfma_f32_16x16x32_bf16 v[96:99], v[220:223], v[144:147], v[96:99]\n\t" \
  "v_mfma_f32_16x16x32_bf16 v[68:71], v[216:219], v[148:151], v[68:71]\n\t" \
  "v_mfma_f32_16x16x32_bf16 v[100:103], v[220:223], v[148:151], v[100:103]\n\t" \
  "v_mfma_f32_16x16x32_bf16 v[72:75], v[216:219], v[152:155], v[72:75]\n\t" \
  "v_mfma_f32_16x16x32_bf16 v[104:107], v[220:223], v[152:155], v[104:107]\n\t" \
  "v_mfma_f32_16x16x32_bf16 v[76:79], v[216:219], v[156:159], v[76:79]\n\t" \
  "v_mfma_f32_16x16x32_bf16 v[108:111], v[220:223], v[156:159], v[108:111]\n\t" \
  "v_mfma_f32_16x16x32_bf16 v[80:83], v[216:219], v[160:163], v[80:83]\n\t" \
  "v_mfma_f32_16x16x32_bf16 v[112:115], v[220:223], v[160:163], v[112:115]\n\t" \
  "v_mfma_f32_16x16x32_bf16 v[84:87], v[216:219], v[164:167], v[84:87]\n\t" \
  "v_mfma_f32_16x16x32_bf16 v[116:119], v[220:223], v[164:167], v[116:119]\n\t" \
  "v_mfma_f32_16x16x32_bf16 v[88:91], v[216:219], v[168:171], v[88:91]\n\t" \
  "v_mfma_f32_16x16x32_bf16 v[120:123], v[220:223], v[168:171], v[120:123]\n\t" \
  "v_mfma_f32_16x16x32_bf16 v[92:95], v[216:219], v[172:175], v[92:95]\n\t" \
  "v_mfma_f32_16x16x32_bf16 v[124:127], v[220:223], v[172:175], v[124:127]\n\t" \
  "v_add_u32 %[pb1], %[s2], %[pb1]\n\t" \
  "Lsa3_%=:\n\t" \
  "ds_read_b64 v[224:225], %[fl]\n\t" \
  "s_sub_i32 %[bud], %[bud], 1\n\t" \
  "s_cmp_lt_i32 %[bud], 0\n\t" \
  "s_cbranch_scc1 Lsb3_%=\n\t" \
  "s_waitcnt lgkmcnt(0)\n\t" \
  "v_or_b32 v224, v224, v225\n\t" \
  "s_nop 0\n\t" \
  "v_readfirstlane_b32 %[s0], v224\n\t" \
  "s_cmp_eq_u32 %[s0], 0\n\t" \
  "s_cbranch_scc1 Lsb3_%=\n\t" \
  "s_branch Lsa3_%=\n\t" \
  "Lsb3_%=:\n\t" \
  "ds_read_b128 v[128:131], %[pa0] offset:0\n\t" \
  "ds_read_b128 v[132:135], %[pa0] offset:8192\n\t" \
  "ds_read_b128 v[136:139], %[pa1] offset:0\n\t" \
  "ds_read_b128 v[140:143], %[pa1] offset:8192\n\t" \
  "ds_read_b128 v[176:179], %[pb0] offset:0\n\t" \
  "ds_read_b128 v[180:183], %[pb0] offset:2048\n\t" \
  "ds_read_b128 v[184:187], %[pb0] offset:4096\n\t" \
  "ds_read_b128 v[188:191], %[pb0] offset:6144\n\t" \
  "ds_read_b128 v[192:195], %[pb0] offset:8192\n\t" \
  "ds_read_b128 v[196:199], %[pb0] offset:10240\n\t" \
  "ds_read_b128 v[200:203], %[pb0] offset:12288\n\t" \
  "ds_read_b128 v[204:207], %[pb0] offset:14336\n\t" \
  "v_add_u32 %[fl], 8, %[fl]\n\t" \
  "s_waitcnt lgkmcnt(8)\n\t" \
  "s_mov_b64 exec, 1\n\t" \
  "buffer_load_dword %[l16], %[srz], 0 offen lds\n\t" \
  "s_mov_b64 exec, -1\n\t" \
  "s_add_u32 m0, m0, 16\n\t" \
  "s_cmp_eq_u32 %[sk], %[ks]\n\t" \
  "s_cbranch_scc0 Lnx3_%=\n\t" \
  "v_add_u32 v244, %[sx], %[xo]\n\t" \
  "v_xor_b32 v245, 16, v244\n\t" \
  "v_xor_b32 v246, 32, v244\n\t" \
  "v_xor_b32 v247, 48, v244\n\t" \
  "ds_read_b64 %[xe00], v244 offset:0\n\t" \
  "ds_read_b64 %[xe10], v244 offset:2048\n\t" \
  "ds_read_b64 %[xe01], v245 offset:128\n\t" \
  "ds_read_b64 %[xe11], v245 offset:2176\n\t" \
  "ds_read_b64 %[xe02], v246 offset:256\n\t" \
  "ds_read_b64 %[xe12], v246 offset:2304\n\t" \
  "ds_read_b64 %[xe03], v247 offset:384\n\t" \
  "ds_read_b64 %[xe13], v247 offset:2432\n\t" \
  "s_branch Lnx3_%=\n\t" \
  "Lx4_%=:\n\t" \
  "v_add_u32 v244, %[sx], %[xo]\n\t" \
  "v_xor_b32 v245, 16, v244\n\t" \
  "v_xor_b32 v246, 32, v244\n\t" \
  "v_xor_b32 v247, 48, v244\n\t" \
  "ds_read_b64 %[xe00], v244 offset:0\n\t" \
  "ds_read_b64 %[xe10], v244 offset:2048\n\t" \
  "ds_read_b64 %[xe01], v245 offset:128\n\t" \
  "ds_read_b64 %[xe11], v245 offset:2176\n\t" \
  "ds_read_b64 %[xe02], v246 offset:256\n\t" \
  "ds_read_b64 %[xe12], v246 offset:2304\n\t" \
  "ds_read_b64 %[xe03], v247 offset:384\n\t" \
  "ds_read_b64 %[xe13], v247 offset:2432\n\t" \
  "s_branch Lxb4_%=\n\t" \
  "Lsp5_%=:\n\t" \
  "ds_read_b64 v[224:225], %[fl]\n\t" \
  "s_sub_i32 %[bud], %[bud], 1\n\t" \
  "s_cmp_lt_i32 %[bud], 0\n\t" \
  "s_cbranch_scc1 Lb5_%=\n\t" \
  "s_waitcnt lgkmcnt(0)\n\t" \
  "v_or_b32 v224, v224, v225\n\t" \
  "s_nop 0\n\t" \
  "v_readfirstlane_b32 %[s0], v224\n\t" \
  "s_cmp_eq_u32 %[s0], 0\n\t" \
  "s_cbranch_scc1 Lb5_%=\n\t" \
  "s_branch Lsp5_%=\n\t" \
  "Lend_%=:\n\t" \
  "s_nop 7\n\t" \
  "s_nop 7\n\t"
#define ASM_MB \
  "s_nop 4\n\t" \
  "s_mov_b32 %[sg], 0\n\t" \
  "s_mov_b32 m0, %[scn]\n\t" \
  "Lc0_%=:\n\t" \
  "ds_read_b32 v192, %[f9]\n\t" \
  "s_sub_i32 %[bud], %[bud], 1\n\t" \
  "s_cmp_lt_i32 %[bud], 0\n\t" \
  "s_cbranch_scc1 Lc0b_%=\n\t" \
  "s_waitcnt lgkmcnt(0)\n\t" \
  "v_cmp_ne_u32 vcc, 0, v192\n\t" \
  "s_cbranch_vccnz Lc0_%=\n\t" \
  "Lc0b_%=:\n\t" \
  "ds_read_b128 v[128:131], %[pc0] offset:0\n\t" \
  "ds_read_b128 v[132:135], %[pc0] offset:2048\n\t" \
  "ds_read_b128 v[136:139], %[pc0] offset:4096\n\t" \
  "ds_read_b128 v[140:143], %[pc0] offset:6144\n\t" \
  "ds_read_b128 v[144:147], %[pc1] offset:0\n\t" \
  "ds_read_b128 v[148:151], %[pc1] offset:2048\n\t" \
  "ds_read_b128 v[152:155], %[pc1] offset:4096\n\t" \
  "ds_read_b128 v[156:159], %[pc1] offset:6144\n\t" \
  "s_waitcnt vmcnt(0)\n\t" \
  "v_readfirstlane_b32 %[s0], %[cnt]\n\t" \
  "s_cmp_eq_u32 %[s0], 31\n\t" \
  "s_cbranch_scc1 Lf2_%=\n\t" \
  "s_cmp_eq_u32 %[w], 0\n\t" \
  "s_cbranch_scc0 Lw_%=\n\t" \
  "Lf_%=:\n\t" \
  "v_mov_b32 v196, 0\n\t" \
  "s_mov_b64 exec, 1\n\t" \
  "buffer_atomic_or v196, off, %[srd], %[sf] sc0 sc1\n\t" \
  "s_mov_b64 exec, -1\n\t" \
  "s_sub_i32 %[bud], %[bud], 16\n\t" \
  "s_cmp_lt_i32 %[bud], 0\n\t" \
  "s_cbranch_scc1 Lf2_%=\n\t" \
  "s_waitcnt vmcnt(0)\n\t" \
  "v_readfirstlane_b32 %[s0], v196\n\t" \
  "s_cmp_lg_u32 %[s0], 32\n\t" \
  "s_cbranch_scc0 Lf2_%=\n\t" \
  "s_sleep 1\n\t" \
  "s_branch Lf_%=\n\t" \
  "Lf2_%=:\n\t" \
  "v_mov_b32 v192, 0\n\t" \
  "v_mov_b32 v193, 0\n\t" \
  "ds_write_b64 %[hr], v[192:193]\n\t" \
  "Lf3_%=:\n\t" \
  "s_setprio 2\n\t" \
  "buffer_load_dwordx4 v[64:67], %[vo], %[srd], %[h0] offen offset:0 sc1\n\t" \
  "buffer_load_dwordx4 v[68:71], %[vo], %[srd], %[h0] offen offset:2048 sc1\n\t" \
  "buffer_load_dwordx4 v[72:75], %[vo], %[srd], %[h1] offen offset:0 sc1\n\t" \
  "buffer_load_dwordx4 v[76:79], %[vo], %[srd], %[h1] offen offset:2048 sc1\n\t" \
  "buffer_load_dwordx4 v[80:83], %[vo], %[srd], %[h2] offen offset:0 sc1\n\t" \
  "buffer_load_dword v194, off, %[srz], 0\n\t" \
  "buffer_load_dwordx4 v[84:87], %[vo], %[srd], %[h2] offen offset:2048 sc1\n\t" \
  "buffer_load_dwordx4 v[88:91], %[vo], %[srd], %[h3] offen offset:0 sc1\n\t" \
  "buffer_load_dwordx4 v[92:95], %[vo], %[srd], %[h3] offen offset:2048 sc1\n\t" \
  "buffer_load_dwordx4 v[96:99], %[vo], %[srd], %[h4] offen offset:0 sc1\n\t" \
  "buffer_load_dwordx4 v[100:103], %[vo], %[srd], %[h4] offen offset:2048 sc1\n\t" \
  "buffer_load_dwordx4 v[104:107], %[vo], %[srd], %[h5] offen offset:0 sc1\n\t" \
  "buffer_load_dwordx4 v[108:111], %[vo], %[srd], %[h5] offen offset:2048 sc1\n\t" \
  "buffer_load_dwordx4 v[112:115], %[vo], %[srd], %[h6] offen offset:0 sc1\n\t" \
  "buffer_load_dword v194, off, %[srz], 0\n\t" \
  "buffer_load_dwordx4 v[116:119], %[vo], %[srd], %[h6] offen offset:2048 sc1\n\t" \
  "s_waitcnt lgkmcnt(0)\n\t" \
  "Lloop_%=:\n\t" \
  "s_waitcnt vmcnt(14) lgkmcnt(4)\n\t" \
  "v_mfma_f32_16x16x32_bf16 %[c00], v[64:67], v[128:131], %[c00]\n\t" \
  "buffer_load_dwordx4 v[120:123], %[vo], %[srd], %[h7] offen offset:0 sc1\n\t" \
  "v_mfma_f32_16x16x32_bf16 %[c10], v[68:71], v[128:131], %[c10]\n\t" \
  "ds_read_b128 v[160:163], %[pc2] offset:0\n\t" \
  "v_add_u32 %[pc0], %[k2], %[pc0]\n\t" \
  "v_mfma_f32_16x16x32_bf16 %[c01], v[64:67], v[132:135], %[c01]\n\t" \
  "v_mfma_f32_16x16x32_bf16 %[c11], v[68:71], v[132:135], %[c11]\n\t" \
  "ds_read_b128 v[164:167], %[pc2] offset:2048\n\t" \
  "v_mfma_f32_16x16x32_bf16 %[c02], v[64:67], v[136:139], %[c02]\n\t" \
  "buffer_load_dwordx4 v[124:127], %[vo], %[srd], %[h7] offen offset:2048 sc1\n\t" \
  "v_mfma_f32_16x16x32_bf16 %[c12], v[68:71], v[136:139], %[c12]\n\t" \
  "ds_read_b128 v[168:171], %[pc2] offset:4096\n\t" \
  "v_mfma_f32_16x16x32_bf16 %[c03], v[64:67], v[140:143], %[c03]\n\t" \
  "v_add_u32 %[vo], 0x20000, %[vo]\n\t" \
  "v_and_b32 %[vo], 0xfffff, %[vo]\n\t" \
  "v_mfma_f32_16x16x32_bf16 %[c13], v[68:71], v[140:143], %[c13]\n\t" \
  "ds_read_b128 v[172:175], %[pc2] offset:6144\n\t" \
  "s_waitcnt vmcnt(14) lgkmcnt(4)\n\t" \
  "v_mfma_f32_16x16x32_bf16 %[c00], v[72:75], v[144:147], %[c00]\n\t" \
  "buffer_load_dwordx4 v[64:67], %[vo], %[srd], %[h0] offen offset:0 sc1\n\t" \
  "v_mfma_f32_16x16x32_bf16 %[c10], v[76:79], v[144:147], %[c10]\n\t" \
  "ds_read_b128 v[176:179], %[pc3] offset:0\n\t" \
  "v_add_u32 %[pc1], %[k2], %[pc1]\n\t" \
  "v_mfma_f32_16x16x32_bf16 %[c01], v[72:75], v[148:151], %[c01]\n\t" \
  "s_cmp_eq_u32 %[sg], 2\n\t" \
  "s_cbranch_scc1 Lh_%=\n\t" \
  "Lhb_%=:\n\t" \
  "v_mfma_f32_16x16x32_bf16 %[c11], v[76:79], v[148:151], %[c11]\n\t" \
  "ds_read_b128 v[180:183], %[pc3] offset:2048\n\t" \
  "v_mfma_f32_16x16x32_bf16 %[c02], v[72:75], v[152:155], %[c02]\n\t" \
  "buffer_load_dwordx4 v[68:71], %[vo], %[srd], %[h0] offen offset:2048 sc1\n\t" \
  "v_mfma_f32_16x16x32_bf16 %[c12], v[76:79], v[152:155], %[c12]\n\t" \
  "ds_read_b128 v[184:187], %[pc3] offset:4096\n\t" \
  "v_mfma_f32_16x16x32_bf16 %[c03], v[72:75], v[156:159], %[c03]\n\t" \
  "v_mfma_f32_16x16x32_bf16 %[c13], v[76:79], v[156:159], %[c13]\n\t" \
  "ds_read_b128 v[188:191], %[pc3] offset:6144\n\t" \
  "s_waitcnt vmcnt(13) lgkmcnt(4)\n\t" \
  "v_mfma_f32_16x16x32_bf16 %[c00], v[80:83], v[160:163], %[c00]\n\t" \
  "buffer_load_dwordx4 v[72:75], %[vo], %[srd], %[h1] offen offset:0 sc1\n\t" \
  "v_mfma_f32_16x16x32_bf16 %[c10], v[84:87], v[160:163], %[c10]\n\t" \
  "ds_read_b128 v[128:131], %[pc0] offset:0\n\t" \
  "v_add_u32 %[pc2], %[k2], %[pc2]\n\t" \
  "v_mfma_f32_16x16x32_bf16 %[c01], v[80:83], v[164:167], %[c01]\n\t" \
  "v_mfma_f32_16x16x32_bf16 %[c11], v[84:87], v[164:167], %[c11]\n\t" \
  "ds_read_b128 v[132:135], %[pc0] offset:2048\n\t" \
  "v_mfma_f32_16x16x32_bf16 %[c02], v[80:83], v[168:171], %[c02]\n\t" \
  "buffer_load_dwordx4 v[76:79], %[vo], %[srd], %[h1] offen offset:2048 sc1\n\t" \
  "v_mfma_f32_16x16x32_bf16 %[c12], v[84:87], v[168:171], %[c12]\n\t" \
  "ds_read_b128 v[136:139], %[pc0] offset:4096\n\t" \
  "v_mfma_f32_16x16x32_bf16 %[c03], v[80:83], v[172:175], %[c03]\n\t" \
  "v_mfma_f32_16x16x32_bf16 %[c13], v[84:87], v[172:175], %[c13]\n\t" \
  "ds_read_b128 v[140:143], %[pc0] offset:6144\n\t" \
  "s_waitcnt vmcnt(13) lgkmcnt(4)\n\t" \
  "v_mfma_f32_16x16x32_bf16 %[c00], v[88:91], v[176:179], %[c00]\n\t" \
  "buffer_load_dwordx4 v[80:83], %[vo], %[srd], %[h2] offen offset:0 sc1\n\t" \
  "v_mfma_f32_16x16x32_bf16 %[c10], v[92:95], v[176:179], %[c10]\n\t" \
  "ds_read_b128 v[144:147], %[pc1] offset:0\n\t" \
  "v_add_u32 %[pc3], %[k2], %[pc3]\n\t" \
  "v_mfma_f32_16x16x32_bf16 %[c01], v[88:91], v[180:183], %[c01]\n\t" \
  "s_mov_b64 exec, 1\n\t" \
  "buffer_load_dword off, %[srz], 0 lds\n\t" \
  "s_mov_b64 exec, -1\n\t" \
  "v_mfma_f32_16x16x32_bf16 %[c11], v[92:95], v[180:183], %[c11]\n\t" \
  "ds_read_b128 v[148:151], %[pc1] offset:2048\n\t" \
  "v_mfma_f32_16x16x32_bf16 %[c02], v[88:91], v[184:187], %[c02]\n\t" \
  "buffer_load_dwordx4 v[84:87], %[vo], %[srd], %[h2] offen offset:2048 sc1\n\t" \
  "v_mfma_f32_16x16x32_bf16 %[c12], v[92:95], v[184:187], %[c12]\n\t" \
  "ds_read_b128 v[152:155], %[pc1] offset:4096\n\t" \
  "s_add_u32 m0, m0, 16\n\t" \
  "v_mfma_f32_16x16x32_bf16 %[c03], v[88:91], v[188:191], %[c03]\n\t" \
  "v_mfma_f32_16x16x32_bf16 %[c13], v[92:95], v[188:191], %[c13]\n\t" \
  "ds_read_b128 v[156:159], %[pc1] offset:6144\n\t" \
  "s_waitcnt vmcnt(14) lgkmcnt(4)\n\t" \
  "v_mfma_f32_16x16x32_bf16 %[c00], v[96:99], v[128:131], %[c00]\n\t" \
  "buffer_load_dwordx4 v[88:91], %[vo], %[srd], %[h3] offen offset:0 sc1\n\t" \
  "v_mfma_f32_16x16x32_bf16 %[c10], v[100:103], v[128:131], %[c10]\n\t" \
  "ds_read_b128 v[160:163], %[pc2] offset:0\n\t" \
  "v_add_u32 %[pc0], %[k2], %[pc0]\n\t" \
  "v_mfma_f32_16x16x32_bf16 %[c01], v[96:99], v[132:135], %[c01]\n\t" \
  "v_mfma_f32_16x16x32_bf16 %[c11], v[100:103], v[132:135], %[c11]\n\t" \
  "ds_read_b128 v[164:167], %[pc2] offset:2048\n\t" \
  "v_mfma_f32_16x16x32_bf16 %[c02], v[96:99], v[136:139], %[c02]\n\t" \
  "buffer_load_dwordx4 v[92:95], %[vo], %[srd], %[h3] offen offset:2048 sc1\n\t" \
  "v_mfma_f32_16x16x32_bf16 %[c12], v[100:103], v[136:139], %[c12]\n\t" \
  "ds_read_b128 v[168:171], %[pc2] offset:4096\n\t" \
  "v_mfma_f32_16x16x32_bf16 %[c03], v[96:99], v[140:143], %[c03]\n\t" \
  "v_mfma_f32_16x16x32_bf16 %[c13], v[100:103], v[140:143], %[c13]\n\t" \
  "ds_read_b128 v[172:175], %[pc2] offset:6144\n\t" \
  "s_waitcnt vmcnt(14) lgkmcnt(4)\n\t" \
  "v_mfma_f32_16x16x32_bf16 %[c00], v[104:107], v[144:147], %[c00]\n\t" \
  "buffer_load_dwordx4 v[96:99], %[vo], %[srd], %[h4] offen offset:0 sc1\n\t" \
  "v_mfma_f32_16x16x32_bf16 %[c10], v[108:111], v[144:147], %[c10]\n\t" \
  "ds_read_b128 v[176:179], %[pc3] offset:0\n\t" \
  "v_add_u32 %[pc1], %[k2], %[pc1]\n\t" \
  "v_mfma_f32_16x16x32_bf16 %[c01], v[104:107], v[148:151], %[c01]\n\t" \
  "v_mfma_f32_16x16x32_bf16 %[c11], v[108:111], v[148:151], %[c11]\n\t" \
  "ds_read_b128 v[180:183], %[pc3] offset:2048\n\t" \
  "v_mfma_f32_16x16x32_bf16 %[c02], v[104:107], v[152:155], %[c02]\n\t" \
  "buffer_load_dwordx4 v[100:103], %[vo], %[srd], %[h4] offen offset:2048 sc1\n\t" \
  "v_mfma_f32_16x16x32_bf16 %[c12], v[108:111], v[152:155], %[c12]\n\t" \
  "ds_read_b128 v[184:187], %[pc3] offset:4096\n\t" \
  "v_mfma_f32_16x16x32_bf16 %[c03], v[104:107], v[156:159], %[c03]\n\t" \
  "v_mfma_f32_16x16x32_bf16 %[c13], v[108:111], v[156:159], %[c13]\n\t" \
  "ds_read_b128 v[188:191], %[pc3] offset:6144\n\t" \
  "s_waitcnt vmcnt(13) lgkmcnt(4)\n\t" \
  "v_mfma_f32_16x16x32_bf16 %[c00], v[112:115], v[160:163], %[c00]\n\t" \
  "buffer_load_dwordx4 v[104:107], %[vo], %[srd], %[h5] offen offset:0 sc1\n\t" \
  "v_mfma_f32_16x16x32_bf16 %[c10], v[116:119], v[160:163], %[c10]\n\t" \
  "ds_read_b128 v[128:131], %[pc0] offset:0\n\t" \
  "v_add_u32 %[pc2], %[k2], %[pc2]\n\t" \
  "v_mfma_f32_16x16x32_bf16 %[c01], v[112:115], v[164:167], %[c01]\n\t" \
  "v_mfma_f32_16x16x32_bf16 %[c11], v[116:119], v[164:167], %[c11]\n\t" \
  "ds_read_b128 v[132:135], %[pc0] offset:2048\n\t" \
  "v_mfma_f32_16x16x32_bf16 %[c02], v[112:115], v[168:171], %[c02]\n\t" \
  "buffer_load_dwordx4 v[108:111], %[vo], %[srd], %[h5] offen offset:2048 sc1\n\t" \
  "v_mfma_f32_16x16x32_bf16 %[c12], v[116:119], v[168:171], %[c12]\n\t" \
  "ds_read_b128 v[136:139], %[pc0] offset:4096\n\t" \
  "v_mfma_f32_16x16x32_bf16 %[c03], v[112:115], v[172:175], %[c03]\n\t" \
  "v_mfma_f32_16x16x32_bf16 %[c13], v[116:119], v[172:175], %[c13]\n\t" \
  "ds_read_b128 v[140:143], %[pc0] offset:6144\n\t" \
  "s_waitcnt vmcnt(13) lgkmcnt(4)\n\t" \
  "v_mfma_f32_16x16x32_bf16 %[c00], v[120:123], v[176:179], %[c00]\n\t" \
  "buffer_load_dwordx4 v[112:115], %[vo], %[srd], %[h6] offen offset:0 sc1\n\t" \
  "v_mfma_f32_16x16x32_bf16 %[c10], v[124:127], v[176:179], %[c10]\n\t" \
  "ds_read_b128 v[144:147], %[pc1] offset:0\n\t" \
  "v_add_u32 %[pc3], %[k2], %[pc3]\n\t" \
  "v_mfma_f32_16x16x32_bf16 %[c01], v[120:123], v[180:183], %[c01]\n\t" \
  "s_mov_b64 exec, 1\n\t" \
  "buffer_load_dword off, %[srz], 0 lds\n\t" \
  "s_mov_b64 exec, -1\n\t" \
  "v_mfma_f32_16x16x32_bf16 %[c11], v[124:127], v[180:183], %[c11]\n\t" \
  "ds_read_b128 v[148:151], %[pc1] offset:2048\n\t" \
  "s_add_u32 %[sg], %[sg], 1\n\t" \
  "v_mfma_f32_16x16x32_bf16 %[c02], v[120:123], v[184:187], %[c02]\n\t" \
  "buffer_load_dwordx4 v[116:119], %[vo], %[srd], %[h6] offen offset:2048 sc1\n\t" \
  "v_mfma_f32_16x16x32_bf16 %[c12], v[124:127], v[184:187], %[c12]\n\t" \
  "ds_read_b128 v[152:155], %[pc1] offset:4096\n\t" \
  "s_add_u32 m0, m0, 16\n\t" \
  "v_mfma_f32_16x16x32_bf16 %[c03], v[120:123], v[188:191], %[c03]\n\t" \
  "s_cmp_eq_u32 %[sg], 3\n\t" \
  "s_cbranch_scc1 Lg_%=\n\t" \
  "Lgb_%=:\n\t" \
  "v_mfma_f32_16x16x32_bf16 %[c13], v[124:127], v[188:191], %[c13]\n\t" \
  "s_cmp_lt_u32 %[sg], 4\n\t" \
  "ds_read_b128 v[156:159], %[pc1] offset:6144\n\t" \
  "s_cbranch_scc1 Lloop_%=\n\t" \
  "s_mov_b32 %[s1], 0xfffe0000\n\t" \
  "Lloop2_%=:\n\t" \
  "s_waitcnt vmcnt(12) lgkmcnt(4)\n\t" \
  "v_mfma_f32_16x16x32_bf16 %[c00], v[64:67], v[128:131], %[c00]\n\t" \
  "ds_read_b64 v[192:193], %[fl] offset:0\n\t" \
  "buffer_load_dwordx4 v[120:123], %[vo], %[srd], %[h7] offen offset:0 sc1\n\t" \
  "v_mfma_f32_16x16x32_bf16 %[c10], v[68:71], v[128:131], %[c10]\n\t" \
  "ds_read_b128 v[160:163], %[pc2] offset:0\n\t" \
  "v_add_u32 %[pc0], %[s1], %[pc0]\n\t" \
  "v_mfma_f32_16x16x32_bf16 %[c01], v[64:67], v[132:135], %[c01]\n\t" \
  "v_mfma_f32_16x16x32_bf16 %[c11], v[68:71], v[132:135], %[c11]\n\t" \
  "ds_read_b128 v[164:167], %[pc2] offset:2048\n\t" \
  "v_mfma_f32_16x16x32_bf16 %[c02], v[64:67], v[136:139], %[c02]\n\t" \
  "buffer_load_dwordx4 v[124:127], %[vo], %[srd], %[h7] offen offset:2048 sc1\n\t" \
  "v_mfma_f32_16x16x32_bf16 %[c12], v[68:71], v[136:139], %[c12]\n\t" \
  "ds_read_b128 v[168:171], %[pc2] offset:4096\n\t" \
  "v_mfma_f32_16x16x32_bf16 %[c03], v[64:67], v[140:143], %[c03]\n\t" \
  "v_add_u32 %[vo], 0x20000, %[vo]\n\t" \
  "v_and_b32 %[vo], 0xfffff, %[vo]\n\t" \
  "v_mfma_f32_16x16x32_bf16 %[c13], v[68:71], v[140:143], %[c13]\n\t" \
  "ds_read_b128 v[172:175], %[pc2] offset:6144\n\t" \
  "s_waitcnt vmcnt(12) lgkmcnt(4)\n\t" \
  "v_mfma_f32_16x16x32_bf16 %[c00], v[72:75], v[144:147], %[c00]\n\t" \
  "buffer_load_dwordx4 v[64:67], %[vo], %[srd], %[h0] offen offset:0 sc1\n\t" \
  "v_mfma_f32_16x16x32_bf16 %[c10], v[76:79], v[144:147], %[c10]\n\t" \
  "ds_read_b128 v[176:179], %[pc3] offset:0\n\t" \
  "v_add_u32 %[pc1], %[s1], %[pc1]\n\t" \
  "v_mfma_f32_16x16x32_bf16 %[c01], v[72:75], v[148:151], %[c01]\n\t" \
  "v_or_b32 v192, v192, v193\n\t" \
  "v_mfma_f32_16x16x32_bf16 %[c11], v[76:79], v[148:151], %[c11]\n\t" \
  "ds_read_b128 v[180:183], %[pc3] offset:2048\n\t" \
  "v_readfirstlane_b32 %[s0], v192\n\t" \
  "v_mfma_f32_16x16x32_bf16 %[c02], v[72:75], v[152:155], %[c02]\n\t" \
  "buffer_load_dwordx4 v[68:71], %[vo], %[srd], %[h0] offen offset:2048 sc1\n\t" \
  "v_mfma_f32_16x16x32_bf16 %[c12], v[76:79], v[152:155], %[c12]\n\t" \
  "ds_read_b128 v[184:187], %[pc3] offset:4096\n\t" \
  "v_mfma_f32_16x16x32_bf16 %[c03], v[72:75], v[156:159], %[c03]\n\t" \
  "s_cmp_eq_u32 %[s0], 0\n\t" \
  "s_cbranch_scc0 Lcb1_%=\n\t" \
  "Lcb1b_%=:\n\t" \
  "v_mfma_f32_16x16x32_bf16 %[c13], v[76:79], v[156:159], %[c13]\n\t" \
  "ds_read_b128 v[188:191], %[pc3] offset:6144\n\t" \
  "s_waitcnt vmcnt(12) lgkmcnt(4)\n\t" \
  "v_mfma_f32_16x16x32_bf16 %[c00], v[80:83], v[160:163], %[c00]\n\t" \
  "buffer_load_dwordx4 v[72:75], %[vo], %[srd], %[h1] offen offset:0 sc1\n\t" \
  "v_mfma_f32_16x16x32_bf16 %[c10], v[84:87], v[160:163], %[c10]\n\t" \
  "ds_read_b128 v[128:131], %[pc0] offset:0\n\t" \
  "v_add_u32 %[pc2], %[s1], %[pc2]\n\t" \
  "v_mfma_f32_16x16x32_bf16 %[c01], v[80:83], v[164:167], %[c01]\n\t" \
  "v_mfma_f32_16x16x32_bf16 %[c11], v[84:87], v[164:167], %[c11]\n\t" \
  "ds_read_b128 v[132:135], %[pc0] offset:2048\n\t" \
  "v_mfma_f32_16x16x32_bf16 %[c02], v[80:83], v[168:171], %[c02]\n\t" \
  "buffer_load_dwordx4 v[76:79], %[vo], %[srd], %[h1] offen offset:2048 sc1\n\t" \
  "v_mfma_f32_16x16x32_bf16 %[c12], v[84:87], v[168:171], %[c12]\n\t" \
  "ds_read_b128 v[136:139], %[pc0] offset:4096\n\t" \
  "v_mfma_f32_16x16x32_bf16 %[c03], v[80:83], v[172:175], %[c03]\n\t" \
  "v_mfma_f32_16x16x32_bf16 %[c13], v[84:87], v[172:175], %[c13]\n\t" \
  "ds_read_b128 v[140:143], %[pc0] offset:6144\n\t" \
  "s_waitcnt vmcnt(12) lgkmcnt(4)\n\t" \
  "v_mfma_f32_16x16x32_bf16 %[c00], v[88:91], v[176:179], %[c00]\n\t" \
  "buffer_load_dwordx4 v[80:83], %[vo], %[srd], %[h2] offen offset:0 sc1\n\t" \
  "v_mfma_f32_16x16x32_bf16 %[c10], v[92:95], v[176:179], %[c10]\n\t" \
  "ds_read_b128 v[144:147], %[pc1] offset:0\n\t" \
  "v_add_u32 %[pc3], %[s1], %[pc3]\n\t" \
  "v_mfma_f32_16x16x32_bf16 %[c01], v[88:91], v[180:183], %[c01]\n\t" \
  "v_mfma_f32_16x16x32_bf16 %[c11], v[92:95], v[180:183], %[c11]\n\t" \
  "ds_read_b128 v[148:151], %[pc1] offset:2048\n\t" \
  "v_mfma_f32_16x16x32_bf16 %[c02], v[88:91], v[184:187], %[c02]\n\t" \
  "buffer_load_dwordx4 v[84:87], %[vo], %[srd], %[h2] offen offset:2048 sc1\n\t" \
  "v_mfma_f32_16x16x32_bf16 %[c12], v[92:95], v[184:187], %[c12]\n\t" \
  "ds_read_b128 v[152:155], %[pc1] offset:4096\n\t" \
  "v_mfma_f32_16x16x32_bf16 %[c03], v[88:91], v[188:191], %[c03]\n\t" \
  "v_mfma_f32_16x16x32_bf16 %[c13], v[92:95], v[188:191], %[c13]\n\t" \
  "ds_read_b128 v[156:159], %[pc1] offset:6144\n\t" \
  "s_waitcnt vmcnt(12) lgkmcnt(4)\n\t" \
  "v_mfma_f32_16x16x32_bf16 %[c00], v[96:99], v[128:131], %[c00]\n\t" \
  "ds_read_b64 v[192:193], %[fl] offset:8\n\t" \
  "buffer_load_dwordx4 v[88:91], %[vo], %[srd], %[h3] offen offset:0 sc1\n\t" \
  "v_mfma_f32_16x16x32_bf16 %[c10], v[100:103], v[128:131], %[c10]\n\t" \
  "ds_read_b128 v[160:163], %[pc2] offset:0\n\t" \
  "v_add_u32 %[pc0], %[k2], %[pc0]\n\t" \
  "v_mfma_f32_16x16x32_bf16 %[c01], v[96:99], v[132:135], %[c01]\n\t" \
  "v_mfma_f32_16x16x32_bf16 %[c11], v[100:103], v[132:135], %[c11]\n\t" \
  "ds_read_b128 v[164:167], %[pc2] offset:2048\n\t" \
  "v_mfma_f32_16x16x32_bf16 %[c02], v[96:99], v[136:139], %[c02]\n\t" \
  "buffer_load_dwordx4 v[92:95], %[vo], %[srd], %[h3] offen offset:2048 sc1\n\t" \
  "v_mfma_f32_16x16x32_bf16 %[c12], v[100:103], v[136:139], %[c12]\n\t" \
  "ds_read_b128 v[168:171], %[pc2] offset:4096\n\t" \
  "v_mfma_f32_16x16x32_bf16 %[c03], v[96:99], v[140:143], %[c03]\n\t" \
  "v_mfma_f32_16x16x32_bf16 %[c13], v[100:103], v[140:143], %[c13]\n\t" \
  "ds_read_b128 v[172:175], %[pc2] offset:6144\n\t" \
  "s_waitcnt vmcnt(12) lgkmcnt(4)\n\t" \
  "v_mfma_f32_16x16x32_bf16 %[c00], v[104:107], v[144:147], %[c00]\n\t" \
  "buffer_load_dwordx4 v[96:99], %[vo], %[srd], %[h4] offen offset:0 sc1\n\t" \
  "v_mfma_f32_16x16x32_bf16 %[c10], v[108:111], v[144:147], %[c10]\n\t" \
  "ds_read_b128 v[176:179], %[pc3] offset:0\n\t" \
  "v_add_u32 %[pc1], %[k2], %[pc1]\n\t" \
  "v_mfma_f32_16x16x32_bf16 %[c01], v[104:107], v[148:151], %[c01]\n\t" \
  "v_or_b32 v192, v192, v193\n\t" \
  "v_mfma_f32_16x16x32_bf16 %[c11], v[108:111], v[148:151], %[c11]\n\t" \
  "ds_read_b128 v[180:183], %[pc3] offset:2048\n\t" \
  "v_readfirstlane_b32 %[s0], v192\n\t" \
  "v_mfma_f32_16x16x32_bf16 %[c02], v[104:107], v[152:155], %[c02]\n\t" \
  "buffer_load_dwordx4 v[100:103], %[vo], %[srd], %[h4] offen offset:2048 sc1\n\t" \
  "v_mfma_f32_16x16x32_bf16 %[c12], v[108:111], v[152:155], %[c12]\n\t" \
  "ds_read_b128 v[184:187], %[pc3] offset:4096\n\t" \
  "v_mfma_f32_16x16x32_bf16 %[c03], v[104:107], v[156:159], %[c03]\n\t" \
  "s_cmp_eq_u32 %[s0], 0\n\t" \
  "s_cbranch_scc0 Lcb5_%=\n\t" \
  "Lcb5b_%=:\n\t" \
  "v_mfma_f32_16x16x32_bf16 %[c13], v[108:111], v[156:159], %[c13]\n\t" \
  "ds_read_b128 v[188:191], %[pc3] offset:6144\n\t" \
  "s_waitcnt vmcnt(12) lgkmcnt(4)\n\t" \
  "v_mfma_f32_16x16x32_bf16 %[c00], v[112:115], v[160:163], %[c00]\n\t" \
  "buffer_load_dwordx4 v[104:107], %[vo], %[srd], %[h5] offen offset:0 sc1\n\t" \
  "v_mfma_f32_16x16x32_bf16 %[c10], v[116:119], v[160:163], %[c10]\n\t" \
  "ds_read_b128 v[128:131], %[pc0] offset:0\n\t" \
  "v_add_u32 %[pc2], %[k2], %[pc2]\n\t" \
  "v_mfma_f32_16x16x32_bf16 %[c01], v[112:115], v[164:167], %[c01]\n\t" \
  "v_add_u32 %[fl], 16, %[fl]\n\t" \
  "s_mov_b32 %[s1], %[k2]\n\t" \
  "v_mfma_f32_16x16x32_bf16 %[c11], v[116:119], v[164:167], %[c11]\n\t" \
  "ds_read_b128 v[132:135], %[pc0] offset:2048\n\t" \
  "v_mfma_f32_16x16x32_bf16 %[c02], v[112:115], v[168:171], %[c02]\n\t" \
  "buffer_load_dwordx4 v[108:111], %[vo], %[srd], %[h5] offen offset:2048 sc1\n\t" \
  "v_mfma_f32_16x16x32_bf16 %[c12], v[116:119], v[168:171], %[c12]\n\t" \
  "ds_read_b128 v[136:139], %[pc0] offset:4096\n\t" \
  "v_mfma_f32_16x16x32_bf16 %[c03], v[112:115], v[172:175], %[c03]\n\t" \
  "v_mfma_f32_16x16x32_bf16 %[c13], v[116:119], v[172:175], %[c13]\n\t" \
  "ds_read_b128 v[140:143], %[pc0] offset:6144\n\t" \
  "s_waitcnt vmcnt(12) lgkmcnt(4)\n\t" \
  "v_mfma_f32_16x16x32_bf16 %[c00], v[120:123], v[176:179], %[c00]\n\t" \
  "buffer_load_dwordx4 v[112:115], %[vo], %[srd], %[h6] offen offset:0 sc1\n\t" \
  "v_mfma_f32_16x16x32_bf16 %[c10], v[124:127], v[176:179], %[c10]\n\t" \
  "ds_read_b128 v[144:147], %[pc1] offset:0\n\t" \
  "v_add_u32 %[pc3], %[k2], %[pc3]\n\t" \
  "v_mfma_f32_16x16x32_bf16 %[c01], v[120:123], v[180:183], %[c01]\n\t" \
  "s_add_u32 %[sg], %[sg], 1\n\t" \
  "s_cmp_lt_u32 %[sg], 7\n\t" \
  "v_mfma_f32_16x16x32_bf16 %[c11], v[124:127], v[180:183], %[c11]\n\t" \
  "ds_read_b128 v[148:151], %[pc1] offset:2048\n\t" \
  "v_mfma_f32_16x16x32_bf16 %[c02], v[120:123], v[184:187], %[c02]\n\t" \
  "buffer_load_dwordx4 v[116:119], %[vo], %[srd], %[h6] offen offset:2048 sc1\n\t" \
  "v_mfma_f32_16x16x32_bf16 %[c12], v[124:127], v[184:187], %[c12]\n\t" \
  "ds_read_b128 v[152:155], %[pc1] offset:4096\n\t" \
  "v_mfma_f32_16x16x32_bf16 %[c03], v[120:123], v[188:191], %[c03]\n\t" \
  "v_mfma_f32_16x16x32_bf16 %[c13], v[124:127], v[188:191], %[c13]\n\t" \
  "ds_read_b128 v[156:159], %[pc1] offset:6144\n\t" \
  "s_cbranch_scc1 Lloop2_%=\n\t" \
  "s_waitcnt vmcnt(12) lgkmcnt(4)\n\t" \
  "v_mfma_f32_16x16x32_bf16 %[c00], v[64:67], v[128:131], %[c00]\n\t" \
  "ds_read_b64 v[192:193], %[fl] offset:0\n\t" \
  "buffer_load_dwordx4 v[120:123], %[vo], %[srd], %[h7] offen offset:0 sc1\n\t" \
  "v_mfma_f32_16x16x32_bf16 %[c10], v[68:71], v[128:131], %[c10]\n\t" \
  "ds_read_b128 v[160:163], %[pc2] offset:0\n\t" \
  "v_add_u32 %[pc0], %[k2], %[pc0]\n\t" \
  "v_mfma_f32_16x16x32_bf16 %[c01], v[64:67], v[132:135], %[c01]\n\t" \
  "v_mfma_f32_16x16x32_bf16 %[c11], v[68:71], v[132:135], %[c11]\n\t" \
  "ds_read_b128 v[164:167], %[pc2] offset:2048\n\t" \
  "v_mfma_f32_16x16x32_bf16 %[c02], v[64:67], v[136:139], %[c02]\n\t" \
  "buffer_load_dwordx4 v[124:127], %[vo], %[srd], %[h7] offen offset:2048 sc1\n\t" \
  "v_mfma_f32_16x16x32_bf16 %[c12], v[68:71], v[136:139], %[c12]\n\t" \
  "ds_read_b128 v[168:171], %[pc2] offset:4096\n\t" \
  "v_mfma_f32_16x16x32_bf16 %[c03], v[64:67], v[140:143], %[c03]\n\t" \
  "v_mfma_f32_16x16x32_bf16 %[c13], v[68:71], v[140:143], %[c13]\n\t" \
  "ds_read_b128 v[172:175], %[pc2] offset:6144\n\t" \
  "s_waitcnt vmcnt(12) lgkmcnt(4)\n\t" \
  "v_mfma_f32_16x16x32_bf16 %[c00], v[72:75], v[144:147], %[c00]\n\t" \
  "v_mfma_f32_16x16x32_bf16 %[c10], v[76:79], v[144:147], %[c10]\n\t" \
  "ds_read_b128 v[176:179], %[pc3] offset:0\n\t" \
  "v_add_u32 %[pc1], %[k2], %[pc1]\n\t" \
  "v_mfma_f32_16x16x32_bf16 %[c01], v[72:75], v[148:151], %[c01]\n\t" \
  "v_or_b32 v192, v192, v193\n\t" \
  "v_mfma_f32_16x16x32_bf16 %[c11], v[76:79], v[148:151], %[c11]\n\t" \
  "ds_read_b128 v[180:183], %[pc3] offset:2048\n\t" \
  "v_readfirstlane_b32 %[s0], v192\n\t" \
  "v_mfma_f32_16x16x32_bf16 %[c02], v[72:75], v[152:155], %[c02]\n\t" \
  "v_mfma_f32_16x16x32_bf16 %[c12], v[76:79], v[152:155], %[c12]\n\t" \
  "ds_read_b128 v[184:187], %[pc3] offset:4096\n\t" \
  "v_mfma_f32_16x16x32_bf16 %[c03], v[72:75], v[156:159], %[c03]\n\t" \
  "s_cmp_eq_u32 %[s0], 0\n\t" \
  "s_cbranch_scc0 Lcf1_%=\n\t" \
  "Lcf1b_%=:\n\t" \
  "v_mfma_f32_16x16x32_bf16 %[c13], v[76:79], v[156:159], %[c13]\n\t" \
  "ds_read_b128 v[188:191], %[pc3] offset:6144\n\t" \
  "s_waitcnt vmcnt(10) lgkmcnt(4)\n\t" \
  "v_mfma_f32_16x16x32_bf16 %[c00], v[80:83], v[160:163], %[c00]\n\t" \
  "v_mfma_f32_16x16x32_bf16 %[c10], v[84:87], v[160:163], %[c10]\n\t" \
  "ds_read_b128 v[128:131], %[pc0] offset:0\n\t" \
  "v_add_u32 %[pc2], %[k2], %[pc2]\n\t" \
  "v_mfma_f32_16x16x32_bf16 %[c01], v[80:83], v[164:167], %[c01]\n\t" \
  "v_mfma_f32_16x16x32_bf16 %[c11], v[84:87], v[164:167], %[c11]\n\t" \
  "ds_read_b128 v[132:135], %[pc0] offset:2048\n\t" \
  "v_mfma_f32_16x16x32_bf16 %[c02], v[80:83], v[168:171], %[c02]\n\t" \
  "v_mfma_f32_16x16x32_bf16 %[c12], v[84:87], v[168:171], %[c12]\n\t" \
  "ds_read_b128 v[136:139], %[pc0] offset:4096\n\t" \
  "v_mfma_f32_16x16x32_bf16 %[c03], v[80:83], v[172:175], %[c03]\n\t" \
  "v_mfma_f32_16x16x32_bf16 %[c13], v[84:87], v[172:175], %[c13]\n\t" \
  "ds_read_b128 v[140:143], %[pc0] offset:6144\n\t" \
  "s_waitcnt vmcnt(8) lgkmcnt(4)\n\t" \
  "v_mfma_f32_16x16x32_bf16 %[c00], v[88:91], v[176:179], %[c00]\n\t" \
  "v_mfma_f32_16x16x32_bf16 %[c10], v[92:95], v[176:179], %[c10]\n\t" \
  "ds_read_b128 v[144:147], %[pc1] offset:0\n\t" \
  "v_add_u32 %[pc3], %[k2], %[pc3]\n\t" \
  "v_mfma_f32_16x16x32_bf16 %[c01], v[88:91], v[180:183], %[c01]\n\t" \
  "v_mfma_f32_16x16x32_bf16 %[c11], v[92:95], v[180:183], %[c11]\n\t" \
  "ds_read_b128 v[148:151], %[pc1] offset:2048\n\t" \
  "v_mfma_f32_16x16x32_bf16 %[c02], v[88:91], v[184:187], %[c02]\n\t" \
  "v_mfma_f32_16x16x32_bf16 %[c12], v[92:95], v[184:187], %[c12]\n\t" \
  "ds_read_b128 v[152:155], %[pc1] offset:4096\n\t" \
  "v_mfma_f32_16x16x32_bf16 %[c03], v[88:91], v[188:191], %[c03]\n\t" \
  "v_mfma_f32_16x16x32_bf16 %[c13], v[92:95], v[188:191], %[c13]\n\t" \
  "ds_read_b128 v[156:159], %[pc1] offset:6144\n\t" \
  "s_waitcnt vmcnt(6) lgkmcnt(4)\n\t" \
  "v_mfma_f32_16x16x32_bf16 %[c00], v[96:99], v[128:131], %[c00]\n\t" \
  "v_mfma_f32_16x16x32_bf16 %[c10], v[100:103], v[128:131], %[c10]\n\t" \
  "ds_read_b128 v[160:163], %[pc2] offset:0\n\t" \
  "v_mfma_f32_16x16x32_bf16 %[c01], v[96:99], v[132:135], %[c01]\n\t" \
  "v_mfma_f32_16x16x32_bf16 %[c11], v[100:103], v[132:135], %[c11]\n\t" \
  "ds_read_b128 v[164:167], %[pc2] offset:2048\n\t" \
  "v_mfma_f32_16x16x32_bf16 %[c02], v[96:99], v[136:139], %[c02]\n\t" \
  "v_mfma_f32_16x16x32_bf16 %[c12], v[100:103], v[136:139], %[c12]\n\t" \
  "ds_read_b128 v[168:171], %[pc2] offset:4096\n\t" \
  "v_mfma_f32_16x16x32_bf16 %[c03], v[96:99], v[140:143], %[c03]\n\t" \
  "v_mfma_f32_16x16x32_bf16 %[c13], v[100:103], v[140:143], %[c13]\n\t" \
  "ds_read_b128 v[172:175], %[pc2] offset:6144\n\t" \
  "s_waitcnt vmcnt(4) lgkmcnt(4)\n\t" \
  "v_mfma_f32_16x16x32_bf16 %[c00], v[104:107], v[144:147], %[c00]\n\t" \
  "v_mfma_f32_16x16x32_bf16 %[c10], v[108:111], v[144:147], %[c10]\n\t" \
  "ds_read_b128 v[176:179], %[pc3] offset:0\n\t" \
  "v_mfma_f32_16x16x32_bf16 %[c01], v[104:107], v[148:151], %[c01]\n\t" \
  "v_mfma_f32_16x16x32_bf16 %[c11], v[108:111], v[148:151], %[c11]\n\t" \
  "ds_read_b128 v[180:183], %[pc3] offset:2048\n\t" \
  "v_mfma_f32_16x16x32_bf16 %[c02], v[104:107], v[152:155], %[c02]\n\t" \
  "v_mfma_f32_16x16x32_bf16 %[c12], v[108:111], v[152:155], %[c12]\n\t" \
  "ds_read_b128 v[184:187], %[pc3] offset:4096\n\t" \
  "v_mfma_f32_16x16x32_bf16 %[c03], v[104:107], v[156:159], %[c03]\n\t" \
  "v_mfma_f32_16x16x32_bf16 %[c13], v[108:111], v[156:159], %[c13]\n\t" \
  "ds_read_b128 v[188:191], %[pc3] offset:6144\n\t" \
  "s_waitcnt vmcnt(2) lgkmcnt(4)\n\t" \
  "v_mfma_f32_16x16x32_bf16 %[c00], v[112:115], v[160:163], %[c00]\n\t" \
  "ds_read_b64 v[192:193], %[xd]\n\t" \
  "v_mfma_f32_16x16x32_bf16 %[c10], v[116:119], v[160:163], %[c10]\n\t" \
  "ds_read_b128 v[128:131], %[d0]\n\t" \
  "v_mfma_f32_16x16x32_bf16 %[c01], v[112:115], v[164:167], %[c01]\n\t" \
  "ds_read_b128 v[132:135], %[d1]\n\t" \
  "v_mfma_f32_16x16x32_bf16 %[c11], v[116:119], v[164:167], %[c11]\n\t" \
  "ds_read_b128 v[136:139], %[d2]\n\t" \
  "v_mfma_f32_16x16x32_bf16 %[c02], v[112:115], v[168:171], %[c02]\n\t" \
  "ds_read_b128 v[140:143], %[d3]\n\t" \
  "v_mfma_f32_16x16x32_bf16 %[c12], v[116:119], v[168:171], %[c12]\n\t" \
  "ds_read_b128 v[144:147], %[d4]\n\t" \
  "v_mfma_f32_16x16x32_bf16 %[c03], v[112:115], v[172:175], %[c03]\n\t" \
  "ds_read_b128 v[148:151], %[d5]\n\t" \
  "v_mfma_f32_16x16x32_bf16 %[c13], v[116:119], v[172:175], %[c13]\n\t" \
  "ds_read_b128 v[152:155], %[d6]\n\t" \
  "ds_read_b128 v[156:159], %[d7]\n\t" \
  "s_waitcnt vmcnt(0) lgkmcnt(9)\n\t" \
  "v_mfma_f32_16x16x32_bf16 %[c00], v[120:123], v[176:179], %[c00]\n\t" \
  "v_mfma_f32_16x16x32_bf16 %[c10], v[124:127], v[176:179], %[c10]\n\t" \
  "v_mfma_f32_16x16x32_bf16 %[c01], v[120:123], v[180:183], %[c01]\n\t" \
  "v_mfma_f32_16x16x32_bf16 %[c11], v[124:127], v[180:183], %[c11]\n\t" \
  "v_mfma_f32_16x16x32_bf16 %[c02], v[120:123], v[184:187], %[c02]\n\t" \
  "v_mfma_f32_16x16x32_bf16 %[c12], v[124:127], v[184:187], %[c12]\n\t" \
  "v_mfma_f32_16x16x32_bf16 %[c03], v[120:123], v[188:191], %[c03]\n\t" \
  "v_mfma_f32_16x16x32_bf16 %[c13], v[124:127], v[188:191], %[c13]\n\t" \
  "s_cmp_eq_u32 %[mine], 0\n\t" \
  "s_cbranch_scc1 Lend_%=\n\t" \
  "Ld_%=:\n\t" \
  "s_waitcnt lgkmcnt(0)\n\t" \
  "v_and_b32 v192, v192, v193\n\t" \
  "s_nop 0\n\t" \
  "v_readfirstlane_b32 %[s0], v192\n\t" \
  "s_cmp_eq_u32 %[s0], 0\n\t" \
  "s_cbranch_scc1 Ld1_%=\n\t" \
  "Ld2_%=:\n\t" \
  "v_add_f32 %[b00], %[b00], v128\n\t" \
  "v_add_f32 %[b01], %[b01], v129\n\t" \
  "v_add_f32 %[b02], %[b02], v130\n\t" \
  "v_add_f32 %[b03], %[b03], v131\n\t" \
  "v_add_f32 %[b10], %[b10], v132\n\t" \
  "v_add_f32 %[b11], %[b11], v133\n\t" \
  "v_add_f32 %[b12], %[b12], v134\n\t" \
  "v_add_f32 %[b13], %[b13], v135\n\t" \
  "v_add_f32 %[b20], %[b20], v136\n\t" \
  "v_add_f32 %[b21], %[b21], v137\n\t" \
  "v_add_f32 %[b22], %[b22], v138\n\t" \
  "v_add_f32 %[b23], %[b23], v139\n\t" \
  "v_add_f32 %[b30], %[b30], v140\n\t" \
  "v_add_f32 %[b31], %[b31], v141\n\t" \
  "v_add_f32 %[b32], %[b32], v142\n\t" \
  "v_add_f32 %[b33], %[b33], v143\n\t" \
  "v_add_f32 %[b40], %[b40], v144\n\t" \
  "v_add_f32 %[b41], %[b41], v145\n\t" \
  "v_add_f32 %[b42], %[b42], v146\n\t" \
  "v_add_f32 %[b43], %[b43], v147\n\t" \
  "v_add_f32 %[b50], %[b50], v148\n\t" \
  "v_add_f32 %[b51], %[b51], v149\n\t" \
  "v_add_f32 %[b52], %[b52], v150\n\t" \
  "v_add_f32 %[b53], %[b53], v151\n\t" \
  "v_add_f32 %[b60], %[b60], v152\n\t" \
  "v_add_f32 %[b61], %[b61], v153\n\t" \
  "v_add_f32 %[b62], %[b62], v154\n\t" \
  "v_add_f32 %[b63], %[b63], v155\n\t" \
  "v_add_f32 %[b70], %[b70], v156\n\t" \
  "v_add_f32 %[b71], %[b71], v157\n\t" \
  "v_add_f32 %[b72], %[b72], v158\n\t" \
  "v_add_f32 %[b73], %[b73], v159\n\t" \
  "s_branch Lend_%=\n\t" \
  "Lw_%=:\n\t" \
  "ds_read_b64 v[192:193], %[hr]\n\t" \
  "s_sub_i32 %[bud], %[bud], 1\n\t" \
  "s_cmp_lt_i32 %[bud], 0\n\t" \
  "s_cbranch_scc1 Lf3_%=\n\t" \
  "s_waitcnt lgkmcnt(0)\n\t" \
  "v_or_b32 v192, v192, v193\n\t" \
  "s_nop 0\n\t" \
  "v_readfirstlane_b32 %[s0], v192\n\t" \
  "s_cmp_eq_u32 %[s0], 0\n\t" \
  "s_cbranch_scc1 Lf3_%=\n\t" \
  "s_branch Lw_%=\n\t" \
  "Lh_%=:\n\t" \
  "buffer_load_dword v196, off, %[srd], %[sf1] sc1\n\t" \
  "s_branch Lhb_%=\n\t" \
  "Lg1_%=:\n\t" \
  "v_mov_b32 v196, 0\n\t" \
  "s_mov_b64 exec, 1\n\t" \
  "buffer_atomic_or v196, off, %[srd], %[sf1] sc0 sc1\n\t" \
  "s_mov_b64 exec, -1\n\t" \
  "s_sub_i32 %[bud], %[bud], 16\n\t" \
  "s_cmp_lt_i32 %[bud], 0\n\t" \
  "s_cbranch_scc1 Lgb_%=\n\t" \
  "s_waitcnt vmcnt(0)\n\t" \
  "s_branch Lg2_%=\n\t" \
  "Lg_%=:\n\t" \
  "s_waitcnt vmcnt(12)\n\t" \
  "Lg2_%=:\n\t" \
  "v_readfirstlane_b32 %[s0], v196\n\t" \
  "s_cmp_lg_u32 %[s0], 32\n\t" \
  "s_cbranch_scc1 Lg1_%=\n\t" \
  "s_branch Lgb_%=\n\t" \
  "Lcb1_%=:\n\t" \
  "ds_read_b64 v[192:193], %[fl] offset:0\n\t" \
  "s_sub_i32 %[bud], %[bud], 1\n\t" \
  "s_cmp_lt_i32 %[bud], 0\n\t" \
  "s_cbranch_scc1 Lcb1b_%=\n\t" \
  "s_waitcnt lgkmcnt(0)\n\t" \
  "v_or_b32 v192, v192, v193\n\t" \
  "s_nop 0\n\t" \
  "v_readfirstlane_b32 %[s0], v192\n\t" \
  "s_cmp_eq_u32 %[s0], 0\n\t" \
  "s_cbranch_scc1 Lcb1b_%=\n\t" \
  "s_branch Lcb1_%=\n\t" \
  "Lcb5_%=:\n\t" \
  "ds_read_b64 v[192:193], %[fl] offset:8\n\t" \
  "s_sub_i32 %[bud], %[bud], 1\n\t" \
  "s_cmp_lt_i32 %[bud], 0\n\t" \
  "s_cbranch_scc1 Lcb5b_%=\n\t" \
  "s_waitcnt lgkmcnt(0)\n\t" \
  "v_or_b32 v192, v192, v193\n\t" \
  "s_nop 0\n\t" \
  "v_readfirstlane_b32 %[s0], v192\n\t" \
  "s_cmp_eq_u32 %[s0], 0\n\t" \
  "s_cbranch_scc1 Lcb5b_%=\n\t" \
  "s_branch Lcb5_%=\n\t" \
  "Lcf1_%=:\n\t" \
  "ds_read_b64 v[192:193], %[fl] offset:0\n\t" \
  "s_sub_i32 %[bud], %[bud], 1\n\t" \
  "s_cmp_lt_i32 %[bud], 0\n\t" \
  "s_cbranch_scc1 Lcf1b_%=\n\t" \
  "s_waitcnt lgkmcnt(0)\n\t" \
  "v_or_b32 v192, v192, v193\n\t" \
  "s_nop 0\n\t" \
  "v_readfirstlane_b32 %[s0], v192\n\t" \
  "s_cmp_eq_u32 %[s0], 0\n\t" \
  "s_cbranch_scc1 Lcf1b_%=\n\t" \
  "s_branch Lcf1_%=\n\t" \
  "Ld1_%=:\n\t" \
  "s_sub_i32 %[bud], %[bud], 1\n\t" \
  "s_cmp_lt_i32 %[bud], 0\n\t" \
  "s_cbranch_scc1 Ld2_%=\n\t" \
  "ds_read_b64 v[192:193], %[xd]\n\t" \
  "ds_read_b128 v[128:131], %[d0]\n\t" \
  "ds_read_b128 v[132:135], %[d1]\n\t" \
  "ds_read_b128 v[136:139], %[d2]\n\t" \
  "ds_read_b128 v[140:143], %[d3]\n\t" \
  "ds_read_b128 v[144:147], %[d4]\n\t" \
  "ds_read_b128 v[148:151], %[d5]\n\t" \
  "ds_read_b128 v[152:155], %[d6]\n\t" \
  "ds_read_b128 v[156:159], %[d7]\n\t" \
  "s_branch Ld_%=\n\t" \
  "Lend_%=:\n\t" \
  "s_waitcnt lgkmcnt(0)\n\t" \
  "s_nop 6\n\t"
#define ASM_EN \
  "s_bfe_u32 %[j], %[bid], 0x30003\n\t" \
  "v_readfirstlane_b32 %[w], %[tid]\n\t" \
  "s_lshr_b32 %[w], %[w], 6\n\t" \
  "s_cmp_lg_u32 %[j], %[w]\n\t" \
  "s_cbranch_scc1 Lnc_%=\n\t" \
  "s_getpc_b64 s[20:21]\n\t" \
  "v_lshlrev_b32 %[v0], 7, %[tid]\n\t" \
  "v_min_u32 %[v0], %[cpf], %[v0]\n\t" \
  "global_load_dword v255, %[v0], s[20:21]\n\t" \
  "Lnc_%=:\n\t" \
  "s_cmp_gt_u32 %[w], 3\n\t" \
  "s_cbranch_scc1 Lend_%=\n\t" \
  "s_lshl3_add_u32 %[t], %[w], 8\n\t" \
  "s_cmp_lt_u32 %[w], 2\n\t" \
  "s_cselect_b32 %[t], 0, %[t]\n\t" \
  "s_load_dwordx2 s[20:21], %[kp], %[t]\n\t" \
  "s_cselect_b32 %[sa], 14, 11\n\t" \
  "s_cselect_b32 %[sc], 11, 15\n\t" \
  "s_cselect_b32 %[m], 16, 0\n\t" \
  "s_bfe_u32 %[r], %[bid], 0x20001\n\t" \
  "s_lshr_b32 %[t], %[bid], 6\n\t" \
  "s_lshl2_add_u32 %[c], %[r], %[t]\n\t" \
  "s_and_b32 %[t], %[bid], 1\n\t" \
  "s_lshl3_add_u32 %[j], %[t], %[j]\n\t" \
  "s_lshl1_add_u32 %[r], %[t], %[r]\n\t" \
  "s_and_b32 %[r], %[r], 3\n\t" \
  "s_lshl_b32 %[c], %[c], 5\n\t" \
  "s_add_u32 %[c], %[c], %[w]\n\t" \
  "s_lshl_b32 %[j], %[j], 4\n\t" \
  "s_cmp_lt_u32 %[w], 2\n\t" \
  "s_cselect_b32 %[c], %[c], %[j]\n\t" \
  "v_bfe_u32 %[v1], %[tid], 3, 3\n\t" \
  "v_xor_b32 %[v0], %[v1], %[tid]\n\t" \
  "v_and_b32 %[v0], 7, %[v0]\n\t" \
  "v_lshlrev_b32 %[v0], 4, %[v0]\n\t" \
  "v_lshl_add_u32 %[v0], %[r], 9, %[v0]\n\t" \
  "v_lshl_add_u32 %[v0], %[v1], %[sa], %[v0]\n\t" \
  "v_lshl_add_u32 %[v0], %[c], 13, %[v0]\n\t" \
  "s_lshl_b32 %[sa], 8, %[sa]\n\t" \
  "s_lshl_b32 %[sc], 1, %[sc]\n\t" \
  "v_add_u32 %[v1], %[sa], %[v0]\n\t" \
  "s_add_u32 %[m], %[m], %[w]\n\t" \
  "s_lshl_b32 m0, %[m], 13\n\t" \
  "s_waitcnt lgkmcnt(0)\n\t" \
  "Ll_%=:\n\t" \
  "global_load_lds_dwordx4 %[v0], s[20:21]\n\t" \
  "s_addk_i32 m0, 0x400\n\t" \
  "v_add_u32 %[v0], %[sc], %[v0]\n\t" \
  "global_load_lds_dwordx4 %[v1], s[20:21]\n\t" \
  "s_addk_i32 m0, 0x400\n\t" \
  "v_add_u32 %[v1], %[sc], %[v1]\n\t" \
  "s_lshl_b32 %[t], m0, 19\n\t" \
  "s_cbranch_scc1 Ll_%=\n\t" \
  "s_lshl2_add_u32 m0, %[w], %[ms0]\n\t" \
  "s_and_b32 s21, s21, 0xffff\n\t" \
  "v_mov_b32 %[v0], m0\n\t" \
  "ds_write_b32 %[v0], %[v0]\n\t" \
  "s_mov_b32 s22, 0\n\t" \
  "s_mov_b32 s23, 0x20000\n\t" \
  "s_mov_b64 exec, 1\n\t" \
  "s_waitcnt lgkmcnt(0)\n\t" \
  "buffer_load_dword off, s[20:23], 0 lds\n\t" \
  "s_mov_b64 exec, -1\n\t" \
  "s_barrier\n\t" \
  "Lend_%=:\n\t"
#define ASM_LA \
  "s_nop 4\n\t" \
  "s_barrier\n\t" \
  "Ltop_%=:\n\t" \
  "s_mov_b32 m0, %[dst]\n\t" \
  "v_mov_b32 %[vc], %[ci]\n\t" \
  "buffer_load_dwordx4 %[v0], %[r0], %[so0] offen lds\n\t" \
  "s_addk_i32 m0, 0x400\n\t" \
  "s_add_u32 %[dst], %[dst], 0x8000\n\t" \
  "buffer_load_dwordx4 %[v0], %[r0], %[so1] offen lds\n\t" \
  "s_addk_i32 m0, 0x400\n\t" \
  "s_cmp_ge_u32 %[dst], 0x24000\n\t" \
  "s_cselect_b32 %[s0], %[wrap], 0\n\t" \
  "buffer_load_dwordx4 %[v0], %[r0], %[so2] offen lds\n\t" \
  "s_addk_i32 m0, 0x400\n\t" \
  "s_sub_u32 %[dst], %[dst], %[s0]\n\t" \
  "buffer_load_dwordx4 %[v0], %[r0], %[so3] offen lds\n\t" \
  "s_addk_i32 m0, 0x400\n\t" \
  "s_add_u32 %[sk], %[sk], 128\n\t" \
  "buffer_load_dwordx4 %[v0], %[r0], %[so4] offen lds\n\t" \
  "s_addk_i32 m0, 0x400\n\t" \
  "s_and_b32 %[sk], %[sk], 2047\n\t" \
  "buffer_load_dwordx4 %[v0], %[r0], %[so5] offen lds\n\t" \
  "s_addk_i32 m0, 0x400\n\t" \
  "s_nop 0\n\t" \
  "buffer_load_dwordx4 %[v0], %[r0], %[so6] offen lds\n\t" \
  "s_addk_i32 m0, 0x400\n\t" \
  "s_add_u32 %[ci], %[ci], 16\n\t" \
  "buffer_load_dwordx4 %[v0], %[r0], %[so7] offen lds\n\t" \
  "s_addk_i32 m0, 0x400\n\t" \
  "s_sub_u32 %[n], %[n], 1\n\t" \
  "buffer_load_dwordx4 %[v0], %[r1], %[so0] offen lds\n\t" \
  "s_addk_i32 m0, 0x400\n\t" \
  "s_nop 0\n\t" \
  "buffer_load_dwordx4 %[v0], %[r1], %[so1] offen lds\n\t" \
  "s_addk_i32 m0, 0x400\n\t" \
  "s_nop 0\n\t" \
  "buffer_load_dwordx4 %[v0], %[r1], %[so2] offen lds\n\t" \
  "s_addk_i32 m0, 0x400\n\t" \
  "s_nop 0\n\t" \
  "buffer_load_dwordx4 %[v0], %[r1], %[so3] offen lds\n\t" \
  "s_addk_i32 m0, 0x400\n\t" \
  "ds_read_b128 v[24:27], %[vc]\n\t" \
  "ds_read_b128 v[28:31], %[vw]\n\t" \
  "buffer_load_dwordx4 %[v0], %[r1], %[so4] offen lds\n\t" \
  "s_addk_i32 m0, 0x400\n\t" \
  "s_nop 0\n\t" \
  "buffer_load_dwordx4 %[v0], %[r1], %[so5] offen lds\n\t" \
  "s_addk_i32 m0, 0x400\n\t" \
  "s_nop 0\n\t" \
  "buffer_load_dwordx4 %[v0], %[r1], %[so6] offen lds\n\t" \
  "s_addk_i32 m0, 0x400\n\t" \
  "s_waitcnt lgkmcnt(0)\n\t" \
  "v_or3_b32 v24, v24, v25, v26\n\t" \
  "v_or_b32 v24, v24, v27\n\t" \
  "v_sub_u32 v28, v30, v28\n\t" \
  "v_sub_u32 v29, v31, v29\n\t" \
  "v_or3_b32 v24, v24, v28, v29\n\t" \
  "buffer_load_dwordx4 %[v0], %[r1], %[so7] offen lds\n\t" \
  "s_mov_b32 m0, %[mk]\n\t" \
  "s_mov_b64 exec, 1\n\t" \
  "buffer_load_dword %[v0], %[rz], 0 offen lds\n\t" \
  "s_mov_b64 exec, -1\n\t" \
  "v_readfirstlane_b32 %[s0], v24\n\t" \
  "v_add_u32 %[v0], %[sk], %[b0]\n\t" \
  "s_add_u32 %[mk], %[mk], 8\n\t" \
  "s_cmp_eq_u32 %[n], 0\n\t" \
  "s_cbranch_scc1 Lend_%=\n\t" \
  "s_cmp_eq_u32 %[s0], 0\n\t" \
  "s_cbranch_scc1 Ltop_%=\n\t" \
  "Lsp_%=:\n\t" \
  "ds_read_b128 v[24:27], %[vc]\n\t" \
  "ds_read_b128 v[28:31], %[vw]\n\t" \
  "s_sub_i32 %[bud], %[bud], 1\n\t" \
  "s_cmp_lt_i32 %[bud], 0\n\t" \
  "s_cbranch_scc1 Ltop_%=\n\t" \
  "s_waitcnt lgkmcnt(0)\n\t" \
  "v_or3_b32 v24, v24, v25, v26\n\t" \
  "v_or_b32 v24, v24, v27\n\t" \
  "v_sub_u32 v28, v30, v28\n\t" \
  "v_sub_u32 v29, v31, v29\n\t" \
  "v_or3_b32 v24, v24, v28, v29\n\t" \
  "s_nop 0\n\t" \
  "v_readfirstlane_b32 %[s0], v24\n\t" \
  "s_cmp_eq_u32 %[s0], 0\n\t" \
  "s_cbranch_scc1 Ltop_%=\n\t" \
  "s_branch Lsp_%=\n\t" \
  "Lend_%=:\n\t"
#define ASM_LB \
  "s_nop 4\n\t" \
  "s_branch Lsp_%=\n\t" \
  "Ltop_%=:\n\t" \
  "s_mov_b32 m0, %[dst]\n\t" \
  "v_mov_b32 %[vc], %[ci]\n\t" \
  "buffer_load_dwordx4 %[v0], %[r0], %[so0] offen lds\n\t" \
  "s_addk_i32 m0, 0x400\n\t" \
  "s_add_u32 %[dst], %[dst], 0x8000\n\t" \
  "buffer_load_dwordx4 %[v1], %[r0], %[so1] offen lds\n\t" \
  "s_addk_i32 m0, 0x400\n\t" \
  "s_cmp_ge_u32 %[dst], 0x24000\n\t" \
  "s_cselect_b32 %[s0], %[wrap], 0\n\t" \
  "buffer_load_dwordx4 %[v0], %[r0], %[so2] offen lds\n\t" \
  "s_addk_i32 m0, 0x400\n\t" \
  "s_sub_u32 %[dst], %[dst], %[s0]\n\t" \
  "buffer_load_dwordx4 %[v1], %[r0], %[so3] offen lds\n\t" \
  "s_addk_i32 m0, 0x400\n\t" \
  "s_add_u32 %[sk], %[sk], 256\n\t" \
  "buffer_load_dwordx4 %[v0], %[r0], %[so4] offen lds\n\t" \
  "s_addk_i32 m0, 0x400\n\t" \
  "s_and_b32 %[sk], %[sk], 1023\n\t" \
  "buffer_load_dwordx4 %[v1], %[r0], %[so5] offen lds\n\t" \
  "s_addk_i32 m0, 0x400\n\t" \
  "s_cmp_ge_u32 %[ci], %[cth]\n\t" \
  "s_cselect_b32 %[s0], 32, 16\n\t" \
  "buffer_load_dwordx4 %[v0], %[r0], %[so6] offen lds\n\t" \
  "s_addk_i32 m0, 0x400\n\t" \
  "s_add_u32 %[ci], %[ci], %[s0]\n\t" \
  "buffer_load_dwordx4 %[v1], %[r0], %[so7] offen lds\n\t" \
  "s_addk_i32 m0, 0x400\n\t" \
  "s_sub_u32 %[n], %[n], 1\n\t" \
  "buffer_load_dwordx4 %[v0], %[r1], %[so0] offen lds\n\t" \
  "s_addk_i32 m0, 0x400\n\t" \
  "s_cmp_eq_u32 %[n], 4\n\t" \
  "s_cselect_b32 %[s0], 0x400, 0\n\t" \
  "buffer_load_dwordx4 %[v1], %[r1], %[so1] offen lds\n\t" \
  "s_addk_i32 m0, 0x400\n\t" \
  "s_xor_b32 %[hi], %[hi], %[s0]\n\t" \
  "buffer_load_dwordx4 %[v0], %[r1], %[so2] offen lds\n\t" \
  "s_addk_i32 m0, 0x400\n\t" \
  "s_or_b32 %[s1], %[sk], %[hi]\n\t" \
  "buffer_load_dwordx4 %[v1], %[r1], %[so3] offen lds\n\t" \
  "s_addk_i32 m0, 0x400\n\t" \
  "ds_read_b128 v[24:27], %[vc]\n\t" \
  "buffer_load_dwordx4 %[v0], %[r1], %[so4] offen lds\n\t" \
  "s_addk_i32 m0, 0x400\n\t" \
  "s_nop 0\n\t" \
  "buffer_load_dwordx4 %[v1], %[r1], %[so5] offen lds\n\t" \
  "s_addk_i32 m0, 0x400\n\t" \
  "s_nop 0\n\t" \
  "buffer_load_dwordx4 %[v0], %[r1], %[so6] offen lds\n\t" \
  "s_addk_i32 m0, 0x400\n\t" \
  "s_waitcnt lgkmcnt(0)\n\t" \
  "v_or3_b32 v24, v24, v25, v26\n\t" \
  "v_or_b32 v24, v24, v27\n\t" \
  "buffer_load_dwordx4 %[v1], %[r1], %[so7] offen lds\n\t" \
  "s_mov_b32 m0, %[mk]\n\t" \
  "s_mov_b64 exec, 3\n\t" \
  "buffer_load_dword %[v0], %[rz], 0 offen lds\n\t" \
  "s_mov_b64 exec, -1\n\t" \
  "v_readfirstlane_b32 %[s0], v24\n\t" \
  "v_add_u32 %[v0], %[s1], %[b0]\n\t" \
  "v_add_u32 %[v1], %[s1], %[b1]\n\t" \
  "s_add_u32 %[mk], %[mk], 16\n\t" \
  "s_cmp_eq_u32 %[n], %[nend]\n\t" \
  "s_cbranch_scc1 Lend_%=\n\t" \
  "s_cmp_eq_u32 %[s0], 0\n\t" \
  "s_cbranch_scc1 Ltop_%=\n\t" \
  "Lsp_%=:\n\t" \
  "ds_read_b128 v[24:27], %[vc]\n\t" \
  "s_sub_i32 %[bud], %[bud], 1\n\t" \
  "s_cmp_lt_i32 %[bud], 0\n\t" \
  "s_cbranch_scc1 Ltop_%=\n\t" \
  "s_waitcnt lgkmcnt(0)\n\t" \
  "v_or3_b32 v24, v24, v25, v26\n\t" \
  "v_or_b32 v24, v24, v27\n\t" \
  "s_nop 0\n\t" \
  "v_readfirstlane_b32 %[s0], v24\n\t" \
  "s_cmp_eq_u32 %[s0], 0\n\t" \
  "s_cbranch_scc1 Ltop_%=\n\t" \
  "s_sleep 1\n\t" \
  "s_branch Lsp_%=\n\t" \
  "Lend_%=:\n\t" \
  "buffer_load_dwordx4 v[32:35], %[p0], %[rp], %[so0] offen offset:128\n\t" \
  "buffer_load_dwordx4 v[36:39], %[p1], %[rp], %[so1] offen offset:128\n\t" \
  "buffer_load_dwordx4 v[40:43], %[p0], %[rp], %[so2] offen offset:128\n\t" \
  "buffer_load_dwordx4 v[44:47], %[p1], %[rp], %[so3] offen offset:128\n\t" \
  "buffer_load_dwordx4 v[48:51], %[p0], %[rp], %[so4] offen offset:128\n\t" \
  "buffer_load_dwordx4 v[52:55], %[p1], %[rp], %[so5] offen offset:128\n\t" \
  "buffer_load_dwordx4 v[56:59], %[p0], %[rp], %[so6] offen offset:128\n\t" \
  "buffer_load_dwordx4 v[60:63], %[p1], %[rp], %[so7] offen offset:128\n\t" \
  "buffer_load_dwordx4 v[64:67], %[p0], %[rp], %[so0] offen offset:256\n\t" \
  "buffer_load_dwordx4 v[68:71], %[p1], %[rp], %[so1] offen offset:256\n\t" \
  "buffer_load_dwordx4 v[72:75], %[p0], %[rp], %[so2] offen offset:256\n\t" \
  "buffer_load_dwordx4 v[76:79], %[p1], %[rp], %[so3] offen offset:256\n\t" \
  "buffer_load_dwordx4 v[80:83], %[p0], %[rp], %[so4] offen offset:256\n\t" \
  "buffer_load_dwordx4 v[84:87], %[p1], %[rp], %[so5] offen offset:256\n\t" \
  "buffer_load_dwordx4 v[88:91], %[p0], %[rp], %[so6] offen offset:256\n\t" \
  "buffer_load_dwordx4 v[92:95], %[p1], %[rp], %[so7] offen offset:256\n\t" \
  "buffer_load_dwordx4 v[96:99], %[p0], %[rp], %[so0] offen offset:384\n\t" \
  "buffer_load_dwordx4 v[100:103], %[p1], %[rp], %[so1] offen offset:384\n\t" \
  "buffer_load_dwordx4 v[104:107], %[p0], %[rp], %[so2] offen offset:384\n\t" \
  "buffer_load_dwordx4 v[108:111], %[p1], %[rp], %[so3] offen offset:384\n\t" \
  "buffer_load_dwordx4 v[112:115], %[p0], %[rp], %[so4] offen offset:384\n\t" \
  "buffer_load_dwordx4 v[116:119], %[p1], %[rp], %[so5] offen offset:384\n\t" \
  "buffer_load_dwordx4 v[120:123], %[p0], %[rp], %[so6] offen offset:384\n\t" \
  "buffer_load_dwordx4 v[124:127], %[p1], %[rp], %[so7] offen offset:384\n\t" \
  "buffer_load_dwordx4 v[128:131], %[p0], %[rp], %[so0] offen offset:512\n\t" \
  "buffer_load_dwordx4 v[132:135], %[p1], %[rp], %[so1] offen offset:512\n\t" \
  "buffer_load_dwordx4 v[136:139], %[p0], %[rp], %[so2] offen offset:512\n\t" \
  "buffer_load_dwordx4 v[140:143], %[p1], %[rp], %[so3] offen offset:512\n\t" \
  "buffer_load_dwordx4 v[144:147], %[p0], %[rp], %[so4] offen offset:512\n\t" \
  "buffer_load_dwordx4 v[148:151], %[p1], %[rp], %[so5] offen offset:512\n\t" \
  "buffer_load_dwordx4 v[152:155], %[p0], %[rp], %[so6] offen offset:512\n\t" \
  "buffer_load_dwordx4 v[156:159], %[p1], %[rp], %[so7] offen offset:512\n\t" \
  "buffer_load_dwordx4 v[160:163], %[p0], %[rp], %[so0] offen offset:640\n\t" \
  "buffer_load_dwordx4 v[164:167], %[p1], %[rp], %[so1] offen offset:640\n\t" \
  "buffer_load_dwordx4 v[168:171], %[p0], %[rp], %[so2] offen offset:640\n\t" \
  "buffer_load_dwordx4 v[172:175], %[p1], %[rp], %[so3] offen offset:640\n\t" \
  "buffer_load_dwordx4 v[176:179], %[p0], %[rp], %[so4] offen offset:640\n\t" \
  "buffer_load_dwordx4 v[180:183], %[p1], %[rp], %[so5] offen offset:640\n\t" \
  "buffer_load_dwordx4 v[184:187], %[p0], %[rp], %[so6] offen offset:640\n\t" \
  "buffer_load_dwordx4 v[188:191], %[p1], %[rp], %[so7] offen offset:640\n\t" \
  "buffer_load_dwordx4 v[192:195], %[p0], %[rp], %[so0] offen offset:768\n\t" \
  "buffer_load_dwordx4 v[196:199], %[p1], %[rp], %[so1] offen offset:768\n\t" \
  "buffer_load_dwordx4 v[200:203], %[p0], %[rp], %[so2] offen offset:768\n\t" \
  "buffer_load_dwordx4 v[204:207], %[p1], %[rp], %[so3] offen offset:768\n\t" \
  "buffer_load_dwordx4 v[208:211], %[p0], %[rp], %[so4] offen offset:768\n\t" \
  "buffer_load_dwordx4 v[212:215], %[p1], %[rp], %[so5] offen offset:768\n\t" \
  "buffer_load_dwordx4 v[216:219], %[p0], %[rp], %[so6] offen offset:768\n\t" \
  "buffer_load_dwordx4 v[220:223], %[p1], %[rp], %[so7] offen offset:768\n\t" \
  "buffer_load_dwordx4 v[224:227], %[p0], %[rp], %[so0] offen offset:896\n\t" \
  "buffer_load_dwordx4 v[228:231], %[p1], %[rp], %[so1] offen offset:896\n\t" \
  "buffer_load_dwordx4 v[232:235], %[p0], %[rp], %[so2] offen offset:896\n\t" \
  "buffer_load_dwordx4 v[236:239], %[p1], %[rp], %[so3] offen offset:896\n\t" \
  "buffer_load_dwordx4 v[240:243], %[p0], %[rp], %[so4] offen offset:896\n\t" \
  "buffer_load_dwordx4 v[244:247], %[p1], %[rp], %[so5] offen offset:896\n\t" \
  "buffer_load_dwordx4 v[248:251], %[p0], %[rp], %[so6] offen offset:896\n\t" \
  "buffer_load_dwordx4 v[252:255], %[p1], %[rp], %[so7] offen offset:896\n\t" \
  "v_mov_b32 v28, 0\n\t" \
  "Lw0_%=:\n\t" \
  "ds_read_b128 v[24:27], %[vm] offset:0\n\t" \
  "s_sub_i32 %[bud], %[bud], 1\n\t" \
  "s_cmp_lt_i32 %[bud], 0\n\t" \
  "s_cbranch_scc1 Lx0_%=\n\t" \
  "s_waitcnt lgkmcnt(0)\n\t" \
  "v_or3_b32 v24, v24, v25, v26\n\t" \
  "v_or_b32 v24, v24, v27\n\t" \
  "s_nop 0\n\t" \
  "v_readfirstlane_b32 %[s0], v24\n\t" \
  "s_cmp_eq_u32 %[s0], 0\n\t" \
  "s_cbranch_scc1 Lx0_%=\n\t" \
  "s_sleep 2\n\t" \
  "s_branch Lw0_%=\n\t" \
  "Lx0_%=:\n\t" \
  "s_waitcnt vmcnt(0)\n\t" \
  "ds_write_b128 %[vd], v[32:35] offset:0\n\t" \
  "ds_write_b128 %[vd], v[36:39] offset:1024\n\t" \
  "ds_write_b128 %[vd], v[40:43] offset:2048\n\t" \
  "ds_write_b128 %[vd], v[44:47] offset:3072\n\t" \
  "ds_write_b128 %[vd], v[48:51] offset:4096\n\t" \
  "ds_write_b128 %[vd], v[52:55] offset:5120\n\t" \
  "ds_write_b128 %[vd], v[56:59] offset:6144\n\t" \
  "ds_write_b128 %[vd], v[60:63] offset:7168\n\t" \
  "s_waitcnt lgkmcnt(0)\n\t" \
  "ds_write_b32 %[vl], v28 offset:0\n\t" \
  "Lw1_%=:\n\t" \
  "ds_read_b128 v[24:27], %[vm] offset:16\n\t" \
  "s_sub_i32 %[bud], %[bud], 1\n\t" \
  "s_cmp_lt_i32 %[bud], 0\n\t" \
  "s_cbranch_scc1 Lx1_%=\n\t" \
  "s_waitcnt lgkmcnt(0)\n\t" \
  "v_or3_b32 v24, v24, v25, v26\n\t" \
  "v_or_b32 v24, v24, v27\n\t" \
  "s_nop 0\n\t" \
  "v_readfirstlane_b32 %[s0], v24\n\t" \
  "s_cmp_eq_u32 %[s0], 0\n\t" \
  "s_cbranch_scc1 Lx1_%=\n\t" \
  "s_sleep 2\n\t" \
  "s_branch Lw1_%=\n\t" \
  "Lx1_%=:\n\t" \
  "ds_write_b128 %[vd], v[64:67] offset:16384\n\t" \
  "ds_write_b128 %[vd], v[68:71] offset:17408\n\t" \
  "ds_write_b128 %[vd], v[72:75] offset:18432\n\t" \
  "ds_write_b128 %[vd], v[76:79] offset:19456\n\t" \
  "ds_write_b128 %[vd], v[80:83] offset:20480\n\t" \
  "ds_write_b128 %[vd], v[84:87] offset:21504\n\t" \
  "ds_write_b128 %[vd], v[88:91] offset:22528\n\t" \
  "ds_write_b128 %[vd], v[92:95] offset:23552\n\t" \
  "s_waitcnt lgkmcnt(0)\n\t" \
  "ds_write_b32 %[vl], v28 offset:8\n\t" \
  "Lw2_%=:\n\t" \
  "ds_read_b128 v[24:27], %[vm] offset:32\n\t" \
  "s_sub_i32 %[bud], %[bud], 1\n\t" \
  "s_cmp_lt_i32 %[bud], 0\n\t" \
  "s_cbranch_scc1 Lx2_%=\n\t" \
  "s_waitcnt lgkmcnt(0)\n\t" \
  "v_or3_b32 v24, v24, v25, v26\n\t" \
  "v_or_b32 v24, v24, v27\n\t" \
  "s_nop 0\n\t" \
  "v_readfirstlane_b32 %[s0], v24\n\t" \
  "s_cmp_eq_u32 %[s0], 0\n\t" \
  "s_cbranch_scc1 Lx2_%=\n\t" \
  "s_sleep 2\n\t" \
  "s_branch Lw2_%=\n\t" \
  "Lx2_%=:\n\t" \
  "ds_write_b128 %[vd], v[96:99] offset:32768\n\t" \
  "ds_write_b128 %[vd], v[100:103] offset:33792\n\t" \
  "ds_write_b128 %[vd], v[104:107] offset:34816\n\t" \
  "ds_write_b128 %[vd], v[108:111] offset:35840\n\t" \
  "ds_write_b128 %[vd], v[112:115] offset:36864\n\t" \
  "ds_write_b128 %[vd], v[116:119] offset:37888\n\t" \
  "ds_write_b128 %[vd], v[120:123] offset:38912\n\t" \
  "ds_write_b128 %[vd], v[124:127] offset:39936\n\t" \
  "s_waitcnt lgkmcnt(0)\n\t" \
  "ds_write_b32 %[vl], v28 offset:16\n\t" \
  "Lw3_%=:\n\t" \
  "ds_read_b128 v[24:27], %[vm] offset:48\n\t" \
  "s_sub_i32 %[bud], %[bud], 1\n\t" \
  "s_cmp_lt_i32 %[bud], 0\n\t" \
  "s_cbranch_scc1 Lx3_%=\n\t" \
  "s_waitcnt lgkmcnt(0)\n\t" \
  "v_or3_b32 v24, v24, v25, v26\n\t" \
  "v_or_b32 v24, v24, v27\n\t" \
  "s_nop 0\n\t" \
  "v_readfirstlane_b32 %[s0], v24\n\t" \
  "s_cmp_eq_u32 %[s0], 0\n\t" \
  "s_cbranch_scc1 Lx3_%=\n\t" \
  "s_sleep 2\n\t" \
  "s_branch Lw3_%=\n\t" \
  "Lx3_%=:\n\t" \
  "ds_write_b128 %[vd], v[128:131] offset:49152\n\t" \
  "ds_write_b128 %[vd], v[132:135] offset:50176\n\t" \
  "ds_write_b128 %[vd], v[136:139] offset:51200\n\t" \
  "ds_write_b128 %[vd], v[140:143] offset:52224\n\t" \
  "ds_write_b128 %[vd], v[144:147] offset:53248\n\t" \
  "ds_write_b128 %[vd], v[148:151] offset:54272\n\t" \
  "ds_write_b128 %[vd], v[152:155] offset:55296\n\t" \
  "ds_write_b128 %[vd], v[156:159] offset:56320\n\t" \
  "v_add_u32 %[vd], 0x10000, %[vd]\n\t" \
  "s_waitcnt lgkmcnt(0)\n\t" \
  "ds_write_b32 %[vl], v28 offset:24\n\t" \
  "Lw4_%=:\n\t" \
  "ds_read_b128 v[24:27], %[vm] offset:64\n\t" \
  "s_sub_i32 %[bud], %[bud], 1\n\t" \
  "s_cmp_lt_i32 %[bud], 0\n\t" \
  "s_cbranch_scc1 Lx4_%=\n\t" \
  "s_waitcnt lgkmcnt(0)\n\t" \
  "v_or3_b32 v24, v24, v25, v26\n\t" \
  "v_or_b32 v24, v24, v27\n\t" \
  "s_nop 0\n\t" \
  "v_readfirstlane_b32 %[s0], v24\n\t" \
  "s_cmp_eq_u32 %[s0], 0\n\t" \
  "s_cbranch_scc1 Lx4_%=\n\t" \
  "s_sleep 2\n\t" \
  "s_branch Lw4_%=\n\t" \
  "Lx4_%=:\n\t" \
  "ds_write_b128 %[vd], v[160:163] offset:0\n\t" \
  "ds_write_b128 %[vd], v[164:167] offset:1024\n\t" \
  "ds_write_b128 %[vd], v[168:171] offset:2048\n\t" \
  "ds_write_b128 %[vd], v[172:175] offset:3072\n\t" \
  "ds_write_b128 %[vd], v[176:179] offset:4096\n\t" \
  "ds_write_b128 %[vd], v[180:183] offset:5120\n\t" \
  "ds_write_b128 %[vd], v[184:187] offset:6144\n\t" \
  "ds_write_b128 %[vd], v[188:191] offset:7168\n\t" \
  "s_waitcnt lgkmcnt(0)\n\t" \
  "ds_write_b32 %[vl], v28 offset:32\n\t" \
  "Lw5_%=:\n\t" \
  "ds_read_b128 v[24:27], %[vm] offset:80\n\t" \
  "s_sub_i32 %[bud], %[bud], 1\n\t" \
  "s_cmp_lt_i32 %[bud], 0\n\t" \
  "s_cbranch_scc1 Lx5_%=\n\t" \
  "s_waitcnt lgkmcnt(0)\n\t" \
  "v_or3_b32 v24, v24, v25, v26\n\t" \
  "v_or_b32 v24, v24, v27\n\t" \
  "s_nop 0\n\t" \
  "v_readfirstlane_b32 %[s0], v24\n\t" \
  "s_cmp_eq_u32 %[s0], 0\n\t" \
  "s_cbranch_scc1 Lx5_%=\n\t" \
  "s_sleep 2\n\t" \
  "s_branch Lw5_%=\n\t" \
  "Lx5_%=:\n\t" \
  "ds_write_b128 %[vd], v[192:195] offset:16384\n\t" \
  "ds_write_b128 %[vd], v[196:199] offset:17408\n\t" \
  "ds_write_b128 %[vd], v[200:203] offset:18432\n\t" \
  "ds_write_b128 %[vd], v[204:207] offset:19456\n\t" \
  "ds_write_b128 %[vd], v[208:211] offset:20480\n\t" \
  "ds_write_b128 %[vd], v[212:215] offset:21504\n\t" \
  "ds_write_b128 %[vd], v[216:219] offset:22528\n\t" \
  "ds_write_b128 %[vd], v[220:223] offset:23552\n\t" \
  "s_waitcnt lgkmcnt(0)\n\t" \
  "ds_write_b32 %[vl], v28 offset:40\n\t" \
  "Lw6_%=:\n\t" \
  "ds_read_b128 v[24:27], %[vm] offset:96\n\t" \
  "s_sub_i32 %[bud], %[bud], 1\n\t" \
  "s_cmp_lt_i32 %[bud], 0\n\t" \
  "s_cbranch_scc1 Lx6_%=\n\t" \
  "s_waitcnt lgkmcnt(0)\n\t" \
  "v_or3_b32 v24, v24, v25, v26\n\t" \
  "v_or_b32 v24, v24, v27\n\t" \
  "s_nop 0\n\t" \
  "v_readfirstlane_b32 %[s0], v24\n\t" \
  "s_cmp_eq_u32 %[s0], 0\n\t" \
  "s_cbranch_scc1 Lx6_%=\n\t" \
  "s_sleep 2\n\t" \
  "s_branch Lw6_%=\n\t" \
  "Lx6_%=:\n\t" \
  "ds_write_b128 %[vd], v[224:227] offset:32768\n\t" \
  "ds_write_b128 %[vd], v[228:231] offset:33792\n\t" \
  "ds_write_b128 %[vd], v[232:235] offset:34816\n\t" \
  "ds_write_b128 %[vd], v[236:239] offset:35840\n\t" \
  "ds_write_b128 %[vd], v[240:243] offset:36864\n\t" \
  "ds_write_b128 %[vd], v[244:247] offset:37888\n\t" \
  "ds_write_b128 %[vd], v[248:251] offset:38912\n\t" \
  "ds_write_b128 %[vd], v[252:255] offset:39936\n\t" \
  "s_waitcnt lgkmcnt(0)\n\t" \
  "ds_write_b32 %[vl], v28 offset:48\n\t" \
  "s_waitcnt lgkmcnt(0)\n\t"

#define OST " sc1"
#define XPRIO0 3
#define XPRIO 1
#define LPRIO 2
#define FPRIO 1
#define MPRIO 2
#define WGRP (~sg & 1)
#define WS0 1
#define WS1 5
#define WSLP 8
#define TKG0 3
#define GEXACT 0
#define YPRIO 3
#define GPRIO 1
#define W2G 14
#define CWS 16
#define HELP 1
#define WRES 1
#define XG -2
#define XSLP 4
#define GUG 25
#define TKG 4
#define YG 1
#define NOFFN 0
#define CODEPF 29952
#define WNT " nt"
constexpr int D_ = 1024, F_ = 3584;
constexpr int MISC = 147456;
constexpr int L_TAB = MISC;
constexpr int L_FLG = L_TAB + 11 * 512;
constexpr int L_ONE = L_FLG + 256;
constexpr int L_G = L_ONE + 1024;
constexpr int L_SEL = L_G + 512;
constexpr int L_BROW = L_SEL + 128;
constexpr int L_ACT = L_BROW + 2048;
constexpr int L_XS = L_ACT + 1024;
constexpr int L_JUNK = L_XS + 4096;
constexpr int LDS_BYTES = 163840;
static_assert(L_JUNK + 768 <= LDS_BYTES, "lds");
constexpr int M_CONS = L_ONE + 16, M_LAND = L_ONE + 544, M_GT = M_LAND + 288, M_HR = M_LAND + 296, M_CAR = M_LAND + 304, M_XS = M_LAND + 320, M_WR = M_LAND + 336;
constexpr int M_S0 = L_SEL + 64;
static_assert(M_S0 - M_LAND == 1056, "offset in ASM_MA");
constexpr int FL_XD = L_FLG + 16, FL_TK = L_FLG + 24;
constexpr unsigned W_EPOCH = 0;
constexpr unsigned W_RG = 256;
constexpr unsigned W_R2 = W_RG + 2048 * 8;
constexpr unsigned W_HF = 32768;
constexpr unsigned W_GF = W_HF + 32 * 128;
constexpr unsigned W_YF = W_GF + 56 * 128;
constexpr unsigned W_HT = W_YF + 8 * 128;
constexpr unsigned W_AGG = 65536;
constexpr unsigned W_GU = W_AGG + 262144;
constexpr unsigned W_YP = W_GU + 56 * 16384;
constexpr unsigned W_H = 4u << 20;
#define HSK 24
#define HOFS(l) ((l) * 16 + (2 > 1 && (l) >= HSK ? 128 : 0))
constexpr unsigned W_DBG = 24u << 20;
constexpr unsigned W_END = 25u << 20;
static_assert(W_R2 + 8192 <= W_HF && W_HT + 8192 <= W_AGG && W_YP + 128 * 56 * 256 <= W_H, "ws");
#define WS_BASE(n) (((n) - W_END) & ~(size_t)0x1fffff)
constexpr int SPIN_MAX = 1 << 18;

DEV float bf2f(unsigned v) { return __uint_as_float(v << 16); }
DEV float bfhi(unsigned v) { return __uint_as_float(v & 0xffff0000u); }
DEV u16 f2bf(float f) { return __builtin_bit_cast(u16, (__bf16)f); }
DEV float rbf(float f) { return bf2f(f2bf(f)); }
typedef __bf16 bf16x2_t __attribute__((ext_vector_type(2)));
DEV unsigned pk2(float a, float b) { return __builtin_bit_cast(unsigned, __builtin_convertvector((f32x2){a, b}, bf16x2_t)); }
DEV float fma1(float a, float b, float c) { float d; asm("v_fma_f32 %0, %1, %2, %3" : "=v"(d) : "v"(a), "v"(b), "v"(c)); return d; }
DEV float fnma1(float a, float b, float c) { float d; asm("v_fma_f32 %0, -%1, %2, %3" : "=v"(d) : "v"(a), "v"(b), "v"(c)); return d; }
DEV float mul1(float a, float b) { float d; asm("v_mul_f32 %0, %1, %2" : "=v"(d) : "v"(a), "v"(b)); return d; }
DEV f32x2 pkfma(f32x2 a, f32x2 b, f32x2 c) { f32x2 d; asm("v_pk_fma_f32 %0, %1, %2, %3" : "=v"(d) : "v"(a), "v"(b), "v"(c)); return d; }
DEV float add1(float a, float b) { float d; asm("v_add_f32 %0, %1, %2" : "=v"(d) : "v"(a), "v"(b)); return d; }
DEV f32x2 pkmul(f32x2 a, f32x2 b) { f32x2 d; asm("v_pk_mul_f32 %0, %1, %2" : "=v"(d) : "v"(a), "v"(b)); return d; }
DEV f32x2 cmul(f32x2 a, f32x2 b) { return (f32x2){fnma1(a.y, b.y, mul1(a.x, b.x)), fma1(a.y, b.x, mul1(a.x, b.y))}; }
template <int N> DEV void waitv() { asm volatile("s_waitcnt vmcnt(%0)" ::"n"(N) : "memory"); }
DEV void bar() { asm volatile("s_barrier" ::: "memory"); }
DEV unsigned lds_rd32(unsigned a) { unsigned v; asm volatile("ds_read_b32 %0, %1\n\ts_waitcnt lgkmcnt(0)" : "=v"(v) : "v"(a) : "memory"); return v; }
DEV u32x2 lds_rd64(unsigned a) { u32x2 v; asm volatile("ds_read_b64 %0, %1\n\ts_waitcnt lgkmcnt(0)" : "=v"(v) : "v"(a) : "memory"); return v; }
DEV u32x4 lds_rd128(unsigned a) { u32x4 v; asm volatile("ds_read_b128 %0, %1\n\ts_waitcnt lgkmcnt(0)" : "=v"(v) : "v"(a) : "memory"); return v; }
DEV void lds_wr32(unsigned a, unsigned v) { asm volatile("ds_write_b32 %0, %1" ::"v"(a), "v"(v) : "memory"); }
DEV void lds_wait_ge(unsigned a, unsigned need) {
#pragma unroll 1
  for (int it = 0; it < SPIN_MAX * 4; ++it) { if (__builtin_amdgcn_readfirstlane(lds_rd32(a)) >= need) break; __builtin_amdgcn_s_sleep(1); }
}
DEV void lds_wait2(unsigned a) {
#pragma unroll 1
  for (int it = 0; it < SPIN_MAX * 4; ++it) { const u32x2 v = lds_rd64(a); if (__builtin_amdgcn_readfirstlane(v.x & v.y)) break; __builtin_amdgcn_s_sleep(1); }
}
DEV void lds_wait0(unsigned a) {
#pragma unroll 1
  for (int it = 0; it < SPIN_MAX * 4; ++it) { if (__builtin_amdgcn_readfirstlane(lds_rd32(a)) == 0u) break; __builtin_amdgcn_s_sleep(XSLP); }
}
typedef __amdgpu_buffer_rsrc_t rsrc_t;
DEV void mark(rsrc_t rz, unsigned a) {
  if ((threadIdx.x & 63) == 0) __builtin_amdgcn_raw_ptr_buffer_load_lds(rz, (LAS void*)(size_t)a, 4, 0, 0, 0, 0);
}
DEV u32x4 ld128(rsrc_t r, unsigned vo, unsigned so = 0) { return __builtin_amdgcn_raw_buffer_load_b128(r, vo, so, 0); }
#define FRESH() asm volatile("" ::: "memory")
DEV u32x4 ld128c(rsrc_t r, unsigned vo) { return __builtin_amdgcn_raw_buffer_load_b128(r, vo, 0, 16); }
DEV void st128c(rsrc_t r, unsigned vo, u32x4 v) {
  asm volatile("s_nop 4\n\tbuffer_store_dwordx4 %0, %1, %2, 0 offen sc1\n\ts_nop 7\n\ts_nop 7" ::"v"(v), "v"(vo), "s"(r) : "memory");
}
DEV void st64c(rsrc_t r, unsigned vo, u32x2 v) { __builtin_amdgcn_raw_buffer_store_b64(v, r, vo, 0, 16); }
DEV void ld8(u32x4 (&w)[8], rsrc_t r, unsigned vo, unsigned so, unsigned step) {
  asm volatile("s_nop 4\n\tbuffer_load_dwordx4 %0, %9, %10, %8 offen\n\ts_add_u32 %8, %8, %11\n\tbuffer_load_dwordx4 %1, %9, %10, %8 offen\n\ts_add_u32 %8, %8, %11\n\t"
               "buffer_load_dwordx4 %2, %9, %10, %8 offen\n\ts_add_u32 %8, %8, %11\n\tbuffer_load_dwordx4 %3, %9, %10, %8 offen\n\ts_add_u32 %8, %8, %11\n\t"
               "buffer_load_dwordx4 %4, %9, %10, %8 offen\n\ts_add_u32 %8, %8, %11\n\tbuffer_load_dwordx4 %5, %9, %10, %8 offen\n\ts_add_u32 %8, %8, %11\n\t"
               "buffer_load_dwordx4 %6, %9, %10, %8 offen\n\ts_add_u32 %8, %8, %11\n\tbuffer_load_dwordx4 %7, %9, %10, %8 offen"
               : "=&v"(w[0]), "=&v"(w[1]), "=&v"(w[2]), "=&v"(w[3]), "=&v"(w[4]), "=&v"(w[5]), "=&v"(w[6]), "=&v"(w[7]), "+&s"(so)
               : "v"(vo), "s"(r), "s"(step) : "memory", "scc");
}
DEV void ld8c(u32x4 (&w)[8], rsrc_t r, unsigned vo, unsigned so, unsigned step, int a, int b) {
  asm volatile("s_nop 4\n\ts_cmp_eq_u32 %12, %13\n\ts_cbranch_scc0 Lskip_%=\n\t"
               "buffer_load_dwordx4 %0, %9, %10, %8 offen" WNT "\n\ts_add_u32 %8, %8, %11\n\tbuffer_load_dwordx4 %1, %9, %10, %8 offen" WNT "\n\ts_add_u32 %8, %8, %11\n\t"
               "buffer_load_dwordx4 %2, %9, %10, %8 offen" WNT "\n\ts_add_u32 %8, %8, %11\n\tbuffer_load_dwordx4 %3, %9, %10, %8 offen" WNT "\n\ts_add_u32 %8, %8, %11\n\t"
               "buffer_load_dwordx4 %4, %9, %10, %8 offen" WNT "\n\ts_add_u32 %8, %8, %11\n\tbuffer_load_dwordx4 %5, %9, %10, %8 offen" WNT "\n\ts_add_u32 %8, %8, %11\n\t"
               "buffer_load_dwordx4 %6, %9, %10, %8 offen" WNT "\n\ts_add_u32 %8, %8, %11\n\tbuffer_load_dwordx4 %7, %9, %10, %8 offen" WNT "\n\tLskip_%=:"
               : "+&v"(w[0]), "+&v"(w[1]), "+&v"(w[2]), "+&v"(w[3]), "+&v"(w[4]), "+&v"(w[5]), "+&v"(w[6]), "+&v"(w[7]), "+&s"(so)
               : "v"(vo), "s"(r), "s"(step), "s"(a), "n"(b) : "memory", "scc");
}
DEV rsrc_t mk(const void* p, unsigned bytes) { return __builtin_amdgcn_make_buffer_rsrc((void*)p, 0, bytes, 0x00020000); }

DEV unsigned wave_maxu(unsigned v) {
  unsigned r;
  asm("s_nop 1\n\tv_max_u32_dpp %1, %1, %1 row_shr:1 row_mask:0xf bank_mask:0xf\n\ts_nop 1\n\tv_max_u32_dpp %1, %1, %1 row_shr:2 row_mask:0xf bank_mask:0xf\n\t"
      "s_nop 1\n\tv_max_u32_dpp %1, %1, %1 row_shr:4 row_mask:0xf bank_mask:0xf\n\ts_nop 1\n\tv_max_u32_dpp %1, %1, %1 row_shr:8 row_mask:0xf bank_mask:0xf\n\t"
      "s_nop 1\n\tv_max_u32_dpp %1, %1, %1 row_bcast:15 row_mask:0xa bank_mask:0xf\n\ts_nop 1\n\tv_max_u32_dpp %1, %1, %1 row_bcast:31 row_mask:0xc bank_mask:0xf\n\t"
      "s_nop 0\n\tv_readlane_b32 %0, %1, 63\n\ts_nop 1" : "=s"(r), "+v"(v));
  return r;
}
DEV float sum4(const float (&a)[4]) {
  auto p = __builtin_amdgcn_permlane16_swap(__float_as_uint(a[0]), __float_as_uint(a[1]), false, false);
  auto q = __builtin_amdgcn_permlane16_swap(__float_as_uint(a[2]), __float_as_uint(a[3]), false, false);
  const float u = __uint_as_float(p[0]) + __uint_as_float(p[1]), w = __uint_as_float(q[0]) + __uint_as_float(q[1]);
  auto r = __builtin_amdgcn_permlane32_swap(__float_as_uint(u), __float_as_uint(w), false, false);
  float v = __uint_as_float(r[0]) + __uint_as_float(r[1]);
  v += __uint_as_float((unsigned)__builtin_amdgcn_update_dpp(0, (int)__float_as_uint(v), 0x128, 0xf, 0xf, false));
  v += __uint_as_float((unsigned)__builtin_amdgcn_update_dpp(0, (int)__float_as_uint(v), 0x124, 0xf, 0xf, false));
  v += __uint_as_float((unsigned)__builtin_amdgcn_update_dpp(0, (int)__float_as_uint(v), 0x122, 0xf, 0xf, false));
  v += __uint_as_float((unsigned)__builtin_amdgcn_update_dpp(0, (int)__float_as_uint(v), 0x121, 0xf, 0xf, false));
  return v;
}
DEV float dot2(unsigned a, unsigned b, float c) { return __builtin_amdgcn_fdot2_f32_bf16(__builtin_bit_cast(bf16x2_t, a), __builtin_bit_cast(bf16x2_t, b), c, false); }
DEV unsigned route_code(unsigned u) { return (u & 0x8000u) ? (~u & 0xffffu) : (u | 0x8000u); }
DEV unsigned med3u(unsigned a, unsigned b, unsigned c) { unsigned d; asm("v_med3_u32 %0, %1, %2, %3" : "=v"(d) : "v"(a), "v"(b), "v"(c)); return d; }
DEV float key_val(unsigned key) {
  unsigned u = key >> 16;
  u = (u & 0x8000u) ? (u & 0x7fffu) : (~u & 0xffffu);
  return __uint_as_float(u << 16);
}
#define STAMP(k)
#define STAMPS()

DEV void unit_mma(const u32x4 (&w)[8], bf16x8 a, f32x4 (&d)[8]) {
#pragma unroll
  for (int j = 0; j < 8; ++j) {
    u32x4 b;
#pragma unroll
    for (int t = 0; t < 4; ++t) b[t] = __builtin_amdgcn_perm(w[2 * t + 1][j >> 1], w[2 * t][j >> 1], (j & 1) ? 0x07060302u : 0x05040100u);
    d[j] = __builtin_amdgcn_mfma_f32_16x16x32_bf16(a, __builtin_bit_cast(bf16x8, b), d[j], 0, 0, 0);
  }
}

DEV void fold(const f32x4 (&d)[8], f32x4 (&o)[2]) {
#pragma unroll
  for (int r = 0; r < 2; ++r)
#pragma unroll
    for (int q = 0; q < 4; ++q) {
      const float a0 = d[q][r], a1 = d[q][r + 2], b0 = d[q + 4][r], b1 = d[q + 4][r + 2];
      auto a = __builtin_amdgcn_permlane32_swap(__float_as_uint(a0), __float_as_uint(a1), false, false);
      auto b = __builtin_amdgcn_permlane32_swap(__float_as_uint(b0), __float_as_uint(b1), false, false);
      float t;
      asm("s_nop 1\n\tv_add_f32_dpp %0, %1, %2 row_ror:8 row_mask:0xf bank_mask:0x3\n\tv_add_f32_dpp %0, %3, %4 row_ror:8 row_mask:0xf bank_mask:0xc"
          : "=&v"(t) : "v"(a[1]), "v"(a[0]), "v"(b[0]), "v"(b[1]));
      o[r][q] = t;
    }
}
DEV void st2x128c(rsrc_t r, unsigned vo, unsigned so, const f32x4 (&o)[2]) {
  asm volatile("s_nop 4\n\tbuffer_store_dwordx4 %0, %2, %3, 0 offen sc1\n\tbuffer_store_dwordx4 %1, %2, %3, %4 offen sc1\n\ts_nop 7\n\ts_nop 7"
               ::"v"(o[0]), "v"(o[1]), "v"(vo), "s"(r), "s"(so) : "memory");
}
DEV void poll32(rsrc_t r, unsigned vo, unsigned tg, bool on) {
#pragma unroll 1
  for (int it = 0; it < SPIN_MAX; ++it) {
    FRESH();
    const unsigned v = __builtin_amdgcn_raw_buffer_load_b32(r, vo, 0, 16);
    if (__all(!on || v == tg)) break;
    __builtin_amdgcn_s_sleep(16);
  }
}

DEV void count1(rsrc_t r, unsigned so) {
  asm volatile("s_nop 4\n\ts_waitcnt vmcnt(0)\n\ts_mov_b64 exec, 1\n\tbuffer_atomic_add %0, off, %1, %2 sc1\n\ts_mov_b64 exec, -1" ::"v"(1u), "s"(r), "s"(so) : "memory");
}
DEV void count_wait(rsrc_t r, unsigned so, unsigned n) {
#pragma unroll 1
  for (int it = 0; it < SPIN_MAX; ++it) {
    unsigned v = 0;
    asm volatile("s_nop 4\n\ts_mov_b64 exec, 1\n\tbuffer_atomic_or %0, off, %1, %2 sc0 sc1\n\ts_mov_b64 exec, -1\n\ts_waitcnt vmcnt(0)" : "+v"(v) : "s"(r), "s"(so) : "memory");
    if (__builtin_amdgcn_readfirstlane(v) == n) break;
    __builtin_amdgcn_s_sleep(CWS);
  }
}

typedef __attribute__((ext_vector_type(8))) u64 u64x8;
__global__ __launch_bounds__(512, 2) void fused(const u16*, const u16*, const u16*, const u16*, const u16*, const u16*, const u16*, const u16*, const u16*,
                                                const u16*, const u16*, const u16*, const u16*, const u16*, char*, float*) {
#if __HIP_DEVICE_COMPILE__
  extern __shared__ __attribute__((aligned(16))) char smem_[];
  LAS char* smem = (LAS char*)smem_;
#define ARGREQ(W) u64x8 ka, kb; \
  asm volatile("s_load_dwordx16 %0, %2, 0x0\n\ts_load_dwordx16 %1, %2, 0x40" W : "=&s"(ka), "=&s"(kb) : "s"(__builtin_amdgcn_kernarg_segment_ptr()) : "memory")
#define ARGS() \
  const u16 *x = (const u16*)ka[0], *Lre = (const u16*)ka[1], *Lim = (const u16*)ka[2], *Bre = (const u16*)ka[3], *Bim = (const u16*)ka[4], *Cre = (const u16*)ka[5], \
            *Cim = (const u16*)ka[6], *logdt = (const u16*)ka[7], *Dskip = (const u16*)kb[0], *w_r = (const u16*)kb[1], *w_gate = (const u16*)kb[2], \
            *w_up = (const u16*)kb[3], *w_down = (const u16*)kb[4], *gate_w = (const u16*)kb[5]; \
  char* ws = (char*)kb[6]; float* out = (float*)kb[7]; \
  const rsrc_t rws = mk(ws, W_END), rx = mk(x, 2048 * 1024 * 2), rz = mk(x, 0)
  STAMP(0);
  int tid = threadIdx.x, bid = blockIdx.x;
  {
    unsigned t_[8], u_[2];
    asm volatile(ASM_EN : [w] "=&s"(t_[0]), [j] "=&s"(t_[1]), [t] "=&s"(t_[2]), [sa] "=&s"(t_[3]), [sc] "=&s"(t_[4]), [m] "=&s"(t_[5]), [r] "=&s"(t_[6]), [c] "=&s"(t_[7]), [v0] "=&v"(u_[0]), [v1] "=&v"(u_[1]) : [kp] "s"(__builtin_amdgcn_kernarg_segment_ptr()), [tid] "v"(threadIdx.x), [bid] "s"(blockIdx.x), [cpf] "n"(CODEPF), [ms0] "n"(M_S0) : "memory", "scc", "m0", "s20", "s21", "s22", "s23", "v255");
    asm volatile("" : "+s"(bid)); asm volatile("" : "+v"(tid));
  }
  const int lane = tid & 63, wid = __builtin_amdgcn_readfirstlane(tid >> 6), xcd = bid & 7, loc = bid >> 3;
  const int c = (xcd >> 1) * 4 + (loc >> 3), j = (xcd & 1) * 8 + (loc & 7);
  const int fr = lane & 15, fq = lane >> 4;

  if (__builtin_expect(wid == 4 || wid == 5, 1)) {
    ARGREQ("");
    const int l = wid - 4;
    __builtin_amdgcn_s_setprio(LPRIO);
    const int l3 = lane >> 3;
    {
      const unsigned one = tid >= 272 ? 1u : 0u;
      asm volatile("ds_write_b128 %0, %1" ::"v"(L_FLG + min(tid - 256, 111) * 16), "v"((u32x4){one, one, one, one}) : "memory");
    }
    int bud = SPIN_MAX * 4;
    unsigned dst = 1 ? l * 49152 : 131072 - l * 114688, ci = M_CONS - 64 + 1 * 16, t_s0, vc = 0;
    unsigned b0 = (c * 128 + l * (j * 64 - c * 128) + l3 * (8 - 7 * l)) * 2048 + ((lane & 7) ^ l3) * 16, wrap = 163840 - l * 32768;
    unsigned so[8];
#pragma unroll
    for (int q = 0; q < 8; ++q) so[q] = (64 * (q & 1) + (q >> 1) + l * (8 * q - 64 * (q & 1) - (q >> 1))) * 2048;
    unsigned sk = (((xcd >> 1) + 2 * (xcd & 1)) & 3) * 512 + 1 * 128, n = 16 - 1, mka = M_LAND + 1 * 8 + l * 4, v0 = b0 + sk;
    asm volatile("" : "+s"(dst), "+v"(b0), "+s"(wrap), "+s"(so[1]), "+s"(so[2]), "+s"(so[3]), "+s"(so[4]), "+s"(so[5]), "+s"(so[6]), "+s"(so[7]),
                 "+s"(sk), "+s"(mka), "+v"(v0));
    asm volatile("s_waitcnt lgkmcnt(0)" : "+s"(ka), "+s"(kb)::"memory");
    ARGS();
    {
      const rsrc_t r0 = mk(l ? Bre : x, (1u << 22) >> l), r1 = mk(l ? Bim : x + 4096, l ? (1u << 21) : (1u << 22) - 8192);
      STAMP(8);
      asm volatile(ASM_LA : [dst] "+&s"(dst), [sk] "+&s"(sk), [ci] "+&s"(ci), [n] "+&s"(n), [mk] "+&s"(mka), [bud] "+&s"(bud), [s0] "=&s"(t_s0), [v0] "+&v"(v0), [vc] "+&v"(vc) : [r0] "s"(r0), [r1] "s"(r1), [rz] "s"(rz), [wrap] "s"(wrap), [b0] "v"(b0), [vw] "v"(M_WR), [so0] "s"(so[0]), [so1] "s"(so[1]), [so2] "s"(so[2]), [so3] "s"(so[3]), [so4] "s"(so[4]), [so5] "s"(so[5]), [so6] "s"(so[6]), [so7] "s"(so[7]) : "memory", "scc", "m0", "v24", "v25", "v26", "v27", "v28", "v29", "v30", "v31");
    }
    STAMP(1);
    {
      const rsrc_t r0 = mk(Cre, 1 << 21), r1 = mk(Cim, 1 << 21);
      const unsigned b0 = (j * 64 + 4 * l3) * 2048 + (((lane & 7) ^ (l3 >> 1)) * 16), b1 = b0 ^ 64, wrap = 147456, cth = M_CONS + (15 - l) * 16;
      unsigned so[8];
#pragma unroll
      for (int q = 0; q < 8; ++q) so[q] = (32 * (q & 1) + (q >> 1)) * 2048;
      unsigned sk = l * 128, hi = (j & 8) * 128, n = 8, mka = M_LAND + (17 + l) * 8, v0 = b0 + sk + hi, v1 = b1 + sk + hi, t_s1;
      const rsrc_t rp = mk(l ? Cim : Cre, 1 << 21);
      const unsigned p0 = b0 + (hi ^ 0x400);
      unsigned vd = l * 8192 + lane * 16;
      asm volatile(ASM_LB : [dst] "+&s"(dst), [sk] "+&s"(sk), [ci] "+&s"(ci), [n] "+&s"(n), [mk] "+&s"(mka), [bud] "+&s"(bud), [s0] "=&s"(t_s0), [v0] "+&v"(v0), [vc] "+&v"(vc), [v1] "+&v"(v1), [hi] "+&s"(hi), [s1] "=&s"(t_s1), [vd] "+&v"(vd) : [r0] "s"(r0), [r1] "s"(r1), [rz] "s"(rz), [wrap] "s"(wrap), [b0] "v"(b0), [vw] "v"(M_WR + 16), [cth] "s"(cth), [b1] "v"(b1), [nend] "s"(3 + l), [rp] "s"(rp), [p0] "v"(p0), [p1] "v"(p0 ^ 64), [vm] "v"(M_CONS + 256), [vl] "v"(M_LAND + 26 * 8 + 4 * l), [so0] "s"(so[0]), [so1] "s"(so[1]), [so2] "s"(so[2]), [so3] "s"(so[3]), [so4] "s"(so[4]), [so5] "s"(so[5]), [so6] "s"(so[6]), [so7] "s"(so[7]) : "memory", "scc", "m0", "v24", "v25", "v26", "v27", "v28", "v29", "v30", "v31", "v32", "v33", "v34", "v35", "v36", "v37", "v38", "v39", "v40", "v41", "v42", "v43", "v44", "v45", "v46", "v47", "v48", "v49", "v50", "v51", "v52", "v53", "v54", "v55", "v56", "v57", "v58", "v59", "v60", "v61", "v62", "v63", "v64", "v65", "v66", "v67", "v68", "v69", "v70", "v71", "v72", "v73", "v74", "v75", "v76", "v77", "v78", "v79", "v80", "v81", "v82", "v83", "v84", "v85", "v86", "v87", "v88", "v89", "v90", "v91", "v92", "v93", "v94", "v95", "v96", "v97", "v98", "v99", "v100", "v101", "v102", "v103", "v104", "v105", "v106", "v107", "v108", "v109", "v110", "v111", "v112", "v113", "v114", "v115", "v116", "v117", "v118", "v119", "v120", "v121", "v122", "v123", "v124", "v125", "v126", "v127", "v128", "v129", "v130", "v131", "v132", "v133", "v134", "v135", "v136", "v137", "v138", "v139", "v140", "v141", "v142", "v143", "v144", "v145", "v146", "v147", "v148", "v149", "v150", "v151", "v152", "v153", "v154", "v155", "v156", "v157", "v158", "v159", "v160", "v161", "v162", "v163", "v164", "v165", "v166", "v167", "v168", "v169", "v170", "v171", "v172", "v173", "v174", "v175", "v176", "v177", "v178", "v179", "v180", "v181", "v182", "v183", "v184", "v185", "v186", "v187", "v188", "v189", "v190", "v191", "v192", "v193", "v194", "v195", "v196", "v197", "v198", "v199", "v200", "v201", "v202", "v203", "v204", "v205", "v206", "v207", "v208", "v209", "v210", "v211", "v212", "v213", "v214", "v215", "v216", "v217", "v218", "v219", "v220", "v221", "v222", "v223", "v224", "v225", "v226", "v227", "v228", "v229", "v230", "v231", "v232", "v233", "v234", "v235", "v236", "v237", "v238", "v239", "v240", "v241", "v242", "v243", "v244", "v245", "v246", "v247", "v248", "v249", "v250", "v251", "v252", "v253", "v254", "v255");
    }
    waitv<0>();
    STAMP(3);
    STAMPS();
    __builtin_amdgcn_endpgm();
  }

  if (__builtin_expect(wid >= 6, 1)) {
    const int xw = wid - 6;
    __builtin_amdgcn_s_setprio(XPRIO0);
    ARGREQ("\n\ts_waitcnt lgkmcnt(0)");
    ARGS();
    const int row0 = c * 128 + j * 8 + xw * 4;
    const int l = xw;
    const bool ffn = !NOFFN && loc < 28;
    const int sg = xcd * 7 + (loc >> 2), kq = loc & 3;
    u32x4 w1[4][8], w2[2][8];
    unsigned tagA, tagB;
    const int sb = HELP ? (3 - (int)((size_t)w_up >> 7)) & 7 : 9, s1 = ffn ? sg : 8 * xcd + sb;
    const bool gu = ffn ? !(xw && (sg & 7) == sb) : HELP && !NOFFN && xw && xcd < 7;
    const int pg = ffn ? WGRP : 0, grp = __builtin_amdgcn_readfirstlane(gu ? pg : 9);
    auto wburst = [&](int p, unsigned gate) __attribute__((always_inline)) {
      const bool on = grp == p;
      const rsrc_t r1 = mk(xw ? w_up : w_gate, D_ * F_ * 2);
      const unsigned o1 = (kq * 256 + 32 * (fr >> 3) + 8 * fq) * (F_ * 2) + (s1 * 64 + 8 * (fr & 7)) * 2;
      if (pg == p) {
        lds_wait0(M_LAND + gate * 8);
        if (on) mark(rz, M_WR + xw * 4);
        __builtin_amdgcn_s_sleep(WSLP);
      }
#pragma unroll
      for (int u = 0; u < 4; ++u) {
        ld8c(w1[u], r1, o1, u * 64 * (F_ * 2), F_ * 2, grp, p);
        if (u == WRES && on) mark(rz, M_WR + 8 + xw * 4);
      }
      if (pg == p)
        __builtin_amdgcn_raw_ptr_buffer_load_lds(mk(xw ? Cim : Cre, 1 << 21), (LAS void*)(smem + L_JUNK + 256 + xw * 256), 4, (j * 64 + lane) * 2048 + ((c + lane) & 15) * 128, 0, 0, 0);
    };
    {
      unsigned ep;
      u32x4 xv[8], wv[2], gv[2];
      unsigned pl[3];
      const rsrc_t rw = mk(w_r, 2048), rg = mk(gate_w, 2048);
      const rsrc_t rp0 = mk(Lre, 2048), rp1 = mk(Lim, 2048), rp2 = mk(logdt, 2048);
      const unsigned zero = 0, l16 = lane * 16, xo = row0 * 2048 + lane * 16, po = (j * 64 + lane) * 2;
      if (XG < -1) { asm volatile("" ::"s"(rg), "s"(rp0), "s"(rp1), "s"(rp2), "s"(rx), "v"(xo), "v"(po)); bar(); }
      asm volatile("s_nop 4\n\tbuffer_load_dword %0, %3, %5, 0 offen sc1\n\tbuffer_load_dwordx4 %1, %4, %6, 0 offen\n\tbuffer_load_dwordx4 %2, %4, %6, 0 offen offset:1024"
                   : "=&v"(ep), "=&v"(wv[0]), "=&v"(wv[1]) : "v"(zero), "v"(l16), "s"(rws), "s"(rw) : "memory");
      asm volatile("s_nop 4\n\tbuffer_load_dwordx4 %3, %9, %10, 0 offen\n\tbuffer_load_dwordx4 %4, %9, %10, 0 offen offset:1024\n\t"
                   "buffer_load_ushort %0, %5, %6, 0 offen\n\tbuffer_load_ushort %1, %5, %7, 0 offen\n\tbuffer_load_ushort %2, %5, %8, 0 offen"
                   : "=&v"(pl[0]), "=&v"(pl[1]), "=&v"(pl[2]), "=&v"(gv[0]), "=&v"(gv[1]) : "v"(po), "s"(rp0), "s"(rp1), "s"(rp2), "v"(l16), "s"(rg) : "memory");
      asm volatile("s_nop 4\n\tbuffer_atomic_swap %0, %1, %2, 0 offen sc1" ::"v"(zero), "v"(tid == 384 && bid < 160 ? W_HF + bid * 128 : 0xf0000000u), "s"(rws) : "memory");
      if (XG >= -1) bar();
      if (XG >= 0) lds_wait0(M_LAND + XG * 8);
      ld8(xv, rx, xo, 0, 1024);
      asm volatile("s_waitcnt vmcnt(0)" : "+v"(ep), "+v"(xv[0]), "+v"(xv[1]), "+v"(xv[2]), "+v"(xv[3]), "+v"(xv[4]), "+v"(xv[5]), "+v"(xv[6]),
                   "+v"(xv[7]), "+v"(wv[0]), "+v"(wv[1]), "+v"(gv[0]), "+v"(gv[1]), "+v"(pl[0]), "+v"(pl[1]), "+v"(pl[2])::"memory");
      tagA = ep + 1u; tagB = ep + 0x40000001u;
      float sr[4];
#pragma unroll
      for (int r = 0; r < 4; ++r) {
        sr[r] = 0.f;
#pragma unroll
        for (int h = 0; h < 2; ++h)
#pragma unroll
          for (int e = 0; e < 4; ++e) { const unsigned xu = xv[2 * r + h][e], wu = wv[h][e]; sr[r] = dot2(xu, wu, sr[r]); }
      }
      {
        const unsigned rb = route_code(f2bf(sum4(sr)));
        const unsigned r01 = (unsigned)__builtin_amdgcn_readlane((int)rb, 0) | ((unsigned)__builtin_amdgcn_readlane((int)rb, 16) << 16);
        const unsigned r23 = (unsigned)__builtin_amdgcn_readlane((int)rb, 32) | ((unsigned)__builtin_amdgcn_readlane((int)rb, 48) << 16);
        st128c(rws, lane == 0 ? W_R2 + row0 * 4 : 0xf0000000u, (u32x4){r01, tagA, r23, tagB});
      }
#pragma unroll
      for (int r = 0; r < 4; ++r) {
        sr[r] = 0.f;
#pragma unroll
        for (int h = 0; h < 2; ++h)
#pragma unroll
          for (int e = 0; e < 4; ++e) {
            const unsigned xu = xv[2 * r + h][e], gu = gv[h][e];
            sr[r] = dot2(xu, gu, sr[r]);
          }
      }
      {
        const float z = rbf(sum4(sr));
        st64c(rws, fr == 0 ? W_RG + (row0 + fq) * 8 : 0xf0000000u, (u32x2){__float_as_uint(rbf(1.f / (1.f + __expf(-z)))), tagB});
      }
      STAMP(8);
      __builtin_amdgcn_s_setprio(XPRIO);
#pragma unroll
      for (int u = 0; u < 4; ++u) asm volatile("" : "=v"(w1[u][0]), "=v"(w1[u][1]), "=v"(w1[u][2]), "=v"(w1[u][3]), "=v"(w1[u][4]), "=v"(w1[u][5]), "=v"(w1[u][6]), "=v"(w1[u][7]));
#pragma unroll
      for (int u = 0; u < 2; ++u) asm volatile("" : "=v"(w2[u][0]), "=v"(w2[u][1]), "=v"(w2[u][2]), "=v"(w2[u][3]), "=v"(w2[u][4]), "=v"(w2[u][5]), "=v"(w2[u][6]), "=v"(w2[u][7]));
      wburst(0, WS0);
      if (xw == 1) {
        const float lre = bf2f(pl[0]), lim = bf2f(pl[1]);
        const float dt = rbf(__expf(bf2f(pl[2])));
        const float er = expf(dt * lre);
        float sn, cs;
        sincosf(dt * lim, &sn, &cs);
        const f32x2 l1 = {mul1(er, cs), mul1(er, sn)};
        const float nr = l1.x - 1.f, ni = l1.y, den = fma1(lre, lre, mul1(lim, lim));
        const f32x2 cf = {fma1(nr, lre, mul1(ni, lim)) / den, fnma1(nr, lim, mul1(ni, lre)) / den};
        const f32x2 l2 = cmul(l1, l1), l4 = cmul(l2, l2), l8 = cmul(l4, l4), l16_ = cmul(l8, l8), l32 = cmul(l16_, l16_), l64 = cmul(l32, l32),
                    l128 = cmul(l64, l64), l256 = cmul(l128, l128), l384 = cmul(l256, l128), l512 = cmul(l256, l256);
        auto put = [&](int e, f32x2 v) { const float re = v.x, im = v.y; asm volatile("ds_write2_b32 %0, %1, %2 offset0:0 offset1:64" ::"v"(L_TAB + e * 512 + lane * 4), "v"(re), "v"(im) : "memory"); };
        put(0, l1); put(1, cf); put(2, l8); put(3, l16_); put(4, l32); put(5, l64); put(6, l512); put(7, (f32x2){1.f, 0.f}); put(8, l128); put(9, l256); put(10, l384);
        lds_wr32(M_LAND + 16 * 8, 0u);
      }
    }
    unsigned m = 0, m2 = 0, m3 = 0, m4 = 0;
    auto gkey = [&](unsigned k) { return (k & 0xffff0000u) | ((k & 28u) << 6) | (k & 3u) | (unsigned)(252 - lane * 4); };
    lds_wait0(M_LAND + (pg == 0 ? TKG0 : TKG) * 8);
    if (xw == 1) {
#pragma unroll
      for (int h = 0; h < 2; ++h) {
        poll32(rws, W_RG + (c * 128 + h * 64 + lane) * 8 + 4, tagB, true);
        __builtin_amdgcn_raw_ptr_buffer_load_lds(rws, (LAS void*)(smem + L_G + h * 256), 4, W_RG + (c * 128 + h * 64 + lane) * 8, 0, 0, 16);
      }
      mark(rz, M_GT);
      STAMP(1);
    } else {
      {
        u32x4 g[8];
#pragma unroll 1
        for (int it = 0; it < SPIN_MAX; ++it) {
          FRESH();
#pragma unroll
          for (int q = 0; q < 8; ++q) g[q] = ld128c(rws, W_R2 + lane * 16 + q * 1024);
          bool ok = true;
#pragma unroll
          for (int q = 0; q < 8; ++q) ok = ok && g[q].y == tagA && g[q].w == tagB;
          if (__all(ok)) break;
          __builtin_amdgcn_s_sleep(16);
        }
        mark(rz, M_LAND + 16 * 8 + 4);
        STAMP(1);
        auto lkey = [&](int i) { const unsigned w = g[i >> 2][i & 2]; return ((i & 1) ? (w & 0xffff0000u) : (w << 16)) | (unsigned)(31 - i); };
        m = lkey(0);
#pragma unroll
        for (int i = 1; i < 32; ++i) {
          const unsigned k = lkey(i);
          m4 = med3u(m3, m4, k); m3 = med3u(m2, m3, k); m2 = med3u(m, m2, k); m = max(m, k);
        }
      }
      m = gkey(m); m2 = gkey(m2); m3 = gkey(m3); m4 = gkey(m4);
    }
    asm volatile("" : "+v"(m), "+v"(m2), "+v"(m3), "+v"(m4));
    wburst(1, WS1);
    if (xw == 0) {
      unsigned mine = 0;
#pragma unroll 1
      for (int s = 0; s < 8; ++s) {
        const unsigned mm = wave_maxu(m);
        mine = (lane >> 3) == s ? mm : mine;
        const bool hit = m == mm;
        m = hit ? m2 : m; m2 = hit ? m3 : m2; m3 = hit ? m4 : m3; m4 = hit ? 0u : m4;
      }
      if (__ballot(m == 0u)) {
        unsigned lim = 0xffffffffu;
#pragma unroll 1
        for (int s = 0; s < 8; ++s) {
          unsigned mm = 0;
#pragma unroll 1
          for (int q = 0; q < 8; ++q) {
            const u32x4 w = ld128c(rws, W_R2 + lane * 16 + q * 1024);
#pragma unroll
            for (int e = 0; e < 4; ++e) {
              const unsigned k = gkey(((e & 1) ? (w[e & 2] & 0xffff0000u) : (w[e & 2] << 16)) | (unsigned)(31 - 4 * q - e));
              mm = max(mm, k < lim || s == 0 ? k : 0u);
            }
          }
          lim = wave_maxu(mm);
          mine = (lane >> 3) == s ? lim : mine;
        }
      }
      const unsigned myidx = 2047u - (mine & 0xffffu);
      {
#pragma unroll
        for (int u = 0; u < 4; ++u)
          __builtin_amdgcn_raw_ptr_buffer_load_lds(rx, (LAS void*)(smem + L_XS + u * 1024), 16, myidx * 2048 + (loc & 3) * 512 + (lane & 7) * 16, u * 128, 0, 0);
      }
      const float myrs = rbf(1.f / (1.f + __expf(-key_val(mine))));
      if ((lane & 7) == 0) asm volatile("ds_write_b64 %0, %1\n\ts_waitcnt lgkmcnt(0)" ::"v"(L_SEL + lane), "v"((u32x2){myidx, __float_as_uint(myrs)}) : "memory");
      mark(rz, M_XS);
    }
    STAMP(2);
    __builtin_amdgcn_s_setprio(FPRIO);
    lds_wait0(M_XS);
    const u32x2 sel = lds_rd64(L_SEL + (lane & 7) * 8);
    const unsigned fa = ((fr & 7) * 8 + 4 * (fr >> 3) + fq) * 16, lp = (fr & 7) * 32 + (fr >> 3) * 16, tk = 4 * (fq & 1) + 2 * (fq >> 1);
    waitv<0>();
    STAMP(10);
    {
      if (0 && ffn) { lds_wait0(M_LAND + (W2G - 2) * 8); mark(rz, M_WR + 16 + xw * 4); }
      if (ffn) lds_wait0(M_LAND + W2G * 8);
      __builtin_amdgcn_s_setprio(3);
      const rsrc_t r2 = mk(w_down, D_ * F_ * 2);
      const unsigned o2 = (sg * 64 + 32 * (fr >> 3) + 8 * fq) * 2048 + (kq * 256 + xw * 128 + 8 * (fr & 7)) * 2;
      const int on = __builtin_amdgcn_readfirstlane(ffn ? 1 : 0);
#pragma unroll
      for (int u = 0; u < 2; ++u) {
        ld8c(w2[u], r2, o2, u * 128, 2048, on, 1);
        if (0 == u + 1 && ffn) mark(rz, M_WR + 24 + xw * 4);
      }
      __builtin_amdgcn_s_setprio(FPRIO);
    }
    f32x4 d[8], o[2];
    if (gu) {
      u32x4 xa[4];
      asm volatile("ds_read_b128 %0, %4\n\tds_read_b128 %1, %4 offset:1024\n\tds_read_b128 %2, %4 offset:2048\n\tds_read_b128 %3, %4 offset:3072\n\ts_waitcnt lgkmcnt(0)"
                   : "=&v"(xa[0]), "=&v"(xa[1]), "=&v"(xa[2]), "=&v"(xa[3]) : "v"(L_XS + fa) : "memory");
#pragma unroll
      for (int q = 0; q < 8; ++q) d[q] = (f32x4){0.f, 0.f, 0.f, 0.f};
#pragma unroll
      for (int u = 0; u < 4; ++u) {
        asm volatile("" : "+v"(w1[u][0]), "+v"(w1[u][1]), "+v"(w1[u][2]), "+v"(w1[u][3]), "+v"(w1[u][4]), "+v"(w1[u][5]), "+v"(w1[u][6]), "+v"(w1[u][7]));
        unit_mma(w1[u], __builtin_bit_cast(bf16x8, xa[u]), d);
        asm volatile("" : "+v"(d[0]), "+v"(d[1]), "+v"(d[2]), "+v"(d[3]), "+v"(d[4]), "+v"(d[5]), "+v"(d[6]), "+v"(d[7]));
      }
      fold(d, o);
      STAMP(11);
      st2x128c(rws, W_GU + ((s1 * 8 + tk) * 8 + kq * 2 + l) * 256 + lp, 2048, o);
      count1(rws, W_GF + s1 * 128);
    }
    STAMP(3);
    if (ffn) {
      {
        if (GUG) lds_wait0(M_LAND + GUG * 8);
        __builtin_amdgcn_s_setprio(GPRIO);
        count_wait(rws, W_GF + sg * 128, 8);
        f32x4 a[8];
#pragma unroll
        for (int q = 0; q < 8; ++q) a[q] = __builtin_bit_cast(f32x4, ld128c(rws, W_GU + (sg * 8 + 4 * l + fq) * 2048 + fr * 16 + q * 256));
        float a2[4];
#pragma unroll
        for (int e = 0; e < 4; ++e) {
          const float g = rbf(add1(add1(a[0][e], a[2][e]), add1(a[4][e], a[6][e]))), u = rbf(add1(add1(a[1][e], a[3][e]), add1(a[5][e], a[7][e])));
          a2[e] = mul1(rbf(g / add1(1.f, __expf(-g))), u);
        }
        asm volatile("ds_write_b64 %0, %1\n\ts_nop 3" ::"v"(L_ACT + (4 * l + fq) * 128 + fr * 8), "v"((u32x2){pk2(a2[0], a2[1]), pk2(a2[2], a2[3])}) : "memory");
        lds_wr32(FL_TK + l * 4, 1u);
        lds_wait_ge(FL_TK + (l ^ 1) * 4, 1u);
      }
      const u32x4 af = lds_rd128(L_ACT + fa);
      STAMP(4);
      waitv<0>();
#pragma unroll
      for (int u = 0; u < 2; ++u)
        asm volatile("" : "+v"(w2[u][0]), "+v"(w2[u][1]), "+v"(w2[u][2]), "+v"(w2[u][3]), "+v"(w2[u][4]), "+v"(w2[u][5]), "+v"(w2[u][6]), "+v"(w2[u][7]));
#pragma unroll
      for (int u = 0; u < 2; ++u) {
#pragma unroll
        for (int q = 0; q < 8; ++q) d[q] = (f32x4){0.f, 0.f, 0.f, 0.f};
        unit_mma(w2[u], __builtin_bit_cast(bf16x8, af), d);
        fold(d, o);
        st2x128c(rws, W_YP + ((tk * 16 + kq * 4 + l * 2 + u) * 56 + sg) * 256 + lp, 16 * 56 * 256, o);
      }
      count1(rws, W_YF + (kq * 2 + l) * 128);
      STAMP(5);
    }
    u64 rows = __ballot(lane < 8 && (sel.x >> 7) == (unsigned)c);
    if (l) rows &= rows - 1;
    if (!NOFFN && rows) {
      lds_wait0(M_CONS + (16 + YG) * 16);
      __builtin_amdgcn_s_setprio(YPRIO);
      count_wait(rws, W_YF + (j >> 1) * 128, 56);
      STAMP(9);
#pragma unroll 1
      for (; rows; rows &= rows - 1, rows &= rows - 1) {
        const int s = __builtin_ctzll(rows);
        const int t = __builtin_amdgcn_readlane(sel.x, s);
        const float rs = __uint_as_float(__builtin_amdgcn_readlane(sel.y, s));
        f32x4 a[14];
#pragma unroll
        for (int k = 0; k < 14; ++k) a[k] = __builtin_bit_cast(f32x4, ld128c(rws, W_YP + (s * 16 + j) * (56 * 256) + lane * 16 + k * 1024));
        const float xf = bf2f(__builtin_amdgcn_raw_buffer_load_b16(rx, t * 2048 + j * 128 + fr * 8 + fq * 2, 0, 0));
        const float omg = rbf(1.f - __uint_as_float(lds_rd32(L_G + (t & 127) * 4)));
        float l0 = a[0][0], l1 = a[0][1], h0 = a[0][2], h1 = a[0][3];
#pragma unroll
        for (int k = 1; k < 14; ++k) { l0 = add1(l0, a[k][0]); l1 = add1(l1, a[k][1]); h0 = add1(h0, a[k][2]); h1 = add1(h1, a[k][3]); }
        asm volatile("s_nop 1" : "+v"(l0), "+v"(l1), "+v"(h0), "+v"(h1));
        auto p = __builtin_amdgcn_permlane16_swap(__float_as_uint(l0), __float_as_uint(l1), false, false);
        auto q = __builtin_amdgcn_permlane16_swap(__float_as_uint(h0), __float_as_uint(h1), false, false);
        const float u = __uint_as_float(p[0]) + __uint_as_float(p[1]), w = __uint_as_float(q[0]) + __uint_as_float(q[1]);
        auto r = __builtin_amdgcn_permlane32_swap(__float_as_uint(u), __float_as_uint(w), false, false);
        const float y = __uint_as_float(r[0]) + __uint_as_float(r[1]);
        lds_wr32(L_BROW + s * 256 + fr * 16 + fq * 4, __float_as_uint(mul1(omg, rbf(xf + rbf(mul1(rbf(y), rs))) - xf)));
      }
    }
    lds_wr32(FL_XD + l * 4, 1u);
    waitv<0>();
    STAMP(6);
    STAMPS();
    __builtin_amdgcn_endpgm();
  }

  __builtin_amdgcn_s_setprio(MPRIO);
  ARGREQ("\n\ts_waitcnt lgkmcnt(0)");
  ARGS();
  unsigned tagA, tagB;
  int bud = SPIN_MAX * 4;
  const int tl0 = 32 * fq + 2 * wid;
  u32x2 xe[2][4], dv;
  {
    const rsrc_t rd = mk(Dskip, 2048);
    const unsigned dofs = (j * 64 + 4 * fr) * 2, xo = (32 * wid + 4 * fq) * 128 + ((fr >> 1) ^ (4 * (fq & 1))) * 16 + (fr & 1) * 8;
    const unsigned ks = (j - 4 * ((xcd >> 1) + 2 * (xcd & 1))) & 15;
    const int x0 = (fq ^ (fr & 7)) * 16, x1 = ((4 + fq) ^ (fr & 7)) * 16;
    const unsigned pa = 16384 + (wid * 16 + fr) * 128, pb = 131072 + fr * 128;
    unsigned pa0 = pa + x0, pa1 = pa + x1, pb0 = pb + x0, pb1 = pb + x1;
    const unsigned tq = L_TAB + (wid * 16 + fq * 4) * 4, tcl = L_TAB + 6 * 512 + (wid * 16 + fr) * 4, tcw = tcl + 512 + fq * 512;
    const unsigned l16 = HOFS(lane), pubo = (fq * 4 + (fr >> 2)) * 16, plo = fr * 16 + (3 - fq) * 1024;
    const unsigned sag = W_AGG + (j * 16 + c) * 1024 + wid * 256, sht = W_HT + (j * 4 + wid) * 128;
    unsigned sh = W_H + (c * 64 + 4 * j + wid) * 8192 * 2, fl = M_LAND, scn = M_CONS - 16 + wid * 4;
    unsigned sl[4];
    u64 em[4];
#pragma unroll
    for (int q = 0; q < 4; ++q) {
      sl[q] = sag - (4 + 4 * q) * 1024;
      const int n = min(max(c - 4 * q, 0), 4);
      em[q] = n == 4 ? ~0ull : (1ull << (16 * n)) - 1ull;
    }
    unsigned t_sk, t_s0, t_s1, t_s2, t_sx;
    asm volatile(ASM_MA : [pa0] "+&v"(pa0), [pa1] "+&v"(pa1), [pb0] "+&v"(pb0), [pb1] "+&v"(pb1), [fl] "+&v"(fl), [sk] "=&s"(t_sk), [s0] "=&s"(t_s0), [s1] "=&s"(t_s1), [s2] "=&s"(t_s2), [sx] "=&s"(t_sx), [bud] "+&s"(bud), [scn] "+&s"(scn), [sh] "+&s"(sh), [tgA] "=&s"(tagA), [tgB] "=&s"(tagB), [xe00] "=&v"(xe[0][0]), [xe01] "=&v"(xe[0][1]), [xe02] "=&v"(xe[0][2]), [xe03] "=&v"(xe[0][3]), [xe10] "=&v"(xe[1][0]), [xe11] "=&v"(xe[1][1]), [xe12] "=&v"(xe[1][2]), [xe13] "=&v"(xe[1][3]), [dv] "=&v"(dv) : [tq] "v"(tq), [tcl] "v"(tcl), [tcw] "v"(tcw), [pubo] "v"(pubo), [plo] "v"(plo), [l16] "v"(l16), [xo] "v"(xo), [dofs] "v"(dofs), [wrap] "s"(-98304), [wrap2] "s"(-131072), [srd] "s"(rws), [ks] "s"(ks), [srdd] "s"(rd), [srz] "s"(rz), [sag] "s"(sag), [mcar] "s"(M_CAR), [eh] "s"((1u << c) - 1u), [sht] "s"(sht), [sl0] "s"(sl[0]), [em0] "s"(em[0]), [sl1] "s"(sl[1]), [em1] "s"(em[1]), [sl2] "s"(sl[2]), [em2] "s"(em[2]), [sl3] "s"(sl[3]), [em3] "s"(em[3]) : "memory", "scc", "vcc", "m0", "v64", "v65", "v66", "v67", "v68", "v69", "v70", "v71", "v72", "v73", "v74", "v75", "v76", "v77", "v78", "v79", "v80", "v81", "v82", "v83", "v84", "v85", "v86", "v87", "v88", "v89", "v90", "v91", "v92", "v93", "v94", "v95", "v96", "v97", "v98", "v99", "v100", "v101", "v102", "v103", "v104", "v105", "v106", "v107", "v108", "v109", "v110", "v111", "v112", "v113", "v114", "v115", "v116", "v117", "v118", "v119", "v120", "v121", "v122", "v123", "v124", "v125", "v126", "v127", "v128", "v129", "v130", "v131", "v132", "v133", "v134", "v135", "v136", "v137", "v138", "v139", "v140", "v141", "v142", "v143", "v144", "v145", "v146", "v147", "v148", "v149", "v150", "v151", "v152", "v153", "v154", "v155", "v156", "v157", "v158", "v159", "v160", "v161", "v162", "v163", "v164", "v165", "v166", "v167", "v168", "v169", "v170", "v171", "v172", "v173", "v174", "v175", "v176", "v177", "v178", "v179", "v180", "v181", "v182", "v183", "v184", "v185", "v186", "v187", "v188", "v189", "v190", "v191", "v192", "v193", "v194", "v195", "v196", "v197", "v198", "v199", "v200", "v201", "v202", "v203", "v204", "v205", "v206", "v207", "v208", "v209", "v210", "v211", "v212", "v213", "v214", "v215", "v216", "v217", "v218", "v219", "v220", "v221", "v222", "v223", "v224", "v225", "v226", "v227", "v228", "v229", "v230", "v231", "v232", "v233", "v234", "v235", "v236", "v237", "v238", "v239", "v240", "v241", "v242", "v243", "v244", "v245", "v246", "v247", "v248", "v249", "v250", "v251", "v252", "v253", "v254", "v255");
  }
  f32x2 g2[4], base[4][4];
  unsigned cnt = 1, mine, dp[8];
  {
    unsigned selu, m0_, m1_;
#pragma unroll 1
    for (int it = 0; it < SPIN_MAX * 4; ++it) {
      asm volatile("ds_read_b32 %5, %7\n\tds_read_b32 %6, %7 offset:%10\n\tds_read_b32 %4, %8\n\tds_read_b64 %0, %9\n\tds_read_b64 %1, %9 offset:32\n\t"
                   "ds_read_b64 %2, %9 offset:64\n\tds_read_b64 %3, %9 offset:96\n\ts_waitcnt lgkmcnt(0)"
                   : "=&v"(g2[0]), "=&v"(g2[1]), "=&v"(g2[2]), "=&v"(g2[3]), "=&v"(selu), "=&v"(m0_), "=&v"(m1_)
                   : "v"(M_GT), "v"(L_SEL + (lane & 7) * 8), "v"(L_G + tl0 * 4), "n"(M_XS - M_GT) : "memory");
      if (__builtin_amdgcn_readfirstlane(m0_ | m1_) == 0u) break;
      __builtin_amdgcn_s_sleep(1);
    }
    f32x2 dd[4];
#pragma unroll
    for (int n = 0; n < 4; ++n) { const unsigned w = dv[n >> 1]; const float f = (n & 1) ? bfhi(w) : bf2f(w & 0xffffu); dd[n] = (f32x2){f, f}; }
#pragma unroll
    for (int r = 0; r < 4; ++r) {
      const unsigned ob = pk2(1.f - g2[r].x, 1.f - g2[r].y);
      const f32x2 omg = {bf2f(ob & 0xffffu), bfhi(ob)};
#pragma unroll
      for (int n = 0; n < 4; ++n) {
        const unsigned w0 = xe[0][r][n >> 1], w1 = xe[1][r][n >> 1];
        const f32x2 xf = {(n & 1) ? bfhi(w0) : bf2f(w0 & 0xffffu), (n & 1) ? bfhi(w1) : bf2f(w1 & 0xffffu)};
        base[r][n] = pkmul(xf, pkfma(g2[r], dd[n], omg));
      }
    }
#pragma unroll
    for (int r = 0; r < 4; ++r) asm volatile("" : "+v"(base[r][0]), "+v"(base[r][1]), "+v"(base[r][2]), "+v"(base[r][3]));
    asm volatile("s_nop 4\n\ts_waitcnt vmcnt(0)\n\ts_mov_b64 exec, 1\n\tbuffer_atomic_add %0, off, %1, %2 sc0 sc1\n\ts_mov_b64 exec, -1"
                 : "+v"(cnt) : "s"(rws), "s"(W_HF + (c * 2 + (j >> 3)) * 128) : "memory");
    STAMP(9);
    mine = (unsigned)__ballot(lane < 8 && (selu >> 7) == (unsigned)c && ((selu >> 1) & 3u) == (unsigned)wid);
#pragma unroll
    for (int q = 0; q < 8; ++q) dp[q] = L_ONE - 16;
#pragma unroll 1
    for (unsigned m = mine; m; m &= m - 1) {
      const int s = __builtin_ctz(m), t = __builtin_amdgcn_readlane((int)selu, s) & 127;
#pragma unroll
      for (int q = 0; q < 8; ++q) dp[q] = t == tl0 + 8 * (q >> 1) + (q & 1) ? L_BROW + s * 256 + fr * 16 : dp[q];
    }
  }
  f32x4 ac2[2][4];
  float bs[8][4];
#pragma unroll
  for (int q = 0; q < 8; ++q)
#pragma unroll
    for (int n = 0; n < 4; ++n) bs[q][n] = base[q >> 1][n][q & 1];
  {
#pragma unroll
    for (int m = 0; m < 2; ++m)
#pragma unroll
      for (int n = 0; n < 4; ++n) ac2[m][n] = (f32x4){0.f, 0.f, 0.f, 0.f};
    unsigned pc[4], fl2 = M_LAND + 26 * 8, vo = ((j & 8) * 32768 + wid * 2048) * 2 + HOFS(lane);
    const unsigned hr = M_HR, scn2 = M_CONS + 256 + wid * 4, hb_ = W_H + c * 524288 * 2, f9 = M_LAND + 17 * 8 + min(lane, 17) * 4;
#pragma unroll
    for (int q = 0; q < 4; ++q) pc[q] = fr * 128 + (((2 * q + (fq >> 1)) ^ ((fr >> 1) & 7)) * 16) + 8192 * (fq & 1);
    const unsigned sf = W_HF + (c * 2 + (j >> 3)) * 128, sf1 = sf ^ 128;
    unsigned t_sk, t_s0, t_s1;
    asm volatile(ASM_MB : [c00] "+&v"(ac2[0][0]), [c01] "+&v"(ac2[0][1]), [c02] "+&v"(ac2[0][2]), [c03] "+&v"(ac2[0][3]), [c10] "+&v"(ac2[1][0]), [c11] "+&v"(ac2[1][1]), [c12] "+&v"(ac2[1][2]), [c13] "+&v"(ac2[1][3]), [pc0] "+&v"(pc[0]), [pc1] "+&v"(pc[1]), [pc2] "+&v"(pc[2]), [pc3] "+&v"(pc[3]), [b00] "+&v"(bs[0][0]), [b01] "+&v"(bs[0][1]), [b02] "+&v"(bs[0][2]), [b03] "+&v"(bs[0][3]), [b10] "+&v"(bs[1][0]), [b11] "+&v"(bs[1][1]), [b12] "+&v"(bs[1][2]), [b13] "+&v"(bs[1][3]), [b20] "+&v"(bs[2][0]), [b21] "+&v"(bs[2][1]), [b22] "+&v"(bs[2][2]), [b23] "+&v"(bs[2][3]), [b30] "+&v"(bs[3][0]), [b31] "+&v"(bs[3][1]), [b32] "+&v"(bs[3][2]), [b33] "+&v"(bs[3][3]), [b40] "+&v"(bs[4][0]), [b41] "+&v"(bs[4][1]), [b42] "+&v"(bs[4][2]), [b43] "+&v"(bs[4][3]), [b50] "+&v"(bs[5][0]), [b51] "+&v"(bs[5][1]), [b52] "+&v"(bs[5][2]), [b53] "+&v"(bs[5][3]), [b60] "+&v"(bs[6][0]), [b61] "+&v"(bs[6][1]), [b62] "+&v"(bs[6][2]), [b63] "+&v"(bs[6][3]), [b70] "+&v"(bs[7][0]), [b71] "+&v"(bs[7][1]), [b72] "+&v"(bs[7][2]), [b73] "+&v"(bs[7][3]), [vo] "+&v"(vo), [fl] "+&v"(fl2), [sg] "=&s"(t_sk), [s0] "=&s"(t_s0), [s1] "=&s"(t_s1), [bud] "+&s"(bud) : [d0] "v"(dp[0]), [d1] "v"(dp[1]), [d2] "v"(dp[2]), [d3] "v"(dp[3]), [d4] "v"(dp[4]), [d5] "v"(dp[5]), [d6] "v"(dp[6]), [d7] "v"(dp[7]), [cnt] "v"(cnt), [xd] "v"(FL_XD), [mine] "s"(mine), [srd] "s"(rws), [srz] "s"(rz), [hr] "v"(hr), [f9] "v"(f9), [w] "s"(wid), [scn] "s"(scn2), [sf] "s"(sf), [sf1] "s"(sf1), [k2] "s"(0x4000), [h0] "s"(hb_ + 0), [h1] "s"(hb_ + 16384), [h2] "s"(hb_ + 32768), [h3] "s"(hb_ + 49152), [h4] "s"(hb_ + 65536), [h5] "s"(hb_ + 81920), [h6] "s"(hb_ + 98304), [h7] "s"(hb_ + 114688) : "memory", "scc", "vcc", "m0", "v64", "v65", "v66", "v67", "v68", "v69", "v70", "v71", "v72", "v73", "v74", "v75", "v76", "v77", "v78", "v79", "v80", "v81", "v82", "v83", "v84", "v85", "v86", "v87", "v88", "v89", "v90", "v91", "v92", "v93", "v94", "v95", "v96", "v97", "v98", "v99", "v100", "v101", "v102", "v103", "v104", "v105", "v106", "v107", "v108", "v109", "v110", "v111", "v112", "v113", "v114", "v115", "v116", "v117", "v118", "v119", "v120", "v121", "v122", "v123", "v124", "v125", "v126", "v127", "v128", "v129", "v130", "v131", "v132", "v133", "v134", "v135", "v136", "v137", "v138", "v139", "v140", "v141", "v142", "v143", "v144", "v145", "v146", "v147", "v148", "v149", "v150", "v151", "v152", "v153", "v154", "v155", "v156", "v157", "v158", "v159", "v160", "v161", "v162", "v163", "v164", "v165", "v166", "v167", "v168", "v169", "v170", "v171", "v172", "v173", "v174", "v175", "v176", "v177", "v178", "v179", "v180", "v181", "v182", "v183", "v184", "v185", "v186", "v187", "v188", "v189", "v190", "v191", "v192", "v193", "v194", "v195", "v196");
  }
  STAMP(6);
  asm volatile("" : "+v"(g2[0]), "+v"(g2[1]), "+v"(g2[2]), "+v"(g2[3]));
  int ln2 = lane;
  asm volatile("" : "+v"(ln2));
  const int fr2 = ln2 & 15, tl2 = 32 * (ln2 >> 4) + 2 * wid;
  f32x4 ov[2][4];
#pragma unroll
  for (int e = 0; e < 2; ++e)
#pragma unroll
    for (int r = 0; r < 4; ++r) {
      const float gg = 2.f * g2[r][e];
#pragma unroll
      for (int n = 0; n < 4; ++n) ov[e][r][n] = fma1(gg, ac2[e][n][r], bs[2 * r + e][n]);
    }
  {
    const rsrc_t ro = mk(out, 2048 * 1024 * 4);
    const unsigned oo = (c * 128 + tl2) * 4096 + (j * 64 + 4 * fr2) * 4, oo2 = oo + 4096;
    asm volatile("s_nop 4\n\t"
                 "buffer_store_dwordx4 %0, %8, %10, 0 offen" OST "\n\tbuffer_store_dwordx4 %4, %9, %10, 0 offen" OST "\n\t"
                 "buffer_store_dwordx4 %1, %8, %10, %11 offen" OST "\n\tbuffer_store_dwordx4 %5, %9, %10, %11 offen" OST "\n\t"
                 "buffer_store_dwordx4 %2, %8, %10, %12 offen" OST "\n\tbuffer_store_dwordx4 %6, %9, %10, %12 offen" OST "\n\t"
                 "buffer_store_dwordx4 %3, %8, %10, %13 offen" OST "\n\tbuffer_store_dwordx4 %7, %9, %10, %13 offen" OST "\n\t"
                 "s_nop 7\n\ts_nop 7"
                 ::"v"(ov[0][0]), "v"(ov[0][1]), "v"(ov[0][2]), "v"(ov[0][3]), "v"(ov[1][0]), "v"(ov[1][1]), "v"(ov[1][2]), "v"(ov[1][3]), "v"(oo), "v"(oo2), "s"(ro),
                 "s"(32768), "s"(65536), "s"(98304) : "memory");
  }
  if (c == 15 && j == 0 && wid == 0 && lane == 0) st64c(rws, W_EPOCH, (u32x2){tagA, tagB});
  STAMP(7);
  STAMPS();
#endif
}

extern "C" void kernel_launch(void* const* d_in, const int* in_sizes, int n_in, void* d_out, int out_size, void* d_ws, size_t ws_size,
                              hipStream_t stream) {
  (void)hipFuncSetAttribute((const void*)fused, hipFuncAttributeMaxDynamicSharedMemorySize, LDS_BYTES);
  fused<<<256, 512, LDS_BYTES, stream>>>((const u16*)d_in[0], (const u16*)d_in[1], (const u16*)d_in[2], (const u16*)d_in[3], (const u16*)d_in[4],
                                         (const u16*)d_in[5], (const u16*)d_in[6], (const u16*)d_in[7], (const u16*)d_in[8], (const u16*)d_in[10],
                                         (const u16*)d_in[11], (const u16*)d_in[12], (const u16*)d_in[13], (const u16*)d_in[14], (char*)d_ws + WS_BASE(ws_size),
                                         (float*)d_out);
}
```

```cpp
#include <hip/hip_runtime.h>
#include <hip/hip_bf16.h>
#include <stdint.h>

typedef __attribute__((ext_vector_type(8))) short bf16x8;
typedef __attribute__((ext_vector_type(4))) float f32x4;
typedef __attribute__((ext_vector_type(2))) float f32x2;
typedef __attribute__((ext_vector_type(4))) unsigned u32x4;
typedef __attribute__((ext_vector_type(2))) unsigned u32x2;
typedef unsigned short u16;
typedef unsigned long long u64;
#define LAS __attribute__((address_space(3)))
#define DEV __device__ __forceinline__

#define ASM_MA \
  "s_nop 4\n\t" \
  "buffer_load_dword v228, off, %[srd], 0 sc1\n\t" \
  "buffer_load_dwordx2 %[dv], %[dofs], %[srdd], 0 offen\n\t" \
  "s_mov_b32 %[sk], 0\n\t" \
  "s_mov_b32 %[sx], 0x20000\n\t" \
  "Lsp0_%=:\n\t" \
  "ds_read_b128 v[128:131], %[fl] offset:1056\n\t" \
  "s_sub_i32 %[bud], %[bud], 1\n\t" \
  "s_cmp_lt_i32 %[bud], 0\n\t" \
  "s_cbranch_scc1 Lb0_%=\n\t" \
  "s_waitcnt lgkmcnt(0)\n\t" \
  "v_or3_b32 v128, v128, v129, v130\n\t" \
  "v_or_b32 v128, v128, v131\n\t" \
  "s_nop 0\n\t" \
  "v_readfirstlane_b32 %[s0], v128\n\t" \
  "s_cmp_eq_u32 %[s0], 0\n\t" \
  "s_cbranch_scc0 Lsp0_%=\n\t" \
  "Lb0_%=:\n\t" \
  "s_cmp_eq_u32 %[sk], %[ks]\n\t" \
  "s_cbranch_scc1 Lx0_%=\n\t" \
  "Lxb0_%=:\n\t" \
  "s_mov_b32 m0, %[scn]\n\t" \
  "v_add_u32 %[fl], 8, %[fl]\n\t" \
  "ds_read_b128 v[208:211], %[pa0] offset:0\n\t" \
  "ds_read_b128 v[212:215], %[pa0] offset:8192\n\t" \
  "ds_read_b128 v[216:219], %[pa1] offset:0\n\t" \
  "ds_read_b128 v[220:223], %[pa1] offset:8192\n\t" \
  "ds_read_b128 v[176:179], %[pb0] offset:0\n\t" \
  "ds_read_b128 v[180:183], %[pb0] offset:2048\n\t" \
  "ds_read_b128 v[184:187], %[pb0] offset:4096\n\t" \
  "ds_read_b128 v[188:191], %[pb0] offset:6144\n\t" \
  "ds_read_b128 v[192:195], %[pb0] offset:8192\n\t" \
  "ds_read_b128 v[196:199], %[pb0] offset:10240\n\t" \
  "ds_read_b128 v[200:203], %[pb0] offset:12288\n\t" \
  "ds_read_b128 v[204:207], %[pb0] offset:14336\n\t" \
  "ds_read_b64 v[224:225], %[fl]\n\t" \
  "s_waitcnt lgkmcnt(7)\n\t" \
  "v_mfma_f32_16x16x32_bf16 v[64:67], v[208:211], v[176:179], 0\n\t" \
  "ds_read_b128 v[144:147], %[pb1] offset:0\n\t" \
  "s_add_u32 %[sk], %[sk], 1\n\t" \
  "v_mfma_f32_16x16x32_bf16 v[96:99], v[212:215], v[176:179], 0\n\t" \
  "s_and_b32 %[s1], %[sk], 3\n\t" \
  "s_cselect_b32 %[s1], 0x8000, %[wrap]\n\t" \
  "v_mfma_f32_16x16x32_bf16 v[68:71], v[208:211], v[180:183], 0\n\t" \
  "ds_read_b128 v[148:151], %[pb1] offset:2048\n\t" \
  "s_add_u32 %[sx], %[sx], 0x8000\n\t" \
  "v_mfma_f32_16x16x32_bf16 v[100:103], v[212:215], v[180:183], 0\n\t" \
  "s_cmp_eq_u32 %[sx], 0x28000\n\t" \
  "s_waitcnt lgkmcnt(7)\n\t" \
  "v_mfma_f32_16x16x32_bf16 v[72:75], v[208:211], v[184:187], 0\n\t" \
  "ds_read_b128 v[152:155], %[pb1] offset:4096\n\t" \
  "s_cselect_b32 %[s2], %[wrap2], 0x8000\n\t" \
  "v_mfma_f32_16x16x32_bf16 v[104:107], v[212:215], v[184:187], 0\n\t" \
  "s_cselect_b32 %[sx], 0, %[sx]\n\t" \
  "v_add_u32 %[pa0], %[s1], %[pa0]\n\t" \
  "v_mfma_f32_16x16x32_bf16 v[76:79], v[208:211], v[188:191], 0\n\t" \
  "ds_read_b128 v[156:159], %[pb1] offset:6144\n\t" \
  "v_add_u32 %[pb0], %[s2], %[pb0]\n\t" \
  "v_mfma_f32_16x16x32_bf16 v[108:111], v[212:215], v[188:191], 0\n\t" \
  "s_cmp_eq_u32 %[sk], %[ks]\n\t" \
  "s_cselect_b32 %[s3], 1, 0\n\t" \
  "s_waitcnt lgkmcnt(5)\n\t" \
  "v_mfma_f32_16x16x32_bf16 v[80:83], v[208:211], v[192:195], 0\n\t" \
  "ds_read_b128 v[160:163], %[pb1] offset:8192\n\t" \
  "v_add_u32 %[pa1], %[s1], %[pa1]\n\t" \
  "v_mfma_f32_16x16x32_bf16 v[112:115], v[212:215], v[192:195], 0\n\t" \
  "s_mov_b64 exec, 1\n\t" \
  "buffer_load_dword %[l16], %[srz], 0 offen lds\n\t" \
  "s_mov_b64 exec, -1\n\t" \
  "v_mfma_f32_16x16x32_bf16 v[84:87], v[208:211], v[196:199], 0\n\t" \
  "ds_read_b128 v[164:167], %[pb1] offset:10240\n\t" \
  "s_add_u32 m0, m0, 16\n\t" \
  "v_mfma_f32_16x16x32_bf16 v[116:119], v[212:215], v[196:199], 0\n\t" \
  "s_waitcnt lgkmcnt(6)\n\t" \
  "v_or_b32 v224, v224, v225\n\t" \
  "v_mfma_f32_16x16x32_bf16 v[88:91], v[208:211], v[200:203], 0\n\t" \
  "ds_read_b128 v[168:171], %[pb1] offset:12288\n\t" \
  "v_readfirstlane_b32 %[s0], v224\n\t" \
  "v_mfma_f32_16x16x32_bf16 v[120:123], v[212:215], v[200:203], 0\n\t" \
  "s_or_b32 %[s0], %[s0], %[s3]\n\t" \
  "s_cbranch_scc1 Lsp1_%=\n\t" \
  "Lb1_%=:\n\t" \
  "v_mfma_f32_16x16x32_bf16 v[92:95], v[208:211], v[204:207], 0\n\t" \
  "ds_read_b128 v[172:175], %[pb1] offset:14336\n\t" \
  "v_add_u32 %[fl], 8, %[fl]\n\t" \
  "v_mfma_f32_16x16x32_bf16 v[124:127], v[212:215], v[204:207], 0\n\t" \
  "s_branch Lmid_%=\n\t" \
  "Lloop_%=:\n\t" \
  "s_waitcnt lgkmcnt(6)\n\t" \
  "v_mfma_f32_16x16x32_bf16 v[64:67], v[136:139], v[144:147], v[64:67]\n\t" \
  "ds_read_b128 v[208:211], %[pa0] offset:0\n\t" \
  "v_mfma_f32_16x16x32_bf16 v[96:99], v[140:143], v[144:147], v[96:99]\n\t" \
  "ds_read_b128 v[212:215], %[pa0] offset:8192\n\t" \
  "v_mfma_f32_16x16x32_bf16 v[68:71], v[136:139], v[148:151], v[68:71]\n\t" \
  "ds_read_b128 v[216:219], %[pa1] offset:0\n\t" \
  "v_mfma_f32_16x16x32_bf16 v[100:103], v[140:143], v[148:151], v[100:103]\n\t" \
  "ds_read_b128 v[220:223], %[pa1] offset:8192\n\t" \
  "s_waitcnt lgkmcnt(8)\n\t" \
  "v_mfma_f32_16x16x32_bf16 v[72:75], v[136:139], v[152:155], v[72:75]\n\t" \
  "ds_read_b128 v[176:179], %[pb0] offset:0\n\t" \
  "v_mfma_f32_16x16x32_bf16 v[104:107], v[140:143], v[152:155], v[104:107]\n\t" \
  "ds_read_b128 v[180:183], %[pb0] offset:2048\n\t" \
  "v_mfma_f32_16x16x32_bf16 v[76:79], v[136:139], v[156:159], v[76:79]\n\t" \
  "v_add_u32 %[pb1], %[s2], %[pb1]\n\t" \
  "v_mfma_f32_16x16x32_bf16 v[108:111], v[140:143], v[156:159], v[108:111]\n\t" \
  "ds_read_b128 v[184:187], %[pb0] offset:4096\n\t" \
  "s_waitcnt lgkmcnt(7)\n\t" \
  "v_mfma_f32_16x16x32_bf16 v[80:83], v[136:139], v[160:163], v[80:83]\n\t" \
  "ds_read_b128 v[188:191], %[pb0] offset:6144\n\t" \
  "v_mfma_f32_16x16x32_bf16 v[112:115], v[140:143], v[160:163], v[112:115]\n\t" \
  "ds_read_b128 v[192:195], %[pb0] offset:8192\n\t" \
  "v_mfma_f32_16x16x32_bf16 v[84:87], v[136:139], v[164:167], v[84:87]\n\t" \
  "s_waitcnt lgkmcnt(5)\n\t" \
  "s_mov_b64 exec, 1\n\t" \
  "buffer_load_dword %[l16], %[srz], 0 offen lds\n\t" \
  "s_mov_b64 exec, -1\n\t" \
  "v_mfma_f32_16x16x32_bf16 v[116:119], v[140:143], v[164:167], v[116:119]\n\t" \
  "ds_read_b128 v[196:199], %[pb0] offset:10240\n\t" \
  "s_add_u32 m0, m0, 16\n\t" \
  "v_mfma_f32_16x16x32_bf16 v[88:91], v[136:139], v[168:171], v[88:91]\n\t" \
  "v_mfma_f32_16x16x32_bf16 v[120:123], v[140:143], v[168:171], v[120:123]\n\t" \
  "ds_read_b128 v[200:203], %[pb0] offset:12288\n\t" \
  "v_mfma_f32_16x16x32_bf16 v[92:95], v[136:139], v[172:175], v[92:95]\n\t" \
  "ds_read_b128 v[204:207], %[pb0] offset:14336\n\t" \
  "v_mfma_f32_16x16x32_bf16 v[124:127], v[140:143], v[172:175], v[124:127]\n\t" \
  "Lnx5_%=:\n\t" \
  "ds_read_b64 v[224:225], %[fl]\n\t" \
  "s_waitcnt lgkmcnt(7)\n\t" \
  "v_mfma_f32_16x16x32_bf16 v[64:67], v[208:211], v[176:179], v[64:67]\n\t" \
  "ds_read_b128 v[144:147], %[pb1] offset:0\n\t" \
  "s_add_u32 %[sk], %[sk], 1\n\t" \
  "v_mfma_f32_16x16x32_bf16 v[96:99], v[212:215], v[176:179], v[96:99]\n\t" \
  "s_and_b32 %[s1], %[sk], 3\n\t" \
  "s_cselect_b32 %[s1], 0x8000, %[wrap]\n\t" \
  "v_mfma_f32_16x16x32_bf16 v[68:71], v[208:211], v[180:183], v[68:71]\n\t" \
  "ds_read_b128 v[148:151], %[pb1] offset:2048\n\t" \
  "s_add_u32 %[sx], %[sx], 0x8000\n\t" \
  "v_mfma_f32_16x16x32_bf16 v[100:103], v[212:215], v[180:183], v[100:103]\n\t" \
  "s_cmp_eq_u32 %[sx], 0x28000\n\t" \
  "s_waitcnt lgkmcnt(7)\n\t" \
  "v_mfma_f32_16x16x32_bf16 v[72:75], v[208:211], v[184:187], v[72:75]\n\t" \
  "ds_read_b128 v[152:155], %[pb1] offset:4096\n\t" \
  "s_cselect_b32 %[s2], %[wrap2], 0x8000\n\t" \
  "v_mfma_f32_16x16x32_bf16 v[104:107], v[212:215], v[184:187], v[104:107]\n\t" \
  "s_cselect_b32 %[sx], 0, %[sx]\n\t" \
  "v_add_u32 %[pa0], %[s1], %[pa0]\n\t" \
  "v_mfma_f32_16x16x32_bf16 v[76:79], v[208:211], v[188:191], v[76:79]\n\t" \
  "ds_read_b128 v[156:159], %[pb1] offset:6144\n\t" \
  "v_add_u32 %[pb0], %[s2], %[pb0]\n\t" \
  "v_mfma_f32_16x16x32_bf16 v[108:111], v[212:215], v[188:191], v[108:111]\n\t" \
  "s_cmp_eq_u32 %[sk], %[ks]\n\t" \
  "s_cselect_b32 %[s3], 1, 0\n\t" \
  "s_waitcnt lgkmcnt(5)\n\t" \
  "v_mfma_f32_16x16x32_bf16 v[80:83], v[208:211], v[192:195], v[80:83]\n\t" \
  "ds_read_b128 v[160:163], %[pb1] offset:8192\n\t" \
  "v_add_u32 %[pa1], %[s1], %[pa1]\n\t" \
  "v_mfma_f32_16x16x32_bf16 v[112:115], v[212:215], v[192:195], v[112:115]\n\t" \
  "v_mfma_f32_16x16x32_bf16 v[84:87], v[208:211], v[196:199], v[84:87]\n\t" \
  "ds_read_b128 v[164:167], %[pb1] offset:10240\n\t" \
  "v_mfma_f32_16x16x32_bf16 v[116:119], v[212:215], v[196:199], v[116:119]\n\t" \
  "s_waitcnt lgkmcnt(6)\n\t" \
  "v_or_b32 v224, v224, v225\n\t" \
  "v_mfma_f32_16x16x32_bf16 v[88:91], v[208:211], v[200:203], v[88:91]\n\t" \
  "ds_read_b128 v[168:171], %[pb1] offset:12288\n\t" \
  "v_readfirstlane_b32 %[s0], v224\n\t" \
  "v_mfma_f32_16x16x32_bf16 v[120:123], v[212:215], v[200:203], v[120:123]\n\t" \
  "s_or_b32 %[s0], %[s0], %[s3]\n\t" \
  "s_cbranch_scc1 Lsp3_%=\n\t" \
  "Lb3_%=:\n\t" \
  "v_mfma_f32_16x16x32_bf16 v[92:95], v[208:211], v[204:207], v[92:95]\n\t" \
  "ds_read_b128 v[172:175], %[pb1] offset:14336\n\t" \
  "v_add_u32 %[fl], 8, %[fl]\n\t" \
  "v_mfma_f32_16x16x32_bf16 v[124:127], v[212:215], v[204:207], v[124:127]\n\t" \
  "Lmid_%=:\n\t" \
  "s_waitcnt lgkmcnt(6)\n\t" \
  "v_mfma_f32_16x16x32_bf16 v[64:67], v[216:219], v[144:147], v[64:67]\n\t" \
  "ds_read_b128 v[128:131], %[pa0] offset:0\n\t" \
  "v_mfma_f32_16x16x32_bf16 v[96:99], v[220:223], v[144:147], v[96:99]\n\t" \
  "ds_read_b128 v[132:135], %[pa0] offset:8192\n\t" \
  "v_mfma_f32_16x16x32_bf16 v[68:71], v[216:219], v[148:151], v[68:71]\n\t" \
  "ds_read_b128 v[136:139], %[pa1] offset:0\n\t" \
  "v_mfma_f32_16x16x32_bf16 v[100:103], v[220:223], v[148:151], v[100:103]\n\t" \
  "ds_read_b128 v[140:143], %[pa1] offset:8192\n\t" \
  "s_waitcnt lgkmcnt(8)\n\t" \
  "v_mfma_f32_16x16x32_bf16 v[72:75], v[216:219], v[152:155], v[72:75]\n\t" \
  "ds_read_b128 v[176:179], %[pb0] offset:0\n\t" \
  "v_mfma_f32_16x16x32_bf16 v[104:107], v[220:223], v[152:155], v[104:107]\n\t" \
  "ds_read_b128 v[180:183], %[pb0] offset:2048\n\t" \
  "v_mfma_f32_16x16x32_bf16 v[76:79], v[216:219], v[156:159], v[76:79]\n\t" \
  "v_add_u32 %[pb1], %[s2], %[pb1]\n\t" \
  "v_mfma_f32_16x16x32_bf16 v[108:111], v[220:223], v[156:159], v[108:111]\n\t" \
  "ds_read_b128 v[184:187], %[pb0] offset:4096\n\t" \
  "s_waitcnt lgkmcnt(7)\n\t" \
  "v_mfma_f32_16x16x32_bf16 v[80:83], v[216:219], v[160:163], v[80:83]\n\t" \
  "ds_read_b128 v[188:191], %[pb0] offset:6144\n\t" \
  "v_mfma_f32_16x16x32_bf16 v[112:115], v[220:223], v[160:163], v[112:115]\n\t" \
  "ds_read_b128 v[192:195], %[pb0] offset:8192\n\t" \
  "v_mfma_f32_16x16x32_bf16 v[84:87], v[216:219], v[164:167], v[84:87]\n\t" \
  "s_waitcnt lgkmcnt(5)\n\t" \
  "s_mov_b64 exec, 1\n\t" \
  "buffer_load_dword %[l16], %[srz], 0 offen lds\n\t" \
  "s_mov_b64 exec, -1\n\t" \
  "v_mfma_f32_16x16x32_bf16 v[116:119], v[220:223], v[164:167], v[116:119]\n\t" \
  "ds_read_b128 v[196:199], %[pb0] offset:10240\n\t" \
  "s_add_u32 m0, m0, 16\n\t" \
  "v_mfma_f32_16x16x32_bf16 v[88:91], v[216:219], v[168:171], v[88:91]\n\t" \
  "v_mfma_f32_16x16x32_bf16 v[120:123], v[220:223], v[168:171], v[120:123]\n\t" \
  "ds_read_b128 v[200:203], %[pb0] offset:12288\n\t" \
  "v_mfma_f32_16x16x32_bf16 v[92:95], v[216:219], v[172:175], v[92:95]\n\t" \
  "ds_read_b128 v[204:207], %[pb0] offset:14336\n\t" \
  "v_mfma_f32_16x16x32_bf16 v[124:127], v[220:223], v[172:175], v[124:127]\n\t" \
  "Lnx3_%=:\n\t" \
  "ds_read_b64 v[224:225], %[fl]\n\t" \
  "s_waitcnt lgkmcnt(7)\n\t" \
  "v_mfma_f32_16x16x32_bf16 v[64:67], v[128:131], v[176:179], v[64:67]\n\t" \
  "ds_read_b128 v[144:147], %[pb1] offset:0\n\t" \
  "s_add_u32 %[sk], %[sk], 1\n\t" \
  "v_mfma_f32_16x16x32_bf16 v[96:99], v[132:135], v[176:179], v[96:99]\n\t" \
  "s_and_b32 %[s1], %[sk], 3\n\t" \
  "s_cselect_b32 %[s1], 0x8000, %[wrap]\n\t" \
  "v_mfma_f32_16x16x32_bf16 v[68:71], v[128:131], v[180:183], v[68:71]\n\t" \
  "ds_read_b128 v[148:151], %[pb1] offset:2048\n\t" \
  "s_add_u32 %[sx], %[sx], 0x8000\n\t" \
  "v_mfma_f32_16x16x32_bf16 v[100:103], v[132:135], v[180:183], v[100:103]\n\t" \
  "s_cmp_eq_u32 %[sx], 0x28000\n\t" \
  "s_waitcnt lgkmcnt(7)\n\t" \
  "v_mfma_f32_16x16x32_bf16 v[72:75], v[128:131], v[184:187], v[72:75]\n\t" \
  "ds_read_b128 v[152:155], %[pb1] offset:4096\n\t" \
  "s_cselect_b32 %[s2], %[wrap2], 0x8000\n\t" \
  "v_mfma_f32_16x16x32_bf16 v[104:107], v[132:135], v[184:187], v[104:107]\n\t" \
  "s_cselect_b32 %[sx], 0, %[sx]\n\t" \
  "v_add_u32 %[pa0], %[s1], %[pa0]\n\t" \
  "v_mfma_f32_16x16x32_bf16 v[76:79], v[128:131], v[188:191], v[76:79]\n\t" \
  "ds_read_b128 v[156:159], %[pb1] offset:6144\n\t" \
  "v_add_u32 %[pb0], %[s2], %[pb0]\n\t" \
  "v_mfma_f32_16x16x32_bf16 v[108:111], v[132:135], v[188:191], v[108:111]\n\t" \
  "s_cmp_eq_u32 %[sk], %[ks]\n\t" \
  "s_cselect_b32 %[s3], 1, 0\n\t" \
  "s_waitcnt lgkmcnt(5)\n\t" \
  "v_mfma_f32_16x16x32_bf16 v[80:83], v[128:131], v[192:195], v[80:83]\n\t" \
  "ds_read_b128 v[160:163], %[pb1] offset:8192\n\t" \
  "v_add_u32 %[pa1], %[s1], %[pa1]\n\t" \
  "v_mfma_f32_16x16x32_bf16 v[112:115], v[132:135], v[192:195], v[112:115]\n\t" \
  "v_mfma_f32_16x16x32_bf16 v[84:87], v[128:131], v[196:199], v[84:87]\n\t" \
  "ds_read_b128 v[164:167], %[pb1] offset:10240\n\t" \
  "v_mfma_f32_16x16x32_bf16 v[116:119], v[132:135], v[196:199], v[116:119]\n\t" \
  "s_waitcnt lgkmcnt(6)\n\t" \
  "v_or_b32 v224, v224, v225\n\t" \
  "v_mfma_f32_16x16x32_bf16 v[88:91], v[128:131], v[200:203], v[88:91]\n\t" \
  "ds_read_b128 v[168:171], %[pb1] offset:12288\n\t" \
  "v_readfirstlane_b32 %[s0], v224\n\t" \
  "v_mfma_f32_16x16x32_bf16 v[120:123], v[132:135], v[200:203], v[120:123]\n\t" \
  "s_or_b32 %[s0], %[s0], %[s3]\n\t" \
  "s_cbranch_scc1 Lsp5_%=\n\t" \
  "Lb5_%=:\n\t" \
  "v_mfma_f32_16x16x32_bf16 v[92:95], v[128:131], v[204:207], v[92:95]\n\t" \
  "ds_read_b128 v[172:175], %[pb1] offset:14336\n\t" \
  "v_add_u32 %[fl], 8, %[fl]\n\t" \
  "v_mfma_f32_16x16x32_bf16 v[124:127], v[132:135], v[204:207], v[124:127]\n\t" \
  "s_cmp_lt_u32 %[sk], 16\n\t" \
  "s_cbranch_scc1 Lloop_%=\n\t" \
  "s_waitcnt lgkmcnt(6)\n\t" \
  "v_mfma_f32_16x16x32_bf16 v[64:67], v[136:139], v[144:147], v[64:67]\n\t" \
  "ds_read_b128 v[176:179], %[tq] offset:0\n\t" \
  "v_mfma_f32_16x16x32_bf16 v[96:99], v[140:143], v[144:147], v[96:99]\n\t" \
  "ds_read_b128 v[180:183], %[tq] offset:256\n\t" \
  "v_mfma_f32_16x16x32_bf16 v[68:71], v[136:139], v[148:151], v[68:71]\n\t" \
  "ds_read_b128 v[192:195], %[tq] offset:1024\n\t" \
  "v_mfma_f32_16x16x32_bf16 v[100:103], v[140:143], v[148:151], v[100:103]\n\t" \
  "ds_read_b128 v[196:199], %[tq] offset:1280\n\t" \
  "s_waitcnt lgkmcnt(8)\n\t" \
  "v_mfma_f32_16x16x32_bf16 v[72:75], v[136:139], v[152:155], v[72:75]\n\t" \
  "ds_read_b128 v[200:203], %[tq] offset:1536\n\t" \
  "v_mfma_f32_16x16x32_bf16 v[104:107], v[140:143], v[152:155], v[104:107]\n\t" \
  "ds_read_b128 v[204:207], %[tq] offset:1792\n\t" \
  "v_mfma_f32_16x16x32_bf16 v[76:79], v[136:139], v[156:159], v[76:79]\n\t" \
  "ds_read_b128 v[208:211], %[tq] offset:2048\n\t" \
  "v_mfma_f32_16x16x32_bf16 v[108:111], v[140:143], v[156:159], v[108:111]\n\t" \
  "ds_read_b128 v[212:215], %[tq] offset:2304\n\t" \
  "s_waitcnt lgkmcnt(8)\n\t" \
  "v_mfma_f32_16x16x32_bf16 v[80:83], v[136:139], v[160:163], v[80:83]\n\t" \
  "ds_read_b128 v[216:219], %[tq] offset:2560\n\t" \
  "v_mfma_f32_16x16x32_bf16 v[112:115], v[140:143], v[160:163], v[112:115]\n\t" \
  "ds_read_b128 v[220:223], %[tq] offset:2816\n\t" \
  "v_mfma_f32_16x16x32_bf16 v[84:87], v[136:139], v[164:167], v[84:87]\n\t" \
  "s_mov_b64 exec, 1\n\t" \
  "buffer_load_dword %[l16], %[srz], 0 offen lds\n\t" \
  "s_mov_b64 exec, -1\n\t" \
  "v_mfma_f32_16x16x32_bf16 v[116:119], v[140:143], v[164:167], v[116:119]\n\t" \
  "ds_read_b128 v[184:187], %[tq] offset:512\n\t" \
  "s_add_u32 m0, m0, 16\n\t" \
  "v_mfma_f32_16x16x32_bf16 v[88:91], v[136:139], v[168:171], v[88:91]\n\t" \
  "ds_read_b128 v[188:191], %[tq] offset:768\n\t" \
  "v_mfma_f32_16x16x32_bf16 v[120:123], v[140:143], v[168:171], v[120:123]\n\t" \
  "ds_read2_b32 v[230:231], %[tcl] offset0:0 offset1:64\n\t" \
  "v_mfma_f32_16x16x32_bf16 v[92:95], v[136:139], v[172:175], v[92:95]\n\t" \
  "ds_read2_b32 v[232:233], %[tcw] offset0:0 offset1:64\n\t" \
  "v_mfma_f32_16x16x32_bf16 v[124:127], v[140:143], v[172:175], v[124:127]\n\t" \
  "s_setprio 3\n\t" \
  "s_waitcnt vmcnt(8) lgkmcnt(4)\n\t" \
  "v_readfirstlane_b32 %[tgB], v228\n\t" \
  "s_add_u32 %[tgA], %[tgB], 1\n\t" \
  "s_add_u32 %[tgB], %[tgB], 0x40000001\n\t" \
  "v_pk_fma_f32 v[244:245], v[180:181], v[96:97], v[68:69] neg_lo:[1,0,0] neg_hi:[1,0,0]\n\t" \
  "v_pk_fma_f32 v[246:247], v[182:183], v[98:99], v[70:71] neg_lo:[1,0,0] neg_hi:[1,0,0]\n\t" \
  "v_pk_fma_f32 v[248:249], v[180:181], v[64:65], v[100:101]\n\t" \
  "v_pk_fma_f32 v[250:251], v[182:183], v[66:67], v[102:103]\n\t" \
  "v_pk_fma_f32 v[236:237], v[176:177], v[64:65], v[244:245]\n\t" \
  "v_pk_fma_f32 v[238:239], v[178:179], v[66:67], v[246:247]\n\t" \
  "v_pk_fma_f32 v[240:241], v[176:177], v[96:97], v[248:249]\n\t" \
  "v_pk_fma_f32 v[242:243], v[178:179], v[98:99], v[250:251]\n\t" \
  "v_pk_fma_f32 v[244:245], v[180:181], v[240:241], v[72:73] neg_lo:[1,0,0] neg_hi:[1,0,0]\n\t" \
  "v_pk_fma_f32 v[246:247], v[182:183], v[242:243], v[74:75] neg_lo:[1,0,0] neg_hi:[1,0,0]\n\t" \
  "v_pk_fma_f32 v[248:249], v[180:181], v[236:237], v[104:105]\n\t" \
  "v_pk_fma_f32 v[250:251], v[182:183], v[238:239], v[106:107]\n\t" \
  "v_pk_fma_f32 v[236:237], v[176:177], v[236:237], v[244:245]\n\t" \
  "v_pk_fma_f32 v[238:239], v[178:179], v[238:239], v[246:247]\n\t" \
  "v_pk_fma_f32 v[240:241], v[176:177], v[240:241], v[248:249]\n\t" \
  "v_pk_fma_f32 v[242:243], v[178:179], v[242:243], v[250:251]\n\t" \
  "v_pk_fma_f32 v[244:245], v[180:181], v[240:241], v[76:77] neg_lo:[1,0,0] neg_hi:[1,0,0]\n\t" \
  "v_pk_fma_f32 v[246:247], v[182:183], v[242:243], v[78:79] neg_lo:[1,0,0] neg_hi:[1,0,0]\n\t" \
  "v_pk_fma_f32 v[248:249], v[180:181], v[236:237], v[108:109]\n\t" \
  "v_pk_fma_f32 v[250:251], v[182:183], v[238:239], v[110:111]\n\t" \
  "v_pk_fma_f32 v[236:237], v[176:177], v[236:237], v[244:245]\n\t" \
  "v_pk_fma_f32 v[238:239], v[178:179], v[238:239], v[246:247]\n\t" \
  "v_pk_fma_f32 v[240:241], v[176:177], v[240:241], v[248:249]\n\t" \
  "v_pk_fma_f32 v[242:243], v[178:179], v[242:243], v[250:251]\n\t" \
  "v_pk_fma_f32 v[244:245], v[180:181], v[240:241], v[80:81] neg_lo:[1,0,0] neg_hi:[1,0,0]\n\t" \
  "v_pk_fma_f32 v[246:247], v[182:183], v[242:243], v[82:83] neg_lo:[1,0,0] neg_hi:[1,0,0]\n\t" \
  "v_pk_fma_f32 v[248:249], v[180:181], v[236:237], v[112:113]\n\t" \
  "v_pk_fma_f32 v[250:251], v[182:183], v[238:239], v[114:115]\n\t" \
  "v_pk_fma_f32 v[236:237], v[176:177], v[236:237], v[244:245]\n\t" \
  "v_pk_fma_f32 v[238:239], v[178:179], v[238:239], v[246:247]\n\t" \
  "v_pk_fma_f32 v[240:241], v[176:177], v[240:241], v[248:249]\n\t" \
  "v_pk_fma_f32 v[242:243], v[178:179], v[242:243], v[250:251]\n\t" \
  "v_pk_fma_f32 v[244:245], v[180:181], v[240:241], v[84:85] neg_lo:[1,0,0] neg_hi:[1,0,0]\n\t" \
  "v_pk_fma_f32 v[246:247], v[182:183], v[242:243], v[86:87] neg_lo:[1,0,0] neg_hi:[1,0,0]\n\t" \
  "v_pk_fma_f32 v[248:249], v[180:181], v[236:237], v[116:117]\n\t" \
  "v_pk_fma_f32 v[250:251], v[182:183], v[238:239], v[118:119]\n\t" \
  "v_pk_fma_f32 v[236:237], v[176:177], v[236:237], v[244:245]\n\t" \
  "v_pk_fma_f32 v[238:239], v[178:179], v[238:239], v[246:247]\n\t" \
  "v_pk_fma_f32 v[240:241], v[176:177], v[240:241], v[248:249]\n\t" \
  "v_pk_fma_f32 v[242:243], v[178:179], v[242:243], v[250:251]\n\t" \
  "v_pk_fma_f32 v[244:245], v[180:181], v[240:241], v[88:89] neg_lo:[1,0,0] neg_hi:[1,0,0]\n\t" \
  "v_pk_fma_f32 v[246:247], v[182:183], v[242:243], v[90:91] neg_lo:[1,0,0] neg_hi:[1,0,0]\n\t" \
  "v_pk_fma_f32 v[248:249], v[180:181], v[236:237], v[120:121]\n\t" \
  "v_pk_fma_f32 v[250:251], v[182:183], v[238:239], v[122:123]\n\t" \
  "v_pk_fma_f32 v[236:237], v[176:177], v[236:237], v[244:245]\n\t" \
  "v_pk_fma_f32 v[238:239], v[178:179], v[238:239], v[246:247]\n\t" \
  "v_pk_fma_f32 v[240:241], v[176:177], v[240:241], v[248:249]\n\t" \
  "v_pk_fma_f32 v[242:243], v[178:179], v[242:243], v[250:251]\n\t" \
  "v_pk_fma_f32 v[244:245], v[180:181], v[240:241], v[92:93] neg_lo:[1,0,0] neg_hi:[1,0,0]\n\t" \
  "v_pk_fma_f32 v[246:247], v[182:183], v[242:243], v[94:95] neg_lo:[1,0,0] neg_hi:[1,0,0]\n\t" \
  "v_pk_fma_f32 v[248:249], v[180:181], v[236:237], v[124:125]\n\t" \
  "v_pk_fma_f32 v[250:251], v[182:183], v[238:239], v[126:127]\n\t" \
  "v_pk_fma_f32 v[236:237], v[176:177], v[236:237], v[244:245]\n\t" \
  "v_pk_fma_f32 v[238:239], v[178:179], v[238:239], v[246:247]\n\t" \
  "v_pk_fma_f32 v[240:241], v[176:177], v[240:241], v[248:249]\n\t" \
  "v_pk_fma_f32 v[242:243], v[178:179], v[242:243], v[250:251]\n\t" \
  "s_add_u32 %[s0], %[eh], 1\n\t" \
  "v_mov_b32 v226, %[s0]\n\t" \
  "v_mov_b32 v253, %[tgA]\n\t" \
  "v_mov_b32 v255, %[tgB]\n\t" \
  "v_mov_b32_dpp v244, v236 row_shr:1 row_mask:0xf bank_mask:0xf bound_ctrl:0\n\t" \
  "v_mov_b32_dpp v245, v237 row_shr:1 row_mask:0xf bank_mask:0xf bound_ctrl:0\n\t" \
  "v_mov_b32_dpp v246, v238 row_shr:1 row_mask:0xf bank_mask:0xf bound_ctrl:0\n\t" \
  "v_mov_b32_dpp v247, v239 row_shr:1 row_mask:0xf bank_mask:0xf bound_ctrl:0\n\t" \
  "v_mov_b32_dpp v248, v240 row_shr:1 row_mask:0xf bank_mask:0xf bound_ctrl:0\n\t" \
  "v_mov_b32_dpp v249, v241 row_shr:1 row_mask:0xf bank_mask:0xf bound_ctrl:0\n\t" \
  "v_mov_b32_dpp v250, v242 row_shr:1 row_mask:0xf bank_mask:0xf bound_ctrl:0\n\t" \
  "v_mov_b32_dpp v251, v243 row_shr:1 row_mask:0xf bank_mask:0xf bound_ctrl:0\n\t" \
  "v_pk_fma_f32 v[236:237], v[192:193], v[244:245], v[236:237]\n\t" \
  "v_pk_fma_f32 v[238:239], v[194:195], v[246:247], v[238:239]\n\t" \
  "v_pk_fma_f32 v[240:241], v[192:193], v[248:249], v[240:241]\n\t" \
  "v_pk_fma_f32 v[242:243], v[194:195], v[250:251], v[242:243]\n\t" \
  "v_pk_fma_f32 v[236:237], v[196:197], v[248:249], v[236:237] neg_lo:[1,0,0] neg_hi:[1,0,0]\n\t" \
  "v_pk_fma_f32 v[238:239], v[198:199], v[250:251], v[238:239] neg_lo:[1,0,0] neg_hi:[1,0,0]\n\t" \
  "v_pk_fma_f32 v[240:241], v[196:197], v[244:245], v[240:241]\n\t" \
  "v_pk_fma_f32 v[242:243], v[198:199], v[246:247], v[242:243]\n\t" \
  "v_mov_b32_dpp v244, v236 row_shr:2 row_mask:0xf bank_mask:0xf bound_ctrl:0\n\t" \
  "v_mov_b32_dpp v245, v237 row_shr:2 row_mask:0xf bank_mask:0xf bound_ctrl:0\n\t" \
  "v_mov_b32_dpp v246, v238 row_shr:2 row_mask:0xf bank_mask:0xf bound_ctrl:0\n\t" \
  "v_mov_b32_dpp v247, v239 row_shr:2 row_mask:0xf bank_mask:0xf bound_ctrl:0\n\t" \
  "v_mov_b32_dpp v248, v240 row_shr:2 row_mask:0xf bank_mask:0xf bound_ctrl:0\n\t" \
  "v_mov_b32_dpp v249, v241 row_shr:2 row_mask:0xf bank_mask:0xf bound_ctrl:0\n\t" \
  "v_mov_b32_dpp v250, v242 row_shr:2 row_mask:0xf bank_mask:0xf bound_ctrl:0\n\t" \
  "v_mov_b32_dpp v251, v243 row_shr:2 row_mask:0xf bank_mask:0xf bound_ctrl:0\n\t" \
  "v_pk_fma_f32 v[236:237], v[200:201], v[244:245], v[236:237]\n\t" \
  "v_pk_fma_f32 v[238:239], v[202:203], v[246:247], v[238:239]\n\t" \
  "v_pk_fma_f32 v[240:241], v[200:201], v[248:249], v[240:241]\n\t" \
  "v_pk_fma_f32 v[242:243], v[202:203], v[250:251], v[242:243]\n\t" \
  "v_pk_fma_f32 v[236:237], v[204:205], v[248:249], v[236:237] neg_lo:[1,0,0] neg_hi:[1,0,0]\n\t" \
  "v_pk_fma_f32 v[238:239], v[206:207], v[250:251], v[238:239] neg_lo:[1,0,0] neg_hi:[1,0,0]\n\t" \
  "v_pk_fma_f32 v[240:241], v[204:205], v[244:245], v[240:241]\n\t" \
  "v_pk_fma_f32 v[242:243], v[206:207], v[246:247], v[242:243]\n\t" \
  "v_mov_b32_dpp v244, v236 row_shr:4 row_mask:0xf bank_mask:0xf bound_ctrl:0\n\t" \
  "v_mov_b32_dpp v245, v237 row_shr:4 row_mask:0xf bank_mask:0xf bound_ctrl:0\n\t" \
  "v_mov_b32_dpp v246, v238 row_shr:4 row_mask:0xf bank_mask:0xf bound_ctrl:0\n\t" \
  "v_mov_b32_dpp v247, v239 row_shr:4 row_mask:0xf bank_mask:0xf bound_ctrl:0\n\t" \
  "v_mov_b32_dpp v248, v240 row_shr:4 row_mask:0xf bank_mask:0xf bound_ctrl:0\n\t" \
  "v_mov_b32_dpp v249, v241 row_shr:4 row_mask:0xf bank_mask:0xf bound_ctrl:0\n\t" \
  "v_mov_b32_dpp v250, v242 row_shr:4 row_mask:0xf bank_mask:0xf bound_ctrl:0\n\t" \
  "v_mov_b32_dpp v251, v243 row_shr:4 row_mask:0xf bank_mask:0xf bound_ctrl:0\n\t" \
  "v_pk_fma_f32 v[236:237], v[208:209], v[244:245], v[236:237]\n\t" \
  "v_pk_fma_f32 v[238:239], v[210:211], v[246:247], v[238:239]\n\t" \
  "v_pk_fma_f32 v[240:241], v[208:209], v[248:249], v[240:241]\n\t" \
  "v_pk_fma_f32 v[242:243], v[210:211], v[250:251], v[242:243]\n\t" \
  "v_pk_fma_f32 v[236:237], v[212:213], v[248:249], v[236:237] neg_lo:[1,0,0] neg_hi:[1,0,0]\n\t" \
  "v_pk_fma_f32 v[238:239], v[214:215], v[250:251], v[238:239] neg_lo:[1,0,0] neg_hi:[1,0,0]\n\t" \
  "v_pk_fma_f32 v[240:241], v[212:213], v[244:245], v[240:241]\n\t" \
  "v_pk_fma_f32 v[242:243], v[214:215], v[246:247], v[242:243]\n\t" \
  "v_mov_b32_dpp v244, v236 row_shr:8 row_mask:0xf bank_mask:0xf bound_ctrl:0\n\t" \
  "v_mov_b32_dpp v245, v237 row_shr:8 row_mask:0xf bank_mask:0xf bound_ctrl:0\n\t" \
  "v_mov_b32_dpp v246, v238 row_shr:8 row_mask:0xf bank_mask:0xf bound_ctrl:0\n\t" \
  "v_mov_b32_dpp v247, v239 row_shr:8 row_mask:0xf bank_mask:0xf bound_ctrl:0\n\t" \
  "v_mov_b32_dpp v248, v240 row_shr:8 row_mask:0xf bank_mask:0xf bound_ctrl:0\n\t" \
  "v_mov_b32_dpp v249, v241 row_shr:8 row_mask:0xf bank_mask:0xf bound_ctrl:0\n\t" \
  "v_mov_b32_dpp v250, v242 row_shr:8 row_mask:0xf bank_mask:0xf bound_ctrl:0\n\t" \
  "v_mov_b32_dpp v251, v243 row_shr:8 row_mask:0xf bank_mask:0xf bound_ctrl:0\n\t" \
  "v_pk_fma_f32 v[236:237], v[216:217], v[244:245], v[236:237]\n\t" \
  "v_pk_fma_f32 v[238:239], v[218:219], v[246:247], v[238:239]\n\t" \
  "v_pk_fma_f32 v[240:241], v[216:217], v[248:249], v[240:241]\n\t" \
  "v_pk_fma_f32 v[242:243], v[218:219], v[250:251], v[242:243]\n\t" \
  "v_pk_fma_f32 v[236:237], v[220:221], v[248:249], v[236:237] neg_lo:[1,0,0] neg_hi:[1,0,0]\n\t" \
  "v_pk_fma_f32 v[238:239], v[222:223], v[250:251], v[238:239] neg_lo:[1,0,0] neg_hi:[1,0,0]\n\t" \
  "v_pk_fma_f32 v[240:241], v[220:221], v[244:245], v[240:241]\n\t" \
  "v_pk_fma_f32 v[242:243], v[222:223], v[246:247], v[242:243]\n\t" \
  "v_mov_b32_dpp v252, v236 row_newbcast:15 row_mask:0xf bank_mask:0x1\n\t" \
  "v_mov_b32_dpp v252, v237 row_newbcast:15 row_mask:0xf bank_mask:0x2\n\t" \
  "v_mov_b32_dpp v252, v238 row_newbcast:15 row_mask:0xf bank_mask:0x4\n\t" \
  "v_mov_b32_dpp v252, v239 row_newbcast:15 row_mask:0xf bank_mask:0x8\n\t" \
  "v_mov_b32_dpp v254, v240 row_newbcast:15 row_mask:0xf bank_mask:0x1\n\t" \
  "v_mov_b32_dpp v254, v241 row_newbcast:15 row_mask:0xf bank_mask:0x2\n\t" \
  "v_mov_b32_dpp v254, v242 row_newbcast:15 row_mask:0xf bank_mask:0x4\n\t" \
  "v_mov_b32_dpp v254, v243 row_newbcast:15 row_mask:0xf bank_mask:0x8\n\t" \
  "s_mov_b32 exec_lo, 0x11111111\n\t" \
  "s_mov_b32 exec_hi, 0x11111111\n\t" \
  "buffer_store_dwordx4 v[252:255], %[pubo], %[srd], %[sag] offen sc1\n\t" \
  "s_mov_b64 exec, 1\n\t" \
  "buffer_atomic_or v226, off, %[srd], %[sht] sc0 sc1\n\t" \
  "s_mov_b64 exec, -1\n\t" \
  "v_mov_b32 v160, 0\n\t" \
  "v_mov_b32 v161, %[tgA]\n\t" \
  "v_mov_b32 v162, 0\n\t" \
  "v_mov_b32 v163, %[tgB]\n\t" \
  "v_mov_b32 v164, 0\n\t" \
  "v_mov_b32 v165, %[tgA]\n\t" \
  "v_mov_b32 v166, 0\n\t" \
  "v_mov_b32 v167, %[tgB]\n\t" \
  "v_mov_b32 v168, 0\n\t" \
  "v_mov_b32 v169, %[tgA]\n\t" \
  "v_mov_b32 v170, 0\n\t" \
  "v_mov_b32 v171, %[tgB]\n\t" \
  "v_mov_b32 v172, 0\n\t" \
  "v_mov_b32 v173, %[tgA]\n\t" \
  "v_mov_b32 v174, 0\n\t" \
  "v_mov_b32 v175, %[tgB]\n\t" \
  "s_waitcnt lgkmcnt(0)\n\t" \
  "v_mov_b32_dpp v244, v236 row_shr:1 row_mask:0xf bank_mask:0xf bound_ctrl:0\n\t" \
  "v_mov_b32_dpp v245, v237 row_shr:1 row_mask:0xf bank_mask:0xf bound_ctrl:0\n\t" \
  "v_mov_b32_dpp v246, v238 row_shr:1 row_mask:0xf bank_mask:0xf bound_ctrl:0\n\t" \
  "v_mov_b32_dpp v247, v239 row_shr:1 row_mask:0xf bank_mask:0xf bound_ctrl:0\n\t" \
  "v_mov_b32_dpp v248, v240 row_shr:1 row_mask:0xf bank_mask:0xf bound_ctrl:0\n\t" \
  "v_mov_b32_dpp v249, v241 row_shr:1 row_mask:0xf bank_mask:0xf bound_ctrl:0\n\t" \
  "v_mov_b32_dpp v250, v242 row_shr:1 row_mask:0xf bank_mask:0xf bound_ctrl:0\n\t" \
  "v_mov_b32_dpp v251, v243 row_shr:1 row_mask:0xf bank_mask:0xf bound_ctrl:0\n\t" \
  "v_pk_mul_f32 v[236:237], v[184:185], v[244:245]\n\t" \
  "v_pk_mul_f32 v[238:239], v[186:187], v[246:247]\n\t" \
  "v_pk_mul_f32 v[240:241], v[184:185], v[248:249]\n\t" \
  "v_pk_mul_f32 v[242:243], v[186:187], v[250:251]\n\t" \
  "v_pk_fma_f32 v[236:237], v[188:189], v[248:249], v[236:237] neg_lo:[1,0,0] neg_hi:[1,0,0]\n\t" \
  "v_pk_fma_f32 v[238:239], v[190:191], v[250:251], v[238:239] neg_lo:[1,0,0] neg_hi:[1,0,0]\n\t" \
  "v_pk_fma_f32 v[240:241], v[188:189], v[244:245], v[240:241]\n\t" \
  "v_pk_fma_f32 v[242:243], v[190:191], v[246:247], v[242:243]\n\t" \
  "v_pk_mul_f32 v[128:129], v[184:185], v[64:65]\n\t" \
  "v_pk_mul_f32 v[130:131], v[186:187], v[66:67]\n\t" \
  "v_pk_fma_f32 v[128:129], v[188:189], v[96:97], v[128:129] neg_lo:[1,0,0] neg_hi:[1,0,0]\n\t" \
  "v_pk_fma_f32 v[130:131], v[190:191], v[98:99], v[130:131] neg_lo:[1,0,0] neg_hi:[1,0,0]\n\t" \
  "v_pk_mul_f32 v[96:97], v[184:185], v[96:97]\n\t" \
  "v_pk_mul_f32 v[98:99], v[186:187], v[98:99]\n\t" \
  "v_pk_fma_f32 v[96:97], v[188:189], v[64:65], v[96:97] neg_lo:[1,0,1] neg_hi:[1,0,1]\n\t" \
  "v_pk_fma_f32 v[98:99], v[190:191], v[66:67], v[98:99] neg_lo:[1,0,1] neg_hi:[1,0,1]\n\t" \
  "v_pk_mul_f32 v[132:133], v[184:185], v[68:69]\n\t" \
  "v_pk_mul_f32 v[134:135], v[186:187], v[70:71]\n\t" \
  "v_pk_fma_f32 v[132:133], v[188:189], v[100:101], v[132:133] neg_lo:[1,0,0] neg_hi:[1,0,0]\n\t" \
  "v_pk_fma_f32 v[134:135], v[190:191], v[102:103], v[134:135] neg_lo:[1,0,0] neg_hi:[1,0,0]\n\t" \
  "v_pk_mul_f32 v[100:101], v[184:185], v[100:101]\n\t" \
  "v_pk_mul_f32 v[102:103], v[186:187], v[102:103]\n\t" \
  "v_pk_fma_f32 v[100:101], v[188:189], v[68:69], v[100:101] neg_lo:[1,0,1] neg_hi:[1,0,1]\n\t" \
  "v_pk_fma_f32 v[102:103], v[190:191], v[70:71], v[102:103] neg_lo:[1,0,1] neg_hi:[1,0,1]\n\t" \
  "v_pk_mul_f32 v[136:137], v[184:185], v[72:73]\n\t" \
  "v_pk_mul_f32 v[138:139], v[186:187], v[74:75]\n\t" \
  "v_pk_fma_f32 v[136:137], v[188:189], v[104:105], v[136:137] neg_lo:[1,0,0] neg_hi:[1,0,0]\n\t" \
  "v_pk_fma_f32 v[138:139], v[190:191], v[106:107], v[138:139] neg_lo:[1,0,0] neg_hi:[1,0,0]\n\t" \
  "v_pk_mul_f32 v[104:105], v[184:185], v[104:105]\n\t" \
  "v_pk_mul_f32 v[106:107], v[186:187], v[106:107]\n\t" \
  "v_pk_fma_f32 v[104:105], v[188:189], v[72:73], v[104:105] neg_lo:[1,0,1] neg_hi:[1,0,1]\n\t" \
  "v_pk_fma_f32 v[106:107], v[190:191], v[74:75], v[106:107] neg_lo:[1,0,1] neg_hi:[1,0,1]\n\t" \
  "v_pk_mul_f32 v[140:141], v[184:185], v[76:77]\n\t" \
  "v_pk_mul_f32 v[142:143], v[186:187], v[78:79]\n\t" \
  "v_pk_fma_f32 v[140:141], v[188:189], v[108:109], v[140:141] neg_lo:[1,0,0] neg_hi:[1,0,0]\n\t" \
  "v_pk_fma_f32 v[142:143], v[190:191], v[110:111], v[142:143] neg_lo:[1,0,0] neg_hi:[1,0,0]\n\t" \
  "v_pk_mul_f32 v[108:109], v[184:185], v[108:109]\n\t" \
  "v_pk_mul_f32 v[110:111], v[186:187], v[110:111]\n\t" \
  "v_pk_fma_f32 v[108:109], v[188:189], v[76:77], v[108:109] neg_lo:[1,0,1] neg_hi:[1,0,1]\n\t" \
  "v_pk_fma_f32 v[110:111], v[190:191], v[78:79], v[110:111] neg_lo:[1,0,1] neg_hi:[1,0,1]\n\t" \
  "v_pk_mul_f32 v[144:145], v[184:185], v[80:81]\n\t" \
  "v_pk_mul_f32 v[146:147], v[186:187], v[82:83]\n\t" \
  "v_pk_fma_f32 v[144:145], v[188:189], v[112:113], v[144:145] neg_lo:[1,0,0] neg_hi:[1,0,0]\n\t" \
  "v_pk_fma_f32 v[146:147], v[190:191], v[114:115], v[146:147] neg_lo:[1,0,0] neg_hi:[1,0,0]\n\t" \
  "v_pk_mul_f32 v[112:113], v[184:185], v[112:113]\n\t" \
  "v_pk_mul_f32 v[114:115], v[186:187], v[114:115]\n\t" \
  "v_pk_fma_f32 v[112:113], v[188:189], v[80:81], v[112:113] neg_lo:[1,0,1] neg_hi:[1,0,1]\n\t" \
  "v_pk_fma_f32 v[114:115], v[190:191], v[82:83], v[114:115] neg_lo:[1,0,1] neg_hi:[1,0,1]\n\t" \
  "v_pk_mul_f32 v[148:149], v[184:185], v[84:85]\n\t" \
  "v_pk_mul_f32 v[150:151], v[186:187], v[86:87]\n\t" \
  "v_pk_fma_f32 v[148:149], v[188:189], v[116:117], v[148:149] neg_lo:[1,0,0] neg_hi:[1,0,0]\n\t" \
  "v_pk_fma_f32 v[150:151], v[190:191], v[118:119], v[150:151] neg_lo:[1,0,0] neg_hi:[1,0,0]\n\t" \
  "v_pk_mul_f32 v[116:117], v[184:185], v[116:117]\n\t" \
  "v_pk_mul_f32 v[118:119], v[186:187], v[118:119]\n\t" \
  "v_pk_fma_f32 v[116:117], v[188:189], v[84:85], v[116:117] neg_lo:[1,0,1] neg_hi:[1,0,1]\n\t" \
  "v_pk_fma_f32 v[118:119], v[190:191], v[86:87], v[118:119] neg_lo:[1,0,1] neg_hi:[1,0,1]\n\t" \
  "v_pk_mul_f32 v[152:153], v[184:185], v[88:89]\n\t" \
  "v_pk_mul_f32 v[154:155], v[186:187], v[90:91]\n\t" \
  "v_pk_fma_f32 v[152:153], v[188:189], v[120:121], v[152:153] neg_lo:[1,0,0] neg_hi:[1,0,0]\n\t" \
  "v_pk_fma_f32 v[154:155], v[190:191], v[122:123], v[154:155] neg_lo:[1,0,0] neg_hi:[1,0,0]\n\t" \
  "v_pk_mul_f32 v[120:121], v[184:185], v[120:121]\n\t" \
  "v_pk_mul_f32 v[122:123], v[186:187], v[122:123]\n\t" \
  "v_pk_fma_f32 v[120:121], v[188:189], v[88:89], v[120:121] neg_lo:[1,0,1] neg_hi:[1,0,1]\n\t" \
  "v_pk_fma_f32 v[122:123], v[190:191], v[90:91], v[122:123] neg_lo:[1,0,1] neg_hi:[1,0,1]\n\t" \
  "v_pk_mul_f32 v[156:157], v[184:185], v[92:93]\n\t" \
  "v_pk_mul_f32 v[158:159], v[186:187], v[94:95]\n\t" \
  "v_pk_fma_f32 v[156:157], v[188:189], v[124:125], v[156:157] neg_lo:[1,0,0] neg_hi:[1,0,0]\n\t" \
  "v_pk_fma_f32 v[158:159], v[190:191], v[126:127], v[158:159] neg_lo:[1,0,0] neg_hi:[1,0,0]\n\t" \
  "v_pk_mul_f32 v[124:125], v[184:185], v[124:125]\n\t" \
  "v_pk_mul_f32 v[126:127], v[186:187], v[126:127]\n\t" \
  "v_pk_fma_f32 v[124:125], v[188:189], v[92:93], v[124:125] neg_lo:[1,0,1] neg_hi:[1,0,1]\n\t" \
  "v_pk_fma_f32 v[126:127], v[190:191], v[94:95], v[126:127] neg_lo:[1,0,1] neg_hi:[1,0,1]\n\t" \
  "s_waitcnt vmcnt(0)\n\t" \
  "v_readfirstlane_b32 %[s0], v226\n\t" \
  "s_andn2_b32 %[s0], %[eh], %[s0]\n\t" \
  "s_cselect_b32 %[s1], 32, 0\n\t" \
  "s_cbranch_scc0 Lh1_%=\n\t" \
  "v_mov_b32 v226, 0\n\t" \
  "s_mov_b64 exec, 1\n\t" \
  "buffer_atomic_or v226, off, %[srd], %[sht] sc0 sc1\n\t" \
  "s_mov_b64 exec, -1\n\t" \
  "s_branch Lpa_%=\n\t" \
  "Lh1_%=:\n\t" \
  "s_mov_b64 exec, %[em0]\n\t" \
  "s_cbranch_execz Lpa_%=\n\t" \
  "buffer_load_dwordx4 v[160:163], %[plo], %[srd], %[sl0] offen sc1\n\t" \
  "s_mov_b64 exec, %[em1]\n\t" \
  "s_cbranch_execz Lpa_%=\n\t" \
  "buffer_load_dwordx4 v[164:167], %[plo], %[srd], %[sl1] offen sc1\n\t" \
  "s_mov_b64 exec, %[em2]\n\t" \
  "s_cbranch_execz Lpa_%=\n\t" \
  "buffer_load_dwordx4 v[168:171], %[plo], %[srd], %[sl2] offen sc1\n\t" \
  "s_mov_b64 exec, %[em3]\n\t" \
  "s_cbranch_execz Lpa_%=\n\t" \
  "buffer_load_dwordx4 v[172:175], %[plo], %[srd], %[sl3] offen sc1\n\t" \
  "Lpa_%=:\n\t" \
  "s_mov_b64 exec, -1\n\t" \
  "s_mov_b32 exec_lo, 0xaaaaaaaa\n\t" \
  "s_mov_b32 exec_hi, 0xaaaaaaaa\n\t" \
  "v_pk_mul_f32 v[244:245], v[196:197], v[188:189]\n\t" \
  "v_pk_mul_f32 v[246:247], v[198:199], v[190:191]\n\t" \
  "v_pk_mul_f32 v[248:249], v[196:197], v[184:185]\n\t" \
  "v_pk_mul_f32 v[250:251], v[198:199], v[186:187]\n\t" \
  "v_pk_fma_f32 v[184:185], v[192:193], v[184:185], v[244:245] neg_lo:[0,0,1] neg_hi:[0,0,1]\n\t" \
  "v_pk_fma_f32 v[186:187], v[194:195], v[186:187], v[246:247] neg_lo:[0,0,1] neg_hi:[0,0,1]\n\t" \
  "v_pk_fma_f32 v[188:189], v[192:193], v[188:189], v[248:249]\n\t" \
  "v_pk_fma_f32 v[190:191], v[194:195], v[190:191], v[250:251]\n\t" \
  "s_mov_b32 exec_lo, 0xcccccccc\n\t" \
  "s_mov_b32 exec_hi, 0xcccccccc\n\t" \
  "v_pk_mul_f32 v[244:245], v[204:205], v[188:189]\n\t" \
  "v_pk_mul_f32 v[246:247], v[206:207], v[190:191]\n\t" \
  "v_pk_mul_f32 v[248:249], v[204:205], v[184:185]\n\t" \
  "v_pk_mul_f32 v[250:251], v[206:207], v[186:187]\n\t" \
  "v_pk_fma_f32 v[184:185], v[200:201], v[184:185], v[244:245] neg_lo:[0,0,1] neg_hi:[0,0,1]\n\t" \
  "v_pk_fma_f32 v[186:187], v[202:203], v[186:187], v[246:247] neg_lo:[0,0,1] neg_hi:[0,0,1]\n\t" \
  "v_pk_fma_f32 v[188:189], v[200:201], v[188:189], v[248:249]\n\t" \
  "v_pk_fma_f32 v[190:191], v[202:203], v[190:191], v[250:251]\n\t" \
  "s_mov_b32 exec_lo, 0xf0f0f0f0\n\t" \
  "s_mov_b32 exec_hi, 0xf0f0f0f0\n\t" \
  "v_pk_mul_f32 v[244:245], v[212:213], v[188:189]\n\t" \
  "v_pk_mul_f32 v[246:247], v[214:215], v[190:191]\n\t" \
  "v_pk_mul_f32 v[248:249], v[212:213], v[184:185]\n\t" \
  "v_pk_mul_f32 v[250:251], v[214:215], v[186:187]\n\t" \
  "v_pk_fma_f32 v[184:185], v[208:209], v[184:185], v[244:245] neg_lo:[0,0,1] neg_hi:[0,0,1]\n\t" \
  "v_pk_fma_f32 v[186:187], v[210:211], v[186:187], v[246:247] neg_lo:[0,0,1] neg_hi:[0,0,1]\n\t" \
  "v_pk_fma_f32 v[188:189], v[208:209], v[188:189], v[248:249]\n\t" \
  "v_pk_fma_f32 v[190:191], v[210:211], v[190:191], v[250:251]\n\t" \
  "s_mov_b32 exec_lo, 0xff00ff00\n\t" \
  "s_mov_b32 exec_hi, 0xff00ff00\n\t" \
  "v_pk_mul_f32 v[244:245], v[220:221], v[188:189]\n\t" \
  "v_pk_mul_f32 v[246:247], v[222:223], v[190:191]\n\t" \
  "v_pk_mul_f32 v[248:249], v[220:221], v[184:185]\n\t" \
  "v_pk_mul_f32 v[250:251], v[222:223], v[186:187]\n\t" \
  "v_pk_fma_f32 v[184:185], v[216:217], v[184:185], v[244:245] neg_lo:[0,0,1] neg_hi:[0,0,1]\n\t" \
  "v_pk_fma_f32 v[186:187], v[218:219], v[186:187], v[246:247] neg_lo:[0,0,1] neg_hi:[0,0,1]\n\t" \
  "v_pk_fma_f32 v[188:189], v[216:217], v[188:189], v[248:249]\n\t" \
  "v_pk_fma_f32 v[190:191], v[218:219], v[190:191], v[250:251]\n\t" \
  "s_mov_b64 exec, -1\n\t" \
  "s_cmp_eq_u32 %[s1], 0\n\t" \
  "s_cbranch_scc1 Lq_%=\n\t" \
  "Lh2_%=:\n\t" \
  "s_waitcnt vmcnt(0)\n\t" \
  "v_readfirstlane_b32 %[s0], v226\n\t" \
  "s_andn2_b32 %[s0], %[eh], %[s0]\n\t" \
  "s_cbranch_scc0 Lh3_%=\n\t" \
  "s_sub_u32 %[s1], %[s1], 1\n\t" \
  "s_cbranch_scc1 Lh3_%=\n\t" \
  "s_sleep 1\n\t" \
  "v_mov_b32 v226, 0\n\t" \
  "s_mov_b64 exec, 1\n\t" \
  "buffer_atomic_or v226, off, %[srd], %[sht] sc0 sc1\n\t" \
  "s_mov_b64 exec, -1\n\t" \
  "s_branch Lh2_%=\n\t" \
  "Lh3_%=:\n\t" \
  "s_mov_b64 exec, %[em0]\n\t" \
  "s_cbranch_execz Lpc_%=\n\t" \
  "buffer_load_dwordx4 v[160:163], %[plo], %[srd], %[sl0] offen sc1\n\t" \
  "s_mov_b64 exec, %[em1]\n\t" \
  "s_cbranch_execz Lpc_%=\n\t" \
  "buffer_load_dwordx4 v[164:167], %[plo], %[srd], %[sl1] offen sc1\n\t" \
  "s_mov_b64 exec, %[em2]\n\t" \
  "s_cbranch_execz Lpc_%=\n\t" \
  "buffer_load_dwordx4 v[168:171], %[plo], %[srd], %[sl2] offen sc1\n\t" \
  "s_mov_b64 exec, %[em3]\n\t" \
  "s_cbranch_execz Lpc_%=\n\t" \
  "buffer_load_dwordx4 v[172:175], %[plo], %[srd], %[sl3] offen sc1\n\t" \
  "Lpc_%=:\n\t" \
  "s_mov_b64 exec, -1\n\t" \
  "Lq_%=:\n\t" \
  "s_waitcnt vmcnt(0)\n\t" \
  "v_cmpx_eq_u32 vcc, %[tgA], v161\n\t" \
  "v_cmpx_eq_u32 vcc, %[tgB], v163\n\t" \
  "v_cmpx_eq_u32 vcc, %[tgA], v165\n\t" \
  "v_cmpx_eq_u32 vcc, %[tgB], v167\n\t" \
  "v_cmpx_eq_u32 vcc, %[tgA], v169\n\t" \
  "v_cmpx_eq_u32 vcc, %[tgB], v171\n\t" \
  "v_cmpx_eq_u32 vcc, %[tgA], v173\n\t" \
  "v_cmpx_eq_u32 vcc, %[tgB], v175\n\t" \
  "s_cmp_eq_u64 exec, -1\n\t" \
  "s_mov_b64 exec, -1\n\t" \
  "s_cbranch_scc1 Lq2_%=\n\t" \
  "s_sub_i32 %[bud], %[bud], 16\n\t" \
  "s_cmp_lt_i32 %[bud], 0\n\t" \
  "s_cbranch_scc1 Lq2_%=\n\t" \
  "s_sleep 4\n\t" \
  "s_mov_b64 exec, %[em0]\n\t" \
  "s_cbranch_execz Lpb_%=\n\t" \
  "buffer_load_dwordx4 v[160:163], %[plo], %[srd], %[sl0] offen sc1\n\t" \
  "s_mov_b64 exec, %[em1]\n\t" \
  "s_cbranch_execz Lpb_%=\n\t" \
  "buffer_load_dwordx4 v[164:167], %[plo], %[srd], %[sl1] offen sc1\n\t" \
  "s_mov_b64 exec, %[em2]\n\t" \
  "s_cbranch_execz Lpb_%=\n\t" \
  "buffer_load_dwordx4 v[168:171], %[plo], %[srd], %[sl2] offen sc1\n\t" \
  "s_mov_b64 exec, %[em3]\n\t" \
  "s_cbranch_execz Lpb_%=\n\t" \
  "buffer_load_dwordx4 v[172:175], %[plo], %[srd], %[sl3] offen sc1\n\t" \
  "Lpb_%=:\n\t" \
  "s_mov_b64 exec, -1\n\t" \
  "s_branch Lq_%=\n\t" \
  "Lq2_%=:\n\t" \
  "s_mov_b32 %[scn], %[mcar]\n\t" \
  "s_mov_b32 m0, %[scn]\n\t" \
  "s_mov_b64 exec, 1\n\t" \
  "buffer_load_dword %[l16], %[srz], 0 offen lds\n\t" \
  "s_mov_b64 exec, -1\n\t" \
  "v_fma_f32 v244, -v231, v174, v168\n\t" \
  "v_fma_f32 v245, v231, v172, v170\n\t" \
  "v_fma_f32 v252, v230, v172, v244\n\t" \
  "v_fma_f32 v253, v230, v174, v245\n\t" \
  "v_fma_f32 v244, -v231, v253, v164\n\t" \
  "v_fma_f32 v245, v231, v252, v166\n\t" \
  "v_fma_f32 v252, v230, v252, v244\n\t" \
  "v_fma_f32 v253, v230, v253, v245\n\t" \
  "v_fma_f32 v244, -v231, v253, v160\n\t" \
  "v_fma_f32 v245, v231, v252, v162\n\t" \
  "v_fma_f32 v252, v230, v252, v244\n\t" \
  "v_fma_f32 v253, v230, v253, v245\n\t" \
  "v_mul_f32 v244, v233, v253\n\t" \
  "v_mul_f32 v245, v233, v252\n\t" \
  "v_fma_f32 v254, v232, v252, -v244\n\t" \
  "v_fma_f32 v255, v232, v253, v245\n\t" \
  "v_fma_f32 v252, v232, v252, -v244\n\t" \
  "v_fma_f32 v253, v232, v253, v245\n\t" \
  "s_nop 1\n\t" \
  "v_permlane16_swap_b32 v252, v254\n\t" \
  "v_permlane16_swap_b32 v253, v255\n\t" \
  "s_nop 0\n\t" \
  "v_add_f32 v244, v252, v254\n\t" \
  "v_add_f32 v245, v253, v255\n\t" \
  "v_add_f32 v252, v252, v254\n\t" \
  "v_add_f32 v253, v253, v255\n\t" \
  "v_mov_b32 v254, v244\n\t" \
  "v_mov_b32 v255, v245\n\t" \
  "s_nop 1\n\t" \
  "v_permlane32_swap_b32 v252, v254\n\t" \
  "v_permlane32_swap_b32 v253, v255\n\t" \
  "s_nop 0\n\t" \
  "v_add_f32 v244, v252, v254\n\t" \
  "v_add_f32 v245, v253, v255\n\t" \
  "v_add_f32 v252, v252, v254\n\t" \
  "v_add_f32 v253, v253, v255\n\t" \
  "v_mov_b32 v254, v244\n\t" \
  "v_mov_b32 v255, v245\n\t" \
  "v_mov_b32_dpp v254, v252 row_ror:12 row_mask:0x2 bank_mask:0xf\n\t" \
  "v_mov_b32_dpp v255, v253 row_ror:12 row_mask:0x2 bank_mask:0xf\n\t" \
  "v_mov_b32_dpp v254, v252 row_ror:8 row_mask:0x4 bank_mask:0xf\n\t" \
  "v_mov_b32_dpp v255, v253 row_ror:8 row_mask:0x4 bank_mask:0xf\n\t" \
  "v_mov_b32_dpp v254, v252 row_ror:4 row_mask:0x8 bank_mask:0xf\n\t" \
  "v_mov_b32_dpp v255, v253 row_ror:4 row_mask:0x8 bank_mask:0xf\n\t" \
  "s_nop 1\n\t" \
  "v_mov_b32_dpp v244, v254 row_newbcast:0 row_mask:0xf bank_mask:0xf\n\t" \
  "v_mov_b32_dpp v245, v254 row_newbcast:1 row_mask:0xf bank_mask:0xf\n\t" \
  "v_mov_b32_dpp v246, v254 row_newbcast:2 row_mask:0xf bank_mask:0xf\n\t" \
  "v_mov_b32_dpp v247, v254 row_newbcast:3 row_mask:0xf bank_mask:0xf\n\t" \
  "v_mov_b32_dpp v248, v255 row_newbcast:0 row_mask:0xf bank_mask:0xf\n\t" \
  "v_mov_b32_dpp v249, v255 row_newbcast:1 row_mask:0xf bank_mask:0xf\n\t" \
  "v_mov_b32_dpp v250, v255 row_newbcast:2 row_mask:0xf bank_mask:0xf\n\t" \
  "v_mov_b32_dpp v251, v255 row_newbcast:3 row_mask:0xf bank_mask:0xf\n\t" \
  "v_pk_fma_f32 v[236:237], v[184:185], v[244:245], v[236:237]\n\t" \
  "v_pk_fma_f32 v[238:239], v[186:187], v[246:247], v[238:239]\n\t" \
  "v_pk_fma_f32 v[240:241], v[184:185], v[248:249], v[240:241]\n\t" \
  "v_pk_fma_f32 v[242:243], v[186:187], v[250:251], v[242:243]\n\t" \
  "v_pk_fma_f32 v[236:237], v[188:189], v[248:249], v[236:237] neg_lo:[1,0,0] neg_hi:[1,0,0]\n\t" \
  "v_pk_fma_f32 v[238:239], v[190:191], v[250:251], v[238:239] neg_lo:[1,0,0] neg_hi:[1,0,0]\n\t" \
  "v_pk_fma_f32 v[240:241], v[188:189], v[244:245], v[240:241]\n\t" \
  "v_pk_fma_f32 v[242:243], v[190:191], v[246:247], v[242:243]\n\t" \
  "v_pk_fma_f32 v[128:129], v[180:181], v[240:241], v[128:129] neg_lo:[1,0,0] neg_hi:[1,0,0]\n\t" \
  "v_pk_fma_f32 v[130:131], v[182:183], v[242:243], v[130:131] neg_lo:[1,0,0] neg_hi:[1,0,0]\n\t" \
  "v_pk_fma_f32 v[96:97], v[180:181], v[236:237], v[96:97] neg_lo:[1,0,0] neg_hi:[1,0,0]\n\t" \
  "v_pk_fma_f32 v[98:99], v[182:183], v[238:239], v[98:99] neg_lo:[1,0,0] neg_hi:[1,0,0]\n\t" \
  "v_pk_fma_f32 v[128:129], v[176:177], v[236:237], v[128:129]\n\t" \
  "v_pk_fma_f32 v[130:131], v[178:179], v[238:239], v[130:131]\n\t" \
  "v_pk_fma_f32 v[96:97], v[176:177], v[240:241], v[96:97] neg_lo:[1,0,0] neg_hi:[1,0,0]\n\t" \
  "v_pk_fma_f32 v[98:99], v[178:179], v[242:243], v[98:99] neg_lo:[1,0,0] neg_hi:[1,0,0]\n\t" \
  "v_cvt_pk_bf16_f32 v160, v128, v129\n\t" \
  "v_cvt_pk_bf16_f32 v161, v130, v131\n\t" \
  "v_cvt_pk_bf16_f32 v162, v96, v97\n\t" \
  "v_cvt_pk_bf16_f32 v163, v98, v99\n\t" \
  "v_pk_fma_f32 v[132:133], v[180:181], v[96:97], v[132:133]\n\t" \
  "v_pk_fma_f32 v[134:135], v[182:183], v[98:99], v[134:135]\n\t" \
  "v_permlane16_swap_b32 v160, v162\n\t" \
  "v_permlane16_swap_b32 v161, v163\n\t" \
  "buffer_store_dwordx4 v[160:163], %[l16], %[srd], %[sh] offen offset:0 sc1\n\t" \
  "v_pk_fma_f32 v[100:101], v[180:181], v[128:129], v[100:101] neg_lo:[1,0,0] neg_hi:[1,0,0]\n\t" \
  "v_pk_fma_f32 v[102:103], v[182:183], v[130:131], v[102:103] neg_lo:[1,0,0] neg_hi:[1,0,0]\n\t" \
  "v_pk_fma_f32 v[132:133], v[176:177], v[128:129], v[132:133]\n\t" \
  "v_pk_fma_f32 v[134:135], v[178:179], v[130:131], v[134:135]\n\t" \
  "v_pk_fma_f32 v[100:101], v[176:177], v[96:97], v[100:101]\n\t" \
  "v_pk_fma_f32 v[102:103], v[178:179], v[98:99], v[102:103]\n\t" \
  "v_cvt_pk_bf16_f32 v164, v132, v133\n\t" \
  "v_cvt_pk_bf16_f32 v165, v134, v135\n\t" \
  "v_cvt_pk_bf16_f32 v166, v100, v101\n\t" \
  "v_cvt_pk_bf16_f32 v167, v102, v103\n\t" \
  "v_pk_fma_f32 v[136:137], v[180:181], v[100:101], v[136:137]\n\t" \
  "v_pk_fma_f32 v[138:139], v[182:183], v[102:103], v[138:139]\n\t" \
  "v_permlane16_swap_b32 v164, v166\n\t" \
  "v_permlane16_swap_b32 v165, v167\n\t" \
  "buffer_store_dwordx4 v[164:167], %[l16], %[srd], %[sh] offen offset:2048 sc1\n\t" \
  "s_add_u32 %[sh], %[sh], 0x1000\n\t" \
  "v_pk_fma_f32 v[104:105], v[180:181], v[132:133], v[104:105] neg_lo:[1,0,0] neg_hi:[1,0,0]\n\t" \
  "v_pk_fma_f32 v[106:107], v[182:183], v[134:135], v[106:107] neg_lo:[1,0,0] neg_hi:[1,0,0]\n\t" \
  "v_pk_fma_f32 v[136:137], v[176:177], v[132:133], v[136:137]\n\t" \
  "v_pk_fma_f32 v[138:139], v[178:179], v[134:135], v[138:139]\n\t" \
  "v_pk_fma_f32 v[104:105], v[176:177], v[100:101], v[104:105]\n\t" \
  "v_pk_fma_f32 v[106:107], v[178:179], v[102:103], v[106:107]\n\t" \
  "v_cvt_pk_bf16_f32 v168, v136, v137\n\t" \
  "v_cvt_pk_bf16_f32 v169, v138, v139\n\t" \
  "v_cvt_pk_bf16_f32 v170, v104, v105\n\t" \
  "v_cvt_pk_bf16_f32 v171, v106, v107\n\t" \
  "v_pk_fma_f32 v[140:141], v[180:181], v[104:105], v[140:141]\n\t" \
  "v_pk_fma_f32 v[142:143], v[182:183], v[106:107], v[142:143]\n\t" \
  "v_permlane16_swap_b32 v168, v170\n\t" \
  "v_permlane16_swap_b32 v169, v171\n\t" \
  "buffer_store_dwordx4 v[168:171], %[l16], %[srd], %[sh] offen offset:0 sc1\n\t" \
  "v_pk_fma_f32 v[108:109], v[180:181], v[136:137], v[108:109] neg_lo:[1,0,0] neg_hi:[1,0,0]\n\t" \
  "v_pk_fma_f32 v[110:111], v[182:183], v[138:139], v[110:111] neg_lo:[1,0,0] neg_hi:[1,0,0]\n\t" \
  "v_pk_fma_f32 v[140:141], v[176:177], v[136:137], v[140:141]\n\t" \
  "v_pk_fma_f32 v[142:143], v[178:179], v[138:139], v[142:143]\n\t" \
  "v_pk_fma_f32 v[108:109], v[176:177], v[104:105], v[108:109]\n\t" \
  "v_pk_fma_f32 v[110:111], v[178:179], v[106:107], v[110:111]\n\t" \
  "v_cvt_pk_bf16_f32 v172, v140, v141\n\t" \
  "v_cvt_pk_bf16_f32 v173, v142, v143\n\t" \
  "v_cvt_pk_bf16_f32 v174, v108, v109\n\t" \
  "v_cvt_pk_bf16_f32 v175, v110, v111\n\t" \
  "v_pk_fma_f32 v[144:145], v[180:181], v[108:109], v[144:145]\n\t" \
  "v_pk_fma_f32 v[146:147], v[182:183], v[110:111], v[146:147]\n\t" \
  "v_permlane16_swap_b32 v172, v174\n\t" \
  "v_permlane16_swap_b32 v173, v175\n\t" \
  "buffer_store_dwordx4 v[172:175], %[l16], %[srd], %[sh] offen offset:2048 sc1\n\t" \
  "s_add_u32 %[sh], %[sh], 0x1000\n\t" \
  "v_pk_fma_f32 v[112:113], v[180:181], v[140:141], v[112:113] neg_lo:[1,0,0] neg_hi:[1,0,0]\n\t" \
  "v_pk_fma_f32 v[114:115], v[182:183], v[142:143], v[114:115] neg_lo:[1,0,0] neg_hi:[1,0,0]\n\t" \
  "v_pk_fma_f32 v[144:145], v[176:177], v[140:141], v[144:145]\n\t" \
  "v_pk_fma_f32 v[146:147], v[178:179], v[142:143], v[146:147]\n\t" \
  "v_pk_fma_f32 v[112:113], v[176:177], v[108:109], v[112:113]\n\t" \
  "v_pk_fma_f32 v[114:115], v[178:179], v[110:111], v[114:115]\n\t" \
  "v_cvt_pk_bf16_f32 v192, v144, v145\n\t" \
  "v_cvt_pk_bf16_f32 v193, v146, v147\n\t" \
  "v_cvt_pk_bf16_f32 v194, v112, v113\n\t" \
  "v_cvt_pk_bf16_f32 v195, v114, v115\n\t" \
  "v_pk_fma_f32 v[148:149], v[180:181], v[112:113], v[148:149]\n\t" \
  "v_pk_fma_f32 v[150:151], v[182:183], v[114:115], v[150:151]\n\t" \
  "v_permlane16_swap_b32 v192, v194\n\t" \
  "v_permlane16_swap_b32 v193, v195\n\t" \
  "buffer_store_dwordx4 v[192:195], %[l16], %[srd], %[sh] offen offset:0 sc1\n\t" \
  "v_pk_fma_f32 v[116:117], v[180:181], v[144:145], v[116:117] neg_lo:[1,0,0] neg_hi:[1,0,0]\n\t" \
  "v_pk_fma_f32 v[118:119], v[182:183], v[146:147], v[118:119] neg_lo:[1,0,0] neg_hi:[1,0,0]\n\t" \
  "v_pk_fma_f32 v[148:149], v[176:177], v[144:145], v[148:149]\n\t" \
  "v_pk_fma_f32 v[150:151], v[178:179], v[146:147], v[150:151]\n\t" \
  "v_pk_fma_f32 v[116:117], v[176:177], v[112:113], v[116:117]\n\t" \
  "v_pk_fma_f32 v[118:119], v[178:179], v[114:115], v[118:119]\n\t" \
  "v_cvt_pk_bf16_f32 v196, v148, v149\n\t" \
  "v_cvt_pk_bf16_f32 v197, v150, v151\n\t" \
  "v_cvt_pk_bf16_f32 v198, v116, v117\n\t" \
  "v_cvt_pk_bf16_f32 v199, v118, v119\n\t" \
  "v_pk_fma_f32 v[152:153], v[180:181], v[116:117], v[152:153]\n\t" \
  "v_pk_fma_f32 v[154:155], v[182:183], v[118:119], v[154:155]\n\t" \
  "v_permlane16_swap_b32 v196, v198\n\t" \
  "v_permlane16_swap_b32 v197, v199\n\t" \
  "buffer_store_dwordx4 v[196:199], %[l16], %[srd], %[sh] offen offset:2048 sc1\n\t" \
  "s_add_u32 %[sh], %[sh], 0x1000\n\t" \
  "v_pk_fma_f32 v[120:121], v[180:181], v[148:149], v[120:121] neg_lo:[1,0,0] neg_hi:[1,0,0]\n\t" \
  "v_pk_fma_f32 v[122:123], v[182:183], v[150:151], v[122:123] neg_lo:[1,0,0] neg_hi:[1,0,0]\n\t" \
  "v_pk_fma_f32 v[152:153], v[176:177], v[148:149], v[152:153]\n\t" \
  "v_pk_fma_f32 v[154:155], v[178:179], v[150:151], v[154:155]\n\t" \
  "v_pk_fma_f32 v[120:121], v[176:177], v[116:117], v[120:121]\n\t" \
  "v_pk_fma_f32 v[122:123], v[178:179], v[118:119], v[122:123]\n\t" \
  "v_cvt_pk_bf16_f32 v200, v152, v153\n\t" \
  "v_cvt_pk_bf16_f32 v201, v154, v155\n\t" \
  "v_cvt_pk_bf16_f32 v202, v120, v121\n\t" \
  "v_cvt_pk_bf16_f32 v203, v122, v123\n\t" \
  "v_pk_fma_f32 v[156:157], v[180:181], v[120:121], v[156:157]\n\t" \
  "v_pk_fma_f32 v[158:159], v[182:183], v[122:123], v[158:159]\n\t" \
  "v_permlane16_swap_b32 v200, v202\n\t" \
  "v_permlane16_swap_b32 v201, v203\n\t" \
  "buffer_store_dwordx4 v[200:203], %[l16], %[srd], %[sh] offen offset:0 sc1\n\t" \
  "v_pk_fma_f32 v[124:125], v[180:181], v[152:153], v[124:125] neg_lo:[1,0,0] neg_hi:[1,0,0]\n\t" \
  "v_pk_fma_f32 v[126:127], v[182:183], v[154:155], v[126:127] neg_lo:[1,0,0] neg_hi:[1,0,0]\n\t" \
  "v_pk_fma_f32 v[156:157], v[176:177], v[152:153], v[156:157]\n\t" \
  "v_pk_fma_f32 v[158:159], v[178:179], v[154:155], v[158:159]\n\t" \
  "v_pk_fma_f32 v[124:125], v[176:177], v[120:121], v[124:125]\n\t" \
  "v_pk_fma_f32 v[126:127], v[178:179], v[122:123], v[126:127]\n\t" \
  "v_cvt_pk_bf16_f32 v204, v156, v157\n\t" \
  "v_cvt_pk_bf16_f32 v205, v158, v159\n\t" \
  "v_cvt_pk_bf16_f32 v206, v124, v125\n\t" \
  "v_cvt_pk_bf16_f32 v207, v126, v127\n\t" \
  "s_nop 1\n\t" \
  "v_permlane16_swap_b32 v204, v206\n\t" \
  "v_permlane16_swap_b32 v205, v207\n\t" \
  "buffer_store_dwordx4 v[204:207], %[l16], %[srd], %[sh] offen offset:2048 sc1\n\t" \
  "s_add_u32 %[scn], %[scn], 8\n\t" \
  "s_mov_b32 m0, %[scn]\n\t" \
  "s_mov_b64 exec, 1\n\t" \
  "buffer_load_dword %[l16], %[srz], 0 offen lds\n\t" \
  "s_mov_b64 exec, -1\n\t" \
  "s_branch Lend_%=\n\t" \
  "Lx0_%=:\n\t" \
  "v_add_u32 v244, %[sx], %[xo]\n\t" \
  "v_xor_b32 v245, 16, v244\n\t" \
  "v_xor_b32 v246, 32, v244\n\t" \
  "v_xor_b32 v247, 48, v244\n\t" \
  "ds_read_b64 %[xe00], v244 offset:0\n\t" \
  "ds_read_b64 %[xe10], v244 offset:2048\n\t" \
  "ds_read_b64 %[xe01], v245 offset:128\n\t" \
  "ds_read_b64 %[xe11], v245 offset:2176\n\t" \
  "ds_read_b64 %[xe02], v246 offset:256\n\t" \
  "ds_read_b64 %[xe12], v246 offset:2304\n\t" \
  "ds_read_b64 %[xe03], v247 offset:384\n\t" \
  "ds_read_b64 %[xe13], v247 offset:2432\n\t" \
  "s_branch Lxb0_%=\n\t" \
  "Lsp1_%=:\n\t" \
  "ds_read_b64 v[224:225], %[fl]\n\t" \
  "s_sub_i32 %[bud], %[bud], 1\n\t" \
  "s_cmp_lt_i32 %[bud], 0\n\t" \
  "s_cbranch_scc1 Lsx1_%=\n\t" \
  "s_waitcnt lgkmcnt(0)\n\t" \
  "v_or_b32 v224, v224, v225\n\t" \
  "s_nop 0\n\t" \
  "v_readfirstlane_b32 %[s0], v224\n\t" \
  "s_cmp_eq_u32 %[s0], 0\n\t" \
  "s_cbranch_scc1 Lsx1_%=\n\t" \
  "s_branch Lsp1_%=\n\t" \
  "Lsx1_%=:\n\t" \
  "s_cmp_eq_u32 %[sk], %[ks]\n\t" \
  "s_cbranch_scc0 Lb1_%=\n\t" \
  "v_add_u32 v244, %[sx], %[xo]\n\t" \
  "v_xor_b32 v245, 16, v244\n\t" \
  "v_xor_b32 v246, 32, v244\n\t" \
  "v_xor_b32 v247, 48, v244\n\t" \
  "ds_read_b64 %[xe00], v244 offset:0\n\t" \
  "ds_read_b64 %[xe10], v244 offset:2048\n\t" \
  "ds_read_b64 %[xe01], v245 offset:128\n\t" \
  "ds_read_b64 %[xe11], v245 offset:2176\n\t" \
  "ds_read_b64 %[xe02], v246 offset:256\n\t" \
  "ds_read_b64 %[xe12], v246 offset:2304\n\t" \
  "ds_read_b64 %[xe03], v247 offset:384\n\t" \
  "ds_read_b64 %[xe13], v247 offset:2432\n\t" \
  "s_branch Lb1_%=\n\t" \
  "Lsp3_%=:\n\t" \
  "v_mfma_f32_16x16x32_bf16 v[92:95], v[208:211], v[204:207], v[92:95]\n\t" \
  "ds_read_b128 v[172:175], %[pb1] offset:14336\n\t" \
  "v_mfma_f32_16x16x32_bf16 v[124:127], v[212:215], v[204:207], v[124:127]\n\t" \
  "s_waitcnt lgkmcnt(0)\n\t" \
  "v_mfma_f32_16x16x32_bf16 v[64:67], v[216:219], v[144:147], v[64:67]\n\t" \
  "v_mfma_f32_16x16x32_bf16 v[96:99], v[220:223], v[144:147], v[96:99]\n\t" \
  "v_mfma_f32_16x16x32_bf16 v[68:71], v[216:219], v[148:151], v[68:71]\n\t" \
  "v_mfma_f32_16x16x32_bf16 v[100:103], v[220:223], v[148:151], v[100:103]\n\t" \
  "v_mfma_f32_16x16x32_bf16 v[72:75], v[216:219], v[152:155], v[72:75]\n\t" \
  "v_mfma_f32_16x16x32_bf16 v[104:107], v[220:223], v[152:155], v[104:107]\n\t" \
  "v_mfma_f32_16x16x32_bf16 v[76:79], v[216:219], v[156:159], v[76:79]\n\t" \
  "v_mfma_f32_16x16x32_bf16 v[108:111], v[220:223], v[156:159], v[108:111]\n\t" \
  "v_mfma_f32_16x16x32_bf16 v[80:83], v[216:219], v[160:163], v[80:83]\n\t" \
  "v_mfma_f32_16x16x32_bf16 v[112:115], v[220:223], v[160:163], v[112:115]\n\t" \
  "v_mfma_f32_16x16x32_bf16 v[84:87], v[216:219], v[164:167], v[84:87]\n\t" \
  "v_mfma_f32_16x16x32_bf16 v[116:119], v[220:223], v[164:167], v[116:119]\n\t" \
  "v_mfma_f32_16x16x32_bf16 v[88:91], v[216:219], v[168:171], v[88:91]\n\t" \
  "v_mfma_f32_16x16x32_bf16 v[120:123], v[220:223], v[168:171], v[120:123]\n\t" \
  "v_mfma_f32_16x16x32_bf16 v[92:95], v[216:219], v[172:175], v[92:95]\n\t" \
  "v_mfma_f32_16x16x32_bf16 v[124:127], v[220:223], v[172:175], v[124:127]\n\t" \
  "v_add_u32 %[pb1], %[s2], %[pb1]\n\t" \
  "Lsa3_%=:\n\t" \
  "ds_read_b64 v[224:225], %[fl]\n\t" \
  "s_sub_i32 %[bud], %[bud], 1\n\t" \
  "s_cmp_lt_i32 %[bud], 0\n\t" \
  "s_cbranch_scc1 Lsb3_%=\n\t" \
  "s_waitcnt lgkmcnt(0)\n\t" \
  "v_or_b32 v224, v224, v225\n\t" \
  "s_nop 0\n\t" \
  "v_readfirstlane_b32 %[s0], v224\n\t" \
  "s_cmp_eq_u32 %[s0], 0\n\t" \
  "s_cbranch_scc1 Lsb3_%=\n\t" \
  "s_branch Lsa3_%=\n\t" \
  "Lsb3_%=:\n\t" \
  "ds_read_b128 v[128:131], %[pa0] offset:0\n\t" \
  "ds_read_b128 v[132:135], %[pa0] offset:8192\n\t" \
  "ds_read_b128 v[136:139], %[pa1] offset:0\n\t" \
  "ds_read_b128 v[140:143], %[pa1] offset:8192\n\t" \
  "ds_read_b128 v[176:179], %[pb0] offset:0\n\t" \
  "ds_read_b128 v[180:183], %[pb0] offset:2048\n\t" \
  "ds_read_b128 v[184:187], %[pb0] offset:4096\n\t" \
  "ds_read_b128 v[188:191], %[pb0] offset:6144\n\t" \
  "ds_read_b128 v[192:195], %[pb0] offset:8192\n\t" \
  "ds_read_b128 v[196:199], %[pb0] offset:10240\n\t" \
  "ds_read_b128 v[200:203], %[pb0] offset:12288\n\t" \
  "ds_read_b128 v[204:207], %[pb0] offset:14336\n\t" \
  "v_add_u32 %[fl], 8, %[fl]\n\t" \
  "s_waitcnt lgkmcnt(8)\n\t" \
  "s_mov_b64 exec, 1\n\t" \
  "buffer_load_dword %[l16], %[srz], 0 offen lds\n\t" \
  "s_mov_b64 exec, -1\n\t" \
  "s_add_u32 m0, m0, 16\n\t" \
  "s_cmp_eq_u32 %[sk], %[ks]\n\t" \
  "s_cbranch_scc0 Lnx3_%=\n\t" \
  "v_add_u32 v244, %[sx], %[xo]\n\t" \
  "v_xor_b32 v245, 16, v244\n\t" \
  "v_xor_b32 v246, 32, v244\n\t" \
  "v_xor_b32 v247, 48, v244\n\t" \
  "ds_read_b64 %[xe00], v244 offset:0\n\t" \
  "ds_read_b64 %[xe10], v244 offset:2048\n\t" \
  "ds_read_b64 %[xe01], v245 offset:128\n\t" \
  "ds_read_b64 %[xe11], v245 offset:2176\n\t" \
  "ds_read_b64 %[xe02], v246 offset:256\n\t" \
  "ds_read_b64 %[xe12], v246 offset:2304\n\t" \
  "ds_read_b64 %[xe03], v247 offset:384\n\t" \
  "ds_read_b64 %[xe13], v247 offset:2432\n\t" \
  "s_branch Lnx3_%=\n\t" \
  "Lsp5_%=:\n\t" \
  "ds_read_b64 v[224:225], %[fl]\n\t" \
  "s_sub_i32 %[bud], %[bud], 1\n\t" \
  "s_cmp_lt_i32 %[bud], 0\n\t" \
  "s_cbranch_scc1 Lsx5_%=\n\t" \
  "s_waitcnt lgkmcnt(0)\n\t" \
  "v_or_b32 v224, v224, v225\n\t" \
  "s_nop 0\n\t" \
  "v_readfirstlane_b32 %[s0], v224\n\t" \
  "s_cmp_eq_u32 %[s0], 0\n\t" \
  "s_cbranch_scc1 Lsx5_%=\n\t" \
  "s_branch Lsp5_%=\n\t" \
  "Lsx5_%=:\n\t" \
  "s_cmp_eq_u32 %[sk], %[ks]\n\t" \
  "s_cbranch_scc0 Lb5_%=\n\t" \
  "v_add_u32 v244, %[sx], %[xo]\n\t" \
  "v_xor_b32 v245, 16, v244\n\t" \
  "v_xor_b32 v246, 32, v244\n\t" \
  "v_xor_b32 v247, 48, v244\n\t" \
  "ds_read_b64 %[xe00], v244 offset:0\n\t" \
  "ds_read_b64 %[xe10], v244 offset:2048\n\t" \
  "ds_read_b64 %[xe01], v245 offset:128\n\t" \
  "ds_read_b64 %[xe11], v245 offset:2176\n\t" \
  "ds_read_b64 %[xe02], v246 offset:256\n\t" \
  "ds_read_b64 %[xe12], v246 offset:2304\n\t" \
  "ds_read_b64 %[xe03], v247 offset:384\n\t" \
  "ds_read_b64 %[xe13], v247 offset:2432\n\t" \
  "s_branch Lb5_%=\n\t" \
  "Lend_%=:\n\t" \
  "s_nop 7\n\t" \
  "s_nop 7\n\t"
#define ASM_MB \
  "s_nop 4\n\t" \
  "s_mov_b32 %[sg], 0\n\t" \
  "s_mov_b32 m0, %[scn]\n\t" \
  "Lc0_%=:\n\t" \
  "ds_read_b32 v192, %[f9]\n\t" \
  "s_sub_i32 %[bud], %[bud], 1\n\t" \
  "s_cmp_lt_i32 %[bud], 0\n\t" \
  "s_cbranch_scc1 Lc0b_%=\n\t" \
  "s_waitcnt lgkmcnt(0)\n\t" \
  "v_cmp_ne_u32 vcc, 0, v192\n\t" \
  "s_cbranch_vccnz Lc0_%=\n\t" \
  "Lc0b_%=:\n\t" \
  "ds_read_b128 v[128:131], %[pc0] offset:0\n\t" \
  "ds_read_b128 v[132:135], %[pc0] offset:2048\n\t" \
  "ds_read_b128 v[136:139], %[pc0] offset:4096\n\t" \
  "ds_read_b128 v[140:143], %[pc0] offset:6144\n\t" \
  "ds_read_b128 v[144:147], %[pc1] offset:0\n\t" \
  "ds_read_b128 v[148:151], %[pc1] offset:2048\n\t" \
  "ds_read_b128 v[152:155], %[pc1] offset:4096\n\t" \
  "ds_read_b128 v[156:159], %[pc1] offset:6144\n\t" \
  "s_waitcnt vmcnt(0)\n\t" \
  "v_readfirstlane_b32 %[s0], %[cnt]\n\t" \
  "s_cmp_eq_u32 %[s0], 31\n\t" \
  "s_cbranch_scc1 Lf2_%=\n\t" \
  "s_cmp_eq_u32 %[w], 0\n\t" \
  "s_cbranch_scc0 Lw_%=\n\t" \
  "Lf_%=:\n\t" \
  "v_mov_b32 v196, 0\n\t" \
  "s_mov_b64 exec, 1\n\t" \
  "buffer_atomic_or v196, off, %[srd], %[sf] sc0 sc1\n\t" \
  "s_mov_b64 exec, -1\n\t" \
  "s_sub_i32 %[bud], %[bud], 16\n\t" \
  "s_cmp_lt_i32 %[bud], 0\n\t" \
  "s_cbranch_scc1 Lf2_%=\n\t" \
  "s_waitcnt vmcnt(0)\n\t" \
  "v_readfirstlane_b32 %[s0], v196\n\t" \
  "s_cmp_lg_u32 %[s0], 32\n\t" \
  "s_cbranch_scc0 Lf2_%=\n\t" \
  "s_sleep 1\n\t" \
  "s_branch Lf_%=\n\t" \
  "Lf2_%=:\n\t" \
  "v_mov_b32 v192, 0\n\t" \
  "v_mov_b32 v193, 0\n\t" \
  "ds_write_b64 %[hr], v[192:193]\n\t" \
  "Lf3_%=:\n\t" \
  "s_setprio 2\n\t" \
  "buffer_load_dwordx4 v[64:67], %[vo], %[srd], %[h0] offen offset:0 sc1\n\t" \
  "buffer_load_dwordx4 v[68:71], %[vo], %[srd], %[h0] offen offset:2048 sc1\n\t" \
  "buffer_load_dwordx4 v[72:75], %[vo], %[srd], %[h1] offen offset:0 sc1\n\t" \
  "buffer_load_dwordx4 v[76:79], %[vo], %[srd], %[h1] offen offset:2048 sc1\n\t" \
  "buffer_load_dwordx4 v[80:83], %[vo], %[srd], %[h2] offen offset:0 sc1\n\t" \
  "buffer_load_dword v194, off, %[srz], 0\n\t" \
  "buffer_load_dwordx4 v[84:87], %[vo], %[srd], %[h2] offen offset:2048 sc1\n\t" \
  "buffer_load_dwordx4 v[88:91], %[vo], %[srd], %[h3] offen offset:0 sc1\n\t" \
  "buffer_load_dwordx4 v[92:95], %[vo], %[srd], %[h3] offen offset:2048 sc1\n\t" \
  "buffer_load_dwordx4 v[96:99], %[vo], %[srd], %[h4] offen offset:0 sc1\n\t" \
  "buffer_load_dwordx4 v[100:103], %[vo], %[srd], %[h4] offen offset:2048 sc1\n\t" \
  "buffer_load_dwordx4 v[104:107], %[vo], %[srd], %[h5] offen offset:0 sc1\n\t" \
  "buffer_load_dwordx4 v[108:111], %[vo], %[srd], %[h5] offen offset:2048 sc1\n\t" \
  "buffer_load_dwordx4 v[112:115], %[vo], %[srd], %[h6] offen offset:0 sc1\n\t" \
  "buffer_load_dword v194, off, %[srz], 0\n\t" \
  "buffer_load_dwordx4 v[116:119], %[vo], %[srd], %[h6] offen offset:2048 sc1\n\t" \
  "s_waitcnt lgkmcnt(0)\n\t" \
  "Lloop_%=:\n\t" \
  "s_waitcnt vmcnt(14) lgkmcnt(4)\n\t" \
  "v_mfma_f32_16x16x32_bf16 %[c00], v[64:67], v[128:131], %[c00]\n\t" \
  "buffer_load_dwordx4 v[120:123], %[vo], %[srd], %[h7] offen offset:0 sc1\n\t" \
  "v_mfma_f32_16x16x32_bf16 %[c10], v[68:71], v[128:131], %[c10]\n\t" \
  "ds_read_b128 v[160:163], %[pc2] offset:0\n\t" \
  "v_add_u32 %[pc0], %[k2], %[pc0]\n\t" \
  "v_mfma_f32_16x16x32_bf16 %[c01], v[64:67], v[132:135], %[c01]\n\t" \
  "v_mfma_f32_16x16x32_bf16 %[c11], v[68:71], v[132:135], %[c11]\n\t" \
  "ds_read_b128 v[164:167], %[pc2] offset:2048\n\t" \
  "v_mfma_f32_16x16x32_bf16 %[c02], v[64:67], v[136:139], %[c02]\n\t" \
  "buffer_load_dwordx4 v[124:127], %[vo], %[srd], %[h7] offen offset:2048 sc1\n\t" \
  "v_mfma_f32_16x16x32_bf16 %[c12], v[68:71], v[136:139], %[c12]\n\t" \
  "ds_read_b128 v[168:171], %[pc2] offset:4096\n\t" \
  "v_mfma_f32_16x16x32_bf16 %[c03], v[64:67], v[140:143], %[c03]\n\t" \
  "v_add_u32 %[vo], 0x20000, %[vo]\n\t" \
  "v_and_b32 %[vo], 0xfffff, %[vo]\n\t" \
  "v_mfma_f32_16x16x32_bf16 %[c13], v[68:71], v[140:143], %[c13]\n\t" \
  "ds_read_b128 v[172:175], %[pc2] offset:6144\n\t" \
  "s_waitcnt vmcnt(14) lgkmcnt(4)\n\t" \
  "v_mfma_f32_16x16x32_bf16 %[c00], v[72:75], v[144:147], %[c00]\n\t" \
  "buffer_load_dwordx4 v[64:67], %[vo], %[srd], %[h0] offen offset:0 sc1\n\t" \
  "v_mfma_f32_16x16x32_bf16 %[c10], v[76:79], v[144:147], %[c10]\n\t" \
  "ds_read_b128 v[176:179], %[pc3] offset:0\n\t" \
  "v_add_u32 %[pc1], %[k2], %[pc1]\n\t" \
  "v_mfma_f32_16x16x32_bf16 %[c01], v[72:75], v[148:151], %[c01]\n\t" \
  "s_cmp_eq_u32 %[sg], 2\n\t" \
  "s_cbranch_scc1 Lh_%=\n\t" \
  "Lhb_%=:\n\t" \
  "v_mfma_f32_16x16x32_bf16 %[c11], v[76:79], v[148:151], %[c11]\n\t" \
  "ds_read_b128 v[180:183], %[pc3] offset:2048\n\t" \
  "v_mfma_f32_16x16x32_bf16 %[c02], v[72:75], v[152:155], %[c02]\n\t" \
  "buffer_load_dwordx4 v[68:71], %[vo], %[srd], %[h0] offen offset:2048 sc1\n\t" \
  "v_mfma_f32_16x16x32_bf16 %[c12], v[76:79], v[152:155], %[c12]\n\t" \
  "ds_read_b128 v[184:187], %[pc3] offset:4096\n\t" \
  "v_mfma_f32_16x16x32_bf16 %[c03], v[72:75], v[156:159], %[c03]\n\t" \
  "v_mfma_f32_16x16x32_bf16 %[c13], v[76:79], v[156:159], %[c13]\n\t" \
  "ds_read_b128 v[188:191], %[pc3] offset:6144\n\t" \
  "s_waitcnt vmcnt(13) lgkmcnt(4)\n\t" \
  "v_mfma_f32_16x16x32_bf16 %[c00], v[80:83], v[160:163], %[c00]\n\t" \
  "buffer_load_dwordx4 v[72:75], %[vo], %[srd], %[h1] offen offset:0 sc1\n\t" \
  "v_mfma_f32_16x16x32_bf16 %[c10], v[84:87], v[160:163], %[c10]\n\t" \
  "ds_read_b128 v[128:131], %[pc0] offset:0\n\t" \
  "v_add_u32 %[pc2], %[k2], %[pc2]\n\t" \
  "v_mfma_f32_16x16x32_bf16 %[c01], v[80:83], v[164:167], %[c01]\n\t" \
  "v_mfma_f32_16x16x32_bf16 %[c11], v[84:87], v[164:167], %[c11]\n\t" \
  "ds_read_b128 v[132:135], %[pc0] offset:2048\n\t" \
  "v_mfma_f32_16x16x32_bf16 %[c02], v[80:83], v[168:171], %[c02]\n\t" \
  "buffer_load_dwordx4 v[76:79], %[vo], %[srd], %[h1] offen offset:2048 sc1\n\t" \
  "v_mfma_f32_16x16x32_bf16 %[c12], v[84:87], v[168:171], %[c12]\n\t" \
  "ds_read_b128 v[136:139], %[pc0] offset:4096\n\t" \
  "v_mfma_f32_16x16x32_bf16 %[c03], v[80:83], v[172:175], %[c03]\n\t" \
  "v_mfma_f32_16x16x32_bf16 %[c13], v[84:87], v[172:175], %[c13]\n\t" \
  "ds_read_b128 v[140:143], %[pc0] offset:6144\n\t" \
  "s_waitcnt vmcnt(13) lgkmcnt(4)\n\t" \
  "v_mfma_f32_16x16x32_bf16 %[c00], v[88:91], v[176:179], %[c00]\n\t" \
  "buffer_load_dwordx4 v[80:83], %[vo], %[srd], %[h2] offen offset:0 sc1\n\t" \
  "v_mfma_f32_16x16x32_bf16 %[c10], v[92:95], v[176:179], %[c10]\n\t" \
  "ds_read_b128 v[144:147], %[pc1] offset:0\n\t" \
  "v_add_u32 %[pc3], %[k2], %[pc3]\n\t" \
  "v_mfma_f32_16x16x32_bf16 %[c01], v[88:91], v[180:183], %[c01]\n\t" \
  "s_mov_b64 exec, 1\n\t" \
  "buffer_load_dword off, %[srz], 0 lds\n\t" \
  "s_mov_b64 exec, -1\n\t" \
  "v_mfma_f32_16x16x32_bf16 %[c11], v[92:95], v[180:183], %[c11]\n\t" \
  "ds_read_b128 v[148:151], %[pc1] offset:2048\n\t" \
  "v_mfma_f32_16x16x32_bf16 %[c02], v[88:91], v[184:187], %[c02]\n\t" \
  "buffer_load_dwordx4 v[84:87], %[vo], %[srd], %[h2] offen offset:2048 sc1\n\t" \
  "v_mfma_f32_16x16x32_bf16 %[c12], v[92:95], v[184:187], %[c12]\n\t" \
  "ds_read_b128 v[152:155], %[pc1] offset:4096\n\t" \
  "s_add_u32 m0, m0, 16\n\t" \
  "v_mfma_f32_16x16x32_bf16 %[c03], v[88:91], v[188:191], %[c03]\n\t" \
  "v_mfma_f32_16x16x32_bf16 %[c13], v[92:95], v[188:191], %[c13]\n\t" \
  "ds_read_b128 v[156:159], %[pc1] offset:6144\n\t" \
  "s_waitcnt vmcnt(14) lgkmcnt(4)\n\t" \
  "v_mfma_f32_16x16x32_bf16 %[c00], v[96:99], v[128:131], %[c00]\n\t" \
  "buffer_load_dwordx4 v[88:91], %[vo], %[srd], %[h3] offen offset:0 sc1\n\t" \
  "v_mfma_f32_16x16x32_bf16 %[c10], v[100:103], v[128:131], %[c10]\n\t" \
  "ds_read_b128 v[160:163], %[pc2] offset:0\n\t" \
  "v_add_u32 %[pc0], %[k2], %[pc0]\n\t" \
  "v_mfma_f32_16x16x32_bf16 %[c01], v[96:99], v[132:135], %[c01]\n\t" \
  "v_mfma_f32_16x16x32_bf16 %[c11], v[100:103], v[132:135], %[c11]\n\t" \
  "ds_read_b128 v[164:167], %[pc2] offset:2048\n\t" \
  "v_mfma_f32_16x16x32_bf16 %[c02], v[96:99], v[136:139], %[c02]\n\t" \
  "buffer_load_dwordx4 v[92:95], %[vo], %[srd], %[h3] offen offset:2048 sc1\n\t" \
  "v_mfma_f32_16x16x32_bf16 %[c12], v[100:103], v[136:139], %[c12]\n\t" \
  "ds_read_b128 v[168:171], %[pc2] offset:4096\n\t" \
  "v_mfma_f32_16x16x32_bf16 %[c03], v[96:99], v[140:143], %[c03]\n\t" \
  "v_mfma_f32_16x16x32_bf16 %[c13], v[100:103], v[140:143], %[c13]\n\t" \
  "ds_read_b128 v[172:175], %[pc2] offset:6144\n\t" \
  "s_waitcnt vmcnt(14) lgkmcnt(4)\n\t" \
  "v_mfma_f32_16x16x32_bf16 %[c00], v[104:107], v[144:147], %[c00]\n\t" \
  "buffer_load_dwordx4 v[96:99], %[vo], %[srd], %[h4] offen offset:0 sc1\n\t" \
  "v_mfma_f32_16x16x32_bf16 %[c10], v[108:111], v[144:147], %[c10]\n\t" \
  "ds_read_b128 v[176:179], %[pc3] offset:0\n\t" \
  "v_add_u32 %[pc1], %[k2], %[pc1]\n\t" \
  "v_mfma_f32_16x16x32_bf16 %[c01], v[104:107], v[148:151], %[c01]\n\t" \
  "v_mfma_f32_16x16x32_bf16 %[c11], v[108:111], v[148:151], %[c11]\n\t" \
  "ds_read_b128 v[180:183], %[pc3] offset:2048\n\t" \
  "v_mfma_f32_16x16x32_bf16 %[c02], v[104:107], v[152:155], %[c02]\n\t" \
  "buffer_load_dwordx4 v[100:103], %[vo], %[srd], %[h4] offen offset:2048 sc1\n\t" \
  "v_mfma_f32_16x16x32_bf16 %[c12], v[108:111], v[152:155], %[c12]\n\t" \
  "ds_read_b128 v[184:187], %[pc3] offset:4096\n\t" \
  "v_mfma_f32_16x16x32_bf16 %[c03], v[104:107], v[156:159], %[c03]\n\t" \
  "v_mfma_f32_16x16x32_bf16 %[c13], v[108:111], v[156:159], %[c13]\n\t" \
  "ds_read_b128 v[188:191], %[pc3] offset:6144\n\t" \
  "s_waitcnt vmcnt(13) lgkmcnt(4)\n\t" \
  "v_mfma_f32_16x16x32_bf16 %[c00], v[112:115], v[160:163], %[c00]\n\t" \
  "buffer_load_dwordx4 v[104:107], %[vo], %[srd], %[h5] offen offset:0 sc1\n\t" \
  "v_mfma_f32_16x16x32_bf16 %[c10], v[116:119], v[160:163], %[c10]\n\t" \
  "ds_read_b128 v[128:131], %[pc0] offset:0\n\t" \
  "v_add_u32 %[pc2], %[k2], %[pc2]\n\t" \
  "v_mfma_f32_16x16x32_bf16 %[c01], v[112:115], v[164:167], %[c01]\n\t" \
  "v_mfma_f32_16x16x32_bf16 %[c11], v[116:119], v[164:167], %[c11]\n\t" \
  "ds_read_b128 v[132:135], %[pc0] offset:2048\n\t" \
  "v_mfma_f32_16x16x32_bf16 %[c02], v[112:115], v[168:171], %[c02]\n\t" \
  "buffer_load_dwordx4 v[108:111], %[vo], %[srd], %[h5] offen offset:2048 sc1\n\t" \
  "v_mfma_f32_16x16x32_bf16 %[c12], v[116:119], v[168:171], %[c12]\n\t" \
  "ds_read_b128 v[136:139], %[pc0] offset:4096\n\t" \
  "v_mfma_f32_16x16x32_bf16 %[c03], v[112:115], v[172:175], %[c03]\n\t" \
  "v_mfma_f32_16x16x32_bf16 %[c13], v[116:119], v[172:175], %[c13]\n\t" \
  "ds_read_b128 v[140:143], %[pc0] offset:6144\n\t" \
  "s_waitcnt vmcnt(13) lgkmcnt(4)\n\t" \
  "v_mfma_f32_16x16x32_bf16 %[c00], v[120:123], v[176:179], %[c00]\n\t" \
  "buffer_load_dwordx4 v[112:115], %[vo], %[srd], %[h6] offen offset:0 sc1\n\t" \
  "v_mfma_f32_16x16x32_bf16 %[c10], v[124:127], v[176:179], %[c10]\n\t" \
  "ds_read_b128 v[144:147], %[pc1] offset:0\n\t" \
  "v_add_u32 %[pc3], %[k2], %[pc3]\n\t" \
  "v_mfma_f32_16x16x32_bf16 %[c01], v[120:123], v[180:183], %[c01]\n\t" \
  "s_mov_b64 exec, 1\n\t" \
  "buffer_load_dword off, %[srz], 0 lds\n\t" \
  "s_mov_b64 exec, -1\n\t" \
  "v_mfma_f32_16x16x32_bf16 %[c11], v[124:127], v[180:183], %[c11]\n\t" \
  "ds_read_b128 v[148:151], %[pc1] offset:2048\n\t" \
  "s_add_u32 %[sg], %[sg], 1\n\t" \
  "v_mfma_f32_16x16x32_bf16 %[c02], v[120:123], v[184:187], %[c02]\n\t" \
  "buffer_load_dwordx4 v[116:119], %[vo], %[srd], %[h6] offen offset:2048 sc1\n\t" \
  "v_mfma_f32_16x16x32_bf16 %[c12], v[124:127], v[184:187], %[c12]\n\t" \
  "ds_read_b128 v[152:155], %[pc1] offset:4096\n\t" \
  "s_add_u32 m0, m0, 16\n\t" \
  "v_mfma_f32_16x16x32_bf16 %[c03], v[120:123], v[188:191], %[c03]\n\t" \
  "s_cmp_eq_u32 %[sg], 3\n\t" \
  "s_cbranch_scc1 Lg_%=\n\t" \
  "Lgb_%=:\n\t" \
  "v_mfma_f32_16x16x32_bf16 %[c13], v[124:127], v[188:191], %[c13]\n\t" \
  "s_cmp_lt_u32 %[sg], 4\n\t" \
  "ds_read_b128 v[156:159], %[pc1] offset:6144\n\t" \
  "s_cbranch_scc1 Lloop_%=\n\t" \
  "s_mov_b32 %[s1], 0xfffe0000\n\t" \
  "Lloop2_%=:\n\t" \
  "s_waitcnt vmcnt(12) lgkmcnt(4)\n\t" \
  "v_mfma_f32_16x16x32_bf16 %[c00], v[64:67], v[128:131], %[c00]\n\t" \
  "ds_read_b64 v[192:193], %[fl] offset:0\n\t" \
  "buffer_load_dwordx4 v[120:123], %[vo], %[srd], %[h7] offen offset:0 sc1\n\t" \
  "v_mfma_f32_16x16x32_bf16 %[c10], v[68:71], v[128:131], %[c10]\n\t" \
  "ds_read_b128 v[160:163], %[pc2] offset:0\n\t" \
  "v_add_u32 %[pc0], %[s1], %[pc0]\n\t" \
  "v_mfma_f32_16x16x32_bf16 %[c01], v[64:67], v[132:135], %[c01]\n\t" \
  "v_mfma_f32_16x16x32_bf16 %[c11], v[68:71], v[132:135], %[c11]\n\t" \
  "ds_read_b128 v[164:167], %[pc2] offset:2048\n\t" \
  "v_mfma_f32_16x16x32_bf16 %[c02], v[64:67], v[136:139], %[c02]\n\t" \
  "buffer_load_dwordx4 v[124:127], %[vo], %[srd], %[h7] offen offset:2048 sc1\n\t" \
  "v_mfma_f32_16x16x32_bf16 %[c12], v[68:71], v[136:139], %[c12]\n\t" \
  "ds_read_b128 v[168:171], %[pc2] offset:4096\n\t" \
  "v_mfma_f32_16x16x32_bf16 %[c03], v[64:67], v[140:143], %[c03]\n\t" \
  "v_add_u32 %[vo], 0x20000, %[vo]\n\t" \
  "v_and_b32 %[vo], 0xfffff, %[vo]\n\t" \
  "v_mfma_f32_16x16x32_bf16 %[c13], v[68:71], v[140:143], %[c13]\n\t" \
  "ds_read_b128 v[172:175], %[pc2] offset:6144\n\t" \
  "s_waitcnt vmcnt(12) lgkmcnt(4)\n\t" \
  "v_mfma_f32_16x16x32_bf16 %[c00], v[72:75], v[144:147], %[c00]\n\t" \
  "buffer_load_dwordx4 v[64:67], %[vo], %[srd], %[h0] offen offset:0 sc1\n\t" \
  "v_mfma_f32_16x16x32_bf16 %[c10], v[76:79], v[144:147], %[c10]\n\t" \
  "ds_read_b128 v[176:179], %[pc3] offset:0\n\t" \
  "v_add_u32 %[pc1], %[s1], %[pc1]\n\t" \
  "v_mfma_f32_16x16x32_bf16 %[c01], v[72:75], v[148:151], %[c01]\n\t" \
  "v_or_b32 v192, v192, v193\n\t" \
  "v_mfma_f32_16x16x32_bf16 %[c11], v[76:79], v[148:151], %[c11]\n\t" \
  "ds_read_b128 v[180:183], %[pc3] offset:2048\n\t" \
  "v_readfirstlane_b32 %[s0], v192\n\t" \
  "v_mfma_f32_16x16x32_bf16 %[c02], v[72:75], v[152:155], %[c02]\n\t" \
  "buffer_load_dwordx4 v[68:71], %[vo], %[srd], %[h0] offen offset:2048 sc1\n\t" \
  "v_mfma_f32_16x16x32_bf16 %[c12], v[76:79], v[152:155], %[c12]\n\t" \
  "ds_read_b128 v[184:187], %[pc3] offset:4096\n\t" \
  "v_mfma_f32_16x16x32_bf16 %[c03], v[72:75], v[156:159], %[c03]\n\t" \
  "s_cmp_eq_u32 %[s0], 0\n\t" \
  "s_cbranch_scc0 Lcb1_%=\n\t" \
  "Lcb1b_%=:\n\t" \
  "v_mfma_f32_16x16x32_bf16 %[c13], v[76:79], v[156:159], %[c13]\n\t" \
  "ds_read_b128 v[188:191], %[pc3] offset:6144\n\t" \
  "s_waitcnt vmcnt(12) lgkmcnt(4)\n\t" \
  "v_mfma_f32_16x16x32_bf16 %[c00], v[80:83], v[160:163], %[c00]\n\t" \
  "buffer_load_dwordx4 v[72:75], %[vo], %[srd], %[h1] offen offset:0 sc1\n\t" \
  "v_mfma_f32_16x16x32_bf16 %[c10], v[84:87], v[160:163], %[c10]\n\t" \
  "ds_read_b128 v[128:131], %[pc0] offset:0\n\t" \
  "v_add_u32 %[pc2], %[s1], %[pc2]\n\t" \
  "v_mfma_f32_16x16x32_bf16 %[c01], v[80:83], v[164:167], %[c01]\n\t" \
  "v_mfma_f32_16x16x32_bf16 %[c11], v[84:87], v[164:167], %[c11]\n\t" \
  "ds_read_b128 v[132:135], %[pc0] offset:2048\n\t" \
  "v_mfma_f32_16x16x32_bf16 %[c02], v[80:83], v[168:171], %[c02]\n\t" \
  "buffer_load_dwordx4 v[76:79], %[vo], %[srd], %[h1] offen offset:2048 sc1\n\t" \
  "v_mfma_f32_16x16x32_bf16 %[c12], v[84:87], v[168:171], %[c12]\n\t" \
  "ds_read_b128 v[136:139], %[pc0] offset:4096\n\t" \
  "v_mfma_f32_16x16x32_bf16 %[c03], v[80:83], v[172:175], %[c03]\n\t" \
  "v_mfma_f32_16x16x32_bf16 %[c13], v[84:87], v[172:175], %[c13]\n\t" \
  "ds_read_b128 v[140:143], %[pc0] offset:6144\n\t" \
  "s_waitcnt vmcnt(12) lgkmcnt(4)\n\t" \
  "v_mfma_f32_16x16x32_bf16 %[c00], v[88:91], v[176:179], %[c00]\n\t" \
  "buffer_load_dwordx4 v[80:83], %[vo], %[srd], %[h2] offen offset:0 sc1\n\t" \
  "v_mfma_f32_16x16x32_bf16 %[c10], v[92:95], v[176:179], %[c10]\n\t" \
  "ds_read_b128 v[144:147], %[pc1] offset:0\n\t" \
  "v_add_u32 %[pc3], %[s1], %[pc3]\n\t" \
  "v_mfma_f32_16x16x32_bf16 %[c01], v[88:91], v[180:183], %[c01]\n\t" \
  "v_mfma_f32_16x16x32_bf16 %[c11], v[92:95], v[180:183], %[c11]\n\t" \
  "ds_read_b128 v[148:151], %[pc1] offset:2048\n\t" \
  "v_mfma_f32_16x16x32_bf16 %[c02], v[88:91], v[184:187], %[c02]\n\t" \
  "buffer_load_dwordx4 v[84:87], %[vo], %[srd], %[h2] offen offset:2048 sc1\n\t" \
  "v_mfma_f32_16x16x32_bf16 %[c12], v[92:95], v[184:187], %[c12]\n\t" \
  "ds_read_b128 v[152:155], %[pc1] offset:4096\n\t" \
  "v_mfma_f32_16x16x32_bf16 %[c03], v[88:91], v[188:191], %[c03]\n\t" \
  "v_mfma_f32_16x16x32_bf16 %[c13], v[92:95], v[188:191], %[c13]\n\t" \
  "ds_read_b128 v[156:159], %[pc1] offset:6144\n\t" \
  "s_waitcnt vmcnt(12) lgkmcnt(4)\n\t" \
  "v_mfma_f32_16x16x32_bf16 %[c00], v[96:99], v[128:131], %[c00]\n\t" \
  "ds_read_b64 v[192:193], %[fl] offset:8\n\t" \
  "buffer_load_dwordx4 v[88:91], %[vo], %[srd], %[h3] offen offset:0 sc1\n\t" \
  "v_mfma_f32_16x16x32_bf16 %[c10], v[100:103], v[128:131], %[c10]\n\t" \
  "ds_read_b128 v[160:163], %[pc2] offset:0\n\t" \
  "v_add_u32 %[pc0], %[k2], %[pc0]\n\t" \
  "v_mfma_f32_16x16x32_bf16 %[c01], v[96:99], v[132:135], %[c01]\n\t" \
  "v_mfma_f32_16x16x32_bf16 %[c11], v[100:103], v[132:135], %[c11]\n\t" \
  "ds_read_b128 v[164:167], %[pc2] offset:2048\n\t" \
  "v_mfma_f32_16x16x32_bf16 %[c02], v[96:99], v[136:139], %[c02]\n\t" \
  "buffer_load_dwordx4 v[92:95], %[vo], %[srd], %[h3] offen offset:2048 sc1\n\t" \
  "v_mfma_f32_16x16x32_bf16 %[c12], v[100:103], v[136:139], %[c12]\n\t" \
  "ds_read_b128 v[168:171], %[pc2] offset:4096\n\t" \
  "v_mfma_f32_16x16x32_bf16 %[c03], v[96:99], v[140:143], %[c03]\n\t" \
  "v_mfma_f32_16x16x32_bf16 %[c13], v[100:103], v[140:143], %[c13]\n\t" \
  "ds_read_b128 v[172:175], %[pc2] offset:6144\n\t" \
  "s_waitcnt vmcnt(12) lgkmcnt(4)\n\t" \
  "v_mfma_f32_16x16x32_bf16 %[c00], v[104:107], v[144:147], %[c00]\n\t" \
  "buffer_load_dwordx4 v[96:99], %[vo], %[srd], %[h4] offen offset:0 sc1\n\t" \
  "v_mfma_f32_16x16x32_bf16 %[c10], v[108:111], v[144:147], %[c10]\n\t" \
  "ds_read_b128 v[176:179], %[pc3] offset:0\n\t" \
  "v_add_u32 %[pc1], %[k2], %[pc1]\n\t" \
  "v_mfma_f32_16x16x32_bf16 %[c01], v[104:107], v[148:151], %[c01]\n\t" \
  "v_or_b32 v192, v192, v193\n\t" \
  "v_mfma_f32_16x16x32_bf16 %[c11], v[108:111], v[148:151], %[c11]\n\t" \
  "ds_read_b128 v[180:183], %[pc3] offset:2048\n\t" \
  "v_readfirstlane_b32 %[s0], v192\n\t" \
  "v_mfma_f32_16x16x32_bf16 %[c02], v[104:107], v[152:155], %[c02]\n\t" \
  "buffer_load_dwordx4 v[100:103], %[vo], %[srd], %[h4] offen offset:2048 sc1\n\t" \
  "v_mfma_f32_16x16x32_bf16 %[c12], v[108:111], v[152:155], %[c12]\n\t" \
  "ds_read_b128 v[184:187], %[pc3] offset:4096\n\t" \
  "v_mfma_f32_16x16x32_bf16 %[c03], v[104:107], v[156:159], %[c03]\n\t" \
  "s_cmp_eq_u32 %[s0], 0\n\t" \
  "s_cbranch_scc0 Lcb5_%=\n\t" \
  "Lcb5b_%=:\n\t" \
  "v_mfma_f32_16x16x32_bf16 %[c13], v[108:111], v[156:159], %[c13]\n\t" \
  "ds_read_b128 v[188:191], %[pc3] offset:6144\n\t" \
  "s_waitcnt vmcnt(12) lgkmcnt(4)\n\t" \
  "v_mfma_f32_16x16x32_bf16 %[c00], v[112:115], v[160:163], %[c00]\n\t" \
  "buffer_load_dwordx4 v[104:107], %[vo], %[srd], %[h5] offen offset:0 sc1\n\t" \
  "v_mfma_f32_16x16x32_bf16 %[c10], v[116:119], v[160:163], %[c10]\n\t" \
  "ds_read_b128 v[128:131], %[pc0] offset:0\n\t" \
  "v_add_u32 %[pc2], %[k2], %[pc2]\n\t" \
  "v_mfma_f32_16x16x32_bf16 %[c01], v[112:115], v[164:167], %[c01]\n\t" \
  "v_add_u32 %[fl], 16, %[fl]\n\t" \
  "s_mov_b32 %[s1], %[k2]\n\t" \
  "v_mfma_f32_16x16x32_bf16 %[c11], v[116:119], v[164:167], %[c11]\n\t" \
  "ds_read_b128 v[132:135], %[pc0] offset:2048\n\t" \
  "v_mfma_f32_16x16x32_bf16 %[c02], v[112:115], v[168:171], %[c02]\n\t" \
  "buffer_load_dwordx4 v[108:111], %[vo], %[srd], %[h5] offen offset:2048 sc1\n\t" \
  "v_mfma_f32_16x16x32_bf16 %[c12], v[116:119], v[168:171], %[c12]\n\t" \
  "ds_read_b128 v[136:139], %[pc0] offset:4096\n\t" \
  "v_mfma_f32_16x16x32_bf16 %[c03], v[112:115], v[172:175], %[c03]\n\t" \
  "v_mfma_f32_16x16x32_bf16 %[c13], v[116:119], v[172:175], %[c13]\n\t" \
  "ds_read_b128 v[140:143], %[pc0] offset:6144\n\t" \
  "s_waitcnt vmcnt(12) lgkmcnt(4)\n\t" \
  "v_mfma_f32_16x16x32_bf16 %[c00], v[120:123], v[176:179], %[c00]\n\t" \
  "buffer_load_dwordx4 v[112:115], %[vo], %[srd], %[h6] offen offset:0 sc1\n\t" \
  "v_mfma_f32_16x16x32_bf16 %[c10], v[124:127], v[176:179], %[c10]\n\t" \
  "ds_read_b128 v[144:147], %[pc1] offset:0\n\t" \
  "v_add_u32 %[pc3], %[k2], %[pc3]\n\t" \
  "v_mfma_f32_16x16x32_bf16 %[c01], v[120:123], v[180:183], %[c01]\n\t" \
  "s_add_u32 %[sg], %[sg], 1\n\t" \
  "s_cmp_lt_u32 %[sg], 7\n\t" \
  "v_mfma_f32_16x16x32_bf16 %[c11], v[124:127], v[180:183], %[c11]\n\t" \
  "ds_read_b128 v[148:151], %[pc1] offset:2048\n\t" \
  "v_mfma_f32_16x16x32_bf16 %[c02], v[120:123], v[184:187], %[c02]\n\t" \
  "buffer_load_dwordx4 v[116:119], %[vo], %[srd], %[h6] offen offset:2048 sc1\n\t" \
  "v_mfma_f32_16x16x32_bf16 %[c12], v[124:127], v[184:187], %[c12]\n\t" \
  "ds_read_b128 v[152:155], %[pc1] offset:4096\n\t" \
  "v_mfma_f32_16x16x32_bf16 %[c03], v[120:123], v[188:191], %[c03]\n\t" \
  "v_mfma_f32_16x16x32_bf16 %[c13], v[124:127], v[188:191], %[c13]\n\t" \
  "ds_read_b128 v[156:159], %[pc1] offset:6144\n\t" \
  "s_cbranch_scc1 Lloop2_%=\n\t" \
  "s_waitcnt vmcnt(12) lgkmcnt(4)\n\t" \
  "v_mfma_f32_16x16x32_bf16 %[c00], v[64:67], v[128:131], %[c00]\n\t" \
  "ds_read_b64 v[192:193], %[fl] offset:0\n\t" \
  "buffer_load_dwordx4 v[120:123], %[vo], %[srd], %[h7] offen offset:0 sc1\n\t" \
  "v_mfma_f32_16x16x32_bf16 %[c10], v[68:71], v[128:131], %[c10]\n\t" \
  "ds_read_b128 v[160:163], %[pc2] offset:0\n\t" \
  "v_add_u32 %[pc0], %[k2], %[pc0]\n\t" \
  "v_mfma_f32_16x16x32_bf16 %[c01], v[64:67], v[132:135], %[c01]\n\t" \
  "v_mfma_f32_16x16x32_bf16 %[c11], v[68:71], v[132:135], %[c11]\n\t" \
  "ds_read_b128 v[164:167], %[pc2] offset:2048\n\t" \
  "v_mfma_f32_16x16x32_bf16 %[c02], v[64:67], v[136:139], %[c02]\n\t" \
  "buffer_load_dwordx4 v[124:127], %[vo], %[srd], %[h7] offen offset:2048 sc1\n\t" \
  "v_mfma_f32_16x16x32_bf16 %[c12], v[68:71], v[136:139], %[c12]\n\t" \
  "ds_read_b128 v[168:171], %[pc2] offset:4096\n\t" \
  "v_mfma_f32_16x16x32_bf16 %[c03], v[64:67], v[140:143], %[c03]\n\t" \
  "v_mfma_f32_16x16x32_bf16 %[c13], v[68:71], v[140:143], %[c13]\n\t" \
  "ds_read_b128 v[172:175], %[pc2] offset:6144\n\t" \
  "s_waitcnt vmcnt(12) lgkmcnt(4)\n\t" \
  "v_mfma_f32_16x16x32_bf16 %[c00], v[72:75], v[144:147], %[c00]\n\t" \
  "v_mfma_f32_16x16x32_bf16 %[c10], v[76:79], v[144:147], %[c10]\n\t" \
  "ds_read_b128 v[176:179], %[pc3] offset:0\n\t" \
  "v_add_u32 %[pc1], %[k2], %[pc1]\n\t" \
  "v_mfma_f32_16x16x32_bf16 %[c01], v[72:75], v[148:151], %[c01]\n\t" \
  "v_or_b32 v192, v192, v193\n\t" \
  "v_mfma_f32_16x16x32_bf16 %[c11], v[76:79], v[148:151], %[c11]\n\t" \
  "ds_read_b128 v[180:183], %[pc3] offset:2048\n\t" \
  "v_readfirstlane_b32 %[s0], v192\n\t" \
  "v_mfma_f32_16x16x32_bf16 %[c02], v[72:75], v[152:155], %[c02]\n\t" \
  "v_mfma_f32_16x16x32_bf16 %[c12], v[76:79], v[152:155], %[c12]\n\t" \
  "ds_read_b128 v[184:187], %[pc3] offset:4096\n\t" \
  "v_mfma_f32_16x16x32_bf16 %[c03], v[72:75], v[156:159], %[c03]\n\t" \
  "s_cmp_eq_u32 %[s0], 0\n\t" \
  "s_cbranch_scc0 Lcf1_%=\n\t" \
  "Lcf1b_%=:\n\t" \
  "v_mfma_f32_16x16x32_bf16 %[c13], v[76:79], v[156:159], %[c13]\n\t" \
  "ds_read_b128 v[188:191], %[pc3] offset:6144\n\t" \
  "s_waitcnt vmcnt(10) lgkmcnt(4)\n\t" \
  "v_mfma_f32_16x16x32_bf16 %[c00], v[80:83], v[160:163], %[c00]\n\t" \
  "v_mfma_f32_16x16x32_bf16 %[c10], v[84:87], v[160:163], %[c10]\n\t" \
  "ds_read_b128 v[128:131], %[pc0] offset:0\n\t" \
  "v_add_u32 %[pc2], %[k2], %[pc2]\n\t" \
  "v_mfma_f32_16x16x32_bf16 %[c01], v[80:83], v[164:167], %[c01]\n\t" \
  "v_mfma_f32_16x16x32_bf16 %[c11], v[84:87], v[164:167], %[c11]\n\t" \
  "ds_read_b128 v[132:135], %[pc0] offset:2048\n\t" \
  "v_mfma_f32_16x16x32_bf16 %[c02], v[80:83], v[168:171], %[c02]\n\t" \
  "v_mfma_f32_16x16x32_bf16 %[c12], v[84:87], v[168:171], %[c12]\n\t" \
  "ds_read_b128 v[136:139], %[pc0] offset:4096\n\t" \
  "v_mfma_f32_16x16x32_bf16 %[c03], v[80:83], v[172:175], %[c03]\n\t" \
  "v_mfma_f32_16x16x32_bf16 %[c13], v[84:87], v[172:175], %[c13]\n\t" \
  "ds_read_b128 v[140:143], %[pc0] offset:6144\n\t" \
  "s_waitcnt vmcnt(8) lgkmcnt(4)\n\t" \
  "v_mfma_f32_16x16x32_bf16 %[c00], v[88:91], v[176:179], %[c00]\n\t" \
  "v_mfma_f32_16x16x32_bf16 %[c10], v[92:95], v[176:179], %[c10]\n\t" \
  "ds_read_b128 v[144:147], %[pc1] offset:0\n\t" \
  "v_add_u32 %[pc3], %[k2], %[pc3]\n\t" \
  "v_mfma_f32_16x16x32_bf16 %[c01], v[88:91], v[180:183], %[c01]\n\t" \
  "v_mfma_f32_16x16x32_bf16 %[c11], v[92:95], v[180:183], %[c11]\n\t" \
  "ds_read_b128 v[148:151], %[pc1] offset:2048\n\t" \
  "v_mfma_f32_16x16x32_bf16 %[c02], v[88:91], v[184:187], %[c02]\n\t" \
  "v_mfma_f32_16x16x32_bf16 %[c12], v[92:95], v[184:187], %[c12]\n\t" \
  "ds_read_b128 v[152:155], %[pc1] offset:4096\n\t" \
  "v_mfma_f32_16x16x32_bf16 %[c03], v[88:91], v[188:191], %[c03]\n\t" \
  "v_mfma_f32_16x16x32_bf16 %[c13], v[92:95], v[188:191], %[c13]\n\t" \
  "ds_read_b128 v[156:159], %[pc1] offset:6144\n\t" \
  "s_waitcnt vmcnt(6) lgkmcnt(4)\n\t" \
  "v_mfma_f32_16x16x32_bf16 %[c00], v[96:99], v[128:131], %[c00]\n\t" \
  "v_mfma_f32_16x16x32_bf16 %[c10], v[100:103], v[128:131], %[c10]\n\t" \
  "ds_read_b128 v[160:163], %[pc2] offset:0\n\t" \
  "v_mfma_f32_16x16x32_bf16 %[c01], v[96:99], v[132:135], %[c01]\n\t" \
  "v_mfma_f32_16x16x32_bf16 %[c11], v[100:103], v[132:135], %[c11]\n\t" \
  "ds_read_b128 v[164:167], %[pc2] offset:2048\n\t" \
  "v_mfma_f32_16x16x32_bf16 %[c02], v[96:99], v[136:139], %[c02]\n\t" \
  "v_mfma_f32_16x16x32_bf16 %[c12], v[100:103], v[136:139], %[c12]\n\t" \
  "ds_read_b128 v[168:171], %[pc2] offset:4096\n\t" \
  "v_mfma_f32_16x16x32_bf16 %[c03], v[96:99], v[140:143], %[c03]\n\t" \
  "v_mfma_f32_16x16x32_bf16 %[c13], v[100:103], v[140:143], %[c13]\n\t" \
  "ds_read_b128 v[172:175], %[pc2] offset:6144\n\t" \
  "s_waitcnt vmcnt(4) lgkmcnt(4)\n\t" \
  "v_mfma_f32_16x16x32_bf16 %[c00], v[104:107], v[144:147], %[c00]\n\t" \
  "v_mfma_f32_16x16x32_bf16 %[c10], v[108:111], v[144:147], %[c10]\n\t" \
  "ds_read_b128 v[176:179], %[pc3] offset:0\n\t" \
  "v_mfma_f32_16x16x32_bf16 %[c01], v[104:107], v[148:151], %[c01]\n\t" \
  "v_mfma_f32_16x16x32_bf16 %[c11], v[108:111], v[148:151], %[c11]\n\t" \
  "ds_read_b128 v[180:183], %[pc3] offset:2048\n\t" \
  "v_mfma_f32_16x16x32_bf16 %[c02], v[104:107], v[152:155], %[c02]\n\t" \
  "v_mfma_f32_16x16x32_bf16 %[c12], v[108:111], v[152:155], %[c12]\n\t" \
  "ds_read_b128 v[184:187], %[pc3] offset:4096\n\t" \
  "v_mfma_f32_16x16x32_bf16 %[c03], v[104:107], v[156:159], %[c03]\n\t" \
  "v_mfma_f32_16x16x32_bf16 %[c13], v[108:111], v[156:159], %[c13]\n\t" \
  "ds_read_b128 v[188:191], %[pc3] offset:6144\n\t" \
  "s_waitcnt vmcnt(2) lgkmcnt(4)\n\t" \
  "v_mfma_f32_16x16x32_bf16 %[c00], v[112:115], v[160:163], %[c00]\n\t" \
  "ds_read_b64 v[192:193], %[xd]\n\t" \
  "v_mfma_f32_16x16x32_bf16 %[c10], v[116:119], v[160:163], %[c10]\n\t" \
  "ds_read_b128 v[128:131], %[d0]\n\t" \
  "v_mfma_f32_16x16x32_bf16 %[c01], v[112:115], v[164:167], %[c01]\n\t" \
  "ds_read_b128 v[132:135], %[d1]\n\t" \
  "v_mfma_f32_16x16x32_bf16 %[c11], v[116:119], v[164:167], %[c11]\n\t" \
  "ds_read_b128 v[136:139], %[d2]\n\t" \
  "v_mfma_f32_16x16x32_bf16 %[c02], v[112:115], v[168:171], %[c02]\n\t" \
  "ds_read_b128 v[140:143], %[d3]\n\t" \
  "v_mfma_f32_16x16x32_bf16 %[c12], v[116:119], v[168:171], %[c12]\n\t" \
  "ds_read_b128 v[144:147], %[d4]\n\t" \
  "v_mfma_f32_16x16x32_bf16 %[c03], v[112:115], v[172:175], %[c03]\n\t" \
  "ds_read_b128 v[148:151], %[d5]\n\t" \
  "v_mfma_f32_16x16x32_bf16 %[c13], v[116:119], v[172:175], %[c13]\n\t" \
  "ds_read_b128 v[152:155], %[d6]\n\t" \
  "ds_read_b128 v[156:159], %[d7]\n\t" \
  "s_waitcnt vmcnt(0) lgkmcnt(9)\n\t" \
  "v_mfma_f32_16x16x32_bf16 %[c00], v[120:123], v[176:179], %[c00]\n\t" \
  "v_mfma_f32_16x16x32_bf16 %[c10], v[124:127], v[176:179], %[c10]\n\t" \
  "v_mfma_f32_16x16x32_bf16 %[c01], v[120:123], v[180:183], %[c01]\n\t" \
  "v_mfma_f32_16x16x32_bf16 %[c11], v[124:127], v[180:183], %[c11]\n\t" \
  "v_mfma_f32_16x16x32_bf16 %[c02], v[120:123], v[184:187], %[c02]\n\t" \
  "v_mfma_f32_16x16x32_bf16 %[c12], v[124:127], v[184:187], %[c12]\n\t" \
  "v_mfma_f32_16x16x32_bf16 %[c03], v[120:123], v[188:191], %[c03]\n\t" \
  "v_mfma_f32_16x16x32_bf16 %[c13], v[124:127], v[188:191], %[c13]\n\t" \
  "s_cmp_eq_u32 %[mine], 0\n\t" \
  "s_cbranch_scc1 Lend_%=\n\t" \
  "Ld_%=:\n\t" \
  "s_waitcnt lgkmcnt(0)\n\t" \
  "v_and_b32 v192, v192, v193\n\t" \
  "s_nop 0\n\t" \
  "v_readfirstlane_b32 %[s0], v192\n\t" \
  "s_cmp_eq_u32 %[s0], 0\n\t" \
  "s_cbranch_scc1 Ld1_%=\n\t" \
  "Ld2_%=:\n\t" \
  "v_add_f32 %[b00], %[b00], v128\n\t" \
  "v_add_f32 %[b01], %[b01], v129\n\t" \
  "v_add_f32 %[b02], %[b02], v130\n\t" \
  "v_add_f32 %[b03], %[b03], v131\n\t" \
  "v_add_f32 %[b10], %[b10], v132\n\t" \
  "v_add_f32 %[b11], %[b11], v133\n\t" \
  "v_add_f32 %[b12], %[b12], v134\n\t" \
  "v_add_f32 %[b13], %[b13], v135\n\t" \
  "v_add_f32 %[b20], %[b20], v136\n\t" \
  "v_add_f32 %[b21], %[b21], v137\n\t" \
  "v_add_f32 %[b22], %[b22], v138\n\t" \
  "v_add_f32 %[b23], %[b23], v139\n\t" \
  "v_add_f32 %[b30], %[b30], v140\n\t" \
  "v_add_f32 %[b31], %[b31], v141\n\t" \
  "v_add_f32 %[b32], %[b32], v142\n\t" \
  "v_add_f32 %[b33], %[b33], v143\n\t" \
  "v_add_f32 %[b40], %[b40], v144\n\t" \
  "v_add_f32 %[b41], %[b41], v145\n\t" \
  "v_add_f32 %[b42], %[b42], v146\n\t" \
  "v_add_f32 %[b43], %[b43], v147\n\t" \
  "v_add_f32 %[b50], %[b50], v148\n\t" \
  "v_add_f32 %[b51], %[b51], v149\n\t" \
  "v_add_f32 %[b52], %[b52], v150\n\t" \
  "v_add_f32 %[b53], %[b53], v151\n\t" \
  "v_add_f32 %[b60], %[b60], v152\n\t" \
  "v_add_f32 %[b61], %[b61], v153\n\t" \
  "v_add_f32 %[b62], %[b62], v154\n\t" \
  "v_add_f32 %[b63], %[b63], v155\n\t" \
  "v_add_f32 %[b70], %[b70], v156\n\t" \
  "v_add_f32 %[b71], %[b71], v157\n\t" \
  "v_add_f32 %[b72], %[b72], v158\n\t" \
  "v_add_f32 %[b73], %[b73], v159\n\t" \
  "s_branch Lend_%=\n\t" \
  "Lw_%=:\n\t" \
  "ds_read_b64 v[192:193], %[hr]\n\t" \
  "s_sub_i32 %[bud], %[bud], 1\n\t" \
  "s_cmp_lt_i32 %[bud], 0\n\t" \
  "s_cbranch_scc1 Lf3_%=\n\t" \
  "s_waitcnt lgkmcnt(0)\n\t" \
  "v_or_b32 v192, v192, v193\n\t" \
  "s_nop 0\n\t" \
  "v_readfirstlane_b32 %[s0], v192\n\t" \
  "s_cmp_eq_u32 %[s0], 0\n\t" \
  "s_cbranch_scc1 Lf3_%=\n\t" \
  "s_branch Lw_%=\n\t" \
  "Lh_%=:\n\t" \
  "buffer_load_dword v196, off, %[srd], %[sf1] sc1\n\t" \
  "s_branch Lhb_%=\n\t" \
  "Lg1_%=:\n\t" \
  "v_mov_b32 v196, 0\n\t" \
  "s_mov_b64 exec, 1\n\t" \
  "buffer_atomic_or v196, off, %[srd], %[sf1] sc0 sc1\n\t" \
  "s_mov_b64 exec, -1\n\t" \
  "s_sub_i32 %[bud], %[bud], 16\n\t" \
  "s_cmp_lt_i32 %[bud], 0\n\t" \
  "s_cbranch_scc1 Lgb_%=\n\t" \
  "s_waitcnt vmcnt(0)\n\t" \
  "s_branch Lg2_%=\n\t" \
  "Lg_%=:\n\t" \
  "s_waitcnt vmcnt(12)\n\t" \
  "Lg2_%=:\n\t" \
  "v_readfirstlane_b32 %[s0], v196\n\t" \
  "s_cmp_lg_u32 %[s0], 32\n\t" \
  "s_cbranch_scc1 Lg1_%=\n\t" \
  "s_branch Lgb_%=\n\t" \
  "Lcb1_%=:\n\t" \
  "ds_read_b64 v[192:193], %[fl] offset:0\n\t" \
  "s_sub_i32 %[bud], %[bud], 1\n\t" \
  "s_cmp_lt_i32 %[bud], 0\n\t" \
  "s_cbranch_scc1 Lcb1b_%=\n\t" \
  "s_waitcnt lgkmcnt(0)\n\t" \
  "v_or_b32 v192, v192, v193\n\t" \
  "s_nop 0\n\t" \
  "v_readfirstlane_b32 %[s0], v192\n\t" \
  "s_cmp_eq_u32 %[s0], 0\n\t" \
  "s_cbranch_scc1 Lcb1b_%=\n\t" \
  "s_branch Lcb1_%=\n\t" \
  "Lcb5_%=:\n\t" \
  "ds_read_b64 v[192:193], %[fl] offset:8\n\t" \
  "s_sub_i32 %[bud], %[bud], 1\n\t" \
  "s_cmp_lt_i32 %[bud], 0\n\t" \
  "s_cbranch_scc1 Lcb5b_%=\n\t" \
  "s_waitcnt lgkmcnt(0)\n\t" \
  "v_or_b32 v192, v192, v193\n\t" \
  "s_nop 0\n\t" \
  "v_readfirstlane_b32 %[s0], v192\n\t" \
  "s_cmp_eq_u32 %[s0], 0\n\t" \
  "s_cbranch_scc1 Lcb5b_%=\n\t" \
  "s_branch Lcb5_%=\n\t" \
  "Lcf1_%=:\n\t" \
  "ds_read_b64 v[192:193], %[fl] offset:0\n\t" \
  "s_sub_i32 %[bud], %[bud], 1\n\t" \
  "s_cmp_lt_i32 %[bud], 0\n\t" \
  "s_cbranch_scc1 Lcf1b_%=\n\t" \
  "s_waitcnt lgkmcnt(0)\n\t" \
  "v_or_b32 v192, v192, v193\n\t" \
  "s_nop 0\n\t" \
  "v_readfirstlane_b32 %[s0], v192\n\t" \
  "s_cmp_eq_u32 %[s0], 0\n\t" \
  "s_cbranch_scc1 Lcf1b_%=\n\t" \
  "s_branch Lcf1_%=\n\t" \
  "Ld1_%=:\n\t" \
  "s_sub_i32 %[bud], %[bud], 1\n\t" \
  "s_cmp_lt_i32 %[bud], 0\n\t" \
  "s_cbranch_scc1 Ld2_%=\n\t" \
  "ds_read_b64 v[192:193], %[xd]\n\t" \
  "ds_read_b128 v[128:131], %[d0]\n\t" \
  "ds_read_b128 v[132:135], %[d1]\n\t" \
  "ds_read_b128 v[136:139], %[d2]\n\t" \
  "ds_read_b128 v[140:143], %[d3]\n\t" \
  "ds_read_b128 v[144:147], %[d4]\n\t" \
  "ds_read_b128 v[148:151], %[d5]\n\t" \
  "ds_read_b128 v[152:155], %[d6]\n\t" \
  "ds_read_b128 v[156:159], %[d7]\n\t" \
  "s_branch Ld_%=\n\t" \
  "Lend_%=:\n\t" \
  "s_waitcnt lgkmcnt(0)\n\t" \
  "s_nop 6\n\t"
#define ASM_EN \
  "s_bfe_u32 %[j], %[bid], 0x30003\n\t" \
  "v_readfirstlane_b32 %[w], %[tid]\n\t" \
  "s_lshr_b32 %[w], %[w], 6\n\t" \
  "s_cmp_lg_u32 %[j], %[w]\n\t" \
  "s_cbranch_scc1 Lnc_%=\n\t" \
  "s_getpc_b64 s[20:21]\n\t" \
  "v_lshlrev_b32 %[v0], 7, %[tid]\n\t" \
  "v_min_u32 %[v0], %[cpf], %[v0]\n\t" \
  "global_load_dword v255, %[v0], s[20:21]\n\t" \
  "Lnc_%=:\n\t" \
  "s_sub_u32 %[w], %[w], 4\n\t" \
  "s_cbranch_scc1 Lend_%=\n\t" \
  "s_lshl3_add_u32 %[t], %[w], 8\n\t" \
  "s_cmp_lt_u32 %[w], 2\n\t" \
  "s_cselect_b32 %[t], 0, %[t]\n\t" \
  "s_load_dwordx2 s[20:21], %[kp], %[t]\n\t" \
  "s_cselect_b32 %[sa], 14, 11\n\t" \
  "s_cselect_b32 %[sc], 11, 15\n\t" \
  "s_cselect_b32 %[m], 16, 0\n\t" \
  "s_bfe_u32 %[r], %[bid], 0x20001\n\t" \
  "s_lshr_b32 %[t], %[bid], 6\n\t" \
  "s_lshl2_add_u32 %[c], %[r], %[t]\n\t" \
  "s_and_b32 %[t], %[bid], 1\n\t" \
  "s_lshl3_add_u32 %[j], %[t], %[j]\n\t" \
  "s_lshl1_add_u32 %[r], %[t], %[r]\n\t" \
  "s_and_b32 %[r], %[r], 3\n\t" \
  "s_lshl_b32 %[c], %[c], 5\n\t" \
  "s_add_u32 %[c], %[c], %[w]\n\t" \
  "s_lshl_b32 %[j], %[j], 4\n\t" \
  "s_cmp_lt_u32 %[w], 2\n\t" \
  "s_cselect_b32 %[c], %[c], %[j]\n\t" \
  "v_bfe_u32 %[v1], %[tid], 3, 3\n\t" \
  "v_xor_b32 %[v0], %[v1], %[tid]\n\t" \
  "v_and_b32 %[v0], 7, %[v0]\n\t" \
  "v_lshlrev_b32 %[v0], 4, %[v0]\n\t" \
  "v_lshl_add_u32 %[v0], %[r], 9, %[v0]\n\t" \
  "v_lshl_add_u32 %[v0], %[v1], %[sa], %[v0]\n\t" \
  "v_lshl_add_u32 %[v0], %[c], 13, %[v0]\n\t" \
  "s_lshl_b32 %[sa], 8, %[sa]\n\t" \
  "s_lshl_b32 %[sc], 1, %[sc]\n\t" \
  "v_add_u32 %[v1], %[sa], %[v0]\n\t" \
  "s_add_u32 %[m], %[m], %[w]\n\t" \
  "s_lshl_b32 m0, %[m], 13\n\t" \
  "s_waitcnt lgkmcnt(0)\n\t" \
  "Ll_%=:\n\t" \
  "global_load_lds_dwordx4 %[v0], s[20:21]\n\t" \
  "s_addk_i32 m0, 0x400\n\t" \
  "v_add_u32 %[v0], %[sc], %[v0]\n\t" \
  "global_load_lds_dwordx4 %[v1], s[20:21]\n\t" \
  "s_addk_i32 m0, 0x400\n\t" \
  "v_add_u32 %[v1], %[sc], %[v1]\n\t" \
  "s_lshl_b32 %[t], m0, 19\n\t" \
  "s_cbranch_scc1 Ll_%=\n\t" \
  "s_lshl2_add_u32 m0, %[w], %[ms0]\n\t" \
  "s_and_b32 s21, s21, 0xffff\n\t" \
  "v_mov_b32 %[v0], m0\n\t" \
  "ds_write_b32 %[v0], %[v0]\n\t" \
  "s_mov_b32 s22, 0\n\t" \
  "s_mov_b32 s23, 0x20000\n\t" \
  "s_mov_b64 exec, 1\n\t" \
  "s_waitcnt lgkmcnt(0)\n\t" \
  "buffer_load_dword off, s[20:23], 0 lds\n\t" \
  "s_mov_b64 exec, -1\n\t" \
  "Lend_%=:\n\t"
#define ASM_LA \
  "s_nop 4\n\t" \
  "s_barrier\n\t" \
  "Ltop_%=:\n\t" \
  "s_mov_b32 m0, %[dst]\n\t" \
  "v_mov_b32 %[vc], %[ci]\n\t" \
  "buffer_load_dwordx4 %[v0], %[r0], %[so0] offen lds\n\t" \
  "s_addk_i32 m0, 0x400\n\t" \
  "s_add_u32 %[dst], %[dst], 0x8000\n\t" \
  "buffer_load_dwordx4 %[v0], %[r0], %[so1] offen lds\n\t" \
  "s_addk_i32 m0, 0x400\n\t" \
  "s_cmp_ge_u32 %[dst], 0x24000\n\t" \
  "s_cselect_b32 %[s0], %[wrap], 0\n\t" \
  "buffer_load_dwordx4 %[v0], %[r0], %[so2] offen lds\n\t" \
  "s_addk_i32 m0, 0x400\n\t" \
  "s_sub_u32 %[dst], %[dst], %[s0]\n\t" \
  "buffer_load_dwordx4 %[v0], %[r0], %[so3] offen lds\n\t" \
  "s_addk_i32 m0, 0x400\n\t" \
  "s_add_u32 %[sk], %[sk], 128\n\t" \
  "buffer_load_dwordx4 %[v0], %[r0], %[so4] offen lds\n\t" \
  "s_addk_i32 m0, 0x400\n\t" \
  "s_and_b32 %[sk], %[sk], 2047\n\t" \
  "buffer_load_dwordx4 %[v0], %[r0], %[so5] offen lds\n\t" \
  "s_addk_i32 m0, 0x400\n\t" \
  "s_nop 0\n\t" \
  "buffer_load_dwordx4 %[v0], %[r0], %[so6] offen lds\n\t" \
  "s_addk_i32 m0, 0x400\n\t" \
  "s_add_u32 %[ci], %[ci], 16\n\t" \
  "buffer_load_dwordx4 %[v0], %[r0], %[so7] offen lds\n\t" \
  "s_addk_i32 m0, 0x400\n\t" \
  "s_sub_u32 %[n], %[n], 1\n\t" \
  "buffer_load_dwordx4 %[v0], %[r1], %[so0] offen lds\n\t" \
  "s_addk_i32 m0, 0x400\n\t" \
  "s_nop 0\n\t" \
  "buffer_load_dwordx4 %[v0], %[r1], %[so1] offen lds\n\t" \
  "s_addk_i32 m0, 0x400\n\t" \
  "s_nop 0\n\t" \
  "buffer_load_dwordx4 %[v0], %[r1], %[so2] offen lds\n\t" \
  "s_addk_i32 m0, 0x400\n\t" \
  "s_nop 0\n\t" \
  "buffer_load_dwordx4 %[v0], %[r1], %[so3] offen lds\n\t" \
  "s_addk_i32 m0, 0x400\n\t" \
  "ds_read_b128 v[24:27], %[vc]\n\t" \
  "ds_read_b128 v[28:31], %[vw]\n\t" \
  "buffer_load_dwordx4 %[v0], %[r1], %[so4] offen lds\n\t" \
  "s_addk_i32 m0, 0x400\n\t" \
  "s_nop 0\n\t" \
  "buffer_load_dwordx4 %[v0], %[r1], %[so5] offen lds\n\t" \
  "s_addk_i32 m0, 0x400\n\t" \
  "s_nop 0\n\t" \
  "buffer_load_dwordx4 %[v0], %[r1], %[so6] offen lds\n\t" \
  "s_addk_i32 m0, 0x400\n\t" \
  "s_waitcnt lgkmcnt(0)\n\t" \
  "v_or3_b32 v24, v24, v25, v26\n\t" \
  "v_or_b32 v24, v24, v27\n\t" \
  "v_sub_u32 v28, v30, v28\n\t" \
  "v_sub_u32 v29, v31, v29\n\t" \
  "v_or3_b32 v24, v24, v28, v29\n\t" \
  "buffer_load_dwordx4 %[v0], %[r1], %[so7] offen lds\n\t" \
  "s_mov_b32 m0, %[mk]\n\t" \
  "s_mov_b64 exec, 1\n\t" \
  "buffer_load_dword %[v0], %[rz], 0 offen lds\n\t" \
  "s_mov_b64 exec, -1\n\t" \
  "v_readfirstlane_b32 %[s0], v24\n\t" \
  "v_add_u32 %[v0], %[sk], %[b0]\n\t" \
  "s_add_u32 %[mk], %[mk], 8\n\t" \
  "s_cmp_eq_u32 %[n], 0\n\t" \
  "s_cbranch_scc1 Lend_%=\n\t" \
  "s_cmp_eq_u32 %[s0], 0\n\t" \
  "s_cbranch_scc1 Ltop_%=\n\t" \
  "Lsp_%=:\n\t" \
  "ds_read_b128 v[24:27], %[vc]\n\t" \
  "ds_read_b128 v[28:31], %[vw]\n\t" \
  "s_sub_i32 %[bud], %[bud], 1\n\t" \
  "s_cmp_lt_i32 %[bud], 0\n\t" \
  "s_cbranch_scc1 Ltop_%=\n\t" \
  "s_waitcnt lgkmcnt(0)\n\t" \
  "v_or3_b32 v24, v24, v25, v26\n\t" \
  "v_or_b32 v24, v24, v27\n\t" \
  "v_sub_u32 v28, v30, v28\n\t" \
  "v_sub_u32 v29, v31, v29\n\t" \
  "v_or3_b32 v24, v24, v28, v29\n\t" \
  "s_nop 0\n\t" \
  "v_readfirstlane_b32 %[s0], v24\n\t" \
  "s_cmp_eq_u32 %[s0], 0\n\t" \
  "s_cbranch_scc1 Ltop_%=\n\t" \
  "s_branch Lsp_%=\n\t" \
  "Lend_%=:\n\t"
#define ASM_LB \
  "s_nop 4\n\t" \
  "s_branch Lsp_%=\n\t" \
  "Ltop_%=:\n\t" \
  "s_mov_b32 m0, %[dst]\n\t" \
  "v_mov_b32 %[vc], %[ci]\n\t" \
  "buffer_load_dwordx4 %[v0], %[r0], %[so0] offen lds\n\t" \
  "s_addk_i32 m0, 0x400\n\t" \
  "s_add_u32 %[dst], %[dst], 0x8000\n\t" \
  "buffer_load_dwordx4 %[v1], %[r0], %[so1] offen lds\n\t" \
  "s_addk_i32 m0, 0x400\n\t" \
  "s_cmp_ge_u32 %[dst], 0x24000\n\t" \
  "s_cselect_b32 %[s0], %[wrap], 0\n\t" \
  "buffer_load_dwordx4 %[v0], %[r0], %[so2] offen lds\n\t" \
  "s_addk_i32 m0, 0x400\n\t" \
  "s_sub_u32 %[dst], %[dst], %[s0]\n\t" \
  "buffer_load_dwordx4 %[v1], %[r0], %[so3] offen lds\n\t" \
  "s_addk_i32 m0, 0x400\n\t" \
  "s_add_u32 %[sk], %[sk], 256\n\t" \
  "buffer_load_dwordx4 %[v0], %[r0], %[so4] offen lds\n\t" \
  "s_addk_i32 m0, 0x400\n\t" \
  "s_and_b32 %[sk], %[sk], 1023\n\t" \
  "buffer_load_dwordx4 %[v1], %[r0], %[so5] offen lds\n\t" \
  "s_addk_i32 m0, 0x400\n\t" \
  "s_cmp_ge_u32 %[ci], %[cth]\n\t" \
  "s_cselect_b32 %[s0], 32, 16\n\t" \
  "buffer_load_dwordx4 %[v0], %[r0], %[so6] offen lds\n\t" \
  "s_addk_i32 m0, 0x400\n\t" \
  "s_add_u32 %[ci], %[ci], %[s0]\n\t" \
  "buffer_load_dwordx4 %[v1], %[r0], %[so7] offen lds\n\t" \
  "s_addk_i32 m0, 0x400\n\t" \
  "s_sub_u32 %[n], %[n], 1\n\t" \
  "buffer_load_dwordx4 %[v0], %[r1], %[so0] offen lds\n\t" \
  "s_addk_i32 m0, 0x400\n\t" \
  "s_cmp_eq_u32 %[n], 4\n\t" \
  "s_cselect_b32 %[s0], 0x400, 0\n\t" \
  "buffer_load_dwordx4 %[v1], %[r1], %[so1] offen lds\n\t" \
  "s_addk_i32 m0, 0x400\n\t" \
  "s_xor_b32 %[hi], %[hi], %[s0]\n\t" \
  "buffer_load_dwordx4 %[v0], %[r1], %[so2] offen lds\n\t" \
  "s_addk_i32 m0, 0x400\n\t" \
  "s_or_b32 %[s1], %[sk], %[hi]\n\t" \
  "buffer_load_dwordx4 %[v1], %[r1], %[so3] offen lds\n\t" \
  "s_addk_i32 m0, 0x400\n\t" \
  "ds_read_b128 v[24:27], %[vc]\n\t" \
  "buffer_load_dwordx4 %[v0], %[r1], %[so4] offen lds\n\t" \
  "s_addk_i32 m0, 0x400\n\t" \
  "s_nop 0\n\t" \
  "buffer_load_dwordx4 %[v1], %[r1], %[so5] offen lds\n\t" \
  "s_addk_i32 m0, 0x400\n\t" \
  "s_nop 0\n\t" \
  "buffer_load_dwordx4 %[v0], %[r1], %[so6] offen lds\n\t" \
  "s_addk_i32 m0, 0x400\n\t" \
  "s_waitcnt lgkmcnt(0)\n\t" \
  "v_or3_b32 v24, v24, v25, v26\n\t" \
  "v_or_b32 v24, v24, v27\n\t" \
  "buffer_load_dwordx4 %[v1], %[r1], %[so7] offen lds\n\t" \
  "s_mov_b32 m0, %[mk]\n\t" \
  "s_mov_b64 exec, 3\n\t" \
  "buffer_load_dword %[v0], %[rz], 0 offen lds\n\t" \
  "s_mov_b64 exec, -1\n\t" \
  "v_readfirstlane_b32 %[s0], v24\n\t" \
  "v_add_u32 %[v0], %[s1], %[b0]\n\t" \
  "v_add_u32 %[v1], %[s1], %[b1]\n\t" \
  "s_add_u32 %[mk], %[mk], 16\n\t" \
  "s_cmp_eq_u32 %[n], %[nend]\n\t" \
  "s_cbranch_scc1 Lend_%=\n\t" \
  "s_cmp_eq_u32 %[s0], 0\n\t" \
  "s_cbranch_scc1 Ltop_%=\n\t" \
  "Lsp_%=:\n\t" \
  "ds_read_b128 v[24:27], %[vc]\n\t" \
  "s_sub_i32 %[bud], %[bud], 1\n\t" \
  "s_cmp_lt_i32 %[bud], 0\n\t" \
  "s_cbranch_scc1 Ltop_%=\n\t" \
  "s_waitcnt lgkmcnt(0)\n\t" \
  "v_or3_b32 v24, v24, v25, v26\n\t" \
  "v_or_b32 v24, v24, v27\n\t" \
  "s_nop 0\n\t" \
  "v_readfirstlane_b32 %[s0], v24\n\t" \
  "s_cmp_eq_u32 %[s0], 0\n\t" \
  "s_cbranch_scc1 Ltop_%=\n\t" \
  "s_sleep 1\n\t" \
  "s_branch Lsp_%=\n\t" \
  "Lend_%=:\n\t" \
  "buffer_load_dwordx4 v[32:35], %[p0], %[rp], %[so0] offen offset:128\n\t" \
  "buffer_load_dwordx4 v[36:39], %[p1], %[rp], %[so1] offen offset:128\n\t" \
  "buffer_load_dwordx4 v[40:43], %[p0], %[rp], %[so2] offen offset:128\n\t" \
  "buffer_load_dwordx4 v[44:47], %[p1], %[rp], %[so3] offen offset:128\n\t" \
  "buffer_load_dwordx4 v[48:51], %[p0], %[rp], %[so4] offen offset:128\n\t" \
  "buffer_load_dwordx4 v[52:55], %[p1], %[rp], %[so5] offen offset:128\n\t" \
  "buffer_load_dwordx4 v[56:59], %[p0], %[rp], %[so6] offen offset:128\n\t" \
  "buffer_load_dwordx4 v[60:63], %[p1], %[rp], %[so7] offen offset:128\n\t" \
  "buffer_load_dwordx4 v[64:67], %[p0], %[rp], %[so0] offen offset:256\n\t" \
  "buffer_load_dwordx4 v[68:71], %[p1], %[rp], %[so1] offen offset:256\n\t" \
  "buffer_load_dwordx4 v[72:75], %[p0], %[rp], %[so2] offen offset:256\n\t" \
  "buffer_load_dwordx4 v[76:79], %[p1], %[rp], %[so3] offen offset:256\n\t" \
  "buffer_load_dwordx4 v[80:83], %[p0], %[rp], %[so4] offen offset:256\n\t" \
  "buffer_load_dwordx4 v[84:87], %[p1], %[rp], %[so5] offen offset:256\n\t" \
  "buffer_load_dwordx4 v[88:91], %[p0], %[rp], %[so6] offen offset:256\n\t" \
  "buffer_load_dwordx4 v[92:95], %[p1], %[rp], %[so7] offen offset:256\n\t" \
  "buffer_load_dwordx4 v[96:99], %[p0], %[rp], %[so0] offen offset:384\n\t" \
  "buffer_load_dwordx4 v[100:103], %[p1], %[rp], %[so1] offen offset:384\n\t" \
  "buffer_load_dwordx4 v[104:107], %[p0], %[rp], %[so2] offen offset:384\n\t" \
  "buffer_load_dwordx4 v[108:111], %[p1], %[rp], %[so3] offen offset:384\n\t" \
  "buffer_load_dwordx4 v[112:115], %[p0], %[rp], %[so4] offen offset:384\n\t" \
  "buffer_load_dwordx4 v[116:119], %[p1], %[rp], %[so5] offen offset:384\n\t" \
  "buffer_load_dwordx4 v[120:123], %[p0], %[rp], %[so6] offen offset:384\n\t" \
  "buffer_load_dwordx4 v[124:127], %[p1], %[rp], %[so7] offen offset:384\n\t" \
  "buffer_load_dwordx4 v[128:131], %[p0], %[rp], %[so0] offen offset:512\n\t" \
  "buffer_load_dwordx4 v[132:135], %[p1], %[rp], %[so1] offen offset:512\n\t" \
  "buffer_load_dwordx4 v[136:139], %[p0], %[rp], %[so2] offen offset:512\n\t" \
  "buffer_load_dwordx4 v[140:143], %[p1], %[rp], %[so3] offen offset:512\n\t" \
  "buffer_load_dwordx4 v[144:147], %[p0], %[rp], %[so4] offen offset:512\n\t" \
  "buffer_load_dwordx4 v[148:151], %[p1], %[rp], %[so5] offen offset:512\n\t" \
  "buffer_load_dwordx4 v[152:155], %[p0], %[rp], %[so6] offen offset:512\n\t" \
  "buffer_load_dwordx4 v[156:159], %[p1], %[rp], %[so7] offen offset:512\n\t" \
  "buffer_load_dwordx4 v[160:163], %[p0], %[rp], %[so0] offen offset:640\n\t" \
  "buffer_load_dwordx4 v[164:167], %[p1], %[rp], %[so1] offen offset:640\n\t" \
  "buffer_load_dwordx4 v[168:171], %[p0], %[rp], %[so2] offen offset:640\n\t" \
  "buffer_load_dwordx4 v[172:175], %[p1], %[rp], %[so3] offen offset:640\n\t" \
  "buffer_load_dwordx4 v[176:179], %[p0], %[rp], %[so4] offen offset:640\n\t" \
  "buffer_load_dwordx4 v[180:183], %[p1], %[rp], %[so5] offen offset:640\n\t" \
  "buffer_load_dwordx4 v[184:187], %[p0], %[rp], %[so6] offen offset:640\n\t" \
  "buffer_load_dwordx4 v[188:191], %[p1], %[rp], %[so7] offen offset:640\n\t" \
  "buffer_load_dwordx4 v[192:195], %[p0], %[rp], %[so0] offen offset:768\n\t" \
  "buffer_load_dwordx4 v[196:199], %[p1], %[rp], %[so1] offen offset:768\n\t" \
  "buffer_load_dwordx4 v[200:203], %[p0], %[rp], %[so2] offen offset:768\n\t" \
  "buffer_load_dwordx4 v[204:207], %[p1], %[rp], %[so3] offen offset:768\n\t" \
  "buffer_load_dwordx4 v[208:211], %[p0], %[rp], %[so4] offen offset:768\n\t" \
  "buffer_load_dwordx4 v[212:215], %[p1], %[rp], %[so5] offen offset:768\n\t" \
  "buffer_load_dwordx4 v[216:219], %[p0], %[rp], %[so6] offen offset:768\n\t" \
  "buffer_load_dwordx4 v[220:223], %[p1], %[rp], %[so7] offen offset:768\n\t" \
  "buffer_load_dwordx4 v[224:227], %[p0], %[rp], %[so0] offen offset:896\n\t" \
  "buffer_load_dwordx4 v[228:231], %[p1], %[rp], %[so1] offen offset:896\n\t" \
  "buffer_load_dwordx4 v[232:235], %[p0], %[rp], %[so2] offen offset:896\n\t" \
  "buffer_load_dwordx4 v[236:239], %[p1], %[rp], %[so3] offen offset:896\n\t" \
  "buffer_load_dwordx4 v[240:243], %[p0], %[rp], %[so4] offen offset:896\n\t" \
  "buffer_load_dwordx4 v[244:247], %[p1], %[rp], %[so5] offen offset:896\n\t" \
  "buffer_load_dwordx4 v[248:251], %[p0], %[rp], %[so6] offen offset:896\n\t" \
  "buffer_load_dwordx4 v[252:255], %[p1], %[rp], %[so7] offen offset:896\n\t" \
  "v_mov_b32 v28, 0\n\t" \
  "Lw0_%=:\n\t" \
  "ds_read_b128 v[24:27], %[vm] offset:0\n\t" \
  "s_sub_i32 %[bud], %[bud], 1\n\t" \
  "s_cmp_lt_i32 %[bud], 0\n\t" \
  "s_cbranch_scc1 Lx0_%=\n\t" \
  "s_waitcnt lgkmcnt(0)\n\t" \
  "v_or3_b32 v24, v24, v25, v26\n\t" \
  "v_or_b32 v24, v24, v27\n\t" \
  "s_nop 0\n\t" \
  "v_readfirstlane_b32 %[s0], v24\n\t" \
  "s_cmp_eq_u32 %[s0], 0\n\t" \
  "s_cbranch_scc1 Lx0_%=\n\t" \
  "s_sleep 2\n\t" \
  "s_branch Lw0_%=\n\t" \
  "Lx0_%=:\n\t" \
  "s_waitcnt vmcnt(0)\n\t" \
  "ds_write_b128 %[vd], v[32:35] offset:0\n\t" \
  "ds_write_b128 %[vd], v[36:39] offset:1024\n\t" \
  "ds_write_b128 %[vd], v[40:43] offset:2048\n\t" \
  "ds_write_b128 %[vd], v[44:47] offset:3072\n\t" \
  "ds_write_b128 %[vd], v[48:51] offset:4096\n\t" \
  "ds_write_b128 %[vd], v[52:55] offset:5120\n\t" \
  "ds_write_b128 %[vd], v[56:59] offset:6144\n\t" \
  "ds_write_b128 %[vd], v[60:63] offset:7168\n\t" \
  "s_waitcnt lgkmcnt(0)\n\t" \
  "ds_write_b32 %[vl], v28 offset:0\n\t" \
  "Lw1_%=:\n\t" \
  "ds_read_b128 v[24:27], %[vm] offset:16\n\t" \
  "s_sub_i32 %[bud], %[bud], 1\n\t" \
  "s_cmp_lt_i32 %[bud], 0\n\t" \
  "s_cbranch_scc1 Lx1_%=\n\t" \
  "s_waitcnt lgkmcnt(0)\n\t" \
  "v_or3_b32 v24, v24, v25, v26\n\t" \
  "v_or_b32 v24, v24, v27\n\t" \
  "s_nop 0\n\t" \
  "v_readfirstlane_b32 %[s0], v24\n\t" \
  "s_cmp_eq_u32 %[s0], 0\n\t" \
  "s_cbranch_scc1 Lx1_%=\n\t" \
  "s_sleep 2\n\t" \
  "s_branch Lw1_%=\n\t" \
  "Lx1_%=:\n\t" \
  "ds_write_b128 %[vd], v[64:67] offset:16384\n\t" \
  "ds_write_b128 %[vd], v[68:71] offset:17408\n\t" \
  "ds_write_b128 %[vd], v[72:75] offset:18432\n\t" \
  "ds_write_b128 %[vd], v[76:79] offset:19456\n\t" \
  "ds_write_b128 %[vd], v[80:83] offset:20480\n\t" \
  "ds_write_b128 %[vd], v[84:87] offset:21504\n\t" \
  "ds_write_b128 %[vd], v[88:91] offset:22528\n\t" \
  "ds_write_b128 %[vd], v[92:95] offset:23552\n\t" \
  "s_waitcnt lgkmcnt(0)\n\t" \
  "ds_write_b32 %[vl], v28 offset:8\n\t" \
  "Lw2_%=:\n\t" \
  "ds_read_b128 v[24:27], %[vm] offset:32\n\t" \
  "s_sub_i32 %[bud], %[bud], 1\n\t" \
  "s_cmp_lt_i32 %[bud], 0\n\t" \
  "s_cbranch_scc1 Lx2_%=\n\t" \
  "s_waitcnt lgkmcnt(0)\n\t" \
  "v_or3_b32 v24, v24, v25, v26\n\t" \
  "v_or_b32 v24, v24, v27\n\t" \
  "s_nop 0\n\t" \
  "v_readfirstlane_b32 %[s0], v24\n\t" \
  "s_cmp_eq_u32 %[s0], 0\n\t" \
  "s_cbranch_scc1 Lx2_%=\n\t" \
  "s_sleep 2\n\t" \
  "s_branch Lw2_%=\n\t" \
  "Lx2_%=:\n\t" \
  "ds_write_b128 %[vd], v[96:99] offset:32768\n\t" \
  "ds_write_b128 %[vd], v[100:103] offset:33792\n\t" \
  "ds_write_b128 %[vd], v[104:107] offset:34816\n\t" \
  "ds_write_b128 %[vd], v[108:111] offset:35840\n\t" \
  "ds_write_b128 %[vd], v[112:115] offset:36864\n\t" \
  "ds_write_b128 %[vd], v[116:119] offset:37888\n\t" \
  "ds_write_b128 %[vd], v[120:123] offset:38912\n\t" \
  "ds_write_b128 %[vd], v[124:127] offset:39936\n\t" \
  "s_waitcnt lgkmcnt(0)\n\t" \
  "ds_write_b32 %[vl], v28 offset:16\n\t" \
  "Lw3_%=:\n\t" \
  "ds_read_b128 v[24:27], %[vm] offset:48\n\t" \
  "s_sub_i32 %[bud], %[bud], 1\n\t" \
  "s_cmp_lt_i32 %[bud], 0\n\t" \
  "s_cbranch_scc1 Lx3_%=\n\t" \
  "s_waitcnt lgkmcnt(0)\n\t" \
  "v_or3_b32 v24, v24, v25, v26\n\t" \
  "v_or_b32 v24, v24, v27\n\t" \
  "s_nop 0\n\t" \
  "v_readfirstlane_b32 %[s0], v24\n\t" \
  "s_cmp_eq_u32 %[s0], 0\n\t" \
  "s_cbranch_scc1 Lx3_%=\n\t" \
  "s_sleep 2\n\t" \
  "s_branch Lw3_%=\n\t" \
  "Lx3_%=:\n\t" \
  "ds_write_b128 %[vd], v[128:131] offset:49152\n\t" \
  "ds_write_b128 %[vd], v[132:135] offset:50176\n\t" \
  "ds_write_b128 %[vd], v[136:139] offset:51200\n\t" \
  "ds_write_b128 %[vd], v[140:143] offset:52224\n\t" \
  "ds_write_b128 %[vd], v[144:147] offset:53248\n\t" \
  "ds_write_b128 %[vd], v[148:151] offset:54272\n\t" \
  "ds_write_b128 %[vd], v[152:155] offset:55296\n\t" \
  "ds_write_b128 %[vd], v[156:159] offset:56320\n\t" \
  "v_add_u32 %[vd], 0x10000, %[vd]\n\t" \
  "s_waitcnt lgkmcnt(0)\n\t" \
  "ds_write_b32 %[vl], v28 offset:24\n\t" \
  "Lw4_%=:\n\t" \
  "ds_read_b128 v[24:27], %[vm] offset:64\n\t" \
  "s_sub_i32 %[bud], %[bud], 1\n\t" \
  "s_cmp_lt_i32 %[bud], 0\n\t" \
  "s_cbranch_scc1 Lx4_%=\n\t" \
  "s_waitcnt lgkmcnt(0)\n\t" \
  "v_or3_b32 v24, v24, v25, v26\n\t" \
  "v_or_b32 v24, v24, v27\n\t" \
  "s_nop 0\n\t" \
  "v_readfirstlane_b32 %[s0], v24\n\t" \
  "s_cmp_eq_u32 %[s0], 0\n\t" \
  "s_cbranch_scc1 Lx4_%=\n\t" \
  "s_sleep 2\n\t" \
  "s_branch Lw4_%=\n\t" \
  "Lx4_%=:\n\t" \
  "ds_write_b128 %[vd], v[160:163] offset:0\n\t" \
  "ds_write_b128 %[vd], v[164:167] offset:1024\n\t" \
  "ds_write_b128 %[vd], v[168:171] offset:2048\n\t" \
  "ds_write_b128 %[vd], v[172:175] offset:3072\n\t" \
  "ds_write_b128 %[vd], v[176:179] offset:4096\n\t" \
  "ds_write_b128 %[vd], v[180:183] offset:5120\n\t" \
  "ds_write_b128 %[vd], v[184:187] offset:6144\n\t" \
  "ds_write_b128 %[vd], v[188:191] offset:7168\n\t" \
  "s_waitcnt lgkmcnt(0)\n\t" \
  "ds_write_b32 %[vl], v28 offset:32\n\t" \
  "Lw5_%=:\n\t" \
  "ds_read_b128 v[24:27], %[vm] offset:80\n\t" \
  "s_sub_i32 %[bud], %[bud], 1\n\t" \
  "s_cmp_lt_i32 %[bud], 0\n\t" \
  "s_cbranch_scc1 Lx5_%=\n\t" \
  "s_waitcnt lgkmcnt(0)\n\t" \
  "v_or3_b32 v24, v24, v25, v26\n\t" \
  "v_or_b32 v24, v24, v27\n\t" \
  "s_nop 0\n\t" \
  "v_readfirstlane_b32 %[s0], v24\n\t" \
  "s_cmp_eq_u32 %[s0], 0\n\t" \
  "s_cbranch_scc1 Lx5_%=\n\t" \
  "s_sleep 2\n\t" \
  "s_branch Lw5_%=\n\t" \
  "Lx5_%=:\n\t" \
  "ds_write_b128 %[vd], v[192:195] offset:16384\n\t" \
  "ds_write_b128 %[vd], v[196:199] offset:17408\n\t" \
  "ds_write_b128 %[vd], v[200:203] offset:18432\n\t" \
  "ds_write_b128 %[vd], v[204:207] offset:19456\n\t" \
  "ds_write_b128 %[vd], v[208:211] offset:20480\n\t" \
  "ds_write_b128 %[vd], v[212:215] offset:21504\n\t" \
  "ds_write_b128 %[vd], v[216:219] offset:22528\n\t" \
  "ds_write_b128 %[vd], v[220:223] offset:23552\n\t" \
  "s_waitcnt lgkmcnt(0)\n\t" \
  "ds_write_b32 %[vl], v28 offset:40\n\t" \
  "Lw6_%=:\n\t" \
  "ds_read_b128 v[24:27], %[vm] offset:96\n\t" \
  "s_sub_i32 %[bud], %[bud], 1\n\t" \
  "s_cmp_lt_i32 %[bud], 0\n\t" \
  "s_cbranch_scc1 Lx6_%=\n\t" \
  "s_waitcnt lgkmcnt(0)\n\t" \
  "v_or3_b32 v24, v24, v25, v26\n\t" \
  "v_or_b32 v24, v24, v27\n\t" \
  "s_nop 0\n\t" \
  "v_readfirstlane_b32 %[s0], v24\n\t" \
  "s_cmp_eq_u32 %[s0], 0\n\t" \
  "s_cbranch_scc1 Lx6_%=\n\t" \
  "s_sleep 2\n\t" \
  "s_branch Lw6_%=\n\t" \
  "Lx6_%=:\n\t" \
  "ds_write_b128 %[vd], v[224:227] offset:32768\n\t" \
  "ds_write_b128 %[vd], v[228:231] offset:33792\n\t" \
  "ds_write_b128 %[vd], v[232:235] offset:34816\n\t" \
  "ds_write_b128 %[vd], v[236:239] offset:35840\n\t" \
  "ds_write_b128 %[vd], v[240:243] offset:36864\n\t" \
  "ds_write_b128 %[vd], v[244:247] offset:37888\n\t" \
  "ds_write_b128 %[vd], v[248:251] offset:38912\n\t" \
  "ds_write_b128 %[vd], v[252:255] offset:39936\n\t" \
  "s_waitcnt lgkmcnt(0)\n\t" \
  "ds_write_b32 %[vl], v28 offset:48\n\t" \
  "s_waitcnt lgkmcnt(0)\n\t"

#define OST " sc1"
#define XPRIO0 3
#define XPRIO 1
#define LPRIO 2
#define FPRIO 1
#define MPRIO 2
#define WGRP (~sg & 1)
#define WS0 1
#define WS1 5
#define WSLP 8
#define TKG0 3
#define GEXACT 0
#define YPRIO 3
#define GPRIO 1
#define W2G 14
#define CWS 16
#define HELP 1
#define WRES 1
#define XG -1
#define XSLP 4
#define GUG 25
#define TKG 4
#define YG 1
#define NOFFN 0
#define CODEPF 30080
#define WNT " nt"
constexpr int D_ = 1024, F_ = 3584;
constexpr int MISC = 147456;
constexpr int L_TAB = MISC;
constexpr int L_FLG = L_TAB + 11 * 512;
constexpr int L_ONE = L_FLG + 256;
constexpr int L_G = L_ONE + 1024;
constexpr int L_SEL = L_G + 512;
constexpr int L_BROW = L_SEL + 128;
constexpr int L_ACT = L_BROW + 2048;
constexpr int L_XS = L_ACT + 1024;
constexpr int L_JUNK = L_XS + 4096;
constexpr int LDS_BYTES = 163840;
static_assert(L_JUNK + 768 <= LDS_BYTES, "lds");
constexpr int M_CONS = L_ONE + 16, M_LAND = L_ONE + 544, M_GT = M_LAND + 288, M_HR = M_LAND + 296, M_CAR = M_LAND + 304, M_XS = M_LAND + 320, M_WR = M_LAND + 336;
constexpr int M_S0 = L_SEL + 64;
static_assert(M_S0 - M_LAND == 1056, "offset in ASM_MA");
constexpr int FL_XD = L_FLG + 16, FL_TK = L_FLG + 24;
constexpr unsigned W_EPOCH = 0;
constexpr unsigned W_RG = 256;
constexpr unsigned W_R2 = W_RG + 2048 * 8;
constexpr unsigned W_HF = 32768;
constexpr unsigned W_GF = W_HF + 32 * 128;
constexpr unsigned W_YF = W_GF + 56 * 128;
constexpr unsigned W_HT = W_YF + 8 * 128;
constexpr unsigned W_AGG = 65536;
constexpr unsigned W_GU = W_AGG + 262144;
constexpr unsigned W_YP = W_GU + 56 * 16384;
constexpr unsigned W_H = 4u << 20;
#define HSK 24
#define HOFS(l) ((l) * 16 + (2 > 1 && (l) >= HSK ? 128 : 0))
constexpr unsigned W_DBG = 24u << 20;
constexpr unsigned W_END = 25u << 20;
static_assert(W_R2 + 8192 <= W_HF && W_HT + 8192 <= W_AGG && W_YP + 128 * 56 * 256 <= W_H, "ws");
#define WS_BASE(n) (((n) - W_END) & ~(size_t)0x1fffff)
constexpr int SPIN_MAX = 1 << 18;

DEV float bf2f(unsigned v) { return __uint_as_float(v << 16); }
DEV float bfhi(unsigned v) { return __uint_as_float(v & 0xffff0000u); }
DEV u16 f2bf(float f) { return __builtin_bit_cast(u16, (__bf16)f); }
DEV float rbf(float f) { return bf2f(f2bf(f)); }
typedef __bf16 bf16x2_t __attribute__((ext_vector_type(2)));
DEV unsigned pk2(float a, float b) { return __builtin_bit_cast(unsigned, __builtin_convertvector((f32x2){a, b}, bf16x2_t)); }
DEV float fma1(float a, float b, float c) { float d; asm("v_fma_f32 %0, %1, %2, %3" : "=v"(d) : "v"(a), "v"(b), "v"(c)); return d; }
DEV float fnma1(float a, float b, float c) { float d; asm("v_fma_f32 %0, -%1, %2, %3" : "=v"(d) : "v"(a), "v"(b), "v"(c)); return d; }
DEV float mul1(float a, float b) { float d; asm("v_mul_f32 %0, %1, %2" : "=v"(d) : "v"(a), "v"(b)); return d; }
DEV f32x2 pkfma(f32x2 a, f32x2 b, f32x2 c) { f32x2 d; asm("v_pk_fma_f32 %0, %1, %2, %3" : "=v"(d) : "v"(a), "v"(b), "v"(c)); return d; }
DEV float add1(float a, float b) { float d; asm("v_add_f32 %0, %1, %2" : "=v"(d) : "v"(a), "v"(b)); return d; }
DEV f32x2 pkmul(f32x2 a, f32x2 b) { f32x2 d; asm("v_pk_mul_f32 %0, %1, %2" : "=v"(d) : "v"(a), "v"(b)); return d; }
DEV f32x2 cmul(f32x2 a, f32x2 b) { return (f32x2){fnma1(a.y, b.y, mul1(a.x, b.x)), fma1(a.y, b.x, mul1(a.x, b.y))}; }
template <int N> DEV void waitv() { asm volatile("s_waitcnt vmcnt(%0)" ::"n"(N) : "memory"); }
DEV void bar() { asm volatile("s_barrier" ::: "memory"); }
DEV unsigned lds_rd32(unsigned a) { unsigned v; asm volatile("ds_read_b32 %0, %1\n\ts_waitcnt lgkmcnt(0)" : "=v"(v) : "v"(a) : "memory"); return v; }
DEV u32x2 lds_rd64(unsigned a) { u32x2 v; asm volatile("ds_read_b64 %0, %1\n\ts_waitcnt lgkmcnt(0)" : "=v"(v) : "v"(a) : "memory"); return v; }
DEV u32x4 lds_rd128(unsigned a) { u32x4 v; asm volatile("ds_read_b128 %0, %1\n\ts_waitcnt lgkmcnt(0)" : "=v"(v) : "v"(a) : "memory"); return v; }
DEV void lds_wr32(unsigned a, unsigned v) { asm volatile("ds_write_b32 %0, %1" ::"v"(a), "v"(v) : "memory"); }
DEV void lds_wait_ge(unsigned a, unsigned need) {
#pragma unroll 1
  for (int it = 0; it < SPIN_MAX * 4; ++it) { if (__builtin_amdgcn_readfirstlane(lds_rd32(a)) >= need) break; __builtin_amdgcn_s_sleep(1); }
}
DEV void lds_wait2(unsigned a) {
#pragma unroll 1
  for (int it = 0; it < SPIN_MAX * 4; ++it) { const u32x2 v = lds_rd64(a); if (__builtin_amdgcn_readfirstlane(v.x & v.y)) break; __builtin_amdgcn_s_sleep(1); }
}
DEV void lds_wait0(unsigned a) {
#pragma unroll 1
  for (int it = 0; it < SPIN_MAX * 4; ++it) { if (__builtin_amdgcn_readfirstlane(lds_rd32(a)) == 0u) break; __builtin_amdgcn_s_sleep(XSLP); }
}
typedef __amdgpu_buffer_rsrc_t rsrc_t;
DEV void mark(rsrc_t rz, unsigned a) {
  if ((threadIdx.x & 63) == 0) __builtin_amdgcn_raw_ptr_buffer_load_lds(rz, (LAS void*)(size_t)a, 4, 0, 0, 0, 0);
}
DEV u32x4 ld128(rsrc_t r, unsigned vo, unsigned so = 0) { return __builtin_amdgcn_raw_buffer_load_b128(r, vo, so, 0); }
#define FRESH() asm volatile("" ::: "memory")
DEV u32x4 ld128c(rsrc_t r, unsigned vo) { return __builtin_amdgcn_raw_buffer_load_b128(r, vo, 0, 16); }
DEV void st128c(rsrc_t r, unsigned vo, u32x4 v) {
  asm volatile("s_nop 4\n\tbuffer_store_dwordx4 %0, %1, %2, 0 offen sc1\n\ts_nop 7\n\ts_nop 7" ::"v"(v), "v"(vo), "s"(r) : "memory");
}
DEV void st64c(rsrc_t r, unsigned vo, u32x2 v) { __builtin_amdgcn_raw_buffer_store_b64(v, r, vo, 0, 16); }
DEV void ld8(u32x4 (&w)[8], rsrc_t r, unsigned vo, unsigned so, unsigned step) {
  asm volatile("s_nop 4\n\tbuffer_load_dwordx4 %0, %9, %10, %8 offen\n\ts_add_u32 %8, %8, %11\n\tbuffer_load_dwordx4 %1, %9, %10, %8 offen\n\ts_add_u32 %8, %8, %11\n\t"
               "buffer_load_dwordx4 %2, %9, %10, %8 offen\n\ts_add_u32 %8, %8, %11\n\tbuffer_load_dwordx4 %3, %9, %10, %8 offen\n\ts_add_u32 %8, %8, %11\n\t"
               "buffer_load_dwordx4 %4, %9, %10, %8 offen\n\ts_add_u32 %8, %8, %11\n\tbuffer_load_dwordx4 %5, %9, %10, %8 offen\n\ts_add_u32 %8, %8, %11\n\t"
               "buffer_load_dwordx4 %6, %9, %10, %8 offen\n\ts_add_u32 %8, %8, %11\n\tbuffer_load_dwordx4 %7, %9, %10, %8 offen"
               : "=&v"(w[0]), "=&v"(w[1]), "=&v"(w[2]), "=&v"(w[3]), "=&v"(w[4]), "=&v"(w[5]), "=&v"(w[6]), "=&v"(w[7]), "+&s"(so)
               : "v"(vo), "s"(r), "s"(step) : "memory", "scc");
}
DEV void ld8c(u32x4 (&w)[8], rsrc_t r, unsigned vo, unsigned so, unsigned step, int a, int b) {
  asm volatile("s_nop 4\n\ts_cmp_eq_u32 %12, %13\n\ts_cbranch_scc0 Lskip_%=\n\t"
               "buffer_load_dwordx4 %0, %9, %10, %8 offen" WNT "\n\ts_add_u32 %8, %8, %11\n\tbuffer_load_dwordx4 %1, %9, %10, %8 offen" WNT "\n\ts_add_u32 %8, %8, %11\n\t"
               "buffer_load_dwordx4 %2, %9, %10, %8 offen" WNT "\n\ts_add_u32 %8, %8, %11\n\tbuffer_load_dwordx4 %3, %9, %10, %8 offen" WNT "\n\ts_add_u32 %8, %8, %11\n\t"
               "buffer_load_dwordx4 %4, %9, %10, %8 offen" WNT "\n\ts_add_u32 %8, %8, %11\n\tbuffer_load_dwordx4 %5, %9, %10, %8 offen" WNT "\n\ts_add_u32 %8, %8, %11\n\t"
               "buffer_load_dwordx4 %6, %9, %10, %8 offen" WNT "\n\ts_add_u32 %8, %8, %11\n\tbuffer_load_dwordx4 %7, %9, %10, %8 offen" WNT "\n\tLskip_%=:"
               : "+&v"(w[0]), "+&v"(w[1]), "+&v"(w[2]), "+&v"(w[3]), "+&v"(w[4]), "+&v"(w[5]), "+&v"(w[6]), "+&v"(w[7]), "+&s"(so)
               : "v"(vo), "s"(r), "s"(step), "s"(a), "n"(b) : "memory", "scc");
}
DEV rsrc_t mk(const void* p, unsigned bytes) { return __builtin_amdgcn_make_buffer_rsrc((void*)p, 0, bytes, 0x00020000); }

DEV unsigned wave_maxu(unsigned v) {
  unsigned r;
  asm("s_nop 1\n\tv_max_u32_dpp %1, %1, %1 row_shr:1 row_mask:0xf bank_mask:0xf\n\ts_nop 1\n\tv_max_u32_dpp %1, %1, %1 row_shr:2 row_mask:0xf bank_mask:0xf\n\t"
      "s_nop 1\n\tv_max_u32_dpp %1, %1, %1 row_shr:4 row_mask:0xf bank_mask:0xf\n\ts_nop 1\n\tv_max_u32_dpp %1, %1, %1 row_shr:8 row_mask:0xf bank_mask:0xf\n\t"
      "s_nop 1\n\tv_max_u32_dpp %1, %1, %1 row_bcast:15 row_mask:0xa bank_mask:0xf\n\ts_nop 1\n\tv_max_u32_dpp %1, %1, %1 row_bcast:31 row_mask:0xc bank_mask:0xf\n\t"
      "s_nop 0\n\tv_readlane_b32 %0, %1, 63\n\ts_nop 1" : "=s"(r), "+v"(v));
  return r;
}
DEV float sum4(const float (&a)[4]) {
  auto p = __builtin_amdgcn_permlane16_swap(__float_as_uint(a[0]), __float_as_uint(a[1]), false, false);
  auto q = __builtin_amdgcn_permlane16_swap(__float_as_uint(a[2]), __float_as_uint(a[3]), false, false);
  const float u = __uint_as_float(p[0]) + __uint_as_float(p[1]), w = __uint_as_float(q[0]) + __uint_as_float(q[1]);
  auto r = __builtin_amdgcn_permlane32_swap(__float_as_uint(u), __float_as_uint(w), false, false);
  float v = __uint_as_float(r[0]) + __uint_as_float(r[1]);
  v += __uint_as_float((unsigned)__builtin_amdgcn_update_dpp(0, (int)__float_as_uint(v), 0x128, 0xf, 0xf, false));
  v += __uint_as_float((unsigned)__builtin_amdgcn_update_dpp(0, (int)__float_as_uint(v), 0x124, 0xf, 0xf, false));
  v += __uint_as_float((unsigned)__builtin_amdgcn_update_dpp(0, (int)__float_as_uint(v), 0x122, 0xf, 0xf, false));
  v += __uint_as_float((unsigned)__builtin_amdgcn_update_dpp(0, (int)__float_as_uint(v), 0x121, 0xf, 0xf, false));
  return v;
}
DEV float dot2(unsigned a, unsigned b, float c) { return __builtin_amdgcn_fdot2_f32_bf16(__builtin_bit_cast(bf16x2_t, a), __builtin_bit_cast(bf16x2_t, b), c, false); }
DEV unsigned route_code(unsigned u) { return (u & 0x8000u) ? (~u & 0xffffu) : (u | 0x8000u); }
DEV unsigned med3u(unsigned a, unsigned b, unsigned c) { unsigned d; asm("v_med3_u32 %0, %1, %2, %3" : "=v"(d) : "v"(a), "v"(b), "v"(c)); return d; }
DEV float key_val(unsigned key) {
  unsigned u = key >> 16;
  u = (u & 0x8000u) ? (u & 0x7fffu) : (~u & 0xffffu);
  return __uint_as_float(u << 16);
}
#define STAMP(k)
#define STAMPS()

DEV void unit_mma(const u32x4 (&w)[8], bf16x8 a, f32x4 (&d)[8]) {
#pragma unroll
  for (int j = 0; j < 8; ++j) {
    u32x4 b;
#pragma unroll
    for (int t = 0; t < 4; ++t) b[t] = __builtin_amdgcn_perm(w[2 * t + 1][j >> 1], w[2 * t][j >> 1], (j & 1) ? 0x07060302u : 0x05040100u);
    d[j] = __builtin_amdgcn_mfma_f32_16x16x32_bf16(a, __builtin_bit_cast(bf16x8, b), d[j], 0, 0, 0);
  }
}

DEV void fold(const f32x4 (&d)[8], f32x4 (&o)[2]) {
#pragma unroll
  for (int r = 0; r < 2; ++r)
#pragma unroll
    for (int q = 0; q < 4; ++q) {
      const float a0 = d[q][r], a1 = d[q][r + 2], b0 = d[q + 4][r], b1 = d[q + 4][r + 2];
      auto a = __builtin_amdgcn_permlane32_swap(__float_as_uint(a0), __float_as_uint(a1), false, false);
      auto b = __builtin_amdgcn_permlane32_swap(__float_as_uint(b0), __float_as_uint(b1), false, false);
      float t;
      asm("s_nop 1\n\tv_add_f32_dpp %0, %1, %2 row_ror:8 row_mask:0xf bank_mask:0x3\n\tv_add_f32_dpp %0, %3, %4 row_ror:8 row_mask:0xf bank_mask:0xc"
          : "=&v"(t) : "v"(a[1]), "v"(a[0]), "v"(b[0]), "v"(b[1]));
      o[r][q] = t;
    }
}
DEV void st2x128c(rsrc_t r, unsigned vo, unsigned so, const f32x4 (&o)[2]) {
  asm volatile("s_nop 4\n\tbuffer_store_dwordx4 %0, %2, %3, 0 offen sc1\n\tbuffer_store_dwordx4 %1, %2, %3, %4 offen sc1\n\ts_nop 7\n\ts_nop 7"
               ::"v"(o[0]), "v"(o[1]), "v"(vo), "s"(r), "s"(so) : "memory");
}
DEV void poll32(rsrc_t r, unsigned vo, unsigned tg, bool on) {
#pragma unroll 1
  for (int it = 0; it < SPIN_MAX; ++it) {
    FRESH();
    const unsigned v = __builtin_amdgcn_raw_buffer_load_b32(r, vo, 0, 16);
    if (__all(!on || v == tg)) break;
    __builtin_amdgcn_s_sleep(16);
  }
}

DEV void count1(rsrc_t r, unsigned so) {
  asm volatile("s_nop 4\n\ts_waitcnt vmcnt(0)\n\ts_mov_b64 exec, 1\n\tbuffer_atomic_add %0, off, %1, %2 sc1\n\ts_mov_b64 exec, -1" ::"v"(1u), "s"(r), "s"(so) : "memory");
}
DEV void count_wait(rsrc_t r, unsigned so, unsigned n) {
#pragma unroll 1
  for (int it = 0; it < SPIN_MAX; ++it) {
    unsigned v = 0;
    asm volatile("s_nop 4\n\ts_mov_b64 exec, 1\n\tbuffer_atomic_or %0, off, %1, %2 sc0 sc1\n\ts_mov_b64 exec, -1\n\ts_waitcnt vmcnt(0)" : "+v"(v) : "s"(r), "s"(so) : "memory");
    if (__builtin_amdgcn_readfirstlane(v) == n) break;
    __builtin_amdgcn_s_sleep(CWS);
  }
}

typedef __attribute__((ext_vector_type(8))) u64 u64x8;
__global__ __launch_bounds__(512, 2) void fused(const u16*, const u16*, const u16*, const u16*, const u16*, const u16*, const u16*, const u16*, const u16*,
                                                const u16*, const u16*, const u16*, const u16*, const u16*, char*, float*) {
#if __HIP_DEVICE_COMPILE__
  extern __shared__ __attribute__((aligned(16))) char smem_[];
  LAS char* smem = (LAS char*)smem_;
#define ARGREQ(W) u64x8 ka, kb; \
  asm volatile("s_load_dwordx16 %0, %2, 0x0\n\ts_load_dwordx16 %1, %2, 0x40" W : "=&s"(ka), "=&s"(kb) : "s"(__builtin_amdgcn_kernarg_segment_ptr()) : "memory")
#define ARGS() \
  const u16 *x = (const u16*)ka[0], *Lre = (const u16*)ka[1], *Lim = (const u16*)ka[2], *Bre = (const u16*)ka[3], *Bim = (const u16*)ka[4], *Cre = (const u16*)ka[5], \
            *Cim = (const u16*)ka[6], *logdt = (const u16*)ka[7], *Dskip = (const u16*)kb[0], *w_r = (const u16*)kb[1], *w_gate = (const u16*)kb[2], \
            *w_up = (const u16*)kb[3], *w_down = (const u16*)kb[4], *gate_w = (const u16*)kb[5]; \
  char* ws = (char*)kb[6]; float* out = (float*)kb[7]; \
  const rsrc_t rws = mk(ws, W_END), rx = mk(x, 2048 * 1024 * 2), rz = mk(x, 0)
  STAMP(0);
  int tid = threadIdx.x, bid = blockIdx.x;
  {
    unsigned t_[8], u_[2];
    asm volatile(ASM_EN : [w] "=&s"(t_[0]), [j] "=&s"(t_[1]), [t] "=&s"(t_[2]), [sa] "=&s"(t_[3]), [sc] "=&s"(t_[4]), [m] "=&s"(t_[5]), [r] "=&s"(t_[6]), [c] "=&s"(t_[7]), [v0] "=&v"(u_[0]), [v1] "=&v"(u_[1]) : [kp] "s"(__builtin_amdgcn_kernarg_segment_ptr()), [tid] "v"(threadIdx.x), [bid] "s"(blockIdx.x), [cpf] "n"(CODEPF), [ms0] "n"(M_S0) : "memory", "scc", "m0", "s20", "s21", "s22", "s23", "v255");
    asm volatile("" : "+s"(bid)); asm volatile("" : "+v"(tid));
  }
  const int lane = tid & 63, wid = __builtin_amdgcn_readfirstlane(tid >> 6), xcd = bid & 7, loc = bid >> 3;
  const int c = (xcd >> 1) * 4 + (loc >> 3), j = (xcd & 1) * 8 + (loc & 7);
  const int fr = lane & 15, fq = lane >> 4;

  if (__builtin_expect(wid == 4 || wid == 5, 1)) {
    ARGREQ("");
    const int l = wid - 4;
    __builtin_amdgcn_s_setprio(LPRIO);
    const int l3 = lane >> 3;
    {
      const unsigned one = tid >= 272 ? 1u : 0u;
      asm volatile("ds_write_b128 %0, %1" ::"v"(L_FLG + min(tid - 256, 111) * 16), "v"((u32x4){one, one, one, one}) : "memory");
    }
    int bud = SPIN_MAX * 4;
    unsigned dst = 1 ? l * 49152 : 131072 - l * 114688, ci = M_CONS - 64 + 1 * 16, t_s0, vc = 0;
    unsigned b0 = (c * 128 + l * (j * 64 - c * 128) + l3 * (8 - 7 * l)) * 2048 + ((lane & 7) ^ l3) * 16, wrap = 163840 - l * 32768;
    unsigned so[8];
#pragma unroll
    for (int q = 0; q < 8; ++q) so[q] = (64 * (q & 1) + (q >> 1) + l * (8 * q - 64 * (q & 1) - (q >> 1))) * 2048;
    unsigned sk = (((xcd >> 1) + 2 * (xcd & 1)) & 3) * 512 + 1 * 128, n = 16 - 1, mka = M_LAND + 1 * 8 + l * 4, v0 = b0 + sk;
    asm volatile("" : "+s"(dst), "+v"(b0), "+s"(wrap), "+s"(so[1]), "+s"(so[2]), "+s"(so[3]), "+s"(so[4]), "+s"(so[5]), "+s"(so[6]), "+s"(so[7]),
                 "+s"(sk), "+s"(mka), "+v"(v0));
    asm volatile("s_waitcnt lgkmcnt(0)" : "+s"(ka), "+s"(kb)::"memory");
    ARGS();
    {
      const rsrc_t r0 = mk(l ? Bre : x, (1u << 22) >> l), r1 = mk(l ? Bim : x + 4096, l ? (1u << 21) : (1u << 22) - 8192);
      STAMP(8);
      asm volatile(ASM_LA : [dst] "+&s"(dst), [sk] "+&s"(sk), [ci] "+&s"(ci), [n] "+&s"(n), [mk] "+&s"(mka), [bud] "+&s"(bud), [s0] "=&s"(t_s0), [v0] "+&v"(v0), [vc] "+&v"(vc) : [r0] "s"(r0), [r1] "s"(r1), [rz] "s"(rz), [wrap] "s"(wrap), [b0] "v"(b0), [vw] "v"(M_WR), [so0] "s"(so[0]), [so1] "s"(so[1]), [so2] "s"(so[2]), [so3] "s"(so[3]), [so4] "s"(so[4]), [so5] "s"(so[5]), [so6] "s"(so[6]), [so7] "s"(so[7]) : "memory", "scc", "m0", "v24", "v25", "v26", "v27", "v28", "v29", "v30", "v31");
    }
    STAMP(1);
    {
      const rsrc_t r0 = mk(Cre, 1 << 21), r1 = mk(Cim, 1 << 21);
      const unsigned b0 = (j * 64 + 4 * l3) * 2048 + (((lane & 7) ^ (l3 >> 1)) * 16), b1 = b0 ^ 64, wrap = 147456, cth = M_CONS + (15 - l) * 16;
      unsigned so[8];
#pragma unroll
      for (int q = 0; q < 8; ++q) so[q] = (32 * (q & 1) + (q >> 1)) * 2048;
      unsigned sk = l * 128, hi = (j & 8) * 128, n = 8, mka = M_LAND + (17 + l) * 8, v0 = b0 + sk + hi, v1 = b1 + sk + hi, t_s1;
      const rsrc_t rp = mk(l ? Cim : Cre, 1 << 21);
      const unsigned p0 = b0 + (hi ^ 0x400);
      unsigned vd = l * 8192 + lane * 16;
      asm volatile(ASM_LB : [dst] "+&s"(dst), [sk] "+&s"(sk), [ci] "+&s"(ci), [n] "+&s"(n), [mk] "+&s"(mka), [bud] "+&s"(bud), [s0] "=&s"(t_s0), [v0] "+&v"(v0), [vc] "+&v"(vc), [v1] "+&v"(v1), [hi] "+&s"(hi), [s1] "=&s"(t_s1), [vd] "+&v"(vd) : [r0] "s"(r0), [r1] "s"(r1), [rz] "s"(rz), [wrap] "s"(wrap), [b0] "v"(b0), [vw] "v"(M_WR + 16), [cth] "s"(cth), [b1] "v"(b1), [nend] "s"(3 + l), [rp] "s"(rp), [p0] "v"(p0), [p1] "v"(p0 ^ 64), [vm] "v"(M_CONS + 256), [vl] "v"(M_LAND + 26 * 8 + 4 * l), [so0] "s"(so[0]), [so1] "s"(so[1]), [so2] "s"(so[2]), [so3] "s"(so[3]), [so4] "s"(so[4]), [so5] "s"(so[5]), [so6] "s"(so[6]), [so7] "s"(so[7]) : "memory", "scc", "m0", "v24", "v25", "v26", "v27", "v28", "v29", "v30", "v31", "v32", "v33", "v34", "v35", "v36", "v37", "v38", "v39", "v40", "v41", "v42", "v43", "v44", "v45", "v46", "v47", "v48", "v49", "v50", "v51", "v52", "v53", "v54", "v55", "v56", "v57", "v58", "v59", "v60", "v61", "v62", "v63", "v64", "v65", "v66", "v67", "v68", "v69", "v70", "v71", "v72", "v73", "v74", "v75", "v76", "v77", "v78", "v79", "v80", "v81", "v82", "v83", "v84", "v85", "v86", "v87", "v88", "v89", "v90", "v91", "v92", "v93", "v94", "v95", "v96", "v97", "v98", "v99", "v100", "v101", "v102", "v103", "v104", "v105", "v106", "v107", "v108", "v109", "v110", "v111", "v112", "v113", "v114", "v115", "v116", "v117", "v118", "v119", "v120", "v121", "v122", "v123", "v124", "v125", "v126", "v127", "v128", "v129", "v130", "v131", "v132", "v133", "v134", "v135", "v136", "v137", "v138", "v139", "v140", "v141", "v142", "v143", "v144", "v145", "v146", "v147", "v148", "v149", "v150", "v151", "v152", "v153", "v154", "v155", "v156", "v157", "v158", "v159", "v160", "v161", "v162", "v163", "v164", "v165", "v166", "v167", "v168", "v169", "v170", "v171", "v172", "v173", "v174", "v175", "v176", "v177", "v178", "v179", "v180", "v181", "v182", "v183", "v184", "v185", "v186", "v187", "v188", "v189", "v190", "v191", "v192", "v193", "v194", "v195", "v196", "v197", "v198", "v199", "v200", "v201", "v202", "v203", "v204", "v205", "v206", "v207", "v208", "v209", "v210", "v211", "v212", "v213", "v214", "v215", "v216", "v217", "v218", "v219", "v220", "v221", "v222", "v223", "v224", "v225", "v226", "v227", "v228", "v229", "v230", "v231", "v232", "v233", "v234", "v235", "v236", "v237", "v238", "v239", "v240", "v241", "v242", "v243", "v244", "v245", "v246", "v247", "v248", "v249", "v250", "v251", "v252", "v253", "v254", "v255");
    }
    waitv<0>();
    STAMP(3);
    STAMPS();
    __builtin_amdgcn_endpgm();
  }

  if (__builtin_expect(wid >= 6, 1)) {
    const int xw = wid - 6;
    __builtin_amdgcn_s_setprio(XPRIO0);
    ARGREQ("\n\ts_waitcnt lgkmcnt(0)");
    ARGS();
    const int row0 = c * 128 + j * 8 + xw * 4;
    const int l = xw;
    const bool ffn = !NOFFN && loc < 28;
    const int sg = xcd * 7 + (loc >> 2), kq = loc & 3;
    u32x4 w1[4][8], w2[2][8];
    unsigned tagA, tagB;
    const int sb = HELP ? (3 - (int)((size_t)w_up >> 7)) & 7 : 9, s1 = ffn ? sg : 8 * xcd + sb;
    const bool gu = ffn ? !(xw && (sg & 7) == sb) : HELP && !NOFFN && xw && xcd < 7;
    const int pg = ffn ? WGRP : 0, grp = __builtin_amdgcn_readfirstlane(gu ? pg : 9);
    auto wburst = [&](int p, unsigned gate) __attribute__((always_inline)) {
      const bool on = grp == p;
      const rsrc_t r1 = mk(xw ? w_up : w_gate, D_ * F_ * 2);
      const unsigned o1 = (kq * 256 + 32 * (fr >> 3) + 8 * fq) * (F_ * 2) + (s1 * 64 + 8 * (fr & 7)) * 2;
      if (pg == p) {
        lds_wait0(M_LAND + gate * 8);
        if (on) mark(rz, M_WR + xw * 4);
        __builtin_amdgcn_s_sleep(WSLP);
      }
#pragma unroll
      for (int u = 0; u < 4; ++u) {
        ld8c(w1[u], r1, o1, u * 64 * (F_ * 2), F_ * 2, grp, p);
        if (u == WRES && on) mark(rz, M_WR + 8 + xw * 4);
      }
      if (pg == p)
        __builtin_amdgcn_raw_ptr_buffer_load_lds(mk(xw ? Cim : Cre, 1 << 21), (LAS void*)(smem + L_JUNK + 256 + xw * 256), 4, (j * 64 + lane) * 2048 + ((c + lane) & 15) * 128, 0, 0, 0);
    };
    {
      unsigned ep;
      u32x4 xv[8], wv[2], gv[2];
      unsigned pl[3];
      const rsrc_t rw = mk(w_r, 2048), rg = mk(gate_w, 2048);
      const rsrc_t rp0 = mk(Lre, 2048), rp1 = mk(Lim, 2048), rp2 = mk(logdt, 2048);
      const unsigned zero = 0, l16 = lane * 16, xo = row0 * 2048 + lane * 16, po = (j * 64 + lane) * 2;
      if (XG < -1) { asm volatile("" ::"s"(rg), "s"(rp0), "s"(rp1), "s"(rp2), "s"(rx), "v"(xo), "v"(po)); bar(); }
      asm volatile("s_nop 4\n\tbuffer_load_dword %0, %3, %5, 0 offen sc1\n\tbuffer_load_dwordx4 %1, %4, %6, 0 offen\n\tbuffer_load_dwordx4 %2, %4, %6, 0 offen offset:1024"
                   : "=&v"(ep), "=&v"(wv[0]), "=&v"(wv[1]) : "v"(zero), "v"(l16), "s"(rws), "s"(rw) : "memory");
      asm volatile("s_nop 4\n\tbuffer_load_dwordx4 %3, %9, %10, 0 offen\n\tbuffer_load_dwordx4 %4, %9, %10, 0 offen offset:1024\n\t"
                   "buffer_load_ushort %0, %5, %6, 0 offen\n\tbuffer_load_ushort %1, %5, %7, 0 offen\n\tbuffer_load_ushort %2, %5, %8, 0 offen"
                   : "=&v"(pl[0]), "=&v"(pl[1]), "=&v"(pl[2]), "=&v"(gv[0]), "=&v"(gv[1]) : "v"(po), "s"(rp0), "s"(rp1), "s"(rp2), "v"(l16), "s"(rg) : "memory");
      asm volatile("s_nop 4\n\tbuffer_atomic_swap %0, %1, %2, 0 offen sc1" ::"v"(zero), "v"(tid == 384 && bid < 160 ? W_HF + bid * 128 : 0xf0000000u), "s"(rws) : "memory");
      if (XG >= -1) bar();
      if (XG >= 0) lds_wait0(M_LAND + XG * 8);
      ld8(xv, rx, xo, 0, 1024);
      asm volatile("s_waitcnt vmcnt(0)" : "+v"(ep), "+v"(xv[0]), "+v"(xv[1]), "+v"(xv[2]), "+v"(xv[3]), "+v"(xv[4]), "+v"(xv[5]), "+v"(xv[6]),
                   "+v"(xv[7]), "+v"(wv[0]), "+v"(wv[1]), "+v"(gv[0]), "+v"(gv[1]), "+v"(pl[0]), "+v"(pl[1]), "+v"(pl[2])::"memory");
      tagA = ep + 1u; tagB = ep + 0x40000001u;
      float sr[4];
#pragma unroll
      for (int r = 0; r < 4; ++r) {
        sr[r] = 0.f;
#pragma unroll
        for (int h = 0; h < 2; ++h)
#pragma unroll
          for (int e = 0; e < 4; ++e) { const unsigned xu = xv[2 * r + h][e], wu = wv[h][e]; sr[r] = dot2(xu, wu, sr[r]); }
      }
      {
        const unsigned rb = route_code(f2bf(sum4(sr)));
        const unsigned r01 = (unsigned)__builtin_amdgcn_readlane((int)rb, 0) | ((unsigned)__builtin_amdgcn_readlane((int)rb, 16) << 16);
        const unsigned r23 = (unsigned)__builtin_amdgcn_readlane((int)rb, 32) | ((unsigned)__builtin_amdgcn_readlane((int)rb, 48) << 16);
        st128c(rws, lane == 0 ? W_R2 + row0 * 4 : 0xf0000000u, (u32x4){r01, tagA, r23, tagB});
      }
#pragma unroll
      for (int r = 0; r < 4; ++r) {
        sr[r] = 0.f;
#pragma unroll
        for (int h = 0; h < 2; ++h)
#pragma unroll
          for (int e = 0; e < 4; ++e) {
            const unsigned xu = xv[2 * r + h][e], gu = gv[h][e];
            sr[r] = dot2(xu, gu, sr[r]);
          }
      }
      {
        const float z = rbf(sum4(sr));
        st64c(rws, fr == 0 ? W_RG + (row0 + fq) * 8 : 0xf0000000u, (u32x2){__float_as_uint(rbf(1.f / (1.f + __expf(-z)))), tagB});
      }
      STAMP(8);
      __builtin_amdgcn_s_setprio(XPRIO);
#pragma unroll
      for (int u = 0; u < 4; ++u) asm volatile("" : "=v"(w1[u][0]), "=v"(w1[u][1]), "=v"(w1[u][2]), "=v"(w1[u][3]), "=v"(w1[u][4]), "=v"(w1[u][5]), "=v"(w1[u][6]), "=v"(w1[u][7]));
#pragma unroll
      for (int u = 0; u < 2; ++u) asm volatile("" : "=v"(w2[u][0]), "=v"(w2[u][1]), "=v"(w2[u][2]), "=v"(w2[u][3]), "=v"(w2[u][4]), "=v"(w2[u][5]), "=v"(w2[u][6]), "=v"(w2[u][7]));
      wburst(0, WS0);
      if (xw == 1) {
        const float lre = bf2f(pl[0]), lim = bf2f(pl[1]);
        const float dt = rbf(__expf(bf2f(pl[2])));
        const float er = expf(dt * lre);
        float sn, cs;
        sincosf(dt * lim, &sn, &cs);
        const f32x2 l1 = {mul1(er, cs), mul1(er, sn)};
        const float nr = l1.x - 1.f, ni = l1.y, den = fma1(lre, lre, mul1(lim, lim));
        const f32x2 cf = {fma1(nr, lre, mul1(ni, lim)) / den, fnma1(nr, lim, mul1(ni, lre)) / den};
        const f32x2 l2 = cmul(l1, l1), l4 = cmul(l2, l2), l8 = cmul(l4, l4), l16_ = cmul(l8, l8), l32 = cmul(l16_, l16_), l64 = cmul(l32, l32),
                    l128 = cmul(l64, l64), l256 = cmul(l128, l128), l384 = cmul(l256, l128), l512 = cmul(l256, l256);
        auto put = [&](int e, f32x2 v) { const float re = v.x, im = v.y; asm volatile("ds_write2_b32 %0, %1, %2 offset0:0 offset1:64" ::"v"(L_TAB + e * 512 + lane * 4), "v"(re), "v"(im) : "memory"); };
        put(0, l1); put(1, cf); put(2, l8); put(3, l16_); put(4, l32); put(5, l64); put(6, l512); put(7, (f32x2){1.f, 0.f}); put(8, l128); put(9, l256); put(10, l384);
        lds_wr32(M_LAND + 16 * 8, 0u);
      }
    }
    unsigned m = 0, m2 = 0, m3 = 0, m4 = 0;
    auto gkey = [&](unsigned k) { return (k & 0xffff0000u) | ((k & 28u) << 6) | (k & 3u) | (unsigned)(252 - lane * 4); };
    lds_wait0(M_LAND + (pg == 0 ? TKG0 : TKG) * 8);
    if (xw == 1) {
#pragma unroll
      for (int h = 0; h < 2; ++h) {
        poll32(rws, W_RG + (c * 128 + h * 64 + lane) * 8 + 4, tagB, true);
        __builtin_amdgcn_raw_ptr_buffer_load_lds(rws, (LAS void*)(smem + L_G + h * 256), 4, W_RG + (c * 128 + h * 64 + lane) * 8, 0, 0, 16);
      }
      mark(rz, M_GT);
      STAMP(1);
    } else {
      {
        u32x4 g[8];
#pragma unroll 1
        for (int it = 0; it < SPIN_MAX; ++it) {
          FRESH();
#pragma unroll
          for (int q = 0; q < 8; ++q) g[q] = ld128c(rws, W_R2 + lane * 16 + q * 1024);
          bool ok = true;
#pragma unroll
          for (int q = 0; q < 8; ++q) ok = ok && g[q].y == tagA && g[q].w == tagB;
          if (__all(ok)) break;
          __builtin_amdgcn_s_sleep(16);
        }
        mark(rz, M_LAND + 16 * 8 + 4);
        STAMP(1);
        auto lkey = [&](int i) { const unsigned w = g[i >> 2][i & 2]; return ((i & 1) ? (w & 0xffff0000u) : (w << 16)) | (unsigned)(31 - i); };
        m = lkey(0);
#pragma unroll
        for (int i = 1; i < 32; ++i) {
          const unsigned k = lkey(i);
          m4 = med3u(m3, m4, k); m3 = med3u(m2, m3, k); m2 = med3u(m, m2, k); m = max(m, k);
        }
      }
      m = gkey(m); m2 = gkey(m2); m3 = gkey(m3); m4 = gkey(m4);
    }
    asm volatile("" : "+v"(m), "+v"(m2), "+v"(m3), "+v"(m4));
    wburst(1, WS1);
    if (xw == 0) {
      unsigned mine = 0;
#pragma unroll 1
      for (int s = 0; s < 8; ++s) {
        const unsigned mm = wave_maxu(m);
        mine = (lane >> 3) == s ? mm : mine;
        const bool hit = m == mm;
        m = hit ? m2 : m; m2 = hit ? m3 : m2; m3 = hit ? m4 : m3; m4 = hit ? 0u : m4;
      }
      if (__ballot(m == 0u)) {
        unsigned lim = 0xffffffffu;
#pragma unroll 1
        for (int s = 0; s < 8; ++s) {
          unsigned mm = 0;
#pragma unroll 1
          for (int q = 0; q < 8; ++q) {
            const u32x4 w = ld128c(rws, W_R2 + lane * 16 + q * 1024);
#pragma unroll
            for (int e = 0; e < 4; ++e) {
              const unsigned k = gkey(((e & 1) ? (w[e & 2] & 0xffff0000u) : (w[e & 2] << 16)) | (unsigned)(31 - 4 * q - e));
              mm = max(mm, k < lim || s == 0 ? k : 0u);
            }
          }
          lim = wave_maxu(mm);
          mine = (lane >> 3) == s ? lim : mine;
        }
      }
      const unsigned myidx = 2047u - (mine & 0xffffu);
      {
#pragma unroll
        for (int u = 0; u < 4; ++u)
          __builtin_amdgcn_raw_ptr_buffer_load_lds(rx, (LAS void*)(smem + L_XS + u * 1024), 16, myidx * 2048 + (loc & 3) * 512 + (lane & 7) * 16, u * 128, 0, 0);
      }
      const float myrs = rbf(1.f / (1.f + __expf(-key_val(mine))));
      if ((lane & 7) == 0) asm volatile("ds_write_b64 %0, %1\n\ts_waitcnt lgkmcnt(0)" ::"v"(L_SEL + lane), "v"((u32x2){myidx, __float_as_uint(myrs)}) : "memory");
      mark(rz, M_XS);
    }
    STAMP(2);
    __builtin_amdgcn_s_setprio(FPRIO);
    lds_wait0(M_XS);
    const u32x2 sel = lds_rd64(L_SEL + (lane & 7) * 8);
    const unsigned fa = ((fr & 7) * 8 + 4 * (fr >> 3) + fq) * 16, lp = (fr & 7) * 32 + (fr >> 3) * 16, tk = 4 * (fq & 1) + 2 * (fq >> 1);
    waitv<0>();
    STAMP(10);
    {
      if (0 && ffn) { lds_wait0(M_LAND + (W2G - 2) * 8); mark(rz, M_WR + 16 + xw * 4); }
      if (ffn) lds_wait0(M_LAND + W2G * 8);
      __builtin_amdgcn_s_setprio(3);
      const rsrc_t r2 = mk(w_down, D_ * F_ * 2);
      const unsigned o2 = (sg * 64 + 32 * (fr >> 3) + 8 * fq) * 2048 + (kq * 256 + xw * 128 + 8 * (fr & 7)) * 2;
      const int on = __builtin_amdgcn_readfirstlane(ffn ? 1 : 0);
#pragma unroll
      for (int u = 0; u < 2; ++u) {
        ld8c(w2[u], r2, o2, u * 128, 2048, on, 1);
        if (0 == u + 1 && ffn) mark(rz, M_WR + 24 + xw * 4);
      }
      __builtin_amdgcn_s_setprio(FPRIO);
    }
    f32x4 d[8], o[2];
    if (gu) {
      u32x4 xa[4];
      asm volatile("ds_read_b128 %0, %4\n\tds_read_b128 %1, %4 offset:1024\n\tds_read_b128 %2, %4 offset:2048\n\tds_read_b128 %3, %4 offset:3072\n\ts_waitcnt lgkmcnt(0)"
                   : "=&v"(xa[0]), "=&v"(xa[1]), "=&v"(xa[2]), "=&v"(xa[3]) : "v"(L_XS + fa) : "memory");
#pragma unroll
      for (int q = 0; q < 8; ++q) d[q] = (f32x4){0.f, 0.f, 0.f, 0.f};
#pragma unroll
      for (int u = 0; u < 4; ++u) {
        asm volatile("" : "+v"(w1[u][0]), "+v"(w1[u][1]), "+v"(w1[u][2]), "+v"(w1[u][3]), "+v"(w1[u][4]), "+v"(w1[u][5]), "+v"(w1[u][6]), "+v"(w1[u][7]));
        unit_mma(w1[u], __builtin_bit_cast(bf16x8, xa[u]), d);
        asm volatile("" : "+v"(d[0]), "+v"(d[1]), "+v"(d[2]), "+v"(d[3]), "+v"(d[4]), "+v"(d[5]), "+v"(d[6]), "+v"(d[7]));
      }
      fold(d, o);
      STAMP(11);
      st2x128c(rws, W_GU + ((s1 * 8 + tk) * 8 + kq * 2 + l) * 256 + lp, 2048, o);
      count1(rws, W_GF + s1 * 128);
    }
    STAMP(3);
    if (ffn) {
      {
        if (GUG) lds_wait0(M_LAND + GUG * 8);
        __builtin_amdgcn_s_setprio(GPRIO);
        count_wait(rws, W_GF + sg * 128, 8);
        f32x4 a[8];
#pragma unroll
        for (int q = 0; q < 8; ++q) a[q] = __builtin_bit_cast(f32x4, ld128c(rws, W_GU + (sg * 8 + 4 * l + fq) * 2048 + fr * 16 + q * 256));
        float a2[4];
#pragma unroll
        for (int e = 0; e < 4; ++e) {
          const float g = rbf(add1(add1(a[0][e], a[2][e]), add1(a[4][e], a[6][e]))), u = rbf(add1(add1(a[1][e], a[3][e]), add1(a[5][e], a[7][e])));
          a2[e] = mul1(rbf(g / add1(1.f, __expf(-g))), u);
        }
        asm volatile("ds_write_b64 %0, %1\n\ts_nop 3" ::"v"(L_ACT + (4 * l + fq) * 128 + fr * 8), "v"((u32x2){pk2(a2[0], a2[1]), pk2(a2[2], a2[3])}) : "memory");
        lds_wr32(FL_TK + l * 4, 1u);
        lds_wait_ge(FL_TK + (l ^ 1) * 4, 1u);
      }
      const u32x4 af = lds_rd128(L_ACT + fa);
      STAMP(4);
      waitv<0>();
#pragma unroll
      for (int u = 0; u < 2; ++u)
        asm volatile("" : "+v"(w2[u][0]), "+v"(w2[u][1]), "+v"(w2[u][2]), "+v"(w2[u][3]), "+v"(w2[u][4]), "+v"(w2[u][5]), "+v"(w2[u][6]), "+v"(w2[u][7]));
#pragma unroll
      for (int u = 0; u < 2; ++u) {
#pragma unroll
        for (int q = 0; q < 8; ++q) d[q] = (f32x4){0.f, 0.f, 0.f, 0.f};
        unit_mma(w2[u], __builtin_bit_cast(bf16x8, af), d);
        fold(d, o);
        st2x128c(rws, W_YP + ((tk * 16 + kq * 4 + l * 2 + u) * 56 + sg) * 256 + lp, 16 * 56 * 256, o);
      }
      count1(rws, W_YF + (kq * 2 + l) * 128);
      STAMP(5);
    }
    u64 rows = __ballot(lane < 8 && (sel.x >> 7) == (unsigned)c);
    if (l) rows &= rows - 1;
    if (!NOFFN && rows) {
      lds_wait0(M_CONS + (16 + YG) * 16);
      __builtin_amdgcn_s_setprio(YPRIO);
      count_wait(rws, W_YF + (j >> 1) * 128, 56);
      STAMP(9);
#pragma unroll 1
      for (; rows; rows &= rows - 1, rows &= rows - 1) {
        const int s = __builtin_ctzll(rows);
        const int t = __builtin_amdgcn_readlane(sel.x, s);
        const float rs = __uint_as_float(__builtin_amdgcn_readlane(sel.y, s));
        f32x4 a[14];
#pragma unroll
        for (int k = 0; k < 14; ++k) a[k] = __builtin_bit_cast(f32x4, ld128c(rws, W_YP + (s * 16 + j) * (56 * 256) + lane * 16 + k * 1024));
        const float xf = bf2f(__builtin_amdgcn_raw_buffer_load_b16(rx, t * 2048 + j * 128 + fr * 8 + fq * 2, 0, 0));
        const float omg = rbf(1.f - __uint_as_float(lds_rd32(L_G + (t & 127) * 4)));
        float l0 = a[0][0], l1 = a[0][1], h0 = a[0][2], h1 = a[0][3];
#pragma unroll
        for (int k = 1; k < 14; ++k) { l0 = add1(l0, a[k][0]); l1 = add1(l1, a[k][1]); h0 = add1(h0, a[k][2]); h1 = add1(h1, a[k][3]); }
        asm volatile("s_nop 1" : "+v"(l0), "+v"(l1), "+v"(h0), "+v"(h1));
        auto p = __builtin_amdgcn_permlane16_swap(__float_as_uint(l0), __float_as_uint(l1), false, false);
        auto q = __builtin_amdgcn_permlane16_swap(__float_as_uint(h0), __float_as_uint(h1), false, false);
        const float u = __uint_as_float(p[0]) + __uint_as_float(p[1]), w = __uint_as_float(q[0]) + __uint_as_float(q[1]);
        auto r = __builtin_amdgcn_permlane32_swap(__float_as_uint(u), __float_as_uint(w), false, false);
        const float y = __uint_as_float(r[0]) + __uint_as_float(r[1]);
        lds_wr32(L_BROW + s * 256 + fr * 16 + fq * 4, __float_as_uint(mul1(omg, rbf(xf + rbf(mul1(rbf(y), rs))) - xf)));
      }
    }
    lds_wr32(FL_XD + l * 4, 1u);
    waitv<0>();
    STAMP(6);
    STAMPS();
    __builtin_amdgcn_endpgm();
  }

  __builtin_amdgcn_s_setprio(MPRIO);
  ARGREQ("\n\ts_waitcnt lgkmcnt(0)\n\ts_barrier");
  ARGS();
  unsigned tagA, tagB;
  int bud = SPIN_MAX * 4;
  const int tl0 = 32 * fq + 2 * wid;
  u32x2 xe[2][4], dv;
  {
    const rsrc_t rd = mk(Dskip, 2048);
    const unsigned dofs = (j * 64 + 4 * fr) * 2, xo = (32 * wid + 4 * fq) * 128 + ((fr >> 1) ^ (4 * (fq & 1))) * 16 + (fr & 1) * 8;
    const unsigned ks = (j - 4 * ((xcd >> 1) + 2 * (xcd & 1))) & 15;
    const int x0 = (fq ^ (fr & 7)) * 16, x1 = ((4 + fq) ^ (fr & 7)) * 16;
    const unsigned pa = 16384 + (wid * 16 + fr) * 128, pb = 131072 + fr * 128;
    unsigned pa0 = pa + x0, pa1 = pa + x1, pb0 = pb + x0, pb1 = pb + x1;
    const unsigned tq = L_TAB + (wid * 16 + fq * 4) * 4, tcl = L_TAB + 6 * 512 + (wid * 16 + fr) * 4, tcw = tcl + 512 + fq * 512;
    const unsigned l16 = HOFS(lane), pubo = (fq * 4 + (fr >> 2)) * 16, plo = fr * 16 + (3 - fq) * 1024;
    const unsigned sag = W_AGG + (j * 16 + c) * 1024 + wid * 256, sht = W_HT + (j * 4 + wid) * 128;
    unsigned sh = W_H + (c * 64 + 4 * j + wid) * 8192 * 2, fl = M_LAND, scn = M_CONS - 16 + wid * 4;
    unsigned sl[4];
    u64 em[4];
#pragma unroll
    for (int q = 0; q < 4; ++q) {
      sl[q] = sag - (4 + 4 * q) * 1024;
      const int n = min(max(c - 4 * q, 0), 4);
      em[q] = n == 4 ? ~0ull : (1ull << (16 * n)) - 1ull;
    }
    unsigned t_sk, t_s0, t_s1, t_s2, t_s3, t_sx;
    asm volatile(ASM_MA : [pa0] "+&v"(pa0), [pa1] "+&v"(pa1), [pb0] "+&v"(pb0), [pb1] "+&v"(pb1), [fl] "+&v"(fl), [sk] "=&s"(t_sk), [s0] "=&s"(t_s0), [s1] "=&s"(t_s1), [s2] "=&s"(t_s2), [s3] "=&s"(t_s3), [sx] "=&s"(t_sx), [bud] "+&s"(bud), [scn] "+&s"(scn), [sh] "+&s"(sh), [tgA] "=&s"(tagA), [tgB] "=&s"(tagB), [xe00] "=&v"(xe[0][0]), [xe01] "=&v"(xe[0][1]), [xe02] "=&v"(xe[0][2]), [xe03] "=&v"(xe[0][3]), [xe10] "=&v"(xe[1][0]), [xe11] "=&v"(xe[1][1]), [xe12] "=&v"(xe[1][2]), [xe13] "=&v"(xe[1][3]), [dv] "=&v"(dv) : [tq] "v"(tq), [tcl] "v"(tcl), [tcw] "v"(tcw), [pubo] "v"(pubo), [plo] "v"(plo), [l16] "v"(l16), [xo] "v"(xo), [dofs] "v"(dofs), [wrap] "s"(-98304), [wrap2] "s"(-131072), [srd] "s"(rws), [ks] "s"(ks), [srdd] "s"(rd), [srz] "s"(rz), [sag] "s"(sag), [mcar] "s"(M_CAR), [eh] "s"((1u << c) - 1u), [sht] "s"(sht), [sl0] "s"(sl[0]), [em0] "s"(em[0]), [sl1] "s"(sl[1]), [em1] "s"(em[1]), [sl2] "s"(sl[2]), [em2] "s"(em[2]), [sl3] "s"(sl[3]), [em3] "s"(em[3]) : "memory", "scc", "vcc", "m0", "v64", "v65", "v66", "v67", "v68", "v69", "v70", "v71", "v72", "v73", "v74", "v75", "v76", "v77", "v78", "v79", "v80", "v81", "v82", "v83", "v84", "v85", "v86", "v87", "v88", "v89", "v90", "v91", "v92", "v93", "v94", "v95", "v96", "v97", "v98", "v99", "v100", "v101", "v102", "v103", "v104", "v105", "v106", "v107", "v108", "v109", "v110", "v111", "v112", "v113", "v114", "v115", "v116", "v117", "v118", "v119", "v120", "v121", "v122", "v123", "v124", "v125", "v126", "v127", "v128", "v129", "v130", "v131", "v132", "v133", "v134", "v135", "v136", "v137", "v138", "v139", "v140", "v141", "v142", "v143", "v144", "v145", "v146", "v147", "v148", "v149", "v150", "v151", "v152", "v153", "v154", "v155", "v156", "v157", "v158", "v159", "v160", "v161", "v162", "v163", "v164", "v165", "v166", "v167", "v168", "v169", "v170", "v171", "v172", "v173", "v174", "v175", "v176", "v177", "v178", "v179", "v180", "v181", "v182", "v183", "v184", "v185", "v186", "v187", "v188", "v189", "v190", "v191", "v192", "v193", "v194", "v195", "v196", "v197", "v198", "v199", "v200", "v201", "v202", "v203", "v204", "v205", "v206", "v207", "v208", "v209", "v210", "v211", "v212", "v213", "v214", "v215", "v216", "v217", "v218", "v219", "v220", "v221", "v222", "v223", "v224", "v225", "v226", "v227", "v228", "v229", "v230", "v231", "v232", "v233", "v234", "v235", "v236", "v237", "v238", "v239", "v240", "v241", "v242", "v243", "v244", "v245", "v246", "v247", "v248", "v249", "v250", "v251", "v252", "v253", "v254", "v255");
  }
  f32x2 g2[4], base[4][4];
  unsigned cnt = 1, mine, dp[8];
  {
    unsigned selu, m0_, m1_;
#pragma unroll 1
    for (int it = 0; it < SPIN_MAX * 4; ++it) {
      asm volatile("ds_read_b32 %5, %7\n\tds_read_b32 %6, %7 offset:%10\n\tds_read_b32 %4, %8\n\tds_read_b64 %0, %9\n\tds_read_b64 %1, %9 offset:32\n\t"
                   "ds_read_b64 %2, %9 offset:64\n\tds_read_b64 %3, %9 offset:96\n\ts_waitcnt lgkmcnt(0)"
                   : "=&v"(g2[0]), "=&v"(g2[1]), "=&v"(g2[2]), "=&v"(g2[3]), "=&v"(selu), "=&v"(m0_), "=&v"(m1_)
                   : "v"(M_GT), "v"(L_SEL + (lane & 7) * 8), "v"(L_G + tl0 * 4), "n"(M_XS - M_GT) : "memory");
      if (__builtin_amdgcn_readfirstlane(m0_ | m1_) == 0u) break;
      __builtin_amdgcn_s_sleep(1);
    }
    f32x2 dd[4];
#pragma unroll
    for (int n = 0; n < 4; ++n) { const unsigned w = dv[n >> 1]; const float f = (n & 1) ? bfhi(w) : bf2f(w & 0xffffu); dd[n] = (f32x2){f, f}; }
#pragma unroll
    for (int r = 0; r < 4; ++r) {
      const unsigned ob = pk2(1.f - g2[r].x, 1.f - g2[r].y);
      const f32x2 omg = {bf2f(ob & 0xffffu), bfhi(ob)};
#pragma unroll
      for (int n = 0; n < 4; ++n) {
        const unsigned w0 = xe[0][r][n >> 1], w1 = xe[1][r][n >> 1];
        const f32x2 xf = {(n & 1) ? bfhi(w0) : bf2f(w0 & 0xffffu), (n & 1) ? bfhi(w1) : bf2f(w1 & 0xffffu)};
        base[r][n] = pkmul(xf, pkfma(g2[r], dd[n], omg));
      }
    }
#pragma unroll
    for (int r = 0; r < 4; ++r) asm volatile("" : "+v"(base[r][0]), "+v"(base[r][1]), "+v"(base[r][2]), "+v"(base[r][3]));
    asm volatile("s_nop 4\n\ts_waitcnt vmcnt(0)\n\ts_mov_b64 exec, 1\n\tbuffer_atomic_add %0, off, %1, %2 sc0 sc1\n\ts_mov_b64 exec, -1"
                 : "+v"(cnt) : "s"(rws), "s"(W_HF + (c * 2 + (j >> 3)) * 128) : "memory");
    STAMP(9);
    mine = (unsigned)__ballot(lane < 8 && (selu >> 7) == (unsigned)c && ((selu >> 1) & 3u) == (unsigned)wid);
#pragma unroll
    for (int q = 0; q < 8; ++q) dp[q] = L_ONE - 16;
#pragma unroll 1
    for (unsigned m = mine; m; m &= m - 1) {
      const int s = __builtin_ctz(m), t = __builtin_amdgcn_readlane((int)selu, s) & 127;
#pragma unroll
      for (int q = 0; q < 8; ++q) dp[q] = t == tl0 + 8 * (q >> 1) + (q & 1) ? L_BROW + s * 256 + fr * 16 : dp[q];
    }
  }
  f32x4 ac2[2][4];
  float bs[8][4];
#pragma unroll
  for (int q = 0; q < 8; ++q)
#pragma unroll
    for (int n = 0; n < 4; ++n) bs[q][n] = base[q >> 1][n][q & 1];
  {
#pragma unroll
    for (int m = 0; m < 2; ++m)
#pragma unroll
      for (int n = 0; n < 4; ++n) ac2[m][n] = (f32x4){0.f, 0.f, 0.f, 0.f};
    unsigned pc[4], fl2 = M_LAND + 26 * 8, vo = ((j & 8) * 32768 + wid * 2048) * 2 + HOFS(lane);
    const unsigned hr = M_HR, scn2 = M_CONS + 256 + wid * 4, hb_ = W_H + c * 524288 * 2, f9 = M_LAND + 17 * 8 + min(lane, 17) * 4;
#pragma unroll
    for (int q = 0; q < 4; ++q) pc[q] = fr * 128 + (((2 * q + (fq >> 1)) ^ ((fr >> 1) & 7)) * 16) + 8192 * (fq & 1);
    const unsigned sf = W_HF + (c * 2 + (j >> 3)) * 128, sf1 = sf ^ 128;
    unsigned t_sk, t_s0, t_s1;
    asm volatile(ASM_MB : [c00] "+&v"(ac2[0][0]), [c01] "+&v"(ac2[0][1]), [c02] "+&v"(ac2[0][2]), [c03] "+&v"(ac2[0][3]), [c10] "+&v"(ac2[1][0]), [c11] "+&v"(ac2[1][1]), [c12] "+&v"(ac2[1][2]), [c13] "+&v"(ac2[1][3]), [pc0] "+&v"(pc[0]), [pc1] "+&v"(pc[1]), [pc2] "+&v"(pc[2]), [pc3] "+&v"(pc[3]), [b00] "+&v"(bs[0][0]), [b01] "+&v"(bs[0][1]), [b02] "+&v"(bs[0][2]), [b03] "+&v"(bs[0][3]), [b10] "+&v"(bs[1][0]), [b11] "+&v"(bs[1][1]), [b12] "+&v"(bs[1][2]), [b13] "+&v"(bs[1][3]), [b20] "+&v"(bs[2][0]), [b21] "+&v"(bs[2][1]), [b22] "+&v"(bs[2][2]), [b23] "+&v"(bs[2][3]), [b30] "+&v"(bs[3][0]), [b31] "+&v"(bs[3][1]), [b32] "+&v"(bs[3][2]), [b33] "+&v"(bs[3][3]), [b40] "+&v"(bs[4][0]), [b41] "+&v"(bs[4][1]), [b42] "+&v"(bs[4][2]), [b43] "+&v"(bs[4][3]), [b50] "+&v"(bs[5][0]), [b51] "+&v"(bs[5][1]), [b52] "+&v"(bs[5][2]), [b53] "+&v"(bs[5][3]), [b60] "+&v"(bs[6][0]), [b61] "+&v"(bs[6][1]), [b62] "+&v"(bs[6][2]), [b63] "+&v"(bs[6][3]), [b70] "+&v"(bs[7][0]), [b71] "+&v"(bs[7][1]), [b72] "+&v"(bs[7][2]), [b73] "+&v"(bs[7][3]), [vo] "+&v"(vo), [fl] "+&v"(fl2), [sg] "=&s"(t_sk), [s0] "=&s"(t_s0), [s1] "=&s"(t_s1), [bud] "+&s"(bud) : [d0] "v"(dp[0]), [d1] "v"(dp[1]), [d2] "v"(dp[2]), [d3] "v"(dp[3]), [d4] "v"(dp[4]), [d5] "v"(dp[5]), [d6] "v"(dp[6]), [d7] "v"(dp[7]), [cnt] "v"(cnt), [xd] "v"(FL_XD), [mine] "s"(mine), [srd] "s"(rws), [srz] "s"(rz), [hr] "v"(hr), [f9] "v"(f9), [w] "s"(wid), [scn] "s"(scn2), [sf] "s"(sf), [sf1] "s"(sf1), [k2] "s"(0x4000), [h0] "s"(hb_ + 0), [h1] "s"(hb_ + 16384), [h2] "s"(hb_ + 32768), [h3] "s"(hb_ + 49152), [h4] "s"(hb_ + 65536), [h5] "s"(hb_ + 81920), [h6] "s"(hb_ + 98304), [h7] "s"(hb_ + 114688) : "memory", "scc", "vcc", "m0", "v64", "v65", "v66", "v67", "v68", "v69", "v70", "v71", "v72", "v73", "v74", "v75", "v76", "v77", "v78", "v79", "v80", "v81", "v82", "v83", "v84", "v85", "v86", "v87", "v88", "v89", "v90", "v91", "v92", "v93", "v94", "v95", "v96", "v97", "v98", "v99", "v100", "v101", "v102", "v103", "v104", "v105", "v106", "v107", "v108", "v109", "v110", "v111", "v112", "v113", "v114", "v115", "v116", "v117", "v118", "v119", "v120", "v121", "v122", "v123", "v124", "v125", "v126", "v127", "v128", "v129", "v130", "v131", "v132", "v133", "v134", "v135", "v136", "v137", "v138", "v139", "v140", "v141", "v142", "v143", "v144", "v145", "v146", "v147", "v148", "v149", "v150", "v151", "v152", "v153", "v154", "v155", "v156", "v157", "v158", "v159", "v160", "v161", "v162", "v163", "v164", "v165", "v166", "v167", "v168", "v169", "v170", "v171", "v172", "v173", "v174", "v175", "v176", "v177", "v178", "v179", "v180", "v181", "v182", "v183", "v184", "v185", "v186", "v187", "v188", "v189", "v190", "v191", "v192", "v193", "v194", "v195", "v196");
  }
  STAMP(6);
  asm volatile("" : "+v"(g2[0]), "+v"(g2[1]), "+v"(g2[2]), "+v"(g2[3]));
  int ln2 = lane;
  asm volatile("" : "+v"(ln2));
  const int fr2 = ln2 & 15, tl2 = 32 * (ln2 >> 4) + 2 * wid;
  f32x4 ov[2][4];
#pragma unroll
  for (int e = 0; e < 2; ++e)
#pragma unroll
    for (int r = 0; r < 4; ++r) {
      const float gg = 2.f * g2[r][e];
#pragma unroll
      for (int n = 0; n < 4; ++n) ov[e][r][n] = fma1(gg, ac2[e][n][r], bs[2 * r + e][n]);
    }
  {
    const rsrc_t ro = mk(out, 2048 * 1024 * 4);
    const unsigned oo = (c * 128 + tl2) * 4096 + (j * 64 + 4 * fr2) * 4, oo2 = oo + 4096;
    asm volatile("s_nop 4\n\t"
                 "buffer_store_dwordx4 %0, %8, %10, 0 offen" OST "\n\tbuffer_store_dwordx4 %4, %9, %10, 0 offen" OST "\n\t"
                 "buffer_store_dwordx4 %1, %8, %10, %11 offen" OST "\n\tbuffer_store_dwordx4 %5, %9, %10, %11 offen" OST "\n\t"
                 "buffer_store_dwordx4 %2, %8, %10, %12 offen" OST "\n\tbuffer_store_dwordx4 %6, %9, %10, %12 offen" OST "\n\t"
                 "buffer_store_dwordx4 %3, %8, %10, %13 offen" OST "\n\tbuffer_store_dwordx4 %7, %9, %10, %13 offen" OST "\n\t"
                 "s_nop 7\n\ts_nop 7"
                 ::"v"(ov[0][0]), "v"(ov[0][1]), "v"(ov[0][2]), "v"(ov[0][3]), "v"(ov[1][0]), "v"(ov[1][1]), "v"(ov[1][2]), "v"(ov[1][3]), "v"(oo), "v"(oo2), "s"(ro),
                 "s"(32768), "s"(65536), "s"(98304) : "memory");
  }
  if (c == 15 && j == 0 && wid == 0 && lane == 0) st64c(rws, W_EPOCH, (u32x2){tagA, tagB});
  STAMP(7);
  STAMPS();
#endif
}

extern "C" void kernel_launch(void* const* d_in, const int* in_sizes, int n_in, void* d_out, int out_size, void* d_ws, size_t ws_size,
                              hipStream_t stream) {
  (void)hipFuncSetAttribute((const void*)fused, hipFuncAttributeMaxDynamicSharedMemorySize, LDS_BYTES);
  fused<<<256, 512, LDS_BYTES, stream>>>((const u16*)d_in[0], (const u16*)d_in[1], (const u16*)d_in[2], (const u16*)d_in[3], (const u16*)d_in[4],
                                         (const u16*)d_in[5], (const u16*)d_in[6], (const u16*)d_in[7], (const u16*)d_in[8], (const u16*)d_in[10],
                                         (const u16*)d_in[11], (const u16*)d_in[12], (const u16*)d_in[13], (const u16*)d_in[14], (char*)d_ws + WS_BASE(ws_size),
                                         (float*)d_out);
}
```

```cpp
#include <hip/hip_runtime.h>
#include <hip/hip_bf16.h>
#include <stdint.h>

typedef __attribute__((ext_vector_type(8))) short bf16x8;
typedef __attribute__((ext_vector_type(4))) float f32x4;
typedef __attribute__((ext_vector_type(2))) float f32x2;
typedef __attribute__((ext_vector_type(4))) unsigned u32x4;
typedef __attribute__((ext_vector_type(2))) unsigned u32x2;
typedef unsigned short u16;
typedef unsigned long long u64;
#define LAS __attribute__((address_space(3)))
#define DEV __device__ __forceinline__

#define ASM_MA \
  "s_nop 4\n\t" \
  "buffer_load_dword v228, off, %[srd], 0 sc1\n\t" \
  "buffer_load_dwordx2 %[dv], %[dofs], %[srdd], 0 offen\n\t" \
  "s_mov_b32 %[sk], 0\n\t" \
  "s_mov_b32 %[sx], 0x20000\n\t" \
  "Lsp0_%=:\n\t" \
  "ds_read_b128 v[128:131], %[fl] offset:1056\n\t" \
  "s_sub_i32 %[bud], %[bud], 1\n\t" \
  "s_cmp_lt_i32 %[bud], 0\n\t" \
  "s_cbranch_scc1 Lb0_%=\n\t" \
  "s_waitcnt lgkmcnt(0)\n\t" \
  "v_or3_b32 v128, v128, v129, v130\n\t" \
  "v_or_b32 v128, v128, v131\n\t" \
  "s_nop 0\n\t" \
  "v_readfirstlane_b32 %[s0], v128\n\t" \
  "s_cmp_eq_u32 %[s0], 0\n\t" \
  "s_cbranch_scc0 Lsp0_%=\n\t" \
  "Lb0_%=:\n\t" \
  "s_cmp_eq_u32 %[sk], %[ks]\n\t" \
  "s_cbranch_scc1 Lx0_%=\n\t" \
  "Lxb0_%=:\n\t" \
  "s_mov_b32 m0, %[scn]\n\t" \
  "v_add_u32 %[fl], 8, %[fl]\n\t" \
  "ds_read_b128 v[208:211], %[pa0] offset:0\n\t" \
  "ds_read_b128 v[212:215], %[pa0] offset:8192\n\t" \
  "ds_read_b128 v[216:219], %[pa1] offset:0\n\t" \
  "ds_read_b128 v[220:223], %[pa1] offset:8192\n\t" \
  "ds_read_b128 v[176:179], %[pb0] offset:0\n\t" \
  "ds_read_b128 v[180:183], %[pb0] offset:2048\n\t" \
  "ds_read_b128 v[184:187], %[pb0] offset:4096\n\t" \
  "ds_read_b128 v[188:191], %[pb0] offset:6144\n\t" \
  "ds_read_b128 v[192:195], %[pb0] offset:8192\n\t" \
  "ds_read_b128 v[196:199], %[pb0] offset:10240\n\t" \
  "ds_read_b128 v[200:203], %[pb0] offset:12288\n\t" \
  "ds_read_b128 v[204:207], %[pb0] offset:14336\n\t" \
  "ds_read_b64 v[224:225], %[fl]\n\t" \
  "s_waitcnt lgkmcnt(7)\n\t" \
  "v_mfma_f32_16x16x32_bf16 v[64:67], v[208:211], v[176:179], 0\n\t" \
  "ds_read_b128 v[144:147], %[pb1] offset:0\n\t" \
  "s_add_u32 %[sk], %[sk], 1\n\t" \
  "v_mfma_f32_16x16x32_bf16 v[96:99], v[212:215], v[176:179], 0\n\t" \
  "s_and_b32 %[s1], %[sk], 3\n\t" \
  "s_cselect_b32 %[s1], 0x8000, %[wrap]\n\t" \
  "v_mfma_f32_16x16x32_bf16 v[68:71], v[208:211], v[180:183], 0\n\t" \
  "ds_read_b128 v[148:151], %[pb1] offset:2048\n\t" \
  "s_add_u32 %[sx], %[sx], 0x8000\n\t" \
  "v_mfma_f32_16x16x32_bf16 v[100:103], v[212:215], v[180:183], 0\n\t" \
  "s_cmp_eq_u32 %[sx], 0x28000\n\t" \
  "s_waitcnt lgkmcnt(7)\n\t" \
  "v_mfma_f32_16x16x32_bf16 v[72:75], v[208:211], v[184:187], 0\n\t" \
  "ds_read_b128 v[152:155], %[pb1] offset:4096\n\t" \
  "s_cselect_b32 %[s2], %[wrap2], 0x8000\n\t" \
  "v_mfma_f32_16x16x32_bf16 v[104:107], v[212:215], v[184:187], 0\n\t" \
  "s_cselect_b32 %[sx], 0, %[sx]\n\t" \
  "v_add_u32 %[pa0], %[s1], %[pa0]\n\t" \
  "v_mfma_f32_16x16x32_bf16 v[76:79], v[208:211], v[188:191], 0\n\t" \
  "ds_read_b128 v[156:159], %[pb1] offset:6144\n\t" \
  "v_add_u32 %[pb0], %[s2], %[pb0]\n\t" \
  "v_mfma_f32_16x16x32_bf16 v[108:111], v[212:215], v[188:191], 0\n\t" \
  "s_cmp_eq_u32 %[sk], %[ks]\n\t" \
  "s_cselect_b32 %[s3], 1, 0\n\t" \
  "s_waitcnt lgkmcnt(5)\n\t" \
  "v_mfma_f32_16x16x32_bf16 v[80:83], v[208:211], v[192:195], 0\n\t" \
  "ds_read_b128 v[160:163], %[pb1] offset:8192\n\t" \
  "v_add_u32 %[pa1], %[s1], %[pa1]\n\t" \
  "v_mfma_f32_16x16x32_bf16 v[112:115], v[212:215], v[192:195], 0\n\t" \
  "s_mov_b64 exec, 1\n\t" \
  "buffer_load_dword %[l16], %[srz], 0 offen lds\n\t" \
  "s_mov_b64 exec, -1\n\t" \
  "v_mfma_f32_16x16x32_bf16 v[84:87], v[208:211], v[196:199], 0\n\t" \
  "ds_read_b128 v[164:167], %[pb1] offset:10240\n\t" \
  "s_add_u32 m0, m0, 16\n\t" \
  "v_mfma_f32_16x16x32_bf16 v[116:119], v[212:215], v[196:199], 0\n\t" \
  "s_waitcnt lgkmcnt(6)\n\t" \
  "v_or_b32 v224, v224, v225\n\t" \
  "v_mfma_f32_16x16x32_bf16 v[88:91], v[208:211], v[200:203], 0\n\t" \
  "ds_read_b128 v[168:171], %[pb1] offset:12288\n\t" \
  "v_readfirstlane_b32 %[s0], v224\n\t" \
  "v_mfma_f32_16x16x32_bf16 v[120:123], v[212:215], v[200:203], 0\n\t" \
  "s_or_b32 %[s0], %[s0], %[s3]\n\t" \
  "s_cbranch_scc1 Lsp1_%=\n\t" \
  "Lb1_%=:\n\t" \
  "v_mfma_f32_16x16x32_bf16 v[92:95], v[208:211], v[204:207], 0\n\t" \
  "ds_read_b128 v[172:175], %[pb1] offset:14336\n\t" \
  "v_add_u32 %[fl], 8, %[fl]\n\t" \
  "v_mfma_f32_16x16x32_bf16 v[124:127], v[212:215], v[204:207], 0\n\t" \
  "s_branch Lmid_%=\n\t" \
  "Lloop_%=:\n\t" \
  "s_waitcnt lgkmcnt(6)\n\t" \
  "v_mfma_f32_16x16x32_bf16 v[64:67], v[136:139], v[144:147], v[64:67]\n\t" \
  "ds_read_b128 v[208:211], %[pa0] offset:0\n\t" \
  "v_mfma_f32_16x16x32_bf16 v[96:99], v[140:143], v[144:147], v[96:99]\n\t" \
  "ds_read_b128 v[212:215], %[pa0] offset:8192\n\t" \
  "v_mfma_f32_16x16x32_bf16 v[68:71], v[136:139], v[148:151], v[68:71]\n\t" \
  "ds_read_b128 v[216:219], %[pa1] offset:0\n\t" \
  "v_mfma_f32_16x16x32_bf16 v[100:103], v[140:143], v[148:151], v[100:103]\n\t" \
  "ds_read_b128 v[220:223], %[pa1] offset:8192\n\t" \
  "s_waitcnt lgkmcnt(8)\n\t" \
  "v_mfma_f32_16x16x32_bf16 v[72:75], v[136:139], v[152:155], v[72:75]\n\t" \
  "ds_read_b128 v[176:179], %[pb0] offset:0\n\t" \
  "v_mfma_f32_16x16x32_bf16 v[104:107], v[140:143], v[152:155], v[104:107]\n\t" \
  "ds_read_b128 v[180:183], %[pb0] offset:2048\n\t" \
  "v_mfma_f32_16x16x32_bf16 v[76:79], v[136:139], v[156:159], v[76:79]\n\t" \
  "v_add_u32 %[pb1], %[s2], %[pb1]\n\t" \
  "v_mfma_f32_16x16x32_bf16 v[108:111], v[140:143], v[156:159], v[108:111]\n\t" \
  "ds_read_b128 v[184:187], %[pb0] offset:4096\n\t" \
  "s_waitcnt lgkmcnt(7)\n\t" \
  "v_mfma_f32_16x16x32_bf16 v[80:83], v[136:139], v[160:163], v[80:83]\n\t" \
  "ds_read_b128 v[188:191], %[pb0] offset:6144\n\t" \
  "v_mfma_f32_16x16x32_bf16 v[112:115], v[140:143], v[160:163], v[112:115]\n\t" \
  "ds_read_b128 v[192:195], %[pb0] offset:8192\n\t" \
  "v_mfma_f32_16x16x32_bf16 v[84:87], v[136:139], v[164:167], v[84:87]\n\t" \
  "s_waitcnt lgkmcnt(5)\n\t" \
  "s_mov_b64 exec, 1\n\t" \
  "buffer_load_dword %[l16], %[srz], 0 offen lds\n\t" \
  "s_mov_b64 exec, -1\n\t" \
  "v_mfma_f32_16x16x32_bf16 v[116:119], v[140:143], v[164:167], v[116:119]\n\t" \
  "ds_read_b128 v[196:199], %[pb0] offset:10240\n\t" \
  "s_add_u32 m0, m0, 16\n\t" \
  "v_mfma_f32_16x16x32_bf16 v[88:91], v[136:139], v[168:171], v[88:91]\n\t" \
  "v_mfma_f32_16x16x32_bf16 v[120:123], v[140:143], v[168:171], v[120:123]\n\t" \
  "ds_read_b128 v[200:203], %[pb0] offset:12288\n\t" \
  "v_mfma_f32_16x16x32_bf16 v[92:95], v[136:139], v[172:175], v[92:95]\n\t" \
  "ds_read_b128 v[204:207], %[pb0] offset:14336\n\t" \
  "v_mfma_f32_16x16x32_bf16 v[124:127], v[140:143], v[172:175], v[124:127]\n\t" \
  "Lnx5_%=:\n\t" \
  "ds_read_b64 v[224:225], %[fl]\n\t" \
  "s_waitcnt lgkmcnt(7)\n\t" \
  "v_mfma_f32_16x16x32_bf16 v[64:67], v[208:211], v[176:179], v[64:67]\n\t" \
  "ds_read_b128 v[144:147], %[pb1] offset:0\n\t" \
  "s_add_u32 %[sk], %[sk], 1\n\t" \
  "v_mfma_f32_16x16x32_bf16 v[96:99], v[212:215], v[176:179], v[96:99]\n\t" \
  "s_and_b32 %[s1], %[sk], 3\n\t" \
  "s_cselect_b32 %[s1], 0x8000, %[wrap]\n\t" \
  "v_mfma_f32_16x16x32_bf16 v[68:71], v[208:211], v[180:183], v[68:71]\n\t" \
  "ds_read_b128 v[148:151], %[pb1] offset:2048\n\t" \
  "s_add_u32 %[sx], %[sx], 0x8000\n\t" \
  "v_mfma_f32_16x16x32_bf16 v[100:103], v[212:215], v[180:183], v[100:103]\n\t" \
  "s_cmp_eq_u32 %[sx], 0x28000\n\t" \
  "s_waitcnt lgkmcnt(7)\n\t" \
  "v_mfma_f32_16x16x32_bf16 v[72:75], v[208:211], v[184:187], v[72:75]\n\t" \
  "ds_read_b128 v[152:155], %[pb1] offset:4096\n\t" \
  "s_cselect_b32 %[s2], %[wrap2], 0x8000\n\t" \
  "v_mfma_f32_16x16x32_bf16 v[104:107], v[212:215], v[184:187], v[104:107]\n\t" \
  "s_cselect_b32 %[sx], 0, %[sx]\n\t" \
  "v_add_u32 %[pa0], %[s1], %[pa0]\n\t" \
  "v_mfma_f32_16x16x32_bf16 v[76:79], v[208:211], v[188:191], v[76:79]\n\t" \
  "ds_read_b128 v[156:159], %[pb1] offset:6144\n\t" \
  "v_add_u32 %[pb0], %[s2], %[pb0]\n\t" \
  "v_mfma_f32_16x16x32_bf16 v[108:111], v[212:215], v[188:191], v[108:111]\n\t" \
  "s_cmp_eq_u32 %[sk], %[ks]\n\t" \
  "s_cselect_b32 %[s3], 1, 0\n\t" \
  "s_waitcnt lgkmcnt(5)\n\t" \
  "v_mfma_f32_16x16x32_bf16 v[80:83], v[208:211], v[192:195], v[80:83]\n\t" \
  "ds_read_b128 v[160:163], %[pb1] offset:8192\n\t" \
  "v_add_u32 %[pa1], %[s1], %[pa1]\n\t" \
  "v_mfma_f32_16x16x32_bf16 v[112:115], v[212:215], v[192:195], v[112:115]\n\t" \
  "v_mfma_f32_16x16x32_bf16 v[84:87], v[208:211], v[196:199], v[84:87]\n\t" \
  "ds_read_b128 v[164:167], %[pb1] offset:10240\n\t" \
  "v_mfma_f32_16x16x32_bf16 v[116:119], v[212:215], v[196:199], v[116:119]\n\t" \
  "s_waitcnt lgkmcnt(6)\n\t" \
  "v_or_b32 v224, v224, v225\n\t" \
  "v_mfma_f32_16x16x32_bf16 v[88:91], v[208:211], v[200:203], v[88:91]\n\t" \
  "ds_read_b128 v[168:171], %[pb1] offset:12288\n\t" \
  "v_readfirstlane_b32 %[s0], v224\n\t" \
  "v_mfma_f32_16x16x32_bf16 v[120:123], v[212:215], v[200:203], v[120:123]\n\t" \
  "s_or_b32 %[s0], %[s0], %[s3]\n\t" \
  "s_cbranch_scc1 Lsp3_%=\n\t" \
  "Lb3_%=:\n\t" \
  "v_mfma_f32_16x16x32_bf16 v[92:95], v[208:211], v[204:207], v[92:95]\n\t" \
  "ds_read_b128 v[172:175], %[pb1] offset:14336\n\t" \
  "v_add_u32 %[fl], 8, %[fl]\n\t" \
  "v_mfma_f32_16x16x32_bf16 v[124:127], v[212:215], v[204:207], v[124:127]\n\t" \
  "Lmid_%=:\n\t" \
  "s_waitcnt lgkmcnt(6)\n\t" \
  "v_mfma_f32_16x16x32_bf16 v[64:67], v[216:219], v[144:147], v[64:67]\n\t" \
  "ds_read_b128 v[128:131], %[pa0] offset:0\n\t" \
  "v_mfma_f32_16x16x32_bf16 v[96:99], v[220:223], v[144:147], v[96:99]\n\t" \
  "ds_read_b128 v[132:135], %[pa0] offset:8192\n\t" \
  "v_mfma_f32_16x16x32_bf16 v[68:71], v[216:219], v[148:151], v[68:71]\n\t" \
  "ds_read_b128 v[136:139], %[pa1] offset:0\n\t" \
  "v_mfma_f32_16x16x32_bf16 v[100:103], v[220:223], v[148:151], v[100:103]\n\t" \
  "ds_read_b128 v[140:143], %[pa1] offset:8192\n\t" \
  "s_waitcnt lgkmcnt(8)\n\t" \
  "v_mfma_f32_16x16x32_bf16 v[72:75], v[216:219], v[152:155], v[72:75]\n\t" \
  "ds_read_b128 v[176:179], %[pb0] offset:0\n\t" \
  "v_mfma_f32_16x16x32_bf16 v[104:107], v[220:223], v[152:155], v[104:107]\n\t" \
  "ds_read_b128 v[180:183], %[pb0] offset:2048\n\t" \
  "v_mfma_f32_16x16x32_bf16 v[76:79], v[216:219], v[156:159], v[76:79]\n\t" \
  "v_add_u32 %[pb1], %[s2], %[pb1]\n\t" \
  "v_mfma_f32_16x16x32_bf16 v[108:111], v[220:223], v[156:159], v[108:111]\n\t" \
  "ds_read_b128 v[184:187], %[pb0] offset:4096\n\t" \
  "s_waitcnt lgkmcnt(7)\n\t" \
  "v_mfma_f32_16x16x32_bf16 v[80:83], v[216:219], v[160:163], v[80:83]\n\t" \
  "ds_read_b128 v[188:191], %[pb0] offset:6144\n\t" \
  "v_mfma_f32_16x16x32_bf16 v[112:115], v[220:223], v[160:163], v[112:115]\n\t" \
  "ds_read_b128 v[192:195], %[pb0] offset:8192\n\t" \
  "v_mfma_f32_16x16x32_bf16 v[84:87], v[216:219], v[164:167], v[84:87]\n\t" \
  "s_waitcnt lgkmcnt(5)\n\t" \
  "s_mov_b64 exec, 1\n\t" \
  "buffer_load_dword %[l16], %[srz], 0 offen lds\n\t" \
  "s_mov_b64 exec, -1\n\t" \
  "v_mfma_f32_16x16x32_bf16 v[116:119], v[220:223], v[164:167], v[116:119]\n\t" \
  "ds_read_b128 v[196:199], %[pb0] offset:10240\n\t" \
  "s_add_u32 m0, m0, 16\n\t" \
  "v_mfma_f32_16x16x32_bf16 v[88:91], v[216:219], v[168:171], v[88:91]\n\t" \
  "v_mfma_f32_16x16x32_bf16 v[120:123], v[220:223], v[168:171], v[120:123]\n\t" \
  "ds_read_b128 v[200:203], %[pb0] offset:12288\n\t" \
  "v_mfma_f32_16x16x32_bf16 v[92:95], v[216:219], v[172:175], v[92:95]\n\t" \
  "ds_read_b128 v[204:207], %[pb0] offset:14336\n\t" \
  "v_mfma_f32_16x16x32_bf16 v[124:127], v[220:223], v[172:175], v[124:127]\n\t" \
  "Lnx3_%=:\n\t" \
  "ds_read_b64 v[224:225], %[fl]\n\t" \
  "s_waitcnt lgkmcnt(7)\n\t" \
  "v_mfma_f32_16x16x32_bf16 v[64:67], v[128:131], v[176:179], v[64:67]\n\t" \
  "ds_read_b128 v[144:147], %[pb1] offset:0\n\t" \
  "s_add_u32 %[sk], %[sk], 1\n\t" \
  "v_mfma_f32_16x16x32_bf16 v[96:99], v[132:135], v[176:179], v[96:99]\n\t" \
  "s_and_b32 %[s1], %[sk], 3\n\t" \
  "s_cselect_b32 %[s1], 0x8000, %[wrap]\n\t" \
  "v_mfma_f32_16x16x32_bf16 v[68:71], v[128:131], v[180:183], v[68:71]\n\t" \
  "ds_read_b128 v[148:151], %[pb1] offset:2048\n\t" \
  "s_add_u32 %[sx], %[sx], 0x8000\n\t" \
  "v_mfma_f32_16x16x32_bf16 v[100:103], v[132:135], v[180:183], v[100:103]\n\t" \
  "s_cmp_eq_u32 %[sx], 0x28000\n\t" \
  "s_waitcnt lgkmcnt(7)\n\t" \
  "v_mfma_f32_16x16x32_bf16 v[72:75], v[128:131], v[184:187], v[72:75]\n\t" \
  "ds_read_b128 v[152:155], %[pb1] offset:4096\n\t" \
  "s_cselect_b32 %[s2], %[wrap2], 0x8000\n\t" \
  "v_mfma_f32_16x16x32_bf16 v[104:107], v[132:135], v[184:187], v[104:107]\n\t" \
  "s_cselect_b32 %[sx], 0, %[sx]\n\t" \
  "v_add_u32 %[pa0], %[s1], %[pa0]\n\t" \
  "v_mfma_f32_16x16x32_bf16 v[76:79], v[128:131], v[188:191], v[76:79]\n\t" \
  "ds_read_b128 v[156:159], %[pb1] offset:6144\n\t" \
  "v_add_u32 %[pb0], %[s2], %[pb0]\n\t" \
  "v_mfma_f32_16x16x32_bf16 v[108:111], v[132:135], v[188:191], v[108:111]\n\t" \
  "s_cmp_eq_u32 %[sk], %[ks]\n\t" \
  "s_cselect_b32 %[s3], 1, 0\n\t" \
  "s_waitcnt lgkmcnt(5)\n\t" \
  "v_mfma_f32_16x16x32_bf16 v[80:83], v[128:131], v[192:195], v[80:83]\n\t" \
  "ds_read_b128 v[160:163], %[pb1] offset:8192\n\t" \
  "v_add_u32 %[pa1], %[s1], %[pa1]\n\t" \
  "v_mfma_f32_16x16x32_bf16 v[112:115], v[132:135], v[192:195], v[112:115]\n\t" \
  "v_mfma_f32_16x16x32_bf16 v[84:87], v[128:131], v[196:199], v[84:87]\n\t" \
  "ds_read_b128 v[164:167], %[pb1] offset:10240\n\t" \
  "v_mfma_f32_16x16x32_bf16 v[116:119], v[132:135], v[196:199], v[116:119]\n\t" \
  "s_waitcnt lgkmcnt(6)\n\t" \
  "v_or_b32 v224, v224, v225\n\t" \
  "v_mfma_f32_16x16x32_bf16 v[88:91], v[128:131], v[200:203], v[88:91]\n\t" \
  "ds_read_b128 v[168:171], %[pb1] offset:12288\n\t" \
  "v_readfirstlane_b32 %[s0], v224\n\t" \
  "v_mfma_f32_16x16x32_bf16 v[120:123], v[132:135], v[200:203], v[120:123]\n\t" \
  "s_or_b32 %[s0], %[s0], %[s3]\n\t" \
  "s_cbranch_scc1 Lsp5_%=\n\t" \
  "Lb5_%=:\n\t" \
  "v_mfma_f32_16x16x32_bf16 v[92:95], v[128:131], v[204:207], v[92:95]\n\t" \
  "ds_read_b128 v[172:175], %[pb1] offset:14336\n\t" \
  "v_add_u32 %[fl], 8, %[fl]\n\t" \
  "v_mfma_f32_16x16x32_bf16 v[124:127], v[132:135], v[204:207], v[124:127]\n\t" \
  "s_cmp_lt_u32 %[sk], 16\n\t" \
  "s_cbranch_scc1 Lloop_%=\n\t" \
  "s_waitcnt lgkmcnt(6)\n\t" \
  "v_mfma_f32_16x16x32_bf16 v[64:67], v[136:139], v[144:147], v[64:67]\n\t" \
  "ds_read_b128 v[176:179], %[tq] offset:0\n\t" \
  "v_mfma_f32_16x16x32_bf16 v[96:99], v[140:143], v[144:147], v[96:99]\n\t" \
  "ds_read_b128 v[180:183], %[tq] offset:256\n\t" \
  "v_mfma_f32_16x16x32_bf16 v[68:71], v[136:139], v[148:151], v[68:71]\n\t" \
  "ds_read_b128 v[192:195], %[tq] offset:1024\n\t" \
  "v_mfma_f32_16x16x32_bf16 v[100:103], v[140:143], v[148:151], v[100:103]\n\t" \
  "ds_read_b128 v[196:199], %[tq] offset:1280\n\t" \
  "s_waitcnt lgkmcnt(8)\n\t" \
  "v_mfma_f32_16x16x32_bf16 v[72:75], v[136:139], v[152:155], v[72:75]\n\t" \
  "ds_read_b128 v[200:203], %[tq] offset:1536\n\t" \
  "v_mfma_f32_16x16x32_bf16 v[104:107], v[140:143], v[152:155], v[104:107]\n\t" \
  "ds_read_b128 v[204:207], %[tq] offset:1792\n\t" \
  "v_mfma_f32_16x16x32_bf16 v[76:79], v[136:139], v[156:159], v[76:79]\n\t" \
  "ds_read_b128 v[208:211], %[tq] offset:2048\n\t" \
  "v_mfma_f32_16x16x32_bf16 v[108:111], v[140:143], v[156:159], v[108:111]\n\t" \
  "ds_read_b128 v[212:215], %[tq] offset:2304\n\t" \
  "s_waitcnt lgkmcnt(8)\n\t" \
  "v_mfma_f32_16x16x32_bf16 v[80:83], v[136:139], v[160:163], v[80:83]\n\t" \
  "ds_read_b128 v[216:219], %[tq] offset:2560\n\t" \
  "v_mfma_f32_16x16x32_bf16 v[112:115], v[140:143], v[160:163], v[112:115]\n\t" \
  "ds_read_b128 v[220:223], %[tq] offset:2816\n\t" \
  "v_mfma_f32_16x16x32_bf16 v[84:87], v[136:139], v[164:167], v[84:87]\n\t" \
  "s_mov_b64 exec, 1\n\t" \
  "buffer_load_dword %[l16], %[srz], 0 offen lds\n\t" \
  "s_mov_b64 exec, -1\n\t" \
  "v_mfma_f32_16x16x32_bf16 v[116:119], v[140:143], v[164:167], v[116:119]\n\t" \
  "ds_read_b128 v[184:187], %[tq] offset:512\n\t" \
  "s_add_u32 m0, m0, 16\n\t" \
  "v_mfma_f32_16x16x32_bf16 v[88:91], v[136:139], v[168:171], v[88:91]\n\t" \
  "ds_read_b128 v[188:191], %[tq] offset:768\n\t" \
  "v_mfma_f32_16x16x32_bf16 v[120:123], v[140:143], v[168:171], v[120:123]\n\t" \
  "ds_read2_b32 v[230:231], %[tcl] offset0:0 offset1:64\n\t" \
  "v_mfma_f32_16x16x32_bf16 v[92:95], v[136:139], v[172:175], v[92:95]\n\t" \
  "ds_read2_b32 v[232:233], %[tcw] offset0:0 offset1:64\n\t" \
  "v_mfma_f32_16x16x32_bf16 v[124:127], v[140:143], v[172:175], v[124:127]\n\t" \
  "s_setprio 3\n\t" \
  "s_waitcnt vmcnt(8) lgkmcnt(4)\n\t" \
  "v_readfirstlane_b32 %[tgB], v228\n\t" \
  "s_add_u32 %[tgA], %[tgB], 1\n\t" \
  "s_add_u32 %[tgB], %[tgB], 0x40000001\n\t" \
  "v_pk_fma_f32 v[244:245], v[180:181], v[96:97], v[68:69] neg_lo:[1,0,0] neg_hi:[1,0,0]\n\t" \
  "v_pk_fma_f32 v[246:247], v[182:183], v[98:99], v[70:71] neg_lo:[1,0,0] neg_hi:[1,0,0]\n\t" \
  "v_pk_fma_f32 v[248:249], v[180:181], v[64:65], v[100:101]\n\t" \
  "v_pk_fma_f32 v[250:251], v[182:183], v[66:67], v[102:103]\n\t" \
  "v_pk_fma_f32 v[236:237], v[176:177], v[64:65], v[244:245]\n\t" \
  "v_pk_fma_f32 v[238:239], v[178:179], v[66:67], v[246:247]\n\t" \
  "v_pk_fma_f32 v[240:241], v[176:177], v[96:97], v[248:249]\n\t" \
  "v_pk_fma_f32 v[242:243], v[178:179], v[98:99], v[250:251]\n\t" \
  "v_pk_fma_f32 v[244:245], v[180:181], v[240:241], v[72:73] neg_lo:[1,0,0] neg_hi:[1,0,0]\n\t" \
  "v_pk_fma_f32 v[246:247], v[182:183], v[242:243], v[74:75] neg_lo:[1,0,0] neg_hi:[1,0,0]\n\t" \
  "v_pk_fma_f32 v[248:249], v[180:181], v[236:237], v[104:105]\n\t" \
  "v_pk_fma_f32 v[250:251], v[182:183], v[238:239], v[106:107]\n\t" \
  "v_pk_fma_f32 v[236:237], v[176:177], v[236:237], v[244:245]\n\t" \
  "v_pk_fma_f32 v[238:239], v[178:179], v[238:239], v[246:247]\n\t" \
  "v_pk_fma_f32 v[240:241], v[176:177], v[240:241], v[248:249]\n\t" \
  "v_pk_fma_f32 v[242:243], v[178:179], v[242:243], v[250:251]\n\t" \
  "v_pk_fma_f32 v[244:245], v[180:181], v[240:241], v[76:77] neg_lo:[1,0,0] neg_hi:[1,0,0]\n\t" \
  "v_pk_fma_f32 v[246:247], v[182:183], v[242:243], v[78:79] neg_lo:[1,0,0] neg_hi:[1,0,0]\n\t" \
  "v_pk_fma_f32 v[248:249], v[180:181], v[236:237], v[108:109]\n\t" \
  "v_pk_fma_f32 v[250:251], v[182:183], v[238:239], v[110:111]\n\t" \
  "v_pk_fma_f32 v[236:237], v[176:177], v[236:237], v[244:245]\n\t" \
  "v_pk_fma_f32 v[238:239], v[178:179], v[238:239], v[246:247]\n\t" \
  "v_pk_fma_f32 v[240:241], v[176:177], v[240:241], v[248:249]\n\t" \
  "v_pk_fma_f32 v[242:243], v[178:179], v[242:243], v[250:251]\n\t" \
  "v_pk_fma_f32 v[244:245], v[180:181], v[240:241], v[80:81] neg_lo:[1,0,0] neg_hi:[1,0,0]\n\t" \
  "v_pk_fma_f32 v[246:247], v[182:183], v[242:243], v[82:83] neg_lo:[1,0,0] neg_hi:[1,0,0]\n\t" \
  "v_pk_fma_f32 v[248:249], v[180:181], v[236:237], v[112:113]\n\t" \
  "v_pk_fma_f32 v[250:251], v[182:183], v[238:239], v[114:115]\n\t" \
  "v_pk_fma_f32 v[236:237], v[176:177], v[236:237], v[244:245]\n\t" \
  "v_pk_fma_f32 v[238:239], v[178:179], v[238:239], v[246:247]\n\t" \
  "v_pk_fma_f32 v[240:241], v[176:177], v[240:241], v[248:249]\n\t" \
  "v_pk_fma_f32 v[242:243], v[178:179], v[242:243], v[250:251]\n\t" \
  "v_pk_fma_f32 v[244:245], v[180:181], v[240:241], v[84:85] neg_lo:[1,0,0] neg_hi:[1,0,0]\n\t" \
  "v_pk_fma_f32 v[246:247], v[182:183], v[242:243], v[86:87] neg_lo:[1,0,0] neg_hi:[1,0,0]\n\t" \
  "v_pk_fma_f32 v[248:249], v[180:181], v[236:237], v[116:117]\n\t" \
  "v_pk_fma_f32 v[250:251], v[182:183], v[238:239], v[118:119]\n\t" \
  "v_pk_fma_f32 v[236:237], v[176:177], v[236:237], v[244:245]\n\t" \
  "v_pk_fma_f32 v[238:239], v[178:179], v[238:239], v[246:247]\n\t" \
  "v_pk_fma_f32 v[240:241], v[176:177], v[240:241], v[248:249]\n\t" \
  "v_pk_fma_f32 v[242:243], v[178:179], v[242:243], v[250:251]\n\t" \
  "v_pk_fma_f32 v[244:245], v[180:181], v[240:241], v[88:89] neg_lo:[1,0,0] neg_hi:[1,0,0]\n\t" \
  "v_pk_fma_f32 v[246:247], v[182:183], v[242:243], v[90:91] neg_lo:[1,0,0] neg_hi:[1,0,0]\n\t" \
  "v_pk_fma_f32 v[248:249], v[180:181], v[236:237], v[120:121]\n\t" \
  "v_pk_fma_f32 v[250:251], v[182:183], v[238:239], v[122:123]\n\t" \
  "v_pk_fma_f32 v[236:237], v[176:177], v[236:237], v[244:245]\n\t" \
  "v_pk_fma_f32 v[238:239], v[178:179], v[238:239], v[246:247]\n\t" \
  "v_pk_fma_f32 v[240:241], v[176:177], v[240:241], v[248:249]\n\t" \
  "v_pk_fma_f32 v[242:243], v[178:179], v[242:243], v[250:251]\n\t" \
  "v_pk_fma_f32 v[244:245], v[180:181], v[240:241], v[92:93] neg_lo:[1,0,0] neg_hi:[1,0,0]\n\t" \
  "v_pk_fma_f32 v[246:247], v[182:183], v[242:243], v[94:95] neg_lo:[1,0,0] neg_hi:[1,0,0]\n\t" \
  "v_pk_fma_f32 v[248:249], v[180:181], v[236:237], v[124:125]\n\t" \
  "v_pk_fma_f32 v[250:251], v[182:183], v[238:239], v[126:127]\n\t" \
  "v_pk_fma_f32 v[236:237], v[176:177], v[236:237], v[244:245]\n\t" \
  "v_pk_fma_f32 v[238:239], v[178:179], v[238:239], v[246:247]\n\t" \
  "v_pk_fma_f32 v[240:241], v[176:177], v[240:241], v[248:249]\n\t" \
  "v_pk_fma_f32 v[242:243], v[178:179], v[242:243], v[250:251]\n\t" \
  "s_add_u32 %[s0], %[eh], 1\n\t" \
  "v_mov_b32 v226, %[s0]\n\t" \
  "v_mov_b32 v253, %[tgA]\n\t" \
  "v_mov_b32 v255, %[tgB]\n\t" \
  "v_mov_b32_dpp v244, v236 row_shr:1 row_mask:0xf bank_mask:0xf bound_ctrl:0\n\t" \
  "v_mov_b32_dpp v245, v237 row_shr:1 row_mask:0xf bank_mask:0xf bound_ctrl:0\n\t" \
  "v_mov_b32_dpp v246, v238 row_shr:1 row_mask:0xf bank_mask:0xf bound_ctrl:0\n\t" \
  "v_mov_b32_dpp v247, v239 row_shr:1 row_mask:0xf bank_mask:0xf bound_ctrl:0\n\t" \
  "v_mov_b32_dpp v248, v240 row_shr:1 row_mask:0xf bank_mask:0xf bound_ctrl:0\n\t" \
  "v_mov_b32_dpp v249, v241 row_shr:1 row_mask:0xf bank_mask:0xf bound_ctrl:0\n\t" \
  "v_mov_b32_dpp v250, v242 row_shr:1 row_mask:0xf bank_mask:0xf bound_ctrl:0\n\t" \
  "v_mov_b32_dpp v251, v243 row_shr:1 row_mask:0xf bank_mask:0xf bound_ctrl:0\n\t" \
  "v_pk_fma_f32 v[236:237], v[192:193], v[244:245], v[236:237]\n\t" \
  "v_pk_fma_f32 v[238:239], v[194:195], v[246:247], v[238:239]\n\t" \
  "v_pk_fma_f32 v[240:241], v[192:193], v[248:249], v[240:241]\n\t" \
  "v_pk_fma_f32 v[242:243], v[194:195], v[250:251], v[242:243]\n\t" \
  "v_pk_fma_f32 v[236:237], v[196:197], v[248:249], v[236:237] neg_lo:[1,0,0] neg_hi:[1,0,0]\n\t" \
  "v_pk_fma_f32 v[238:239], v[198:199], v[250:251], v[238:239] neg_lo:[1,0,0] neg_hi:[1,0,0]\n\t" \
  "v_pk_fma_f32 v[240:241], v[196:197], v[244:245], v[240:241]\n\t" \
  "v_pk_fma_f32 v[242:243], v[198:199], v[246:247], v[242:243]\n\t" \
  "v_mov_b32_dpp v244, v236 row_shr:2 row_mask:0xf bank_mask:0xf bound_ctrl:0\n\t" \
  "v_mov_b32_dpp v245, v237 row_shr:2 row_mask:0xf bank_mask:0xf bound_ctrl:0\n\t" \
  "v_mov_b32_dpp v246, v238 row_shr:2 row_mask:0xf bank_mask:0xf bound_ctrl:0\n\t" \
  "v_mov_b32_dpp v247, v239 row_shr:2 row_mask:0xf bank_mask:0xf bound_ctrl:0\n\t" \
  "v_mov_b32_dpp v248, v240 row_shr:2 row_mask:0xf bank_mask:0xf bound_ctrl:0\n\t" \
  "v_mov_b32_dpp v249, v241 row_shr:2 row_mask:0xf bank_mask:0xf bound_ctrl:0\n\t" \
  "v_mov_b32_dpp v250, v242 row_shr:2 row_mask:0xf bank_mask:0xf bound_ctrl:0\n\t" \
  "v_mov_b32_dpp v251, v243 row_shr:2 row_mask:0xf bank_mask:0xf bound_ctrl:0\n\t" \
  "v_pk_fma_f32 v[236:237], v[200:201], v[244:245], v[236:237]\n\t" \
  "v_pk_fma_f32 v[238:239], v[202:203], v[246:247], v[238:239]\n\t" \
  "v_pk_fma_f32 v[240:241], v[200:201], v[248:249], v[240:241]\n\t" \
  "v_pk_fma_f32 v[242:243], v[202:203], v[250:251], v[242:243]\n\t" \
  "v_pk_fma_f32 v[236:237], v[204:205], v[248:249], v[236:237] neg_lo:[1,0,0] neg_hi:[1,0,0]\n\t" \
  "v_pk_fma_f32 v[238:239], v[206:207], v[250:251], v[238:239] neg_lo:[1,0,0] neg_hi:[1,0,0]\n\t" \
  "v_pk_fma_f32 v[240:241], v[204:205], v[244:245], v[240:241]\n\t" \
  "v_pk_fma_f32 v[242:243], v[206:207], v[246:247], v[242:243]\n\t" \
  "v_mov_b32_dpp v244, v236 row_shr:4 row_mask:0xf bank_mask:0xf bound_ctrl:0\n\t" \
  "v_mov_b32_dpp v245, v237 row_shr:4 row_mask:0xf bank_mask:0xf bound_ctrl:0\n\t" \
  "v_mov_b32_dpp v246, v238 row_shr:4 row_mask:0xf bank_mask:0xf bound_ctrl:0\n\t" \
  "v_mov_b32_dpp v247, v239 row_shr:4 row_mask:0xf bank_mask:0xf bound_ctrl:0\n\t" \
  "v_mov_b32_dpp v248, v240 row_shr:4 row_mask:0xf bank_mask:0xf bound_ctrl:0\n\t" \
  "v_mov_b32_dpp v249, v241 row_shr:4 row_mask:0xf bank_mask:0xf bound_ctrl:0\n\t" \
  "v_mov_b32_dpp v250, v242 row_shr:4 row_mask:0xf bank_mask:0xf bound_ctrl:0\n\t" \
  "v_mov_b32_dpp v251, v243 row_shr:4 row_mask:0xf bank_mask:0xf bound_ctrl:0\n\t" \
  "v_pk_fma_f32 v[236:237], v[208:209], v[244:245], v[236:237]\n\t" \
  "v_pk_fma_f32 v[238:239], v[210:211], v[246:247], v[238:239]\n\t" \
  "v_pk_fma_f32 v[240:241], v[208:209], v[248:249], v[240:241]\n\t" \
  "v_pk_fma_f32 v[242:243], v[210:211], v[250:251], v[242:243]\n\t" \
  "v_pk_fma_f32 v[236:237], v[212:213], v[248:249], v[236:237] neg_lo:[1,0,0] neg_hi:[1,0,0]\n\t" \
  "v_pk_fma_f32 v[238:239], v[214:215], v[250:251], v[238:239] neg_lo:[1,0,0] neg_hi:[1,0,0]\n\t" \
  "v_pk_fma_f32 v[240:241], v[212:213], v[244:245], v[240:241]\n\t" \
  "v_pk_fma_f32 v[242:243], v[214:215], v[246:247], v[242:243]\n\t" \
  "v_mov_b32_dpp v244, v236 row_shr:8 row_mask:0xf bank_mask:0xf bound_ctrl:0\n\t" \
  "v_mov_b32_dpp v245, v237 row_shr:8 row_mask:0xf bank_mask:0xf bound_ctrl:0\n\t" \
  "v_mov_b32_dpp v246, v238 row_shr:8 row_mask:0xf bank_mask:0xf bound_ctrl:0\n\t" \
  "v_mov_b32_dpp v247, v239 row_shr:8 row_mask:0xf bank_mask:0xf bound_ctrl:0\n\t" \
  "v_mov_b32_dpp v248, v240 row_shr:8 row_mask:0xf bank_mask:0xf bound_ctrl:0\n\t" \
  "v_mov_b32_dpp v249, v241 row_shr:8 row_mask:0xf bank_mask:0xf bound_ctrl:0\n\t" \
  "v_mov_b32_dpp v250, v242 row_shr:8 row_mask:0xf bank_mask:0xf bound_ctrl:0\n\t" \
  "v_mov_b32_dpp v251, v243 row_shr:8 row_mask:0xf bank_mask:0xf bound_ctrl:0\n\t" \
  "v_pk_fma_f32 v[236:237], v[216:217], v[244:245], v[236:237]\n\t" \
  "v_pk_fma_f32 v[238:239], v[218:219], v[246:247], v[238:239]\n\t" \
  "v_pk_fma_f32 v[240:241], v[216:217], v[248:249], v[240:241]\n\t" \
  "v_pk_fma_f32 v[242:243], v[218:219], v[250:251], v[242:243]\n\t" \
  "v_pk_fma_f32 v[236:237], v[220:221], v[248:249], v[236:237] neg_lo:[1,0,0] neg_hi:[1,0,0]\n\t" \
  "v_pk_fma_f32 v[238:239], v[222:223], v[250:251], v[238:239] neg_lo:[1,0,0] neg_hi:[1,0,0]\n\t" \
  "v_pk_fma_f32 v[240:241], v[220:221], v[244:245], v[240:241]\n\t" \
  "v_pk_fma_f32 v[242:243], v[222:223], v[246:247], v[242:243]\n\t" \
  "v_mov_b32_dpp v252, v236 row_newbcast:15 row_mask:0xf bank_mask:0x1\n\t" \
  "v_mov_b32_dpp v252, v237 row_newbcast:15 row_mask:0xf bank_mask:0x2\n\t" \
  "v_mov_b32_dpp v252, v238 row_newbcast:15 row_mask:0xf bank_mask:0x4\n\t" \
  "v_mov_b32_dpp v252, v239 row_newbcast:15 row_mask:0xf bank_mask:0x8\n\t" \
  "v_mov_b32_dpp v254, v240 row_newbcast:15 row_mask:0xf bank_mask:0x1\n\t" \
  "v_mov_b32_dpp v254, v241 row_newbcast:15 row_mask:0xf bank_mask:0x2\n\t" \
  "v_mov_b32_dpp v254, v242 row_newbcast:15 row_mask:0xf bank_mask:0x4\n\t" \
  "v_mov_b32_dpp v254, v243 row_newbcast:15 row_mask:0xf bank_mask:0x8\n\t" \
  "s_mov_b32 exec_lo, 0x11111111\n\t" \
  "s_mov_b32 exec_hi, 0x11111111\n\t" \
  "buffer_store_dwordx4 v[252:255], %[pubo], %[srd], %[sag] offen sc1\n\t" \
  "s_mov_b64 exec, 1\n\t" \
  "buffer_atomic_or v226, off, %[srd], %[sht] sc0 sc1\n\t" \
  "s_mov_b64 exec, -1\n\t" \
  "v_mov_b32 v160, 0\n\t" \
  "v_mov_b32 v161, %[tgA]\n\t" \
  "v_mov_b32 v162, 0\n\t" \
  "v_mov_b32 v163, %[tgB]\n\t" \
  "v_mov_b32 v164, 0\n\t" \
  "v_mov_b32 v165, %[tgA]\n\t" \
  "v_mov_b32 v166, 0\n\t" \
  "v_mov_b32 v167, %[tgB]\n\t" \
  "v_mov_b32 v168, 0\n\t" \
  "v_mov_b32 v169, %[tgA]\n\t" \
  "v_mov_b32 v170, 0\n\t" \
  "v_mov_b32 v171, %[tgB]\n\t" \
  "v_mov_b32 v172, 0\n\t" \
  "v_mov_b32 v173, %[tgA]\n\t" \
  "v_mov_b32 v174, 0\n\t" \
  "v_mov_b32 v175, %[tgB]\n\t" \
  "s_waitcnt lgkmcnt(0)\n\t" \
  "v_mov_b32_dpp v244, v236 row_shr:1 row_mask:0xf bank_mask:0xf bound_ctrl:0\n\t" \
  "v_mov_b32_dpp v245, v237 row_shr:1 row_mask:0xf bank_mask:0xf bound_ctrl:0\n\t" \
  "v_mov_b32_dpp v246, v238 row_shr:1 row_mask:0xf bank_mask:0xf bound_ctrl:0\n\t" \
  "v_mov_b32_dpp v247, v239 row_shr:1 row_mask:0xf bank_mask:0xf bound_ctrl:0\n\t" \
  "v_mov_b32_dpp v248, v240 row_shr:1 row_mask:0xf bank_mask:0xf bound_ctrl:0\n\t" \
  "v_mov_b32_dpp v249, v241 row_shr:1 row_mask:0xf bank_mask:0xf bound_ctrl:0\n\t" \
  "v_mov_b32_dpp v250, v242 row_shr:1 row_mask:0xf bank_mask:0xf bound_ctrl:0\n\t" \
  "v_mov_b32_dpp v251, v243 row_shr:1 row_mask:0xf bank_mask:0xf bound_ctrl:0\n\t" \
  "v_pk_mul_f32 v[236:237], v[184:185], v[244:245]\n\t" \
  "v_pk_mul_f32 v[238:239], v[186:187], v[246:247]\n\t" \
  "v_pk_mul_f32 v[240:241], v[184:185], v[248:249]\n\t" \
  "v_pk_mul_f32 v[242:243], v[186:187], v[250:251]\n\t" \
  "v_pk_fma_f32 v[236:237], v[188:189], v[248:249], v[236:237] neg_lo:[1,0,0] neg_hi:[1,0,0]\n\t" \
  "v_pk_fma_f32 v[238:239], v[190:191], v[250:251], v[238:239] neg_lo:[1,0,0] neg_hi:[1,0,0]\n\t" \
  "v_pk_fma_f32 v[240:241], v[188:189], v[244:245], v[240:241]\n\t" \
  "v_pk_fma_f32 v[242:243], v[190:191], v[246:247], v[242:243]\n\t" \
  "v_pk_mul_f32 v[128:129], v[184:185], v[64:65]\n\t" \
  "v_pk_mul_f32 v[130:131], v[186:187], v[66:67]\n\t" \
  "v_pk_fma_f32 v[128:129], v[188:189], v[96:97], v[128:129] neg_lo:[1,0,0] neg_hi:[1,0,0]\n\t" \
  "v_pk_fma_f32 v[130:131], v[190:191], v[98:99], v[130:131] neg_lo:[1,0,0] neg_hi:[1,0,0]\n\t" \
  "v_pk_mul_f32 v[96:97], v[184:185], v[96:97]\n\t" \
  "v_pk_mul_f32 v[98:99], v[186:187], v[98:99]\n\t" \
  "v_pk_fma_f32 v[96:97], v[188:189], v[64:65], v[96:97] neg_lo:[1,0,1] neg_hi:[1,0,1]\n\t" \
  "v_pk_fma_f32 v[98:99], v[190:191], v[66:67], v[98:99] neg_lo:[1,0,1] neg_hi:[1,0,1]\n\t" \
  "v_pk_mul_f32 v[132:133], v[184:185], v[68:69]\n\t" \
  "v_pk_mul_f32 v[134:135], v[186:187], v[70:71]\n\t" \
  "v_pk_fma_f32 v[132:133], v[188:189], v[100:101], v[132:133] neg_lo:[1,0,0] neg_hi:[1,0,0]\n\t" \
  "v_pk_fma_f32 v[134:135], v[190:191], v[102:103], v[134:135] neg_lo:[1,0,0] neg_hi:[1,0,0]\n\t" \
  "v_pk_mul_f32 v[100:101], v[184:185], v[100:101]\n\t" \
  "v_pk_mul_f32 v[102:103], v[186:187], v[102:103]\n\t" \
  "v_pk_fma_f32 v[100:101], v[188:189], v[68:69], v[100:101] neg_lo:[1,0,1] neg_hi:[1,0,1]\n\t" \
  "v_pk_fma_f32 v[102:103], v[190:191], v[70:71], v[102:103] neg_lo:[1,0,1] neg_hi:[1,0,1]\n\t" \
  "v_pk_mul_f32 v[136:137], v[184:185], v[72:73]\n\t" \
  "v_pk_mul_f32 v[138:139], v[186:187], v[74:75]\n\t" \
  "v_pk_fma_f32 v[136:137], v[188:189], v[104:105], v[136:137] neg_lo:[1,0,0] neg_hi:[1,0,0]\n\t" \
  "v_pk_fma_f32 v[138:139], v[190:191], v[106:107], v[138:139] neg_lo:[1,0,0] neg_hi:[1,0,0]\n\t" \
  "v_pk_mul_f32 v[104:105], v[184:185], v[104:105]\n\t" \
  "v_pk_mul_f32 v[106:107], v[186:187], v[106:107]\n\t" \
  "v_pk_fma_f32 v[104:105], v[188:189], v[72:73], v[104:105] neg_lo:[1,0,1] neg_hi:[1,0,1]\n\t" \
  "v_pk_fma_f32 v[106:107], v[190:191], v[74:75], v[106:107] neg_lo:[1,0,1] neg_hi:[1,0,1]\n\t" \
  "v_pk_mul_f32 v[140:141], v[184:185], v[76:77]\n\t" \
  "v_pk_mul_f32 v[142:143], v[186:187], v[78:79]\n\t" \
  "v_pk_fma_f32 v[140:141], v[188:189], v[108:109], v[140:141] neg_lo:[1,0,0] neg_hi:[1,0,0]\n\t" \
  "v_pk_fma_f32 v[142:143], v[190:191], v[110:111], v[142:143] neg_lo:[1,0,0] neg_hi:[1,0,0]\n\t" \
  "v_pk_mul_f32 v[108:109], v[184:185], v[108:109]\n\t" \
  "v_pk_mul_f32 v[110:111], v[186:187], v[110:111]\n\t" \
  "v_pk_fma_f32 v[108:109], v[188:189], v[76:77], v[108:109] neg_lo:[1,0,1] neg_hi:[1,0,1]\n\t" \
  "v_pk_fma_f32 v[110:111], v[190:191], v[78:79], v[110:111] neg_lo:[1,0,1] neg_hi:[1,0,1]\n\t" \
  "v_pk_mul_f32 v[144:145], v[184:185], v[80:81]\n\t" \
  "v_pk_mul_f32 v[146:147], v[186:187], v[82:83]\n\t" \
  "v_pk_fma_f32 v[144:145], v[188:189], v[112:113], v[144:145] neg_lo:[1,0,0] neg_hi:[1,0,0]\n\t" \
  "v_pk_fma_f32 v[146:147], v[190:191], v[114:115], v[146:147] neg_lo:[1,0,0] neg_hi:[1,0,0]\n\t" \
  "v_pk_mul_f32 v[112:113], v[184:185], v[112:113]\n\t" \
  "v_pk_mul_f32 v[114:115], v[186:187], v[114:115]\n\t" \
  "v_pk_fma_f32 v[112:113], v[188:189], v[80:81], v[112:113] neg_lo:[1,0,1] neg_hi:[1,0,1]\n\t" \
  "v_pk_fma_f32 v[114:115], v[190:191], v[82:83], v[114:115] neg_lo:[1,0,1] neg_hi:[1,0,1]\n\t" \
  "v_pk_mul_f32 v[148:149], v[184:185], v[84:85]\n\t" \
  "v_pk_mul_f32 v[150:151], v[186:187], v[86:87]\n\t" \
  "v_pk_fma_f32 v[148:149], v[188:189], v[116:117], v[148:149] neg_lo:[1,0,0] neg_hi:[1,0,0]\n\t" \
  "v_pk_fma_f32 v[150:151], v[190:191], v[118:119], v[150:151] neg_lo:[1,0,0] neg_hi:[1,0,0]\n\t" \
  "v_pk_mul_f32 v[116:117], v[184:185], v[116:117]\n\t" \
  "v_pk_mul_f32 v[118:119], v[186:187], v[118:119]\n\t" \
  "v_pk_fma_f32 v[116:117], v[188:189], v[84:85], v[116:117] neg_lo:[1,0,1] neg_hi:[1,0,1]\n\t" \
  "v_pk_fma_f32 v[118:119], v[190:191], v[86:87], v[118:119] neg_lo:[1,0,1] neg_hi:[1,0,1]\n\t" \
  "v_pk_mul_f32 v[152:153], v[184:185], v[88:89]\n\t" \
  "v_pk_mul_f32 v[154:155], v[186:187], v[90:91]\n\t" \
  "v_pk_fma_f32 v[152:153], v[188:189], v[120:121], v[152:153] neg_lo:[1,0,0] neg_hi:[1,0,0]\n\t" \
  "v_pk_fma_f32 v[154:155], v[190:191], v[122:123], v[154:155] neg_lo:[1,0,0] neg_hi:[1,0,0]\n\t" \
  "v_pk_mul_f32 v[120:121], v[184:185], v[120:121]\n\t" \
  "v_pk_mul_f32 v[122:123], v[186:187], v[122:123]\n\t" \
  "v_pk_fma_f32 v[120:121], v[188:189], v[88:89], v[120:121] neg_lo:[1,0,1] neg_hi:[1,0,1]\n\t" \
  "v_pk_fma_f32 v[122:123], v[190:191], v[90:91], v[122:123] neg_lo:[1,0,1] neg_hi:[1,0,1]\n\t" \
  "v_pk_mul_f32 v[156:157], v[184:185], v[92:93]\n\t" \
  "v_pk_mul_f32 v[158:159], v[186:187], v[94:95]\n\t" \
  "v_pk_fma_f32 v[156:157], v[188:189], v[124:125], v[156:157] neg_lo:[1,0,0] neg_hi:[1,0,0]\n\t" \
  "v_pk_fma_f32 v[158:159], v[190:191], v[126:127], v[158:159] neg_lo:[1,0,0] neg_hi:[1,0,0]\n\t" \
  "v_pk_mul_f32 v[124:125], v[184:185], v[124:125]\n\t" \
  "v_pk_mul_f32 v[126:127], v[186:187], v[126:127]\n\t" \
  "v_pk_fma_f32 v[124:125], v[188:189], v[92:93], v[124:125] neg_lo:[1,0,1] neg_hi:[1,0,1]\n\t" \
  "v_pk_fma_f32 v[126:127], v[190:191], v[94:95], v[126:127] neg_lo:[1,0,1] neg_hi:[1,0,1]\n\t" \
  "s_waitcnt vmcnt(0)\n\t" \
  "v_readfirstlane_b32 %[s0], v226\n\t" \
  "s_andn2_b32 %[s0], %[eh], %[s0]\n\t" \
  "s_cselect_b32 %[s1], 32, 0\n\t" \
  "s_cbranch_scc0 Lh1_%=\n\t" \
  "v_mov_b32 v226, 0\n\t" \
  "s_mov_b64 exec, 1\n\t" \
  "buffer_atomic_or v226, off, %[srd], %[sht] sc0 sc1\n\t" \
  "s_mov_b64 exec, -1\n\t" \
  "s_branch Lpa_%=\n\t" \
  "Lh1_%=:\n\t" \
  "s_mov_b64 exec, %[em0]\n\t" \
  "s_cbranch_execz Lpa_%=\n\t" \
  "buffer_load_dwordx4 v[160:163], %[plo], %[srd], %[sl0] offen sc1\n\t" \
  "s_mov_b64 exec, %[em1]\n\t" \
  "s_cbranch_execz Lpa_%=\n\t" \
  "buffer_load_dwordx4 v[164:167], %[plo], %[srd], %[sl1] offen sc1\n\t" \
  "s_mov_b64 exec, %[em2]\n\t" \
  "s_cbranch_execz Lpa_%=\n\t" \
  "buffer_load_dwordx4 v[168:171], %[plo], %[srd], %[sl2] offen sc1\n\t" \
  "s_mov_b64 exec, %[em3]\n\t" \
  "s_cbranch_execz Lpa_%=\n\t" \
  "buffer_load_dwordx4 v[172:175], %[plo], %[srd], %[sl3] offen sc1\n\t" \
  "Lpa_%=:\n\t" \
  "s_mov_b64 exec, -1\n\t" \
  "s_mov_b32 exec_lo, 0xaaaaaaaa\n\t" \
  "s_mov_b32 exec_hi, 0xaaaaaaaa\n\t" \
  "v_pk_mul_f32 v[244:245], v[196:197], v[188:189]\n\t" \
  "v_pk_mul_f32 v[246:247], v[198:199], v[190:191]\n\t" \
  "v_pk_mul_f32 v[248:249], v[196:197], v[184:185]\n\t" \
  "v_pk_mul_f32 v[250:251], v[198:199], v[186:187]\n\t" \
  "v_pk_fma_f32 v[184:185], v[192:193], v[184:185], v[244:245] neg_lo:[0,0,1] neg_hi:[0,0,1]\n\t" \
  "v_pk_fma_f32 v[186:187], v[194:195], v[186:187], v[246:247] neg_lo:[0,0,1] neg_hi:[0,0,1]\n\t" \
  "v_pk_fma_f32 v[188:189], v[192:193], v[188:189], v[248:249]\n\t" \
  "v_pk_fma_f32 v[190:191], v[194:195], v[190:191], v[250:251]\n\t" \
  "s_mov_b32 exec_lo, 0xcccccccc\n\t" \
  "s_mov_b32 exec_hi, 0xcccccccc\n\t" \
  "v_pk_mul_f32 v[244:245], v[204:205], v[188:189]\n\t" \
  "v_pk_mul_f32 v[246:247], v[206:207], v[190:191]\n\t" \
  "v_pk_mul_f32 v[248:249], v[204:205], v[184:185]\n\t" \
  "v_pk_mul_f32 v[250:251], v[206:207], v[186:187]\n\t" \
  "v_pk_fma_f32 v[184:185], v[200:201], v[184:185], v[244:245] neg_lo:[0,0,1] neg_hi:[0,0,1]\n\t" \
  "v_pk_fma_f32 v[186:187], v[202:203], v[186:187], v[246:247] neg_lo:[0,0,1] neg_hi:[0,0,1]\n\t" \
  "v_pk_fma_f32 v[188:189], v[200:201], v[188:189], v[248:249]\n\t" \
  "v_pk_fma_f32 v[190:191], v[202:203], v[190:191], v[250:251]\n\t" \
  "s_mov_b32 exec_lo, 0xf0f0f0f0\n\t" \
  "s_mov_b32 exec_hi, 0xf0f0f0f0\n\t" \
  "v_pk_mul_f32 v[244:245], v[212:213], v[188:189]\n\t" \
  "v_pk_mul_f32 v[246:247], v[214:215], v[190:191]\n\t" \
  "v_pk_mul_f32 v[248:249], v[212:213], v[184:185]\n\t" \
  "v_pk_mul_f32 v[250:251], v[214:215], v[186:187]\n\t" \
  "v_pk_fma_f32 v[184:185], v[208:209], v[184:185], v[244:245] neg_lo:[0,0,1] neg_hi:[0,0,1]\n\t" \
  "v_pk_fma_f32 v[186:187], v[210:211], v[186:187], v[246:247] neg_lo:[0,0,1] neg_hi:[0,0,1]\n\t" \
  "v_pk_fma_f32 v[188:189], v[208:209], v[188:189], v[248:249]\n\t" \
  "v_pk_fma_f32 v[190:191], v[210:211], v[190:191], v[250:251]\n\t" \
  "s_mov_b32 exec_lo, 0xff00ff00\n\t" \
  "s_mov_b32 exec_hi, 0xff00ff00\n\t" \
  "v_pk_mul_f32 v[244:245], v[220:221], v[188:189]\n\t" \
  "v_pk_mul_f32 v[246:247], v[222:223], v[190:191]\n\t" \
  "v_pk_mul_f32 v[248:249], v[220:221], v[184:185]\n\t" \
  "v_pk_mul_f32 v[250:251], v[222:223], v[186:187]\n\t" \
  "v_pk_fma_f32 v[184:185], v[216:217], v[184:185], v[244:245] neg_lo:[0,0,1] neg_hi:[0,0,1]\n\t" \
  "v_pk_fma_f32 v[186:187], v[218:219], v[186:187], v[246:247] neg_lo:[0,0,1] neg_hi:[0,0,1]\n\t" \
  "v_pk_fma_f32 v[188:189], v[216:217], v[188:189], v[248:249]\n\t" \
  "v_pk_fma_f32 v[190:191], v[218:219], v[190:191], v[250:251]\n\t" \
  "s_mov_b64 exec, -1\n\t" \
  "s_cmp_eq_u32 %[s1], 0\n\t" \
  "s_cbranch_scc1 Lq_%=\n\t" \
  "Lh2_%=:\n\t" \
  "s_waitcnt vmcnt(0)\n\t" \
  "v_readfirstlane_b32 %[s0], v226\n\t" \
  "s_andn2_b32 %[s0], %[eh], %[s0]\n\t" \
  "s_cbranch_scc0 Lh3_%=\n\t" \
  "s_sub_u32 %[s1], %[s1], 1\n\t" \
  "s_cbranch_scc1 Lh3_%=\n\t" \
  "s_sleep 1\n\t" \
  "v_mov_b32 v226, 0\n\t" \
  "s_mov_b64 exec, 1\n\t" \
  "buffer_atomic_or v226, off, %[srd], %[sht] sc0 sc1\n\t" \
  "s_mov_b64 exec, -1\n\t" \
  "s_branch Lh2_%=\n\t" \
  "Lh3_%=:\n\t" \
  "s_mov_b64 exec, %[em0]\n\t" \
  "s_cbranch_execz Lpc_%=\n\t" \
  "buffer_load_dwordx4 v[160:163], %[plo], %[srd], %[sl0] offen sc1\n\t" \
  "s_mov_b64 exec, %[em1]\n\t" \
  "s_cbranch_execz Lpc_%=\n\t" \
  "buffer_load_dwordx4 v[164:167], %[plo], %[srd], %[sl1] offen sc1\n\t" \
  "s_mov_b64 exec, %[em2]\n\t" \
  "s_cbranch_execz Lpc_%=\n\t" \
  "buffer_load_dwordx4 v[168:171], %[plo], %[srd], %[sl2] offen sc1\n\t" \
  "s_mov_b64 exec, %[em3]\n\t" \
  "s_cbranch_execz Lpc_%=\n\t" \
  "buffer_load_dwordx4 v[172:175], %[plo], %[srd], %[sl3] offen sc1\n\t" \
  "Lpc_%=:\n\t" \
  "s_mov_b64 exec, -1\n\t" \
  "Lq_%=:\n\t" \
  "s_waitcnt vmcnt(0)\n\t" \
  "v_cmpx_eq_u32 vcc, %[tgA], v161\n\t" \
  "v_cmpx_eq_u32 vcc, %[tgB], v163\n\t" \
  "v_cmpx_eq_u32 vcc, %[tgA], v165\n\t" \
  "v_cmpx_eq_u32 vcc, %[tgB], v167\n\t" \
  "v_cmpx_eq_u32 vcc, %[tgA], v169\n\t" \
  "v_cmpx_eq_u32 vcc, %[tgB], v171\n\t" \
  "v_cmpx_eq_u32 vcc, %[tgA], v173\n\t" \
  "v_cmpx_eq_u32 vcc, %[tgB], v175\n\t" \
  "s_cmp_eq_u64 exec, -1\n\t" \
  "s_mov_b64 exec, -1\n\t" \
  "s_cbranch_scc1 Lq2_%=\n\t" \
  "s_sub_i32 %[bud], %[bud], 16\n\t" \
  "s_cmp_lt_i32 %[bud], 0\n\t" \
  "s_cbranch_scc1 Lq2_%=\n\t" \
  "s_sleep 4\n\t" \
  "s_mov_b64 exec, %[em0]\n\t" \
  "s_cbranch_execz Lpb_%=\n\t" \
  "buffer_load_dwordx4 v[160:163], %[plo], %[srd], %[sl0] offen sc1\n\t" \
  "s_mov_b64 exec, %[em1]\n\t" \
  "s_cbranch_execz Lpb_%=\n\t" \
  "buffer_load_dwordx4 v[164:167], %[plo], %[srd], %[sl1] offen sc1\n\t" \
  "s_mov_b64 exec, %[em2]\n\t" \
  "s_cbranch_execz Lpb_%=\n\t" \
  "buffer_load_dwordx4 v[168:171], %[plo], %[srd], %[sl2] offen sc1\n\t" \
  "s_mov_b64 exec, %[em3]\n\t" \
  "s_cbranch_execz Lpb_%=\n\t" \
  "buffer_load_dwordx4 v[172:175], %[plo], %[srd], %[sl3] offen sc1\n\t" \
  "Lpb_%=:\n\t" \
  "s_mov_b64 exec, -1\n\t" \
  "s_branch Lq_%=\n\t" \
  "Lq2_%=:\n\t" \
  "s_mov_b32 %[scn], %[mcar]\n\t" \
  "s_mov_b32 m0, %[scn]\n\t" \
  "s_mov_b64 exec, 1\n\t" \
  "buffer_load_dword %[l16], %[srz], 0 offen lds\n\t" \
  "s_mov_b64 exec, -1\n\t" \
  "v_fma_f32 v244, -v231, v174, v168\n\t" \
  "v_fma_f32 v245, v231, v172, v170\n\t" \
  "v_fma_f32 v252, v230, v172, v244\n\t" \
  "v_fma_f32 v253, v230, v174, v245\n\t" \
  "v_fma_f32 v244, -v231, v253, v164\n\t" \
  "v_fma_f32 v245, v231, v252, v166\n\t" \
  "v_fma_f32 v252, v230, v252, v244\n\t" \
  "v_fma_f32 v253, v230, v253, v245\n\t" \
  "v_fma_f32 v244, -v231, v253, v160\n\t" \
  "v_fma_f32 v245, v231, v252, v162\n\t" \
  "v_fma_f32 v252, v230, v252, v244\n\t" \
  "v_fma_f32 v253, v230, v253, v245\n\t" \
  "v_mul_f32 v244, v233, v253\n\t" \
  "v_mul_f32 v245, v233, v252\n\t" \
  "v_fma_f32 v254, v232, v252, -v244\n\t" \
  "v_fma_f32 v255, v232, v253, v245\n\t" \
  "v_fma_f32 v252, v232, v252, -v244\n\t" \
  "v_fma_f32 v253, v232, v253, v245\n\t" \
  "s_nop 1\n\t" \
  "v_permlane16_swap_b32 v252, v254\n\t" \
  "v_permlane16_swap_b32 v253, v255\n\t" \
  "s_nop 0\n\t" \
  "v_add_f32 v244, v252, v254\n\t" \
  "v_add_f32 v245, v253, v255\n\t" \
  "v_add_f32 v252, v252, v254\n\t" \
  "v_add_f32 v253, v253, v255\n\t" \
  "v_mov_b32 v254, v244\n\t" \
  "v_mov_b32 v255, v245\n\t" \
  "s_nop 1\n\t" \
  "v_permlane32_swap_b32 v252, v254\n\t" \
  "v_permlane32_swap_b32 v253, v255\n\t" \
  "s_nop 0\n\t" \
  "v_add_f32 v244, v252, v254\n\t" \
  "v_add_f32 v245, v253, v255\n\t" \
  "v_add_f32 v252, v252, v254\n\t" \
  "v_add_f32 v253, v253, v255\n\t" \
  "v_mov_b32 v254, v244\n\t" \
  "v_mov_b32 v255, v245\n\t" \
  "v_mov_b32_dpp v254, v252 row_ror:12 row_mask:0x2 bank_mask:0xf\n\t" \
  "v_mov_b32_dpp v255, v253 row_ror:12 row_mask:0x2 bank_mask:0xf\n\t" \
  "v_mov_b32_dpp v254, v252 row_ror:8 row_mask:0x4 bank_mask:0xf\n\t" \
  "v_mov_b32_dpp v255, v253 row_ror:8 row_mask:0x4 bank_mask:0xf\n\t" \
  "v_mov_b32_dpp v254, v252 row_ror:4 row_mask:0x8 bank_mask:0xf\n\t" \
  "v_mov_b32_dpp v255, v253 row_ror:4 row_mask:0x8 bank_mask:0xf\n\t" \
  "s_nop 1\n\t" \
  "v_mov_b32_dpp v244, v254 row_newbcast:0 row_mask:0xf bank_mask:0xf\n\t" \
  "v_mov_b32_dpp v245, v254 row_newbcast:1 row_mask:0xf bank_mask:0xf\n\t" \
  "v_mov_b32_dpp v246, v254 row_newbcast:2 row_mask:0xf bank_mask:0xf\n\t" \
  "v_mov_b32_dpp v247, v254 row_newbcast:3 row_mask:0xf bank_mask:0xf\n\t" \
  "v_mov_b32_dpp v248, v255 row_newbcast:0 row_mask:0xf bank_mask:0xf\n\t" \
  "v_mov_b32_dpp v249, v255 row_newbcast:1 row_mask:0xf bank_mask:0xf\n\t" \
  "v_mov_b32_dpp v250, v255 row_newbcast:2 row_mask:0xf bank_mask:0xf\n\t" \
  "v_mov_b32_dpp v251, v255 row_newbcast:3 row_mask:0xf bank_mask:0xf\n\t" \
  "v_pk_fma_f32 v[236:237], v[184:185], v[244:245], v[236:237]\n\t" \
  "v_pk_fma_f32 v[238:239], v[186:187], v[246:247], v[238:239]\n\t" \
  "v_pk_fma_f32 v[240:241], v[184:185], v[248:249], v[240:241]\n\t" \
  "v_pk_fma_f32 v[242:243], v[186:187], v[250:251], v[242:243]\n\t" \
  "v_pk_fma_f32 v[236:237], v[188:189], v[248:249], v[236:237] neg_lo:[1,0,0] neg_hi:[1,0,0]\n\t" \
  "v_pk_fma_f32 v[238:239], v[190:191], v[250:251], v[238:239] neg_lo:[1,0,0] neg_hi:[1,0,0]\n\t" \
  "v_pk_fma_f32 v[240:241], v[188:189], v[244:245], v[240:241]\n\t" \
  "v_pk_fma_f32 v[242:243], v[190:191], v[246:247], v[242:243]\n\t" \
  "v_pk_fma_f32 v[128:129], v[180:181], v[240:241], v[128:129] neg_lo:[1,0,0] neg_hi:[1,0,0]\n\t" \
  "v_pk_fma_f32 v[130:131], v[182:183], v[242:243], v[130:131] neg_lo:[1,0,0] neg_hi:[1,0,0]\n\t" \
  "v_pk_fma_f32 v[96:97], v[180:181], v[236:237], v[96:97] neg_lo:[1,0,0] neg_hi:[1,0,0]\n\t" \
  "v_pk_fma_f32 v[98:99], v[182:183], v[238:239], v[98:99] neg_lo:[1,0,0] neg_hi:[1,0,0]\n\t" \
  "v_pk_fma_f32 v[128:129], v[176:177], v[236:237], v[128:129]\n\t" \
  "v_pk_fma_f32 v[130:131], v[178:179], v[238:239], v[130:131]\n\t" \
  "v_pk_fma_f32 v[96:97], v[176:177], v[240:241], v[96:97] neg_lo:[1,0,0] neg_hi:[1,0,0]\n\t" \
  "v_pk_fma_f32 v[98:99], v[178:179], v[242:243], v[98:99] neg_lo:[1,0,0] neg_hi:[1,0,0]\n\t" \
  "v_cvt_pk_bf16_f32 v160, v128, v129\n\t" \
  "v_cvt_pk_bf16_f32 v161, v130, v131\n\t" \
  "v_cvt_pk_bf16_f32 v162, v96, v97\n\t" \
  "v_cvt_pk_bf16_f32 v163, v98, v99\n\t" \
  "v_pk_fma_f32 v[132:133], v[180:181], v[96:97], v[132:133]\n\t" \
  "v_pk_fma_f32 v[134:135], v[182:183], v[98:99], v[134:135]\n\t" \
  "v_permlane16_swap_b32 v160, v162\n\t" \
  "v_permlane16_swap_b32 v161, v163\n\t" \
  "buffer_store_dwordx4 v[160:163], %[l16], %[srd], %[sh] offen offset:0 sc1\n\t" \
  "v_pk_fma_f32 v[100:101], v[180:181], v[128:129], v[100:101] neg_lo:[1,0,0] neg_hi:[1,0,0]\n\t" \
  "v_pk_fma_f32 v[102:103], v[182:183], v[130:131], v[102:103] neg_lo:[1,0,0] neg_hi:[1,0,0]\n\t" \
  "v_pk_fma_f32 v[132:133], v[176:177], v[128:129], v[132:133]\n\t" \
  "v_pk_fma_f32 v[134:135], v[178:179], v[130:131], v[134:135]\n\t" \
  "v_pk_fma_f32 v[100:101], v[176:177], v[96:97], v[100:101]\n\t" \
  "v_pk_fma_f32 v[102:103], v[178:179], v[98:99], v[102:103]\n\t" \
  "v_cvt_pk_bf16_f32 v164, v132, v133\n\t" \
  "v_cvt_pk_bf16_f32 v165, v134, v135\n\t" \
  "v_cvt_pk_bf16_f32 v166, v100, v101\n\t" \
  "v_cvt_pk_bf16_f32 v167, v102, v103\n\t" \
  "v_pk_fma_f32 v[136:137], v[180:181], v[100:101], v[136:137]\n\t" \
  "v_pk_fma_f32 v[138:139], v[182:183], v[102:103], v[138:139]\n\t" \
  "v_permlane16_swap_b32 v164, v166\n\t" \
  "v_permlane16_swap_b32 v165, v167\n\t" \
  "buffer_store_dwordx4 v[164:167], %[l16], %[srd], %[sh] offen offset:2048 sc1\n\t" \
  "s_add_u32 %[sh], %[sh], 0x1000\n\t" \
  "v_pk_fma_f32 v[104:105], v[180:181], v[132:133], v[104:105] neg_lo:[1,0,0] neg_hi:[1,0,0]\n\t" \
  "v_pk_fma_f32 v[106:107], v[182:183], v[134:135], v[106:107] neg_lo:[1,0,0] neg_hi:[1,0,0]\n\t" \
  "v_pk_fma_f32 v[136:137], v[176:177], v[132:133], v[136:137]\n\t" \
  "v_pk_fma_f32 v[138:139], v[178:179], v[134:135], v[138:139]\n\t" \
  "v_pk_fma_f32 v[104:105], v[176:177], v[100:101], v[104:105]\n\t" \
  "v_pk_fma_f32 v[106:107], v[178:179], v[102:103], v[106:107]\n\t" \
  "v_cvt_pk_bf16_f32 v168, v136, v137\n\t" \
  "v_cvt_pk_bf16_f32 v169, v138, v139\n\t" \
  "v_cvt_pk_bf16_f32 v170, v104, v105\n\t" \
  "v_cvt_pk_bf16_f32 v171, v106, v107\n\t" \
  "v_pk_fma_f32 v[140:141], v[180:181], v[104:105], v[140:141]\n\t" \
  "v_pk_fma_f32 v[142:143], v[182:183], v[106:107], v[142:143]\n\t" \
  "v_permlane16_swap_b32 v168, v170\n\t" \
  "v_permlane16_swap_b32 v169, v171\n\t" \
  "buffer_store_dwordx4 v[168:171], %[l16], %[srd], %[sh] offen offset:0 sc1\n\t" \
  "v_pk_fma_f32 v[108:109], v[180:181], v[136:137], v[108:109] neg_lo:[1,0,0] neg_hi:[1,0,0]\n\t" \
  "v_pk_fma_f32 v[110:111], v[182:183], v[138:139], v[110:111] neg_lo:[1,0,0] neg_hi:[1,0,0]\n\t" \
  "v_pk_fma_f32 v[140:141], v[176:177], v[136:137], v[140:141]\n\t" \
  "v_pk_fma_f32 v[142:143], v[178:179], v[138:139], v[142:143]\n\t" \
  "v_pk_fma_f32 v[108:109], v[176:177], v[104:105], v[108:109]\n\t" \
  "v_pk_fma_f32 v[110:111], v[178:179], v[106:107], v[110:111]\n\t" \
  "v_cvt_pk_bf16_f32 v172, v140, v141\n\t" \
  "v_cvt_pk_bf16_f32 v173, v142, v143\n\t" \
  "v_cvt_pk_bf16_f32 v174, v108, v109\n\t" \
  "v_cvt_pk_bf16_f32 v175, v110, v111\n\t" \
  "v_pk_fma_f32 v[144:145], v[180:181], v[108:109], v[144:145]\n\t" \
  "v_pk_fma_f32 v[146:147], v[182:183], v[110:111], v[146:147]\n\t" \
  "v_permlane16_swap_b32 v172, v174\n\t" \
  "v_permlane16_swap_b32 v173, v175\n\t" \
  "buffer_store_dwordx4 v[172:175], %[l16], %[srd], %[sh] offen offset:2048 sc1\n\t" \
  "s_add_u32 %[sh], %[sh], 0x1000\n\t" \
  "v_pk_fma_f32 v[112:113], v[180:181], v[140:141], v[112:113] neg_lo:[1,0,0] neg_hi:[1,0,0]\n\t" \
  "v_pk_fma_f32 v[114:115], v[182:183], v[142:143], v[114:115] neg_lo:[1,0,0] neg_hi:[1,0,0]\n\t" \
  "v_pk_fma_f32 v[144:145], v[176:177], v[140:141], v[144:145]\n\t" \
  "v_pk_fma_f32 v[146:147], v[178:179], v[142:143], v[146:147]\n\t" \
  "v_pk_fma_f32 v[112:113], v[176:177], v[108:109], v[112:113]\n\t" \
  "v_pk_fma_f32 v[114:115], v[178:179], v[110:111], v[114:115]\n\t" \
  "v_cvt_pk_bf16_f32 v192, v144, v145\n\t" \
  "v_cvt_pk_bf16_f32 v193, v146, v147\n\t" \
  "v_cvt_pk_bf16_f32 v194, v112, v113\n\t" \
  "v_cvt_pk_bf16_f32 v195, v114, v115\n\t" \
  "v_pk_fma_f32 v[148:149], v[180:181], v[112:113], v[148:149]\n\t" \
  "v_pk_fma_f32 v[150:151], v[182:183], v[114:115], v[150:151]\n\t" \
  "v_permlane16_swap_b32 v192, v194\n\t" \
  "v_permlane16_swap_b32 v193, v195\n\t" \
  "buffer_store_dwordx4 v[192:195], %[l16], %[srd], %[sh] offen offset:0 sc1\n\t" \
  "v_pk_fma_f32 v[116:117], v[180:181], v[144:145], v[116:117] neg_lo:[1,0,0] neg_hi:[1,0,0]\n\t" \
  "v_pk_fma_f32 v[118:119], v[182:183], v[146:147], v[118:119] neg_lo:[1,0,0] neg_hi:[1,0,0]\n\t" \
  "v_pk_fma_f32 v[148:149], v[176:177], v[144:145], v[148:149]\n\t" \
  "v_pk_fma_f32 v[150:151], v[178:179], v[146:147], v[150:151]\n\t" \
  "v_pk_fma_f32 v[116:117], v[176:177], v[112:113], v[116:117]\n\t" \
  "v_pk_fma_f32 v[118:119], v[178:179], v[114:115], v[118:119]\n\t" \
  "v_cvt_pk_bf16_f32 v196, v148, v149\n\t" \
  "v_cvt_pk_bf16_f32 v197, v150, v151\n\t" \
  "v_cvt_pk_bf16_f32 v198, v116, v117\n\t" \
  "v_cvt_pk_bf16_f32 v199, v118, v119\n\t" \
  "v_pk_fma_f32 v[152:153], v[180:181], v[116:117], v[152:153]\n\t" \
  "v_pk_fma_f32 v[154:155], v[182:183], v[118:119], v[154:155]\n\t" \
  "v_permlane16_swap_b32 v196, v198\n\t" \
  "v_permlane16_swap_b32 v197, v199\n\t" \
  "buffer_store_dwordx4 v[196:199], %[l16], %[srd], %[sh] offen offset:2048 sc1\n\t" \
  "s_add_u32 %[sh], %[sh], 0x1000\n\t" \
  "v_pk_fma_f32 v[120:121], v[180:181], v[148:149], v[120:121] neg_lo:[1,0,0] neg_hi:[1,0,0]\n\t" \
  "v_pk_fma_f32 v[122:123], v[182:183], v[150:151], v[122:123] neg_lo:[1,0,0] neg_hi:[1,0,0]\n\t" \
  "v_pk_fma_f32 v[152:153], v[176:177], v[148:149], v[152:153]\n\t" \
  "v_pk_fma_f32 v[154:155], v[178:179], v[150:151], v[154:155]\n\t" \
  "v_pk_fma_f32 v[120:121], v[176:177], v[116:117], v[120:121]\n\t" \
  "v_pk_fma_f32 v[122:123], v[178:179], v[118:119], v[122:123]\n\t" \
  "v_cvt_pk_bf16_f32 v200, v152, v153\n\t" \
  "v_cvt_pk_bf16_f32 v201, v154, v155\n\t" \
  "v_cvt_pk_bf16_f32 v202, v120, v121\n\t" \
  "v_cvt_pk_bf16_f32 v203, v122, v123\n\t" \
  "v_pk_fma_f32 v[156:157], v[180:181], v[120:121], v[156:157]\n\t" \
  "v_pk_fma_f32 v[158:159], v[182:183], v[122:123], v[158:159]\n\t" \
  "v_permlane16_swap_b32 v200, v202\n\t" \
  "v_permlane16_swap_b32 v201, v203\n\t" \
  "buffer_store_dwordx4 v[200:203], %[l16], %[srd], %[sh] offen offset:0 sc1\n\t" \
  "v_pk_fma_f32 v[124:125], v[180:181], v[152:153], v[124:125] neg_lo:[1,0,0] neg_hi:[1,0,0]\n\t" \
  "v_pk_fma_f32 v[126:127], v[182:183], v[154:155], v[126:127] neg_lo:[1,0,0] neg_hi:[1,0,0]\n\t" \
  "v_pk_fma_f32 v[156:157], v[176:177], v[152:153], v[156:157]\n\t" \
  "v_pk_fma_f32 v[158:159], v[178:179], v[154:155], v[158:159]\n\t" \
  "v_pk_fma_f32 v[124:125], v[176:177], v[120:121], v[124:125]\n\t" \
  "v_pk_fma_f32 v[126:127], v[178:179], v[122:123], v[126:127]\n\t" \
  "v_cvt_pk_bf16_f32 v204, v156, v157\n\t" \
  "v_cvt_pk_bf16_f32 v205, v158, v159\n\t" \
  "v_cvt_pk_bf16_f32 v206, v124, v125\n\t" \
  "v_cvt_pk_bf16_f32 v207, v126, v127\n\t" \
  "s_nop 1\n\t" \
  "v_permlane16_swap_b32 v204, v206\n\t" \
  "v_permlane16_swap_b32 v205, v207\n\t" \
  "buffer_store_dwordx4 v[204:207], %[l16], %[srd], %[sh] offen offset:2048 sc1\n\t" \
  "s_add_u32 %[scn], %[scn], 8\n\t" \
  "s_mov_b32 m0, %[scn]\n\t" \
  "s_mov_b64 exec, 1\n\t" \
  "buffer_load_dword %[l16], %[srz], 0 offen lds\n\t" \
  "s_mov_b64 exec, -1\n\t" \
  "s_branch Lend_%=\n\t" \
  "Lx0_%=:\n\t" \
  "v_add_u32 v244, %[sx], %[xo]\n\t" \
  "v_xor_b32 v245, 16, v244\n\t" \
  "v_xor_b32 v246, 32, v244\n\t" \
  "v_xor_b32 v247, 48, v244\n\t" \
  "ds_read_b64 %[xe00], v244 offset:0\n\t" \
  "ds_read_b64 %[xe10], v244 offset:2048\n\t" \
  "ds_read_b64 %[xe01], v245 offset:128\n\t" \
  "ds_read_b64 %[xe11], v245 offset:2176\n\t" \
  "ds_read_b64 %[xe02], v246 offset:256\n\t" \
  "ds_read_b64 %[xe12], v246 offset:2304\n\t" \
  "ds_read_b64 %[xe03], v247 offset:384\n\t" \
  "ds_read_b64 %[xe13], v247 offset:2432\n\t" \
  "s_branch Lxb0_%=\n\t" \
  "Lsp1_%=:\n\t" \
  "ds_read_b64 v[224:225], %[fl]\n\t" \
  "s_sub_i32 %[bud], %[bud], 1\n\t" \
  "s_cmp_lt_i32 %[bud], 0\n\t" \
  "s_cbranch_scc1 Lsx1_%=\n\t" \
  "s_waitcnt lgkmcnt(0)\n\t" \
  "v_or_b32 v224, v224, v225\n\t" \
  "s_nop 0\n\t" \
  "v_readfirstlane_b32 %[s0], v224\n\t" \
  "s_cmp_eq_u32 %[s0], 0\n\t" \
  "s_cbranch_scc1 Lsx1_%=\n\t" \
  "s_branch Lsp1_%=\n\t" \
  "Lsx1_%=:\n\t" \
  "s_cmp_eq_u32 %[sk], %[ks]\n\t" \
  "s_cbranch_scc0 Lb1_%=\n\t" \
  "v_add_u32 v244, %[sx], %[xo]\n\t" \
  "v_xor_b32 v245, 16, v244\n\t" \
  "v_xor_b32 v246, 32, v244\n\t" \
  "v_xor_b32 v247, 48, v244\n\t" \
  "ds_read_b64 %[xe00], v244 offset:0\n\t" \
  "ds_read_b64 %[xe10], v244 offset:2048\n\t" \
  "ds_read_b64 %[xe01], v245 offset:128\n\t" \
  "ds_read_b64 %[xe11], v245 offset:2176\n\t" \
  "ds_read_b64 %[xe02], v246 offset:256\n\t" \
  "ds_read_b64 %[xe12], v246 offset:2304\n\t" \
  "ds_read_b64 %[xe03], v247 offset:384\n\t" \
  "ds_read_b64 %[xe13], v247 offset:2432\n\t" \
  "s_branch Lb1_%=\n\t" \
  "Lsp3_%=:\n\t" \
  "v_mfma_f32_16x16x32_bf16 v[92:95], v[208:211], v[204:207], v[92:95]\n\t" \
  "ds_read_b128 v[172:175], %[pb1] offset:14336\n\t" \
  "v_mfma_f32_16x16x32_bf16 v[124:127], v[212:215], v[204:207], v[124:127]\n\t" \
  "s_waitcnt lgkmcnt(0)\n\t" \
  "v_mfma_f32_16x16x32_bf16 v[64:67], v[216:219], v[144:147], v[64:67]\n\t" \
  "v_mfma_f32_16x16x32_bf16 v[96:99], v[220:223], v[144:147], v[96:99]\n\t" \
  "v_mfma_f32_16x16x32_bf16 v[68:71], v[216:219], v[148:151], v[68:71]\n\t" \
  "v_mfma_f32_16x16x32_bf16 v[100:103], v[220:223], v[148:151], v[100:103]\n\t" \
  "v_mfma_f32_16x16x32_bf16 v[72:75], v[216:219], v[152:155], v[72:75]\n\t" \
  "v_mfma_f32_16x16x32_bf16 v[104:107], v[220:223], v[152:155], v[104:107]\n\t" \
  "v_mfma_f32_16x16x32_bf16 v[76:79], v[216:219], v[156:159], v[76:79]\n\t" \
  "v_mfma_f32_16x16x32_bf16 v[108:111], v[220:223], v[156:159], v[108:111]\n\t" \
  "v_mfma_f32_16x16x32_bf16 v[80:83], v[216:219], v[160:163], v[80:83]\n\t" \
  "v_mfma_f32_16x16x32_bf16 v[112:115], v[220:223], v[160:163], v[112:115]\n\t" \
  "v_mfma_f32_16x16x32_bf16 v[84:87], v[216:219], v[164:167], v[84:87]\n\t" \
  "v_mfma_f32_16x16x32_bf16 v[116:119], v[220:223], v[164:167], v[116:119]\n\t" \
  "v_mfma_f32_16x16x32_bf16 v[88:91], v[216:219], v[168:171], v[88:91]\n\t" \
  "v_mfma_f32_16x16x32_bf16 v[120:123], v[220:223], v[168:171], v[120:123]\n\t" \
  "v_mfma_f32_16x16x32_bf16 v[92:95], v[216:219], v[172:175], v[92:95]\n\t" \
  "v_mfma_f32_16x16x32_bf16 v[124:127], v[220:223], v[172:175], v[124:127]\n\t" \
  "v_add_u32 %[pb1], %[s2], %[pb1]\n\t" \
  "Lsa3_%=:\n\t" \
  "ds_read_b64 v[224:225], %[fl]\n\t" \
  "s_sub_i32 %[bud], %[bud], 1\n\t" \
  "s_cmp_lt_i32 %[bud], 0\n\t" \
  "s_cbranch_scc1 Lsb3_%=\n\t" \
  "s_waitcnt lgkmcnt(0)\n\t" \
  "v_or_b32 v224, v224, v225\n\t" \
  "s_nop 0\n\t" \
  "v_readfirstlane_b32 %[s0], v224\n\t" \
  "s_cmp_eq_u32 %[s0], 0\n\t" \
  "s_cbranch_scc1 Lsb3_%=\n\t" \
  "s_branch Lsa3_%=\n\t" \
  "Lsb3_%=:\n\t" \
  "ds_read_b128 v[128:131], %[pa0] offset:0\n\t" \
  "ds_read_b128 v[132:135], %[pa0] offset:8192\n\t" \
  "ds_read_b128 v[136:139], %[pa1] offset:0\n\t" \
  "ds_read_b128 v[140:143], %[pa1] offset:8192\n\t" \
  "ds_read_b128 v[176:179], %[pb0] offset:0\n\t" \
  "ds_read_b128 v[180:183], %[pb0] offset:2048\n\t" \
  "ds_read_b128 v[184:187], %[pb0] offset:4096\n\t" \
  "ds_read_b128 v[188:191], %[pb0] offset:6144\n\t" \
  "ds_read_b128 v[192:195], %[pb0] offset:8192\n\t" \
  "ds_read_b128 v[196:199], %[pb0] offset:10240\n\t" \
  "ds_read_b128 v[200:203], %[pb0] offset:12288\n\t" \
  "ds_read_b128 v[204:207], %[pb0] offset:14336\n\t" \
  "v_add_u32 %[fl], 8, %[fl]\n\t" \
  "s_waitcnt lgkmcnt(8)\n\t" \
  "s_mov_b64 exec, 1\n\t" \
  "buffer_load_dword %[l16], %[srz], 0 offen lds\n\t" \
  "s_mov_b64 exec, -1\n\t" \
  "s_add_u32 m0, m0, 16\n\t" \
  "s_cmp_eq_u32 %[sk], %[ks]\n\t" \
  "s_cbranch_scc0 Lnx3_%=\n\t" \
  "v_add_u32 v244, %[sx], %[xo]\n\t" \
  "v_xor_b32 v245, 16, v244\n\t" \
  "v_xor_b32 v246, 32, v244\n\t" \
  "v_xor_b32 v247, 48, v244\n\t" \
  "ds_read_b64 %[xe00], v244 offset:0\n\t" \
  "ds_read_b64 %[xe10], v244 offset:2048\n\t" \
  "ds_read_b64 %[xe01], v245 offset:128\n\t" \
  "ds_read_b64 %[xe11], v245 offset:2176\n\t" \
  "ds_read_b64 %[xe02], v246 offset:256\n\t" \
  "ds_read_b64 %[xe12], v246 offset:2304\n\t" \
  "ds_read_b64 %[xe03], v247 offset:384\n\t" \
  "ds_read_b64 %[xe13], v247 offset:2432\n\t" \
  "s_branch Lnx3_%=\n\t" \
  "Lsp5_%=:\n\t" \
  "ds_read_b64 v[224:225], %[fl]\n\t" \
  "s_sub_i32 %[bud], %[bud], 1\n\t" \
  "s_cmp_lt_i32 %[bud], 0\n\t" \
  "s_cbranch_scc1 Lsx5_%=\n\t" \
  "s_waitcnt lgkmcnt(0)\n\t" \
  "v_or_b32 v224, v224, v225\n\t" \
  "s_nop 0\n\t" \
  "v_readfirstlane_b32 %[s0], v224\n\t" \
  "s_cmp_eq_u32 %[s0], 0\n\t" \
  "s_cbranch_scc1 Lsx5_%=\n\t" \
  "s_branch Lsp5_%=\n\t" \
  "Lsx5_%=:\n\t" \
  "s_cmp_eq_u32 %[sk], %[ks]\n\t" \
  "s_cbranch_scc0 Lb5_%=\n\t" \
  "v_add_u32 v244, %[sx], %[xo]\n\t" \
  "v_xor_b32 v245, 16, v244\n\t" \
  "v_xor_b32 v246, 32, v244\n\t" \
  "v_xor_b32 v247, 48, v244\n\t" \
  "ds_read_b64 %[xe00], v244 offset:0\n\t" \
  "ds_read_b64 %[xe10], v244 offset:2048\n\t" \
  "ds_read_b64 %[xe01], v245 offset:128\n\t" \
  "ds_read_b64 %[xe11], v245 offset:2176\n\t" \
  "ds_read_b64 %[xe02], v246 offset:256\n\t" \
  "ds_read_b64 %[xe12], v246 offset:2304\n\t" \
  "ds_read_b64 %[xe03], v247 offset:384\n\t" \
  "ds_read_b64 %[xe13], v247 offset:2432\n\t" \
  "s_branch Lb5_%=\n\t" \
  "Lend_%=:\n\t" \
  "s_nop 7\n\t" \
  "s_nop 7\n\t"
#define ASM_MB \
  "s_nop 4\n\t" \
  "s_mov_b32 %[sg], 0\n\t" \
  "s_mov_b32 m0, %[scn]\n\t" \
  "Lc0_%=:\n\t" \
  "ds_read_b32 v192, %[f9]\n\t" \
  "s_sub_i32 %[bud], %[bud], 1\n\t" \
  "s_cmp_lt_i32 %[bud], 0\n\t" \
  "s_cbranch_scc1 Lc0b_%=\n\t" \
  "s_waitcnt lgkmcnt(0)\n\t" \
  "v_cmp_ne_u32 vcc, 0, v192\n\t" \
  "s_cbranch_vccnz Lc0_%=\n\t" \
  "Lc0b_%=:\n\t" \
  "ds_read_b128 v[128:131], %[pc0] offset:0\n\t" \
  "ds_read_b128 v[132:135], %[pc0] offset:2048\n\t" \
  "ds_read_b128 v[136:139], %[pc0] offset:4096\n\t" \
  "ds_read_b128 v[140:143], %[pc0] offset:6144\n\t" \
  "ds_read_b128 v[144:147], %[pc1] offset:0\n\t" \
  "ds_read_b128 v[148:151], %[pc1] offset:2048\n\t" \
  "ds_read_b128 v[152:155], %[pc1] offset:4096\n\t" \
  "ds_read_b128 v[156:159], %[pc1] offset:6144\n\t" \
  "s_waitcnt vmcnt(0)\n\t" \
  "v_readfirstlane_b32 %[s0], %[cnt]\n\t" \
  "s_cmp_eq_u32 %[s0], 31\n\t" \
  "s_cbranch_scc1 Lf2_%=\n\t" \
  "s_cmp_eq_u32 %[w], 0\n\t" \
  "s_cbranch_scc0 Lw_%=\n\t" \
  "Lf_%=:\n\t" \
  "v_mov_b32 v196, 0\n\t" \
  "s_mov_b64 exec, 1\n\t" \
  "buffer_atomic_or v196, off, %[srd], %[sf] sc0 sc1\n\t" \
  "s_mov_b64 exec, -1\n\t" \
  "s_sub_i32 %[bud], %[bud], 16\n\t" \
  "s_cmp_lt_i32 %[bud], 0\n\t" \
  "s_cbranch_scc1 Lf2_%=\n\t" \
  "s_waitcnt vmcnt(0)\n\t" \
  "v_readfirstlane_b32 %[s0], v196\n\t" \
  "s_cmp_lg_u32 %[s0], 32\n\t" \
  "s_cbranch_scc0 Lf2_%=\n\t" \
  "s_sleep 1\n\t" \
  "s_branch Lf_%=\n\t" \
  "Lf2_%=:\n\t" \
  "v_mov_b32 v192, 0\n\t" \
  "v_mov_b32 v193, 0\n\t" \
  "ds_write_b64 %[hr], v[192:193]\n\t" \
  "Lf3_%=:\n\t" \
  "s_setprio 2\n\t" \
  "buffer_load_dwordx4 v[64:67], %[vo], %[srd], %[h0] offen offset:0 sc1\n\t" \
  "buffer_load_dwordx4 v[68:71], %[vo], %[srd], %[h0] offen offset:2048 sc1\n\t" \
  "buffer_load_dwordx4 v[72:75], %[vo], %[srd], %[h1] offen offset:0 sc1\n\t" \
  "buffer_load_dwordx4 v[76:79], %[vo], %[srd], %[h1] offen offset:2048 sc1\n\t" \
  "buffer_load_dwordx4 v[80:83], %[vo], %[srd], %[h2] offen offset:0 sc1\n\t" \
  "buffer_load_dword v194, off, %[srz], 0\n\t" \
  "buffer_load_dwordx4 v[84:87], %[vo], %[srd], %[h2] offen offset:2048 sc1\n\t" \
  "buffer_load_dwordx4 v[88:91], %[vo], %[srd], %[h3] offen offset:0 sc1\n\t" \
  "buffer_load_dwordx4 v[92:95], %[vo], %[srd], %[h3] offen offset:2048 sc1\n\t" \
  "buffer_load_dwordx4 v[96:99], %[vo], %[srd], %[h4] offen offset:0 sc1\n\t" \
  "buffer_load_dwordx4 v[100:103], %[vo], %[srd], %[h4] offen offset:2048 sc1\n\t" \
  "buffer_load_dwordx4 v[104:107], %[vo], %[srd], %[h5] offen offset:0 sc1\n\t" \
  "buffer_load_dwordx4 v[108:111], %[vo], %[srd], %[h5] offen offset:2048 sc1\n\t" \
  "buffer_load_dwordx4 v[112:115], %[vo], %[srd], %[h6] offen offset:0 sc1\n\t" \
  "buffer_load_dword v194, off, %[srz], 0\n\t" \
  "buffer_load_dwordx4 v[116:119], %[vo], %[srd], %[h6] offen offset:2048 sc1\n\t" \
  "s_waitcnt lgkmcnt(0)\n\t" \
  "Lloop_%=:\n\t" \
  "s_waitcnt vmcnt(14) lgkmcnt(4)\n\t" \
  "v_mfma_f32_16x16x32_bf16 %[c00], v[64:67], v[128:131], %[c00]\n\t" \
  "buffer_load_dwordx4 v[120:123], %[vo], %[srd], %[h7] offen offset:0 sc1\n\t" \
  "v_mfma_f32_16x16x32_bf16 %[c10], v[68:71], v[128:131], %[c10]\n\t" \
  "ds_read_b128 v[160:163], %[pc2] offset:0\n\t" \
  "v_add_u32 %[pc0], %[k2], %[pc0]\n\t" \
  "v_mfma_f32_16x16x32_bf16 %[c01], v[64:67], v[132:135], %[c01]\n\t" \
  "v_mfma_f32_16x16x32_bf16 %[c11], v[68:71], v[132:135], %[c11]\n\t" \
  "ds_read_b128 v[164:167], %[pc2] offset:2048\n\t" \
  "v_mfma_f32_16x16x32_bf16 %[c02], v[64:67], v[136:139], %[c02]\n\t" \
  "buffer_load_dwordx4 v[124:127], %[vo], %[srd], %[h7] offen offset:2048 sc1\n\t" \
  "v_mfma_f32_16x16x32_bf16 %[c12], v[68:71], v[136:139], %[c12]\n\t" \
  "ds_read_b128 v[168:171], %[pc2] offset:4096\n\t" \
  "v_mfma_f32_16x16x32_bf16 %[c03], v[64:67], v[140:143], %[c03]\n\t" \
  "v_add_u32 %[vo], 0x20000, %[vo]\n\t" \
  "v_and_b32 %[vo], 0xfffff, %[vo]\n\t" \
  "v_mfma_f32_16x16x32_bf16 %[c13], v[68:71], v[140:143], %[c13]\n\t" \
  "ds_read_b128 v[172:175], %[pc2] offset:6144\n\t" \
  "s_waitcnt vmcnt(14) lgkmcnt(4)\n\t" \
  "v_mfma_f32_16x16x32_bf16 %[c00], v[72:75], v[144:147], %[c00]\n\t" \
  "buffer_load_dwordx4 v[64:67], %[vo], %[srd], %[h0] offen offset:0 sc1\n\t" \
  "v_mfma_f32_16x16x32_bf16 %[c10], v[76:79], v[144:147], %[c10]\n\t" \
  "ds_read_b128 v[176:179], %[pc3] offset:0\n\t" \
  "v_add_u32 %[pc1], %[k2], %[pc1]\n\t" \
  "v_mfma_f32_16x16x32_bf16 %[c01], v[72:75], v[148:151], %[c01]\n\t" \
  "s_cmp_eq_u32 %[sg], 2\n\t" \
  "s_cbranch_scc1 Lh_%=\n\t" \
  "Lhb_%=:\n\t" \
  "v_mfma_f32_16x16x32_bf16 %[c11], v[76:79], v[148:151], %[c11]\n\t" \
  "ds_read_b128 v[180:183], %[pc3] offset:2048\n\t" \
  "v_mfma_f32_16x16x32_bf16 %[c02], v[72:75], v[152:155], %[c02]\n\t" \
  "buffer_load_dwordx4 v[68:71], %[vo], %[srd], %[h0] offen offset:2048 sc1\n\t" \
  "v_mfma_f32_16x16x32_bf16 %[c12], v[76:79], v[152:155], %[c12]\n\t" \
  "ds_read_b128 v[184:187], %[pc3] offset:4096\n\t" \
  "v_mfma_f32_16x16x32_bf16 %[c03], v[72:75], v[156:159], %[c03]\n\t" \
  "v_mfma_f32_16x16x32_bf16 %[c13], v[76:79], v[156:159], %[c13]\n\t" \
  "ds_read_b128 v[188:191], %[pc3] offset:6144\n\t" \
  "s_waitcnt vmcnt(13) lgkmcnt(4)\n\t" \
  "v_mfma_f32_16x16x32_bf16 %[c00], v[80:83], v[160:163], %[c00]\n\t" \
  "buffer_load_dwordx4 v[72:75], %[vo], %[srd], %[h1] offen offset:0 sc1\n\t" \
  "v_mfma_f32_16x16x32_bf16 %[c10], v[84:87], v[160:163], %[c10]\n\t" \
  "ds_read_b128 v[128:131], %[pc0] offset:0\n\t" \
  "v_add_u32 %[pc2], %[k2], %[pc2]\n\t" \
  "v_mfma_f32_16x16x32_bf16 %[c01], v[80:83], v[164:167], %[c01]\n\t" \
  "v_mfma_f32_16x16x32_bf16 %[c11], v[84:87], v[164:167], %[c11]\n\t" \
  "ds_read_b128 v[132:135], %[pc0] offset:2048\n\t" \
  "v_mfma_f32_16x16x32_bf16 %[c02], v[80:83], v[168:171], %[c02]\n\t" \
  "buffer_load_dwordx4 v[76:79], %[vo], %[srd], %[h1] offen offset:2048 sc1\n\t" \
  "v_mfma_f32_16x16x32_bf16 %[c12], v[84:87], v[168:171], %[c12]\n\t" \
  "ds_read_b128 v[136:139], %[pc0] offset:4096\n\t" \
  "v_mfma_f32_16x16x32_bf16 %[c03], v[80:83], v[172:175], %[c03]\n\t" \
  "v_mfma_f32_16x16x32_bf16 %[c13], v[84:87], v[172:175], %[c13]\n\t" \
  "ds_read_b128 v[140:143], %[pc0] offset:6144\n\t" \
  "s_waitcnt vmcnt(13) lgkmcnt(4)\n\t" \
  "v_mfma_f32_16x16x32_bf16 %[c00], v[88:91], v[176:179], %[c00]\n\t" \
  "buffer_load_dwordx4 v[80:83], %[vo], %[srd], %[h2] offen offset:0 sc1\n\t" \
  "v_mfma_f32_16x16x32_bf16 %[c10], v[92:95], v[176:179], %[c10]\n\t" \
  "ds_read_b128 v[144:147], %[pc1] offset:0\n\t" \
  "v_add_u32 %[pc3], %[k2], %[pc3]\n\t" \
  "v_mfma_f32_16x16x32_bf16 %[c01], v[88:91], v[180:183], %[c01]\n\t" \
  "s_mov_b64 exec, 1\n\t" \
  "buffer_load_dword off, %[srz], 0 lds\n\t" \
  "s_mov_b64 exec, -1\n\t" \
  "v_mfma_f32_16x16x32_bf16 %[c11], v[92:95], v[180:183], %[c11]\n\t" \
  "ds_read_b128 v[148:151], %[pc1] offset:2048\n\t" \
  "v_mfma_f32_16x16x32_bf16 %[c02], v[88:91], v[184:187], %[c02]\n\t" \
  "buffer_load_dwordx4 v[84:87], %[vo], %[srd], %[h2] offen offset:2048 sc1\n\t" \
  "v_mfma_f32_16x16x32_bf16 %[c12], v[92:95], v[184:187], %[c12]\n\t" \
  "ds_read_b128 v[152:155], %[pc1] offset:4096\n\t" \
  "s_add_u32 m0, m0, 16\n\t" \
  "v_mfma_f32_16x16x32_bf16 %[c03], v[88:91], v[188:191], %[c03]\n\t" \
  "v_mfma_f32_16x16x32_bf16 %[c13], v[92:95], v[188:191], %[c13]\n\t" \
  "ds_read_b128 v[156:159], %[pc1] offset:6144\n\t" \
  "s_waitcnt vmcnt(14) lgkmcnt(4)\n\t" \
  "v_mfma_f32_16x16x32_bf16 %[c00], v[96:99], v[128:131], %[c00]\n\t" \
  "buffer_load_dwordx4 v[88:91], %[vo], %[srd], %[h3] offen offset:0 sc1\n\t" \
  "v_mfma_f32_16x16x32_bf16 %[c10], v[100:103], v[128:131], %[c10]\n\t" \
  "ds_read_b128 v[160:163], %[pc2] offset:0\n\t" \
  "v_add_u32 %[pc0], %[k2], %[pc0]\n\t" \
  "v_mfma_f32_16x16x32_bf16 %[c01], v[96:99], v[132:135], %[c01]\n\t" \
  "v_mfma_f32_16x16x32_bf16 %[c11], v[100:103], v[132:135], %[c11]\n\t" \
  "ds_read_b128 v[164:167], %[pc2] offset:2048\n\t" \
  "v_mfma_f32_16x16x32_bf16 %[c02], v[96:99], v[136:139], %[c02]\n\t" \
  "buffer_load_dwordx4 v[92:95], %[vo], %[srd], %[h3] offen offset:2048 sc1\n\t" \
  "v_mfma_f32_16x16x32_bf16 %[c12], v[100:103], v[136:139], %[c12]\n\t" \
  "ds_read_b128 v[168:171], %[pc2] offset:4096\n\t" \
  "v_mfma_f32_16x16x32_bf16 %[c03], v[96:99], v[140:143], %[c03]\n\t" \
  "v_mfma_f32_16x16x32_bf16 %[c13], v[100:103], v[140:143], %[c13]\n\t" \
  "ds_read_b128 v[172:175], %[pc2] offset:6144\n\t" \
  "s_waitcnt vmcnt(14) lgkmcnt(4)\n\t" \
  "v_mfma_f32_16x16x32_bf16 %[c00], v[104:107], v[144:147], %[c00]\n\t" \
  "buffer_load_dwordx4 v[96:99], %[vo], %[srd], %[h4] offen offset:0 sc1\n\t" \
  "v_mfma_f32_16x16x32_bf16 %[c10], v[108:111], v[144:147], %[c10]\n\t" \
  "ds_read_b128 v[176:179], %[pc3] offset:0\n\t" \
  "v_add_u32 %[pc1], %[k2], %[pc1]\n\t" \
  "v_mfma_f32_16x16x32_bf16 %[c01], v[104:107], v[148:151], %[c01]\n\t" \
  "v_mfma_f32_16x16x32_bf16 %[c11], v[108:111], v[148:151], %[c11]\n\t" \
  "ds_read_b128 v[180:183], %[pc3] offset:2048\n\t" \
  "v_mfma_f32_16x16x32_bf16 %[c02], v[104:107], v[152:155], %[c02]\n\t" \
  "buffer_load_dwordx4 v[100:103], %[vo], %[srd], %[h4] offen offset:2048 sc1\n\t" \
  "v_mfma_f32_16x16x32_bf16 %[c12], v[108:111], v[152:155], %[c12]\n\t" \
  "ds_read_b128 v[184:187], %[pc3] offset:4096\n\t" \
  "v_mfma_f32_16x16x32_bf16 %[c03], v[104:107], v[156:159], %[c03]\n\t" \
  "v_mfma_f32_16x16x32_bf16 %[c13], v[108:111], v[156:159], %[c13]\n\t" \
  "ds_read_b128 v[188:191], %[pc3] offset:6144\n\t" \
  "s_waitcnt vmcnt(13) lgkmcnt(4)\n\t" \
  "v_mfma_f32_16x16x32_bf16 %[c00], v[112:115], v[160:163], %[c00]\n\t" \
  "buffer_load_dwordx4 v[104:107], %[vo], %[srd], %[h5] offen offset:0 sc1\n\t" \
  "v_mfma_f32_16x16x32_bf16 %[c10], v[116:119], v[160:163], %[c10]\n\t" \
  "ds_read_b128 v[128:131], %[pc0] offset:0\n\t" \
  "v_add_u32 %[pc2], %[k2], %[pc2]\n\t" \
  "v_mfma_f32_16x16x32_bf16 %[c01], v[112:115], v[164:167], %[c01]\n\t" \
  "v_mfma_f32_16x16x32_bf16 %[c11], v[116:119], v[164:167], %[c11]\n\t" \
  "ds_read_b128 v[132:135], %[pc0] offset:2048\n\t" \
  "v_mfma_f32_16x16x32_bf16 %[c02], v[112:115], v[168:171], %[c02]\n\t" \
  "buffer_load_dwordx4 v[108:111], %[vo], %[srd], %[h5] offen offset:2048 sc1\n\t" \
  "v_mfma_f32_16x16x32_bf16 %[c12], v[116:119], v[168:171], %[c12]\n\t" \
  "ds_read_b128 v[136:139], %[pc0] offset:4096\n\t" \
  "v_mfma_f32_16x16x32_bf16 %[c03], v[112:115], v[172:175], %[c03]\n\t" \
  "v_mfma_f32_16x16x32_bf16 %[c13], v[116:119], v[172:175], %[c13]\n\t" \
  "ds_read_b128 v[140:143], %[pc0] offset:6144\n\t" \
  "s_waitcnt vmcnt(13) lgkmcnt(4)\n\t" \
  "v_mfma_f32_16x16x32_bf16 %[c00], v[120:123], v[176:179], %[c00]\n\t" \
  "buffer_load_dwordx4 v[112:115], %[vo], %[srd], %[h6] offen offset:0 sc1\n\t" \
  "v_mfma_f32_16x16x32_bf16 %[c10], v[124:127], v[176:179], %[c10]\n\t" \
  "ds_read_b128 v[144:147], %[pc1] offset:0\n\t" \
  "v_add_u32 %[pc3], %[k2], %[pc3]\n\t" \
  "v_mfma_f32_16x16x32_bf16 %[c01], v[120:123], v[180:183], %[c01]\n\t" \
  "s_mov_b64 exec, 1\n\t" \
  "buffer_load_dword off, %[srz], 0 lds\n\t" \
  "s_mov_b64 exec, -1\n\t" \
  "v_mfma_f32_16x16x32_bf16 %[c11], v[124:127], v[180:183], %[c11]\n\t" \
  "ds_read_b128 v[148:151], %[pc1] offset:2048\n\t" \
  "s_add_u32 %[sg], %[sg], 1\n\t" \
  "v_mfma_f32_16x16x32_bf16 %[c02], v[120:123], v[184:187], %[c02]\n\t" \
  "buffer_load_dwordx4 v[116:119], %[vo], %[srd], %[h6] offen offset:2048 sc1\n\t" \
  "v_mfma_f32_16x16x32_bf16 %[c12], v[124:127], v[184:187], %[c12]\n\t" \
  "ds_read_b128 v[152:155], %[pc1] offset:4096\n\t" \
  "s_add_u32 m0, m0, 16\n\t" \
  "v_mfma_f32_16x16x32_bf16 %[c03], v[120:123], v[188:191], %[c03]\n\t" \
  "s_cmp_eq_u32 %[sg], 3\n\t" \
  "s_cbranch_scc1 Lg_%=\n\t" \
  "Lgb_%=:\n\t" \
  "v_mfma_f32_16x16x32_bf16 %[c13], v[124:127], v[188:191], %[c13]\n\t" \
  "s_cmp_lt_u32 %[sg], 4\n\t" \
  "ds_read_b128 v[156:159], %[pc1] offset:6144\n\t" \
  "s_cbranch_scc1 Lloop_%=\n\t" \
  "s_mov_b32 %[s1], 0xfffe0000\n\t" \
  "Lloop2_%=:\n\t" \
  "s_waitcnt vmcnt(12) lgkmcnt(4)\n\t" \
  "v_mfma_f32_16x16x32_bf16 %[c00], v[64:67], v[128:131], %[c00]\n\t" \
  "ds_read_b64 v[192:193], %[fl] offset:0\n\t" \
  "buffer_load_dwordx4 v[120:123], %[vo], %[srd], %[h7] offen offset:0 sc1\n\t" \
  "v_mfma_f32_16x16x32_bf16 %[c10], v[68:71], v[128:131], %[c10]\n\t" \
  "ds_read_b128 v[160:163], %[pc2] offset:0\n\t" \
  "v_add_u32 %[pc0], %[s1], %[pc0]\n\t" \
  "v_mfma_f32_16x16x32_bf16 %[c01], v[64:67], v[132:135], %[c01]\n\t" \
  "v_mfma_f32_16x16x32_bf16 %[c11], v[68:71], v[132:135], %[c11]\n\t" \
  "ds_read_b128 v[164:167], %[pc2] offset:2048\n\t" \
  "v_mfma_f32_16x16x32_bf16 %[c02], v[64:67], v[136:139], %[c02]\n\t" \
  "buffer_load_dwordx4 v[124:127], %[vo], %[srd], %[h7] offen offset:2048 sc1\n\t" \
  "v_mfma_f32_16x16x32_bf16 %[c12], v[68:71], v[136:139], %[c12]\n\t" \
  "ds_read_b128 v[168:171], %[pc2] offset:4096\n\t" \
  "v_mfma_f32_16x16x32_bf16 %[c03], v[64:67], v[140:143], %[c03]\n\t" \
  "v_add_u32 %[vo], 0x20000, %[vo]\n\t" \
  "v_and_b32 %[vo], 0xfffff, %[vo]\n\t" \
  "v_mfma_f32_16x16x32_bf16 %[c13], v[68:71], v[140:143], %[c13]\n\t" \
  "ds_read_b128 v[172:175], %[pc2] offset:6144\n\t" \
  "s_waitcnt vmcnt(12) lgkmcnt(4)\n\t" \
  "v_mfma_f32_16x16x32_bf16 %[c00], v[72:75], v[144:147], %[c00]\n\t" \
  "buffer_load_dwordx4 v[64:67], %[vo], %[srd], %[h0] offen offset:0 sc1\n\t" \
  "v_mfma_f32_16x16x32_bf16 %[c10], v[76:79], v[144:147], %[c10]\n\t" \
  "ds_read_b128 v[176:179], %[pc3] offset:0\n\t" \
  "v_add_u32 %[pc1], %[s1], %[pc1]\n\t" \
  "v_mfma_f32_16x16x32_bf16 %[c01], v[72:75], v[148:151], %[c01]\n\t" \
  "v_or_b32 v192, v192, v193\n\t" \
  "v_mfma_f32_16x16x32_bf16 %[c11], v[76:79], v[148:151], %[c11]\n\t" \
  "ds_read_b128 v[180:183], %[pc3] offset:2048\n\t" \
  "v_readfirstlane_b32 %[s0], v192\n\t" \
  "v_mfma_f32_16x16x32_bf16 %[c02], v[72:75], v[152:155], %[c02]\n\t" \
  "buffer_load_dwordx4 v[68:71], %[vo], %[srd], %[h0] offen offset:2048 sc1\n\t" \
  "v_mfma_f32_16x16x32_bf16 %[c12], v[76:79], v[152:155], %[c12]\n\t" \
  "ds_read_b128 v[184:187], %[pc3] offset:4096\n\t" \
  "v_mfma_f32_16x16x32_bf16 %[c03], v[72:75], v[156:159], %[c03]\n\t" \
  "s_cmp_eq_u32 %[s0], 0\n\t" \
  "s_cbranch_scc0 Lcb1_%=\n\t" \
  "Lcb1b_%=:\n\t" \
  "v_mfma_f32_16x16x32_bf16 %[c13], v[76:79], v[156:159], %[c13]\n\t" \
  "ds_read_b128 v[188:191], %[pc3] offset:6144\n\t" \
  "s_waitcnt vmcnt(12) lgkmcnt(4)\n\t" \
  "v_mfma_f32_16x16x32_bf16 %[c00], v[80:83], v[160:163], %[c00]\n\t" \
  "buffer_load_dwordx4 v[72:75], %[vo], %[srd], %[h1] offen offset:0 sc1\n\t" \
  "v_mfma_f32_16x16x32_bf16 %[c10], v[84:87], v[160:163], %[c10]\n\t" \
  "ds_read_b128 v[128:131], %[pc0] offset:0\n\t" \
  "v_add_u32 %[pc2], %[s1], %[pc2]\n\t" \
  "v_mfma_f32_16x16x32_bf16 %[c01], v[80:83], v[164:167], %[c01]\n\t" \
  "v_mfma_f32_16x16x32_bf16 %[c11], v[84:87], v[164:167], %[c11]\n\t" \
  "ds_read_b128 v[132:135], %[pc0] offset:2048\n\t" \
  "v_mfma_f32_16x16x32_bf16 %[c02], v[80:83], v[168:171], %[c02]\n\t" \
  "buffer_load_dwordx4 v[76:79], %[vo], %[srd], %[h1] offen offset:2048 sc1\n\t" \
  "v_mfma_f32_16x16x32_bf16 %[c12], v[84:87], v[168:171], %[c12]\n\t" \
  "ds_read_b128 v[136:139], %[pc0] offset:4096\n\t" \
  "v_mfma_f32_16x16x32_bf16 %[c03], v[80:83], v[172:175], %[c03]\n\t" \
  "v_mfma_f32_16x16x32_bf16 %[c13], v[84:87], v[172:175], %[c13]\n\t" \
  "ds_read_b128 v[140:143], %[pc0] offset:6144\n\t" \
  "s_waitcnt vmcnt(12) lgkmcnt(4)\n\t" \
  "v_mfma_f32_16x16x32_bf16 %[c00], v[88:91], v[176:179], %[c00]\n\t" \
  "buffer_load_dwordx4 v[80:83], %[vo], %[srd], %[h2] offen offset:0 sc1\n\t" \
  "v_mfma_f32_16x16x32_bf16 %[c10], v[92:95], v[176:179], %[c10]\n\t" \
  "ds_read_b128 v[144:147], %[pc1] offset:0\n\t" \
  "v_add_u32 %[pc3], %[s1], %[pc3]\n\t" \
  "v_mfma_f32_16x16x32_bf16 %[c01], v[88:91], v[180:183], %[c01]\n\t" \
  "v_mfma_f32_16x16x32_bf16 %[c11], v[92:95], v[180:183], %[c11]\n\t" \
  "ds_read_b128 v[148:151], %[pc1] offset:2048\n\t" \
  "v_mfma_f32_16x16x32_bf16 %[c02], v[88:91], v[184:187], %[c02]\n\t" \
  "buffer_load_dwordx4 v[84:87], %[vo], %[srd], %[h2] offen offset:2048 sc1\n\t" \
  "v_mfma_f32_16x16x32_bf16 %[c12], v[92:95], v[184:187], %[c12]\n\t" \
  "ds_read_b128 v[152:155], %[pc1] offset:4096\n\t" \
  "v_mfma_f32_16x16x32_bf16 %[c03], v[88:91], v[188:191], %[c03]\n\t" \
  "v_mfma_f32_16x16x32_bf16 %[c13], v[92:95], v[188:191], %[c13]\n\t" \
  "ds_read_b128 v[156:159], %[pc1] offset:6144\n\t" \
  "s_waitcnt vmcnt(12) lgkmcnt(4)\n\t" \
  "v_mfma_f32_16x16x32_bf16 %[c00], v[96:99], v[128:131], %[c00]\n\t" \
  "ds_read_b64 v[192:193], %[fl] offset:8\n\t" \
  "buffer_load_dwordx4 v[88:91], %[vo], %[srd], %[h3] offen offset:0 sc1\n\t" \
  "v_mfma_f32_16x16x32_bf16 %[c10], v[100:103], v[128:131], %[c10]\n\t" \
  "ds_read_b128 v[160:163], %[pc2] offset:0\n\t" \
  "v_add_u32 %[pc0], %[k2], %[pc0]\n\t" \
  "v_mfma_f32_16x16x32_bf16 %[c01], v[96:99], v[132:135], %[c01]\n\t" \
  "v_mfma_f32_16x16x32_bf16 %[c11], v[100:103], v[132:135], %[c11]\n\t" \
  "ds_read_b128 v[164:167], %[pc2] offset:2048\n\t" \
  "v_mfma_f32_16x16x32_bf16 %[c02], v[96:99], v[136:139], %[c02]\n\t" \
  "buffer_load_dwordx4 v[92:95], %[vo], %[srd], %[h3] offen offset:2048 sc1\n\t" \
  "v_mfma_f32_16x16x32_bf16 %[c12], v[100:103], v[136:139], %[c12]\n\t" \
  "ds_read_b128 v[168:171], %[pc2] offset:4096\n\t" \
  "v_mfma_f32_16x16x32_bf16 %[c03], v[96:99], v[140:143], %[c03]\n\t" \
  "v_mfma_f32_16x16x32_bf16 %[c13], v[100:103], v[140:143], %[c13]\n\t" \
  "ds_read_b128 v[172:175], %[pc2] offset:6144\n\t" \
  "s_waitcnt vmcnt(12) lgkmcnt(4)\n\t" \
  "v_mfma_f32_16x16x32_bf16 %[c00], v[104:107], v[144:147], %[c00]\n\t" \
  "buffer_load_dwordx4 v[96:99], %[vo], %[srd], %[h4] offen offset:0 sc1\n\t" \
  "v_mfma_f32_16x16x32_bf16 %[c10], v[108:111], v[144:147], %[c10]\n\t" \
  "ds_read_b128 v[176:179], %[pc3] offset:0\n\t" \
  "v_add_u32 %[pc1], %[k2], %[pc1]\n\t" \
  "v_mfma_f32_16x16x32_bf16 %[c01], v[104:107], v[148:151], %[c01]\n\t" \
  "v_or_b32 v192, v192, v193\n\t" \
  "v_mfma_f32_16x16x32_bf16 %[c11], v[108:111], v[148:151], %[c11]\n\t" \
  "ds_read_b128 v[180:183], %[pc3] offset:2048\n\t" \
  "v_readfirstlane_b32 %[s0], v192\n\t" \
  "v_mfma_f32_16x16x32_bf16 %[c02], v[104:107], v[152:155], %[c02]\n\t" \
  "buffer_load_dwordx4 v[100:103], %[vo], %[srd], %[h4] offen offset:2048 sc1\n\t" \
  "v_mfma_f32_16x16x32_bf16 %[c12], v[108:111], v[152:155], %[c12]\n\t" \
  "ds_read_b128 v[184:187], %[pc3] offset:4096\n\t" \
  "v_mfma_f32_16x16x32_bf16 %[c03], v[104:107], v[156:159], %[c03]\n\t" \
  "s_cmp_eq_u32 %[s0], 0\n\t" \
  "s_cbranch_scc0 Lcb5_%=\n\t" \
  "Lcb5b_%=:\n\t" \
  "v_mfma_f32_16x16x32_bf16 %[c13], v[108:111], v[156:159], %[c13]\n\t" \
  "ds_read_b128 v[188:191], %[pc3] offset:6144\n\t" \
  "s_waitcnt vmcnt(12) lgkmcnt(4)\n\t" \
  "v_mfma_f32_16x16x32_bf16 %[c00], v[112:115], v[160:163], %[c00]\n\t" \
  "buffer_load_dwordx4 v[104:107], %[vo], %[srd], %[h5] offen offset:0 sc1\n\t" \
  "v_mfma_f32_16x16x32_bf16 %[c10], v[116:119], v[160:163], %[c10]\n\t" \
  "ds_read_b128 v[128:131], %[pc0] offset:0\n\t" \
  "v_add_u32 %[pc2], %[k2], %[pc2]\n\t" \
  "v_mfma_f32_16x16x32_bf16 %[c01], v[112:115], v[164:167], %[c01]\n\t" \
  "v_add_u32 %[fl], 16, %[fl]\n\t" \
  "s_mov_b32 %[s1], %[k2]\n\t" \
  "v_mfma_f32_16x16x32_bf16 %[c11], v[116:119], v[164:167], %[c11]\n\t" \
  "ds_read_b128 v[132:135], %[pc0] offset:2048\n\t" \
  "v_mfma_f32_16x16x32_bf16 %[c02], v[112:115], v[168:171], %[c02]\n\t" \
  "buffer_load_dwordx4 v[108:111], %[vo], %[srd], %[h5] offen offset:2048 sc1\n\t" \
  "v_mfma_f32_16x16x32_bf16 %[c12], v[116:119], v[168:171], %[c12]\n\t" \
  "ds_read_b128 v[136:139], %[pc0] offset:4096\n\t" \
  "v_mfma_f32_16x16x32_bf16 %[c03], v[112:115], v[172:175], %[c03]\n\t" \
  "v_mfma_f32_16x16x32_bf16 %[c13], v[116:119], v[172:175], %[c13]\n\t" \
  "ds_read_b128 v[140:143], %[pc0] offset:6144\n\t" \
  "s_waitcnt vmcnt(12) lgkmcnt(4)\n\t" \
  "v_mfma_f32_16x16x32_bf16 %[c00], v[120:123], v[176:179], %[c00]\n\t" \
  "buffer_load_dwordx4 v[112:115], %[vo], %[srd], %[h6] offen offset:0 sc1\n\t" \
  "v_mfma_f32_16x16x32_bf16 %[c10], v[124:127], v[176:179], %[c10]\n\t" \
  "ds_read_b128 v[144:147], %[pc1] offset:0\n\t" \
  "v_add_u32 %[pc3], %[k2], %[pc3]\n\t" \
  "v_mfma_f32_16x16x32_bf16 %[c01], v[120:123], v[180:183], %[c01]\n\t" \
  "s_add_u32 %[sg], %[sg], 1\n\t" \
  "s_cmp_lt_u32 %[sg], 7\n\t" \
  "v_mfma_f32_16x16x32_bf16 %[c11], v[124:127], v[180:183], %[c11]\n\t" \
  "ds_read_b128 v[148:151], %[pc1] offset:2048\n\t" \
  "v_mfma_f32_16x16x32_bf16 %[c02], v[120:123], v[184:187], %[c02]\n\t" \
  "buffer_load_dwordx4 v[116:119], %[vo], %[srd], %[h6] offen offset:2048 sc1\n\t" \
  "v_mfma_f32_16x16x32_bf16 %[c12], v[124:127], v[184:187], %[c12]\n\t" \
  "ds_read_b128 v[152:155], %[pc1] offset:4096\n\t" \
  "v_mfma_f32_16x16x32_bf16 %[c03], v[120:123], v[188:191], %[c03]\n\t" \
  "v_mfma_f32_16x16x32_bf16 %[c13], v[124:127], v[188:191], %[c13]\n\t" \
  "ds_read_b128 v[156:159], %[pc1] offset:6144\n\t" \
  "s_cbranch_scc1 Lloop2_%=\n\t" \
  "s_waitcnt vmcnt(12) lgkmcnt(4)\n\t" \
  "v_mfma_f32_16x16x32_bf16 %[c00], v[64:67], v[128:131], %[c00]\n\t" \
  "ds_read_b64 v[192:193], %[fl] offset:0\n\t" \
  "buffer_load_dwordx4 v[120:123], %[vo], %[srd], %[h7] offen offset:0 sc1\n\t" \
  "v_mfma_f32_16x16x32_bf16 %[c10], v[68:71], v[128:131], %[c10]\n\t" \
  "ds_read_b128 v[160:163], %[pc2] offset:0\n\t" \
  "v_add_u32 %[pc0], %[k2], %[pc0]\n\t" \
  "v_mfma_f32_16x16x32_bf16 %[c01], v[64:67], v[132:135], %[c01]\n\t" \
  "v_mfma_f32_16x16x32_bf16 %[c11], v[68:71], v[132:135], %[c11]\n\t" \
  "ds_read_b128 v[164:167], %[pc2] offset:2048\n\t" \
  "v_mfma_f32_16x16x32_bf16 %[c02], v[64:67], v[136:139], %[c02]\n\t" \
  "buffer_load_dwordx4 v[124:127], %[vo], %[srd], %[h7] offen offset:2048 sc1\n\t" \
  "v_mfma_f32_16x16x32_bf16 %[c12], v[68:71], v[136:139], %[c12]\n\t" \
  "ds_read_b128 v[168:171], %[pc2] offset:4096\n\t" \
  "v_mfma_f32_16x16x32_bf16 %[c03], v[64:67], v[140:143], %[c03]\n\t" \
  "v_mfma_f32_16x16x32_bf16 %[c13], v[68:71], v[140:143], %[c13]\n\t" \
  "ds_read_b128 v[172:175], %[pc2] offset:6144\n\t" \
  "s_waitcnt vmcnt(12) lgkmcnt(4)\n\t" \
  "v_mfma_f32_16x16x32_bf16 %[c00], v[72:75], v[144:147], %[c00]\n\t" \
  "v_mfma_f32_16x16x32_bf16 %[c10], v[76:79], v[144:147], %[c10]\n\t" \
  "ds_read_b128 v[176:179], %[pc3] offset:0\n\t" \
  "v_add_u32 %[pc1], %[k2], %[pc1]\n\t" \
  "v_mfma_f32_16x16x32_bf16 %[c01], v[72:75], v[148:151], %[c01]\n\t" \
  "v_or_b32 v192, v192, v193\n\t" \
  "v_mfma_f32_16x16x32_bf16 %[c11], v[76:79], v[148:151], %[c11]\n\t" \
  "ds_read_b128 v[180:183], %[pc3] offset:2048\n\t" \
  "v_readfirstlane_b32 %[s0], v192\n\t" \
  "v_mfma_f32_16x16x32_bf16 %[c02], v[72:75], v[152:155], %[c02]\n\t" \
  "v_mfma_f32_16x16x32_bf16 %[c12], v[76:79], v[152:155], %[c12]\n\t" \
  "ds_read_b128 v[184:187], %[pc3] offset:4096\n\t" \
  "v_mfma_f32_16x16x32_bf16 %[c03], v[72:75], v[156:159], %[c03]\n\t" \
  "s_cmp_eq_u32 %[s0], 0\n\t" \
  "s_cbranch_scc0 Lcf1_%=\n\t" \
  "Lcf1b_%=:\n\t" \
  "v_mfma_f32_16x16x32_bf16 %[c13], v[76:79], v[156:159], %[c13]\n\t" \
  "ds_read_b128 v[188:191], %[pc3] offset:6144\n\t" \
  "s_waitcnt vmcnt(10) lgkmcnt(4)\n\t" \
  "v_mfma_f32_16x16x32_bf16 %[c00], v[80:83], v[160:163], %[c00]\n\t" \
  "v_mfma_f32_16x16x32_bf16 %[c10], v[84:87], v[160:163], %[c10]\n\t" \
  "ds_read_b128 v[128:131], %[pc0] offset:0\n\t" \
  "v_add_u32 %[pc2], %[k2], %[pc2]\n\t" \
  "v_mfma_f32_16x16x32_bf16 %[c01], v[80:83], v[164:167], %[c01]\n\t" \
  "v_mfma_f32_16x16x32_bf16 %[c11], v[84:87], v[164:167], %[c11]\n\t" \
  "ds_read_b128 v[132:135], %[pc0] offset:2048\n\t" \
  "v_mfma_f32_16x16x32_bf16 %[c02], v[80:83], v[168:171], %[c02]\n\t" \
  "v_mfma_f32_16x16x32_bf16 %[c12], v[84:87], v[168:171], %[c12]\n\t" \
  "ds_read_b128 v[136:139], %[pc0] offset:4096\n\t" \
  "v_mfma_f32_16x16x32_bf16 %[c03], v[80:83], v[172:175], %[c03]\n\t" \
  "v_mfma_f32_16x16x32_bf16 %[c13], v[84:87], v[172:175], %[c13]\n\t" \
  "ds_read_b128 v[140:143], %[pc0] offset:6144\n\t" \
  "s_waitcnt vmcnt(8) lgkmcnt(4)\n\t" \
  "v_mfma_f32_16x16x32_bf16 %[c00], v[88:91], v[176:179], %[c00]\n\t" \
  "v_mfma_f32_16x16x32_bf16 %[c10], v[92:95], v[176:179], %[c10]\n\t" \
  "ds_read_b128 v[144:147], %[pc1] offset:0\n\t" \
  "v_add_u32 %[pc3], %[k2], %[pc3]\n\t" \
  "v_mfma_f32_16x16x32_bf16 %[c01], v[88:91], v[180:183], %[c01]\n\t" \
  "v_mfma_f32_16x16x32_bf16 %[c11], v[92:95], v[180:183], %[c11]\n\t" \
  "ds_read_b128 v[148:151], %[pc1] offset:2048\n\t" \
  "v_mfma_f32_16x16x32_bf16 %[c02], v[88:91], v[184:187], %[c02]\n\t" \
  "v_mfma_f32_16x16x32_bf16 %[c12], v[92:95], v[184:187], %[c12]\n\t" \
  "ds_read_b128 v[152:155], %[pc1] offset:4096\n\t" \
  "v_mfma_f32_16x16x32_bf16 %[c03], v[88:91], v[188:191], %[c03]\n\t" \
  "v_mfma_f32_16x16x32_bf16 %[c13], v[92:95], v[188:191], %[c13]\n\t" \
  "ds_read_b128 v[156:159], %[pc1] offset:6144\n\t" \
  "s_waitcnt vmcnt(6) lgkmcnt(4)\n\t" \
  "v_mfma_f32_16x16x32_bf16 %[c00], v[96:99], v[128:131], %[c00]\n\t" \
  "v_mfma_f32_16x16x32_bf16 %[c10], v[100:103], v[128:131], %[c10]\n\t" \
  "ds_read_b128 v[160:163], %[pc2] offset:0\n\t" \
  "v_mfma_f32_16x16x32_bf16 %[c01], v[96:99], v[132:135], %[c01]\n\t" \
  "v_mfma_f32_16x16x32_bf16 %[c11], v[100:103], v[132:135], %[c11]\n\t" \
  "ds_read_b128 v[164:167], %[pc2] offset:2048\n\t" \
  "v_mfma_f32_16x16x32_bf16 %[c02], v[96:99], v[136:139], %[c02]\n\t" \
  "v_mfma_f32_16x16x32_bf16 %[c12], v[100:103], v[136:139], %[c12]\n\t" \
  "ds_read_b128 v[168:171], %[pc2] offset:4096\n\t" \
  "v_mfma_f32_16x16x32_bf16 %[c03], v[96:99], v[140:143], %[c03]\n\t" \
  "v_mfma_f32_16x16x32_bf16 %[c13], v[100:103], v[140:143], %[c13]\n\t" \
  "ds_read_b128 v[172:175], %[pc2] offset:6144\n\t" \
  "s_waitcnt vmcnt(4) lgkmcnt(4)\n\t" \
  "v_mfma_f32_16x16x32_bf16 %[c00], v[104:107], v[144:147], %[c00]\n\t" \
  "v_mfma_f32_16x16x32_bf16 %[c10], v[108:111], v[144:147], %[c10]\n\t" \
  "ds_read_b128 v[176:179], %[pc3] offset:0\n\t" \
  "v_mfma_f32_16x16x32_bf16 %[c01], v[104:107], v[148:151], %[c01]\n\t" \
  "v_mfma_f32_16x16x32_bf16 %[c11], v[108:111], v[148:151], %[c11]\n\t" \
  "ds_read_b128 v[180:183], %[pc3] offset:2048\n\t" \
  "v_mfma_f32_16x16x32_bf16 %[c02], v[104:107], v[152:155], %[c02]\n\t" \
  "v_mfma_f32_16x16x32_bf16 %[c12], v[108:111], v[152:155], %[c12]\n\t" \
  "ds_read_b128 v[184:187], %[pc3] offset:4096\n\t" \
  "v_mfma_f32_16x16x32_bf16 %[c03], v[104:107], v[156:159], %[c03]\n\t" \
  "v_mfma_f32_16x16x32_bf16 %[c13], v[108:111], v[156:159], %[c13]\n\t" \
  "ds_read_b128 v[188:191], %[pc3] offset:6144\n\t" \
  "s_waitcnt vmcnt(2) lgkmcnt(4)\n\t" \
  "v_mfma_f32_16x16x32_bf16 %[c00], v[112:115], v[160:163], %[c00]\n\t" \
  "ds_read_b64 v[192:193], %[xd]\n\t" \
  "v_mfma_f32_16x16x32_bf16 %[c10], v[116:119], v[160:163], %[c10]\n\t" \
  "ds_read_b128 v[128:131], %[d0]\n\t" \
  "v_mfma_f32_16x16x32_bf16 %[c01], v[112:115], v[164:167], %[c01]\n\t" \
  "ds_read_b128 v[132:135], %[d1]\n\t" \
  "v_mfma_f32_16x16x32_bf16 %[c11], v[116:119], v[164:167], %[c11]\n\t" \
  "ds_read_b128 v[136:139], %[d2]\n\t" \
  "v_mfma_f32_16x16x32_bf16 %[c02], v[112:115], v[168:171], %[c02]\n\t" \
  "ds_read_b128 v[140:143], %[d3]\n\t" \
  "v_mfma_f32_16x16x32_bf16 %[c12], v[116:119], v[168:171], %[c12]\n\t" \
  "ds_read_b128 v[144:147], %[d4]\n\t" \
  "v_mfma_f32_16x16x32_bf16 %[c03], v[112:115], v[172:175], %[c03]\n\t" \
  "ds_read_b128 v[148:151], %[d5]\n\t" \
  "v_mfma_f32_16x16x32_bf16 %[c13], v[116:119], v[172:175], %[c13]\n\t" \
  "ds_read_b128 v[152:155], %[d6]\n\t" \
  "ds_read_b128 v[156:159], %[d7]\n\t" \
  "s_waitcnt vmcnt(0) lgkmcnt(9)\n\t" \
  "v_mfma_f32_16x16x32_bf16 %[c00], v[120:123], v[176:179], %[c00]\n\t" \
  "v_mfma_f32_16x16x32_bf16 %[c10], v[124:127], v[176:179], %[c10]\n\t" \
  "v_mfma_f32_16x16x32_bf16 %[c01], v[120:123], v[180:183], %[c01]\n\t" \
  "v_mfma_f32_16x16x32_bf16 %[c11], v[124:127], v[180:183], %[c11]\n\t" \
  "v_mfma_f32_16x16x32_bf16 %[c02], v[120:123], v[184:187], %[c02]\n\t" \
  "v_mfma_f32_16x16x32_bf16 %[c12], v[124:127], v[184:187], %[c12]\n\t" \
  "v_mfma_f32_16x16x32_bf16 %[c03], v[120:123], v[188:191], %[c03]\n\t" \
  "v_mfma_f32_16x16x32_bf16 %[c13], v[124:127], v[188:191], %[c13]\n\t" \
  "s_cmp_eq_u32 %[mine], 0\n\t" \
  "s_cbranch_scc1 Lend_%=\n\t" \
  "Ld_%=:\n\t" \
  "s_waitcnt lgkmcnt(0)\n\t" \
  "v_and_b32 v192, v192, v193\n\t" \
  "s_nop 0\n\t" \
  "v_readfirstlane_b32 %[s0], v192\n\t" \
  "s_cmp_eq_u32 %[s0], 0\n\t" \
  "s_cbranch_scc1 Ld1_%=\n\t" \
  "Ld2_%=:\n\t" \
  "v_add_f32 %[b00], %[b00], v128\n\t" \
  "v_add_f32 %[b01], %[b01], v129\n\t" \
  "v_add_f32 %[b02], %[b02], v130\n\t" \
  "v_add_f32 %[b03], %[b03], v131\n\t" \
  "v_add_f32 %[b10], %[b10], v132\n\t" \
  "v_add_f32 %[b11], %[b11], v133\n\t" \
  "v_add_f32 %[b12], %[b12], v134\n\t" \
  "v_add_f32 %[b13], %[b13], v135\n\t" \
  "v_add_f32 %[b20], %[b20], v136\n\t" \
  "v_add_f32 %[b21], %[b21], v137\n\t" \
  "v_add_f32 %[b22], %[b22], v138\n\t" \
  "v_add_f32 %[b23], %[b23], v139\n\t" \
  "v_add_f32 %[b30], %[b30], v140\n\t" \
  "v_add_f32 %[b31], %[b31], v141\n\t" \
  "v_add_f32 %[b32], %[b32], v142\n\t" \
  "v_add_f32 %[b33], %[b33], v143\n\t" \
  "v_add_f32 %[b40], %[b40], v144\n\t" \
  "v_add_f32 %[b41], %[b41], v145\n\t" \
  "v_add_f32 %[b42], %[b42], v146\n\t" \
  "v_add_f32 %[b43], %[b43], v147\n\t" \
  "v_add_f32 %[b50], %[b50], v148\n\t" \
  "v_add_f32 %[b51], %[b51], v149\n\t" \
  "v_add_f32 %[b52], %[b52], v150\n\t" \
  "v_add_f32 %[b53], %[b53], v151\n\t" \
  "v_add_f32 %[b60], %[b60], v152\n\t" \
  "v_add_f32 %[b61], %[b61], v153\n\t" \
  "v_add_f32 %[b62], %[b62], v154\n\t" \
  "v_add_f32 %[b63], %[b63], v155\n\t" \
  "v_add_f32 %[b70], %[b70], v156\n\t" \
  "v_add_f32 %[b71], %[b71], v157\n\t" \
  "v_add_f32 %[b72], %[b72], v158\n\t" \
  "v_add_f32 %[b73], %[b73], v159\n\t" \
  "s_branch Lend_%=\n\t" \
  "Lw_%=:\n\t" \
  "ds_read_b64 v[192:193], %[hr]\n\t" \
  "s_sub_i32 %[bud], %[bud], 1\n\t" \
  "s_cmp_lt_i32 %[bud], 0\n\t" \
  "s_cbranch_scc1 Lf3_%=\n\t" \
  "s_waitcnt lgkmcnt(0)\n\t" \
  "v_or_b32 v192, v192, v193\n\t" \
  "s_nop 0\n\t" \
  "v_readfirstlane_b32 %[s0], v192\n\t" \
  "s_cmp_eq_u32 %[s0], 0\n\t" \
  "s_cbranch_scc1 Lf3_%=\n\t" \
  "s_branch Lw_%=\n\t" \
  "Lh_%=:\n\t" \
  "buffer_load_dword v196, off, %[srd], %[sf1] sc1\n\t" \
  "s_branch Lhb_%=\n\t" \
  "Lg1_%=:\n\t" \
  "v_mov_b32 v196, 0\n\t" \
  "s_mov_b64 exec, 1\n\t" \
  "buffer_atomic_or v196, off, %[srd], %[sf1] sc0 sc1\n\t" \
  "s_mov_b64 exec, -1\n\t" \
  "s_sub_i32 %[bud], %[bud], 16\n\t" \
  "s_cmp_lt_i32 %[bud], 0\n\t" \
  "s_cbranch_scc1 Lgb_%=\n\t" \
  "s_waitcnt vmcnt(0)\n\t" \
  "s_branch Lg2_%=\n\t" \
  "Lg_%=:\n\t" \
  "s_waitcnt vmcnt(12)\n\t" \
  "Lg2_%=:\n\t" \
  "v_readfirstlane_b32 %[s0], v196\n\t" \
  "s_cmp_lg_u32 %[s0], 32\n\t" \
  "s_cbranch_scc1 Lg1_%=\n\t" \
  "s_branch Lgb_%=\n\t" \
  "Lcb1_%=:\n\t" \
  "ds_read_b64 v[192:193], %[fl] offset:0\n\t" \
  "s_sub_i32 %[bud], %[bud], 1\n\t" \
  "s_cmp_lt_i32 %[bud], 0\n\t" \
  "s_cbranch_scc1 Lcb1b_%=\n\t" \
  "s_waitcnt lgkmcnt(0)\n\t" \
  "v_or_b32 v192, v192, v193\n\t" \
  "s_nop 0\n\t" \
  "v_readfirstlane_b32 %[s0], v192\n\t" \
  "s_cmp_eq_u32 %[s0], 0\n\t" \
  "s_cbranch_scc1 Lcb1b_%=\n\t" \
  "s_branch Lcb1_%=\n\t" \
  "Lcb5_%=:\n\t" \
  "ds_read_b64 v[192:193], %[fl] offset:8\n\t" \
  "s_sub_i32 %[bud], %[bud], 1\n\t" \
  "s_cmp_lt_i32 %[bud], 0\n\t" \
  "s_cbranch_scc1 Lcb5b_%=\n\t" \
  "s_waitcnt lgkmcnt(0)\n\t" \
  "v_or_b32 v192, v192, v193\n\t" \
  "s_nop 0\n\t" \
  "v_readfirstlane_b32 %[s0], v192\n\t" \
  "s_cmp_eq_u32 %[s0], 0\n\t" \
  "s_cbranch_scc1 Lcb5b_%=\n\t" \
  "s_branch Lcb5_%=\n\t" \
  "Lcf1_%=:\n\t" \
  "ds_read_b64 v[192:193], %[fl] offset:0\n\t" \
  "s_sub_i32 %[bud], %[bud], 1\n\t" \
  "s_cmp_lt_i32 %[bud], 0\n\t" \
  "s_cbranch_scc1 Lcf1b_%=\n\t" \
  "s_waitcnt lgkmcnt(0)\n\t" \
  "v_or_b32 v192, v192, v193\n\t" \
  "s_nop 0\n\t" \
  "v_readfirstlane_b32 %[s0], v192\n\t" \
  "s_cmp_eq_u32 %[s0], 0\n\t" \
  "s_cbranch_scc1 Lcf1b_%=\n\t" \
  "s_branch Lcf1_%=\n\t" \
  "Ld1_%=:\n\t" \
  "s_sub_i32 %[bud], %[bud], 1\n\t" \
  "s_cmp_lt_i32 %[bud], 0\n\t" \
  "s_cbranch_scc1 Ld2_%=\n\t" \
  "ds_read_b64 v[192:193], %[xd]\n\t" \
  "ds_read_b128 v[128:131], %[d0]\n\t" \
  "ds_read_b128 v[132:135], %[d1]\n\t" \
  "ds_read_b128 v[136:139], %[d2]\n\t" \
  "ds_read_b128 v[140:143], %[d3]\n\t" \
  "ds_read_b128 v[144:147], %[d4]\n\t" \
  "ds_read_b128 v[148:151], %[d5]\n\t" \
  "ds_read_b128 v[152:155], %[d6]\n\t" \
  "ds_read_b128 v[156:159], %[d7]\n\t" \
  "s_branch Ld_%=\n\t" \
  "Lend_%=:\n\t" \
  "s_waitcnt lgkmcnt(0)\n\t" \
  "s_nop 6\n\t"
#define ASM_EN \
  "s_bfe_u32 %[j], %[bid], 0x30003\n\t" \
  "v_readfirstlane_b32 %[w], %[tid]\n\t" \
  "s_lshr_b32 %[w], %[w], 6\n\t" \
  "s_cmp_lg_u32 %[j], %[w]\n\t" \
  "s_cbranch_scc1 Lnc_%=\n\t" \
  "s_getpc_b64 s[20:21]\n\t" \
  "v_lshlrev_b32 %[v0], 7, %[tid]\n\t" \
  "v_min_u32 %[v0], %[cpf], %[v0]\n\t" \
  "global_load_dword v255, %[v0], s[20:21]\n\t" \
  "Lnc_%=:\n\t" \
  "s_cmp_gt_u32 %[w], 3\n\t" \
  "s_cbranch_scc1 Lend_%=\n\t" \
  "s_lshl3_add_u32 %[t], %[w], 8\n\t" \
  "s_cmp_lt_u32 %[w], 2\n\t" \
  "s_cselect_b32 %[t], 0, %[t]\n\t" \
  "s_load_dwordx2 s[20:21], %[kp], %[t]\n\t" \
  "s_cselect_b32 %[sa], 14, 11\n\t" \
  "s_cselect_b32 %[sc], 11, 15\n\t" \
  "s_cselect_b32 %[m], 16, 0\n\t" \
  "s_bfe_u32 %[r], %[bid], 0x20001\n\t" \
  "s_lshr_b32 %[t], %[bid], 6\n\t" \
  "s_lshl2_add_u32 %[c], %[r], %[t]\n\t" \
  "s_and_b32 %[t], %[bid], 1\n\t" \
  "s_lshl3_add_u32 %[j], %[t], %[j]\n\t" \
  "s_lshl1_add_u32 %[r], %[t], %[r]\n\t" \
  "s_and_b32 %[r], %[r], 3\n\t" \
  "s_lshl_b32 %[c], %[c], 5\n\t" \
  "s_add_u32 %[c], %[c], %[w]\n\t" \
  "s_lshl_b32 %[j], %[j], 4\n\t" \
  "s_cmp_lt_u32 %[w], 2\n\t" \
  "s_cselect_b32 %[c], %[c], %[j]\n\t" \
  "v_bfe_u32 %[v1], %[tid], 3, 3\n\t" \
  "v_xor_b32 %[v0], %[v1], %[tid]\n\t" \
  "v_and_b32 %[v0], 7, %[v0]\n\t" \
  "v_lshlrev_b32 %[v0], 4, %[v0]\n\t" \
  "v_lshl_add_u32 %[v0], %[r], 9, %[v0]\n\t" \
  "v_lshl_add_u32 %[v0], %[v1], %[sa], %[v0]\n\t" \
  "v_lshl_add_u32 %[v0], %[c], 13, %[v0]\n\t" \
  "s_lshl_b32 %[sa], 8, %[sa]\n\t" \
  "s_lshl_b32 %[sc], 1, %[sc]\n\t" \
  "v_add_u32 %[v1], %[sa], %[v0]\n\t" \
  "s_add_u32 %[m], %[m], %[w]\n\t" \
  "s_lshl_b32 m0, %[m], 13\n\t" \
  "s_waitcnt lgkmcnt(0)\n\t" \
  "s_load_dword %[r], s[20:21], 0x0\n\t" \
  "Ll_%=:\n\t" \
  "global_load_lds_dwordx4 %[v0], s[20:21]\n\t" \
  "s_addk_i32 m0, 0x400\n\t" \
  "v_add_u32 %[v0], %[sc], %[v0]\n\t" \
  "global_load_lds_dwordx4 %[v1], s[20:21]\n\t" \
  "s_addk_i32 m0, 0x400\n\t" \
  "v_add_u32 %[v1], %[sc], %[v1]\n\t" \
  "s_lshl_b32 %[t], m0, 19\n\t" \
  "s_cbranch_scc1 Ll_%=\n\t" \
  "s_lshl2_add_u32 m0, %[w], %[ms0]\n\t" \
  "s_and_b32 s21, s21, 0xffff\n\t" \
  "v_mov_b32 %[v0], m0\n\t" \
  "ds_write_b32 %[v0], %[v0]\n\t" \
  "s_mov_b32 s22, 0\n\t" \
  "s_mov_b32 s23, 0x20000\n\t" \
  "s_mov_b64 exec, 1\n\t" \
  "s_waitcnt lgkmcnt(0)\n\t" \
  "buffer_load_dword off, s[20:23], 0 lds\n\t" \
  "s_mov_b64 exec, -1\n\t" \
  "s_barrier\n\t" \
  "Lend_%=:\n\t"
#define ASM_LA \
  "s_nop 4\n\t" \
  "s_barrier\n\t" \
  "Ltop_%=:\n\t" \
  "s_mov_b32 m0, %[dst]\n\t" \
  "v_mov_b32 %[vc], %[ci]\n\t" \
  "buffer_load_dwordx4 %[v0], %[r0], %[so0] offen lds\n\t" \
  "s_addk_i32 m0, 0x400\n\t" \
  "s_add_u32 %[dst], %[dst], 0x8000\n\t" \
  "buffer_load_dwordx4 %[v0], %[r0], %[so1] offen lds\n\t" \
  "s_addk_i32 m0, 0x400\n\t" \
  "s_cmp_ge_u32 %[dst], 0x24000\n\t" \
  "s_cselect_b32 %[s0], %[wrap], 0\n\t" \
  "buffer_load_dwordx4 %[v0], %[r0], %[so2] offen lds\n\t" \
  "s_addk_i32 m0, 0x400\n\t" \
  "s_sub_u32 %[dst], %[dst], %[s0]\n\t" \
  "buffer_load_dwordx4 %[v0], %[r0], %[so3] offen lds\n\t" \
  "s_addk_i32 m0, 0x400\n\t" \
  "s_add_u32 %[sk], %[sk], 128\n\t" \
  "buffer_load_dwordx4 %[v0], %[r0], %[so4] offen lds\n\t" \
  "s_addk_i32 m0, 0x400\n\t" \
  "s_and_b32 %[sk], %[sk], 2047\n\t" \
  "buffer_load_dwordx4 %[v0], %[r0], %[so5] offen lds\n\t" \
  "s_addk_i32 m0, 0x400\n\t" \
  "s_nop 0\n\t" \
  "buffer_load_dwordx4 %[v0], %[r0], %[so6] offen lds\n\t" \
  "s_addk_i32 m0, 0x400\n\t" \
  "s_add_u32 %[ci], %[ci], 16\n\t" \
  "buffer_load_dwordx4 %[v0], %[r0], %[so7] offen lds\n\t" \
  "s_addk_i32 m0, 0x400\n\t" \
  "s_sub_u32 %[n], %[n], 1\n\t" \
  "buffer_load_dwordx4 %[v0], %[r1], %[so0] offen lds\n\t" \
  "s_addk_i32 m0, 0x400\n\t" \
  "s_nop 0\n\t" \
  "buffer_load_dwordx4 %[v0], %[r1], %[so1] offen lds\n\t" \
  "s_addk_i32 m0, 0x400\n\t" \
  "s_nop 0\n\t" \
  "buffer_load_dwordx4 %[v0], %[r1], %[so2] offen lds\n\t" \
  "s_addk_i32 m0, 0x400\n\t" \
  "s_nop 0\n\t" \
  "buffer_load_dwordx4 %[v0], %[r1], %[so3] offen lds\n\t" \
  "s_addk_i32 m0, 0x400\n\t" \
  "ds_read_b128 v[24:27], %[vc]\n\t" \
  "ds_read_b128 v[28:31], %[vw]\n\t" \
  "buffer_load_dwordx4 %[v0], %[r1], %[so4] offen lds\n\t" \
  "s_addk_i32 m0, 0x400\n\t" \
  "s_nop 0\n\t" \
  "buffer_load_dwordx4 %[v0], %[r1], %[so5] offen lds\n\t" \
  "s_addk_i32 m0, 0x400\n\t" \
  "s_nop 0\n\t" \
  "buffer_load_dwordx4 %[v0], %[r1], %[so6] offen lds\n\t" \
  "s_addk_i32 m0, 0x400\n\t" \
  "s_waitcnt lgkmcnt(0)\n\t" \
  "v_or3_b32 v24, v24, v25, v26\n\t" \
  "v_or_b32 v24, v24, v27\n\t" \
  "v_sub_u32 v28, v30, v28\n\t" \
  "v_sub_u32 v29, v31, v29\n\t" \
  "v_or3_b32 v24, v24, v28, v29\n\t" \
  "buffer_load_dwordx4 %[v0], %[r1], %[so7] offen lds\n\t" \
  "s_mov_b32 m0, %[mk]\n\t" \
  "s_mov_b64 exec, 1\n\t" \
  "buffer_load_dword %[v0], %[rz], 0 offen lds\n\t" \
  "s_mov_b64 exec, -1\n\t" \
  "v_readfirstlane_b32 %[s0], v24\n\t" \
  "v_add_u32 %[v0], %[sk], %[b0]\n\t" \
  "s_add_u32 %[mk], %[mk], 8\n\t" \
  "s_cmp_eq_u32 %[n], 0\n\t" \
  "s_cbranch_scc1 Lend_%=\n\t" \
  "s_cmp_eq_u32 %[s0], 0\n\t" \
  "s_cbranch_scc1 Ltop_%=\n\t" \
  "Lsp_%=:\n\t" \
  "ds_read_b128 v[24:27], %[vc]\n\t" \
  "ds_read_b128 v[28:31], %[vw]\n\t" \
  "s_sub_i32 %[bud], %[bud], 1\n\t" \
  "s_cmp_lt_i32 %[bud], 0\n\t" \
  "s_cbranch_scc1 Ltop_%=\n\t" \
  "s_waitcnt lgkmcnt(0)\n\t" \
  "v_or3_b32 v24, v24, v25, v26\n\t" \
  "v_or_b32 v24, v24, v27\n\t" \
  "v_sub_u32 v28, v30, v28\n\t" \
  "v_sub_u32 v29, v31, v29\n\t" \
  "v_or3_b32 v24, v24, v28, v29\n\t" \
  "s_nop 0\n\t" \
  "v_readfirstlane_b32 %[s0], v24\n\t" \
  "s_cmp_eq_u32 %[s0], 0\n\t" \
  "s_cbranch_scc1 Ltop_%=\n\t" \
  "s_branch Lsp_%=\n\t" \
  "Lend_%=:\n\t"
#define ASM_LB \
  "s_nop 4\n\t" \
  "s_branch Lsp_%=\n\t" \
  "Ltop_%=:\n\t" \
  "s_mov_b32 m0, %[dst]\n\t" \
  "v_mov_b32 %[vc], %[ci]\n\t" \
  "buffer_load_dwordx4 %[v0], %[r0], %[so0] offen lds\n\t" \
  "s_addk_i32 m0, 0x400\n\t" \
  "s_add_u32 %[dst], %[dst], 0x8000\n\t" \
  "buffer_load_dwordx4 %[v1], %[r0], %[so1] offen lds\n\t" \
  "s_addk_i32 m0, 0x400\n\t" \
  "s_cmp_ge_u32 %[dst], 0x24000\n\t" \
  "s_cselect_b32 %[s0], %[wrap], 0\n\t" \
  "buffer_load_dwordx4 %[v0], %[r0], %[so2] offen lds\n\t" \
  "s_addk_i32 m0, 0x400\n\t" \
  "s_sub_u32 %[dst], %[dst], %[s0]\n\t" \
  "buffer_load_dwordx4 %[v1], %[r0], %[so3] offen lds\n\t" \
  "s_addk_i32 m0, 0x400\n\t" \
  "s_add_u32 %[sk], %[sk], 256\n\t" \
  "buffer_load_dwordx4 %[v0], %[r0], %[so4] offen lds\n\t" \
  "s_addk_i32 m0, 0x400\n\t" \
  "s_and_b32 %[sk], %[sk], 1023\n\t" \
  "buffer_load_dwordx4 %[v1], %[r0], %[so5] offen lds\n\t" \
  "s_addk_i32 m0, 0x400\n\t" \
  "s_cmp_ge_u32 %[ci], %[cth]\n\t" \
  "s_cselect_b32 %[s0], 32, 16\n\t" \
  "buffer_load_dwordx4 %[v0], %[r0], %[so6] offen lds\n\t" \
  "s_addk_i32 m0, 0x400\n\t" \
  "s_add_u32 %[ci], %[ci], %[s0]\n\t" \
  "buffer_load_dwordx4 %[v1], %[r0], %[so7] offen lds\n\t" \
  "s_addk_i32 m0, 0x400\n\t" \
  "s_sub_u32 %[n], %[n], 1\n\t" \
  "buffer_load_dwordx4 %[v0], %[r1], %[so0] offen lds\n\t" \
  "s_addk_i32 m0, 0x400\n\t" \
  "s_cmp_eq_u32 %[n], 4\n\t" \
  "s_cselect_b32 %[s0], 0x400, 0\n\t" \
  "buffer_load_dwordx4 %[v1], %[r1], %[so1] offen lds\n\t" \
  "s_addk_i32 m0, 0x400\n\t" \
  "s_xor_b32 %[hi], %[hi], %[s0]\n\t" \
  "buffer_load_dwordx4 %[v0], %[r1], %[so2] offen lds\n\t" \
  "s_addk_i32 m0, 0x400\n\t" \
  "s_or_b32 %[s1], %[sk], %[hi]\n\t" \
  "buffer_load_dwordx4 %[v1], %[r1], %[so3] offen lds\n\t" \
  "s_addk_i32 m0, 0x400\n\t" \
  "ds_read_b128 v[24:27], %[vc]\n\t" \
  "buffer_load_dwordx4 %[v0], %[r1], %[so4] offen lds\n\t" \
  "s_addk_i32 m0, 0x400\n\t" \
  "s_nop 0\n\t" \
  "buffer_load_dwordx4 %[v1], %[r1], %[so5] offen lds\n\t" \
  "s_addk_i32 m0, 0x400\n\t" \
  "s_nop 0\n\t" \
  "buffer_load_dwordx4 %[v0], %[r1], %[so6] offen lds\n\t" \
  "s_addk_i32 m0, 0x400\n\t" \
  "s_waitcnt lgkmcnt(0)\n\t" \
  "v_or3_b32 v24, v24, v25, v26\n\t" \
  "v_or_b32 v24, v24, v27\n\t" \
  "buffer_load_dwordx4 %[v1], %[r1], %[so7] offen lds\n\t" \
  "s_mov_b32 m0, %[mk]\n\t" \
  "s_mov_b64 exec, 3\n\t" \
  "buffer_load_dword %[v0], %[rz], 0 offen lds\n\t" \
  "s_mov_b64 exec, -1\n\t" \
  "v_readfirstlane_b32 %[s0], v24\n\t" \
  "v_add_u32 %[v0], %[s1], %[b0]\n\t" \
  "v_add_u32 %[v1], %[s1], %[b1]\n\t" \
  "s_add_u32 %[mk], %[mk], 16\n\t" \
  "s_cmp_eq_u32 %[n], %[nend]\n\t" \
  "s_cbranch_scc1 Lend_%=\n\t" \
  "s_cmp_eq_u32 %[s0], 0\n\t" \
  "s_cbranch_scc1 Ltop_%=\n\t" \
  "Lsp_%=:\n\t" \
  "ds_read_b128 v[24:27], %[vc]\n\t" \
  "s_sub_i32 %[bud], %[bud], 1\n\t" \
  "s_cmp_lt_i32 %[bud], 0\n\t" \
  "s_cbranch_scc1 Ltop_%=\n\t" \
  "s_waitcnt lgkmcnt(0)\n\t" \
  "v_or3_b32 v24, v24, v25, v26\n\t" \
  "v_or_b32 v24, v24, v27\n\t" \
  "s_nop 0\n\t" \
  "v_readfirstlane_b32 %[s0], v24\n\t" \
  "s_cmp_eq_u32 %[s0], 0\n\t" \
  "s_cbranch_scc1 Ltop_%=\n\t" \
  "s_sleep 1\n\t" \
  "s_branch Lsp_%=\n\t" \
  "Lend_%=:\n\t" \
  "buffer_load_dwordx4 v[32:35], %[p0], %[rp], %[so0] offen offset:128\n\t" \
  "buffer_load_dwordx4 v[36:39], %[p1], %[rp], %[so1] offen offset:128\n\t" \
  "buffer_load_dwordx4 v[40:43], %[p0], %[rp], %[so2] offen offset:128\n\t" \
  "buffer_load_dwordx4 v[44:47], %[p1], %[rp], %[so3] offen offset:128\n\t" \
  "buffer_load_dwordx4 v[48:51], %[p0], %[rp], %[so4] offen offset:128\n\t" \
  "buffer_load_dwordx4 v[52:55], %[p1], %[rp], %[so5] offen offset:128\n\t" \
  "buffer_load_dwordx4 v[56:59], %[p0], %[rp], %[so6] offen offset:128\n\t" \
  "buffer_load_dwordx4 v[60:63], %[p1], %[rp], %[so7] offen offset:128\n\t" \
  "buffer_load_dwordx4 v[64:67], %[p0], %[rp], %[so0] offen offset:256\n\t" \
  "buffer_load_dwordx4 v[68:71], %[p1], %[rp], %[so1] offen offset:256\n\t" \
  "buffer_load_dwordx4 v[72:75], %[p0], %[rp], %[so2] offen offset:256\n\t" \
  "buffer_load_dwordx4 v[76:79], %[p1], %[rp], %[so3] offen offset:256\n\t" \
  "buffer_load_dwordx4 v[80:83], %[p0], %[rp], %[so4] offen offset:256\n\t" \
  "buffer_load_dwordx4 v[84:87], %[p1], %[rp], %[so5] offen offset:256\n\t" \
  "buffer_load_dwordx4 v[88:91], %[p0], %[rp], %[so6] offen offset:256\n\t" \
  "buffer_load_dwordx4 v[92:95], %[p1], %[rp], %[so7] offen offset:256\n\t" \
  "buffer_load_dwordx4 v[96:99], %[p0], %[rp], %[so0] offen offset:384\n\t" \
  "buffer_load_dwordx4 v[100:103], %[p1], %[rp], %[so1] offen offset:384\n\t" \
  "buffer_load_dwordx4 v[104:107], %[p0], %[rp], %[so2] offen offset:384\n\t" \
  "buffer_load_dwordx4 v[108:111], %[p1], %[rp], %[so3] offen offset:384\n\t" \
  "buffer_load_dwordx4 v[112:115], %[p0], %[rp], %[so4] offen offset:384\n\t" \
  "buffer_load_dwordx4 v[116:119], %[p1], %[rp], %[so5] offen offset:384\n\t" \
  "buffer_load_dwordx4 v[120:123], %[p0], %[rp], %[so6] offen offset:384\n\t" \
  "buffer_load_dwordx4 v[124:127], %[p1], %[rp], %[so7] offen offset:384\n\t" \
  "buffer_load_dwordx4 v[128:131], %[p0], %[rp], %[so0] offen offset:512\n\t" \
  "buffer_load_dwordx4 v[132:135], %[p1], %[rp], %[so1] offen offset:512\n\t" \
  "buffer_load_dwordx4 v[136:139], %[p0], %[rp], %[so2] offen offset:512\n\t" \
  "buffer_load_dwordx4 v[140:143], %[p1], %[rp], %[so3] offen offset:512\n\t" \
  "buffer_load_dwordx4 v[144:147], %[p0], %[rp], %[so4] offen offset:512\n\t" \
  "buffer_load_dwordx4 v[148:151], %[p1], %[rp], %[so5] offen offset:512\n\t" \
  "buffer_load_dwordx4 v[152:155], %[p0], %[rp], %[so6] offen offset:512\n\t" \
  "buffer_load_dwordx4 v[156:159], %[p1], %[rp], %[so7] offen offset:512\n\t" \
  "buffer_load_dwordx4 v[160:163], %[p0], %[rp], %[so0] offen offset:640\n\t" \
  "buffer_load_dwordx4 v[164:167], %[p1], %[rp], %[so1] offen offset:640\n\t" \
  "buffer_load_dwordx4 v[168:171], %[p0], %[rp], %[so2] offen offset:640\n\t" \
  "buffer_load_dwordx4 v[172:175], %[p1], %[rp], %[so3] offen offset:640\n\t" \
  "buffer_load_dwordx4 v[176:179], %[p0], %[rp], %[so4] offen offset:640\n\t" \
  "buffer_load_dwordx4 v[180:183], %[p1], %[rp], %[so5] offen offset:640\n\t" \
  "buffer_load_dwordx4 v[184:187], %[p0], %[rp], %[so6] offen offset:640\n\t" \
  "buffer_load_dwordx4 v[188:191], %[p1], %[rp], %[so7] offen offset:640\n\t" \
  "buffer_load_dwordx4 v[192:195], %[p0], %[rp], %[so0] offen offset:768\n\t" \
  "buffer_load_dwordx4 v[196:199], %[p1], %[rp], %[so1] offen offset:768\n\t" \
  "buffer_load_dwordx4 v[200:203], %[p0], %[rp], %[so2] offen offset:768\n\t" \
  "buffer_load_dwordx4 v[204:207], %[p1], %[rp], %[so3] offen offset:768\n\t" \
  "buffer_load_dwordx4 v[208:211], %[p0], %[rp], %[so4] offen offset:768\n\t" \
  "buffer_load_dwordx4 v[212:215], %[p1], %[rp], %[so5] offen offset:768\n\t" \
  "buffer_load_dwordx4 v[216:219], %[p0], %[rp], %[so6] offen offset:768\n\t" \
  "buffer_load_dwordx4 v[220:223], %[p1], %[rp], %[so7] offen offset:768\n\t" \
  "buffer_load_dwordx4 v[224:227], %[p0], %[rp], %[so0] offen offset:896\n\t" \
  "buffer_load_dwordx4 v[228:231], %[p1], %[rp], %[so1] offen offset:896\n\t" \
  "buffer_load_dwordx4 v[232:235], %[p0], %[rp], %[so2] offen offset:896\n\t" \
  "buffer_load_dwordx4 v[236:239], %[p1], %[rp], %[so3] offen offset:896\n\t" \
  "buffer_load_dwordx4 v[240:243], %[p0], %[rp], %[so4] offen offset:896\n\t" \
  "buffer_load_dwordx4 v[244:247], %[p1], %[rp], %[so5] offen offset:896\n\t" \
  "buffer_load_dwordx4 v[248:251], %[p0], %[rp], %[so6] offen offset:896\n\t" \
  "buffer_load_dwordx4 v[252:255], %[p1], %[rp], %[so7] offen offset:896\n\t" \
  "v_mov_b32 v28, 0\n\t" \
  "Lw0_%=:\n\t" \
  "ds_read_b128 v[24:27], %[vm] offset:0\n\t" \
  "s_sub_i32 %[bud], %[bud], 1\n\t" \
  "s_cmp_lt_i32 %[bud], 0\n\t" \
  "s_cbranch_scc1 Lx0_%=\n\t" \
  "s_waitcnt lgkmcnt(0)\n\t" \
  "v_or3_b32 v24, v24, v25, v26\n\t" \
  "v_or_b32 v24, v24, v27\n\t" \
  "s_nop 0\n\t" \
  "v_readfirstlane_b32 %[s0], v24\n\t" \
  "s_cmp_eq_u32 %[s0], 0\n\t" \
  "s_cbranch_scc1 Lx0_%=\n\t" \
  "s_sleep 2\n\t" \
  "s_branch Lw0_%=\n\t" \
  "Lx0_%=:\n\t" \
  "s_waitcnt vmcnt(0)\n\t" \
  "ds_write_b128 %[vd], v[32:35] offset:0\n\t" \
  "ds_write_b128 %[vd], v[36:39] offset:1024\n\t" \
  "ds_write_b128 %[vd], v[40:43] offset:2048\n\t" \
  "ds_write_b128 %[vd], v[44:47] offset:3072\n\t" \
  "ds_write_b128 %[vd], v[48:51] offset:4096\n\t" \
  "ds_write_b128 %[vd], v[52:55] offset:5120\n\t" \
  "ds_write_b128 %[vd], v[56:59] offset:6144\n\t" \
  "ds_write_b128 %[vd], v[60:63] offset:7168\n\t" \
  "s_waitcnt lgkmcnt(0)\n\t" \
  "ds_write_b32 %[vl], v28 offset:0\n\t" \
  "Lw1_%=:\n\t" \
  "ds_read_b128 v[24:27], %[vm] offset:16\n\t" \
  "s_sub_i32 %[bud], %[bud], 1\n\t" \
  "s_cmp_lt_i32 %[bud], 0\n\t" \
  "s_cbranch_scc1 Lx1_%=\n\t" \
  "s_waitcnt lgkmcnt(0)\n\t" \
  "v_or3_b32 v24, v24, v25, v26\n\t" \
  "v_or_b32 v24, v24, v27\n\t" \
  "s_nop 0\n\t" \
  "v_readfirstlane_b32 %[s0], v24\n\t" \
  "s_cmp_eq_u32 %[s0], 0\n\t" \
  "s_cbranch_scc1 Lx1_%=\n\t" \
  "s_sleep 2\n\t" \
  "s_branch Lw1_%=\n\t" \
  "Lx1_%=:\n\t" \
  "ds_write_b128 %[vd], v[64:67] offset:16384\n\t" \
  "ds_write_b128 %[vd], v[68:71] offset:17408\n\t" \
  "ds_write_b128 %[vd], v[72:75] offset:18432\n\t" \
  "ds_write_b128 %[vd], v[76:79] offset:19456\n\t" \
  "ds_write_b128 %[vd], v[80:83] offset:20480\n\t" \
  "ds_write_b128 %[vd], v[84:87] offset:21504\n\t" \
  "ds_write_b128 %[vd], v[88:91] offset:22528\n\t" \
  "ds_write_b128 %[vd], v[92:95] offset:23552\n\t" \
  "s_waitcnt lgkmcnt(0)\n\t" \
  "ds_write_b32 %[vl], v28 offset:8\n\t" \
  "Lw2_%=:\n\t" \
  "ds_read_b128 v[24:27], %[vm] offset:32\n\t" \
  "s_sub_i32 %[bud], %[bud], 1\n\t" \
  "s_cmp_lt_i32 %[bud], 0\n\t" \
  "s_cbranch_scc1 Lx2_%=\n\t" \
  "s_waitcnt lgkmcnt(0)\n\t" \
  "v_or3_b32 v24, v24, v25, v26\n\t" \
  "v_or_b32 v24, v24, v27\n\t" \
  "s_nop 0\n\t" \
  "v_readfirstlane_b32 %[s0], v24\n\t" \
  "s_cmp_eq_u32 %[s0], 0\n\t" \
  "s_cbranch_scc1 Lx2_%=\n\t" \
  "s_sleep 2\n\t" \
  "s_branch Lw2_%=\n\t" \
  "Lx2_%=:\n\t" \
  "ds_write_b128 %[vd], v[96:99] offset:32768\n\t" \
  "ds_write_b128 %[vd], v[100:103] offset:33792\n\t" \
  "ds_write_b128 %[vd], v[104:107] offset:34816\n\t" \
  "ds_write_b128 %[vd], v[108:111] offset:35840\n\t" \
  "ds_write_b128 %[vd], v[112:115] offset:36864\n\t" \
  "ds_write_b128 %[vd], v[116:119] offset:37888\n\t" \
  "ds_write_b128 %[vd], v[120:123] offset:38912\n\t" \
  "ds_write_b128 %[vd], v[124:127] offset:39936\n\t" \
  "s_waitcnt lgkmcnt(0)\n\t" \
  "ds_write_b32 %[vl], v28 offset:16\n\t" \
  "Lw3_%=:\n\t" \
  "ds_read_b128 v[24:27], %[vm] offset:48\n\t" \
  "s_sub_i32 %[bud], %[bud], 1\n\t" \
  "s_cmp_lt_i32 %[bud], 0\n\t" \
  "s_cbranch_scc1 Lx3_%=\n\t" \
  "s_waitcnt lgkmcnt(0)\n\t" \
  "v_or3_b32 v24, v24, v25, v26\n\t" \
  "v_or_b32 v24, v24, v27\n\t" \
  "s_nop 0\n\t" \
  "v_readfirstlane_b32 %[s0], v24\n\t" \
  "s_cmp_eq_u32 %[s0], 0\n\t" \
  "s_cbranch_scc1 Lx3_%=\n\t" \
  "s_sleep 2\n\t" \
  "s_branch Lw3_%=\n\t" \
  "Lx3_%=:\n\t" \
  "ds_write_b128 %[vd], v[128:131] offset:49152\n\t" \
  "ds_write_b128 %[vd], v[132:135] offset:50176\n\t" \
  "ds_write_b128 %[vd], v[136:139] offset:51200\n\t" \
  "ds_write_b128 %[vd], v[140:143] offset:52224\n\t" \
  "ds_write_b128 %[vd], v[144:147] offset:53248\n\t" \
  "ds_write_b128 %[vd], v[148:151] offset:54272\n\t" \
  "ds_write_b128 %[vd], v[152:155] offset:55296\n\t" \
  "ds_write_b128 %[vd], v[156:159] offset:56320\n\t" \
  "v_add_u32 %[vd], 0x10000, %[vd]\n\t" \
  "s_waitcnt lgkmcnt(0)\n\t" \
  "ds_write_b32 %[vl], v28 offset:24\n\t" \
  "Lw4_%=:\n\t" \
  "ds_read_b128 v[24:27], %[vm] offset:64\n\t" \
  "s_sub_i32 %[bud], %[bud], 1\n\t" \
  "s_cmp_lt_i32 %[bud], 0\n\t" \
  "s_cbranch_scc1 Lx4_%=\n\t" \
  "s_waitcnt lgkmcnt(0)\n\t" \
  "v_or3_b32 v24, v24, v25, v26\n\t" \
  "v_or_b32 v24, v24, v27\n\t" \
  "s_nop 0\n\t" \
  "v_readfirstlane_b32 %[s0], v24\n\t" \
  "s_cmp_eq_u32 %[s0], 0\n\t" \
  "s_cbranch_scc1 Lx4_%=\n\t" \
  "s_sleep 2\n\t" \
  "s_branch Lw4_%=\n\t" \
  "Lx4_%=:\n\t" \
  "ds_write_b128 %[vd], v[160:163] offset:0\n\t" \
  "ds_write_b128 %[vd], v[164:167] offset:1024\n\t" \
  "ds_write_b128 %[vd], v[168:171] offset:2048\n\t" \
  "ds_write_b128 %[vd], v[172:175] offset:3072\n\t" \
  "ds_write_b128 %[vd], v[176:179] offset:4096\n\t" \
  "ds_write_b128 %[vd], v[180:183] offset:5120\n\t" \
  "ds_write_b128 %[vd], v[184:187] offset:6144\n\t" \
  "ds_write_b128 %[vd], v[188:191] offset:7168\n\t" \
  "s_waitcnt lgkmcnt(0)\n\t" \
  "ds_write_b32 %[vl], v28 offset:32\n\t" \
  "Lw5_%=:\n\t" \
  "ds_read_b128 v[24:27], %[vm] offset:80\n\t" \
  "s_sub_i32 %[bud], %[bud], 1\n\t" \
  "s_cmp_lt_i32 %[bud], 0\n\t" \
  "s_cbranch_scc1 Lx5_%=\n\t" \
  "s_waitcnt lgkmcnt(0)\n\t" \
  "v_or3_b32 v24, v24, v25, v26\n\t" \
  "v_or_b32 v24, v24, v27\n\t" \
  "s_nop 0\n\t" \
  "v_readfirstlane_b32 %[s0], v24\n\t" \
  "s_cmp_eq_u32 %[s0], 0\n\t" \
  "s_cbranch_scc1 Lx5_%=\n\t" \
  "s_sleep 2\n\t" \
  "s_branch Lw5_%=\n\t" \
  "Lx5_%=:\n\t" \
  "ds_write_b128 %[vd], v[192:195] offset:16384\n\t" \
  "ds_write_b128 %[vd], v[196:199] offset:17408\n\t" \
  "ds_write_b128 %[vd], v[200:203] offset:18432\n\t" \
  "ds_write_b128 %[vd], v[204:207] offset:19456\n\t" \
  "ds_write_b128 %[vd], v[208:211] offset:20480\n\t" \
  "ds_write_b128 %[vd], v[212:215] offset:21504\n\t" \
  "ds_write_b128 %[vd], v[216:219] offset:22528\n\t" \
  "ds_write_b128 %[vd], v[220:223] offset:23552\n\t" \
  "s_waitcnt lgkmcnt(0)\n\t" \
  "ds_write_b32 %[vl], v28 offset:40\n\t" \
  "Lw6_%=:\n\t" \
  "ds_read_b128 v[24:27], %[vm] offset:96\n\t" \
  "s_sub_i32 %[bud], %[bud], 1\n\t" \
  "s_cmp_lt_i32 %[bud], 0\n\t" \
  "s_cbranch_scc1 Lx6_%=\n\t" \
  "s_waitcnt lgkmcnt(0)\n\t" \
  "v_or3_b32 v24, v24, v25, v26\n\t" \
  "v_or_b32 v24, v24, v27\n\t" \
  "s_nop 0\n\t" \
  "v_readfirstlane_b32 %[s0], v24\n\t" \
  "s_cmp_eq_u32 %[s0], 0\n\t" \
  "s_cbranch_scc1 Lx6_%=\n\t" \
  "s_sleep 2\n\t" \
  "s_branch Lw6_%=\n\t" \
  "Lx6_%=:\n\t" \
  "ds_write_b128 %[vd], v[224:227] offset:32768\n\t" \
  "ds_write_b128 %[vd], v[228:231] offset:33792\n\t" \
  "ds_write_b128 %[vd], v[232:235] offset:34816\n\t" \
  "ds_write_b128 %[vd], v[236:239] offset:35840\n\t" \
  "ds_write_b128 %[vd], v[240:243] offset:36864\n\t" \
  "ds_write_b128 %[vd], v[244:247] offset:37888\n\t" \
  "ds_write_b128 %[vd], v[248:251] offset:38912\n\t" \
  "ds_write_b128 %[vd], v[252:255] offset:39936\n\t" \
  "s_waitcnt lgkmcnt(0)\n\t" \
  "ds_write_b32 %[vl], v28 offset:48\n\t" \
  "s_waitcnt lgkmcnt(0)\n\t"

#define OST " sc1"
#define XPRIO0 3
#define XPRIO 1
#define LPRIO 2
#define FPRIO 1
#define MPRIO 2
#define WGRP (~sg & 1)
#define WS0 1
#define WS1 5
#define WSLP 8
#define TKG0 3
#define GEXACT 0
#define YPRIO 3
#define GPRIO 1
#define W2G 14
#define CWS 16
#define HELP 1
#define WRES 1
#define XG -2
#define XSLP 4
#define PT 1
#define GUG 25
#define TKG 4
#define YG 1
#define NOFFN 0
#define CODEPF 30336
#define WNT " nt"
constexpr int D_ = 1024, F_ = 3584;
constexpr int MISC = 147456;
constexpr int L_TAB = MISC;
constexpr int L_FLG = L_TAB + 11 * 512;
constexpr int L_ONE = L_FLG + 256;
constexpr int L_G = L_ONE + 1024;
constexpr int L_SEL = L_G + 512;
constexpr int L_BROW = L_SEL + 128;
constexpr int L_ACT = L_BROW + 2048;
constexpr int L_XS = L_ACT + 1024;
constexpr int L_JUNK = L_XS + 4096;
constexpr int LDS_BYTES = 163840;
static_assert(L_JUNK + 768 <= LDS_BYTES, "lds");
constexpr int M_CONS = L_ONE + 16, M_LAND = L_ONE + 544, M_GT = M_LAND + 288, M_HR = M_LAND + 296, M_CAR = M_LAND + 304, M_XS = M_LAND + 320, M_WR = M_LAND + 336;
constexpr int M_S0 = L_SEL + 64;
static_assert(M_S0 - M_LAND == 1056, "offset in ASM_MA");
constexpr int FL_XD = L_FLG + 16, FL_TK = L_FLG + 24;
constexpr unsigned W_EPOCH = 0;
constexpr unsigned W_RG = 256;
constexpr unsigned W_R2 = W_RG + 2048 * 8;
constexpr unsigned W_HF = 32768;
constexpr unsigned W_GF = W_HF + 32 * 128;
constexpr unsigned W_YF = W_GF + 56 * 128;
constexpr unsigned W_HT = W_YF + 8 * 128;
constexpr unsigned W_AGG = 65536;
constexpr unsigned W_GU = W_AGG + 262144;
constexpr unsigned W_YP = W_GU + 56 * 16384;
constexpr unsigned W_H = 4u << 20;
#define HSK 24
#define HOFS(l) ((l) * 16 + (2 > 1 && (l) >= HSK ? 128 : 0))
constexpr unsigned W_DBG = 24u << 20;
constexpr unsigned W_END = 25u << 20;
static_assert(W_R2 + 8192 <= W_HF && W_HT + 8192 <= W_AGG && W_YP + 128 * 56 * 256 <= W_H, "ws");
#define WS_BASE(n) (((n) - W_END) & ~(size_t)0x1fffff)
constexpr int SPIN_MAX = 1 << 18;

DEV float bf2f(unsigned v) { return __uint_as_float(v << 16); }
DEV float bfhi(unsigned v) { return __uint_as_float(v & 0xffff0000u); }
DEV u16 f2bf(float f) { return __builtin_bit_cast(u16, (__bf16)f); }
DEV float rbf(float f) { return bf2f(f2bf(f)); }
typedef __bf16 bf16x2_t __attribute__((ext_vector_type(2)));
DEV unsigned pk2(float a, float b) { return __builtin_bit_cast(unsigned, __builtin_convertvector((f32x2){a, b}, bf16x2_t)); }
DEV float fma1(float a, float b, float c) { float d; asm("v_fma_f32 %0, %1, %2, %3" : "=v"(d) : "v"(a), "v"(b), "v"(c)); return d; }
DEV float fnma1(float a, float b, float c) { float d; asm("v_fma_f32 %0, -%1, %2, %3" : "=v"(d) : "v"(a), "v"(b), "v"(c)); return d; }
DEV float mul1(float a, float b) { float d; asm("v_mul_f32 %0, %1, %2" : "=v"(d) : "v"(a), "v"(b)); return d; }
DEV f32x2 pkfma(f32x2 a, f32x2 b, f32x2 c) { f32x2 d; asm("v_pk_fma_f32 %0, %1, %2, %3" : "=v"(d) : "v"(a), "v"(b), "v"(c)); return d; }
DEV float add1(float a, float b) { float d; asm("v_add_f32 %0, %1, %2" : "=v"(d) : "v"(a), "v"(b)); return d; }
DEV f32x2 pkmul(f32x2 a, f32x2 b) { f32x2 d; asm("v_pk_mul_f32 %0, %1, %2" : "=v"(d) : "v"(a), "v"(b)); return d; }
DEV f32x2 cmul(f32x2 a, f32x2 b) { return (f32x2){fnma1(a.y, b.y, mul1(a.x, b.x)), fma1(a.y, b.x, mul1(a.x, b.y))}; }
template <int N> DEV void waitv() { asm volatile("s_waitcnt vmcnt(%0)" ::"n"(N) : "memory"); }
DEV void bar() { asm volatile("s_barrier" ::: "memory"); }
DEV unsigned lds_rd32(unsigned a) { unsigned v; asm volatile("ds_read_b32 %0, %1\n\ts_waitcnt lgkmcnt(0)" : "=v"(v) : "v"(a) : "memory"); return v; }
DEV u32x2 lds_rd64(unsigned a) { u32x2 v; asm volatile("ds_read_b64 %0, %1\n\ts_waitcnt lgkmcnt(0)" : "=v"(v) : "v"(a) : "memory"); return v; }
DEV u32x4 lds_rd128(unsigned a) { u32x4 v; asm volatile("ds_read_b128 %0, %1\n\ts_waitcnt lgkmcnt(0)" : "=v"(v) : "v"(a) : "memory"); return v; }
DEV void lds_wr32(unsigned a, unsigned v) { asm volatile("ds_write_b32 %0, %1" ::"v"(a), "v"(v) : "memory"); }
DEV void lds_wait_ge(unsigned a, unsigned need) {
#pragma unroll 1
  for (int it = 0; it < SPIN_MAX * 4; ++it) { if (__builtin_amdgcn_readfirstlane(lds_rd32(a)) >= need) break; __builtin_amdgcn_s_sleep(1); }
}
DEV void lds_wait2(unsigned a) {
#pragma unroll 1
  for (int it = 0; it < SPIN_MAX * 4; ++it) { const u32x2 v = lds_rd64(a); if (__builtin_amdgcn_readfirstlane(v.x & v.y)) break; __builtin_amdgcn_s_sleep(1); }
}
DEV void lds_wait0(unsigned a) {
#pragma unroll 1
  for (int it = 0; it < SPIN_MAX * 4; ++it) { if (__builtin_amdgcn_readfirstlane(lds_rd32(a)) == 0u) break; __builtin_amdgcn_s_sleep(XSLP); }
}
typedef __amdgpu_buffer_rsrc_t rsrc_t;
DEV void mark(rsrc_t rz, unsigned a) {
  if ((threadIdx.x & 63) == 0) __builtin_amdgcn_raw_ptr_buffer_load_lds(rz, (LAS void*)(size_t)a, 4, 0, 0, 0, 0);
}
DEV u32x4 ld128(rsrc_t r, unsigned vo, unsigned so = 0) { return __builtin_amdgcn_raw_buffer_load_b128(r, vo, so, 0); }
#define FRESH() asm volatile("" ::: "memory")
DEV u32x4 ld128c(rsrc_t r, unsigned vo) { return __builtin_amdgcn_raw_buffer_load_b128(r, vo, 0, 16); }
DEV void st128c(rsrc_t r, unsigned vo, u32x4 v) {
  asm volatile("s_nop 4\n\tbuffer_store_dwordx4 %0, %1, %2, 0 offen sc1\n\ts_nop 7\n\ts_nop 7" ::"v"(v), "v"(vo), "s"(r) : "memory");
}
DEV void st64c(rsrc_t r, unsigned vo, u32x2 v) { __builtin_amdgcn_raw_buffer_store_b64(v, r, vo, 0, 16); }
DEV void ld8(u32x4 (&w)[8], rsrc_t r, unsigned vo, unsigned so, unsigned step) {
  asm volatile("s_nop 4\n\tbuffer_load_dwordx4 %0, %9, %10, %8 offen\n\ts_add_u32 %8, %8, %11\n\tbuffer_load_dwordx4 %1, %9, %10, %8 offen\n\ts_add_u32 %8, %8, %11\n\t"
               "buffer_load_dwordx4 %2, %9, %10, %8 offen\n\ts_add_u32 %8, %8, %11\n\tbuffer_load_dwordx4 %3, %9, %10, %8 offen\n\ts_add_u32 %8, %8, %11\n\t"
               "buffer_load_dwordx4 %4, %9, %10, %8 offen\n\ts_add_u32 %8, %8, %11\n\tbuffer_load_dwordx4 %5, %9, %10, %8 offen\n\ts_add_u32 %8, %8, %11\n\t"
               "buffer_load_dwordx4 %6, %9, %10, %8 offen\n\ts_add_u32 %8, %8, %11\n\tbuffer_load_dwordx4 %7, %9, %10, %8 offen"
               : "=&v"(w[0]), "=&v"(w[1]), "=&v"(w[2]), "=&v"(w[3]), "=&v"(w[4]), "=&v"(w[5]), "=&v"(w[6]), "=&v"(w[7]), "+&s"(so)
               : "v"(vo), "s"(r), "s"(step) : "memory", "scc");
}
DEV void ld8c(u32x4 (&w)[8], rsrc_t r, unsigned vo, unsigned so, unsigned step, int a, int b) {
  asm volatile("s_nop 4\n\ts_cmp_eq_u32 %12, %13\n\ts_cbranch_scc0 Lskip_%=\n\t"
               "buffer_load_dwordx4 %0, %9, %10, %8 offen" WNT "\n\ts_add_u32 %8, %8, %11\n\tbuffer_load_dwordx4 %1, %9, %10, %8 offen" WNT "\n\ts_add_u32 %8, %8, %11\n\t"
               "buffer_load_dwordx4 %2, %9, %10, %8 offen" WNT "\n\ts_add_u32 %8, %8, %11\n\tbuffer_load_dwordx4 %3, %9, %10, %8 offen" WNT "\n\ts_add_u32 %8, %8, %11\n\t"
               "buffer_load_dwordx4 %4, %9, %10, %8 offen" WNT "\n\ts_add_u32 %8, %8, %11\n\tbuffer_load_dwordx4 %5, %9, %10, %8 offen" WNT "\n\ts_add_u32 %8, %8, %11\n\t"
               "buffer_load_dwordx4 %6, %9, %10, %8 offen" WNT "\n\ts_add_u32 %8, %8, %11\n\tbuffer_load_dwordx4 %7, %9, %10, %8 offen" WNT "\n\tLskip_%=:"
               : "+&v"(w[0]), "+&v"(w[1]), "+&v"(w[2]), "+&v"(w[3]), "+&v"(w[4]), "+&v"(w[5]), "+&v"(w[6]), "+&v"(w[7]), "+&s"(so)
               : "v"(vo), "s"(r), "s"(step), "s"(a), "n"(b) : "memory", "scc");
}
DEV rsrc_t mk(const void* p, unsigned bytes) { return __builtin_amdgcn_make_buffer_rsrc((void*)p, 0, bytes, 0x00020000); }

DEV unsigned wave_maxu(unsigned v) {
  unsigned r;
  asm("s_nop 1\n\tv_max_u32_dpp %1, %1, %1 row_shr:1 row_mask:0xf bank_mask:0xf\n\ts_nop 1\n\tv_max_u32_dpp %1, %1, %1 row_shr:2 row_mask:0xf bank_mask:0xf\n\t"
      "s_nop 1\n\tv_max_u32_dpp %1, %1, %1 row_shr:4 row_mask:0xf bank_mask:0xf\n\ts_nop 1\n\tv_max_u32_dpp %1, %1, %1 row_shr:8 row_mask:0xf bank_mask:0xf\n\t"
      "s_nop 1\n\tv_max_u32_dpp %1, %1, %1 row_bcast:15 row_mask:0xa bank_mask:0xf\n\ts_nop 1\n\tv_max_u32_dpp %1, %1, %1 row_bcast:31 row_mask:0xc bank_mask:0xf\n\t"
      "s_nop 0\n\tv_readlane_b32 %0, %1, 63\n\ts_nop 1" : "=s"(r), "+v"(v));
  return r;
}
DEV float sum4(const float (&a)[4]) {
  auto p = __builtin_amdgcn_permlane16_swap(__float_as_uint(a[0]), __float_as_uint(a[1]), false, false);
  auto q = __builtin_amdgcn_permlane16_swap(__float_as_uint(a[2]), __float_as_uint(a[3]), false, false);
  const float u = __uint_as_float(p[0]) + __uint_as_float(p[1]), w = __uint_as_float(q[0]) + __uint_as_float(q[1]);
  auto r = __builtin_amdgcn_permlane32_swap(__float_as_uint(u), __float_as_uint(w), false, false);
  float v = __uint_as_float(r[0]) + __uint_as_float(r[1]);
  v += __uint_as_float((unsigned)__builtin_amdgcn_update_dpp(0, (int)__float_as_uint(v), 0x128, 0xf, 0xf, false));
  v += __uint_as_float((unsigned)__builtin_amdgcn_update_dpp(0, (int)__float_as_uint(v), 0x124, 0xf, 0xf, false));
  v += __uint_as_float((unsigned)__builtin_amdgcn_update_dpp(0, (int)__float_as_uint(v), 0x122, 0xf, 0xf, false));
  v += __uint_as_float((unsigned)__builtin_amdgcn_update_dpp(0, (int)__float_as_uint(v), 0x121, 0xf, 0xf, false));
  return v;
}
DEV float dot2(unsigned a, unsigned b, float c) { return __builtin_amdgcn_fdot2_f32_bf16(__builtin_bit_cast(bf16x2_t, a), __builtin_bit_cast(bf16x2_t, b), c, false); }
DEV unsigned route_code(unsigned u) { return (u & 0x8000u) ? (~u & 0xffffu) : (u | 0x8000u); }
DEV unsigned med3u(unsigned a, unsigned b, unsigned c) { unsigned d; asm("v_med3_u32 %0, %1, %2, %3" : "=v"(d) : "v"(a), "v"(b), "v"(c)); return d; }
DEV float key_val(unsigned key) {
  unsigned u = key >> 16;
  u = (u & 0x8000u) ? (u & 0x7fffu) : (~u & 0xffffu);
  return __uint_as_float(u << 16);
}
#define STAMP(k)
#define STAMPS()

DEV void unit_mma(const u32x4 (&w)[8], bf16x8 a, f32x4 (&d)[8]) {
#pragma unroll
  for (int j = 0; j < 8; ++j) {
    u32x4 b;
#pragma unroll
    for (int t = 0; t < 4; ++t) b[t] = __builtin_amdgcn_perm(w[2 * t + 1][j >> 1], w[2 * t][j >> 1], (j & 1) ? 0x07060302u : 0x05040100u);
    d[j] = __builtin_amdgcn_mfma_f32_16x16x32_bf16(a, __builtin_bit_cast(bf16x8, b), d[j], 0, 0, 0);
  }
}

DEV void fold(const f32x4 (&d)[8], f32x4 (&o)[2]) {
#pragma unroll
  for (int r = 0; r < 2; ++r)
#pragma unroll
    for (int q = 0; q < 4; ++q) {
      const float a0 = d[q][r], a1 = d[q][r + 2], b0 = d[q + 4][r], b1 = d[q + 4][r + 2];
      auto a = __builtin_amdgcn_permlane32_swap(__float_as_uint(a0), __float_as_uint(a1), false, false);
      auto b = __builtin_amdgcn_permlane32_swap(__float_as_uint(b0), __float_as_uint(b1), false, false);
      float t;
      asm("s_nop 1\n\tv_add_f32_dpp %0, %1, %2 row_ror:8 row_mask:0xf bank_mask:0x3\n\tv_add_f32_dpp %0, %3, %4 row_ror:8 row_mask:0xf bank_mask:0xc"
          : "=&v"(t) : "v"(a[1]), "v"(a[0]), "v"(b[0]), "v"(b[1]));
      o[r][q] = t;
    }
}
DEV void st2x128c(rsrc_t r, unsigned vo, unsigned so, const f32x4 (&o)[2]) {
  asm volatile("s_nop 4\n\tbuffer_store_dwordx4 %0, %2, %3, 0 offen sc1\n\tbuffer_store_dwordx4 %1, %2, %3, %4 offen sc1\n\ts_nop 7\n\ts_nop 7"
               ::"v"(o[0]), "v"(o[1]), "v"(vo), "s"(r), "s"(so) : "memory");
}
DEV void poll32(rsrc_t r, unsigned vo, unsigned tg, bool on) {
#pragma unroll 1
  for (int it = 0; it < SPIN_MAX; ++it) {
    FRESH();
    const unsigned v = __builtin_amdgcn_raw_buffer_load_b32(r, vo, 0, 16);
    if (__all(!on || v == tg)) break;
    __builtin_amdgcn_s_sleep(16);
  }
}

DEV void count1(rsrc_t r, unsigned so) {
  asm volatile("s_nop 4\n\ts_waitcnt vmcnt(0)\n\ts_mov_b64 exec, 1\n\tbuffer_atomic_add %0, off, %1, %2 sc1\n\ts_mov_b64 exec, -1" ::"v"(1u), "s"(r), "s"(so) : "memory");
}
DEV void count_wait(rsrc_t r, unsigned so, unsigned n) {
#pragma unroll 1
  for (int it = 0; it < SPIN_MAX; ++it) {
    unsigned v = 0;
    asm volatile("s_nop 4\n\ts_mov_b64 exec, 1\n\tbuffer_atomic_or %0, off, %1, %2 sc0 sc1\n\ts_mov_b64 exec, -1\n\ts_waitcnt vmcnt(0)" : "+v"(v) : "s"(r), "s"(so) : "memory");
    if (__builtin_amdgcn_readfirstlane(v) == n) break;
    __builtin_amdgcn_s_sleep(CWS);
  }
}

typedef __attribute__((ext_vector_type(8))) u64 u64x8;
__global__ __launch_bounds__(512, 2) void fused(const u16*, const u16*, const u16*, const u16*, const u16*, const u16*, const u16*, const u16*, const u16*,
                                                const u16*, const u16*, const u16*, const u16*, const u16*, char*, float*) {
#if __HIP_DEVICE_COMPILE__
  extern __shared__ __attribute__((aligned(16))) char smem_[];
  LAS char* smem = (LAS char*)smem_;
#define ARGREQ(W) u64x8 ka, kb; \
  asm volatile("s_load_dwordx16 %0, %2, 0x0\n\ts_load_dwordx16 %1, %2, 0x40" W : "=&s"(ka), "=&s"(kb) : "s"(__builtin_amdgcn_kernarg_segment_ptr()) : "memory")
#define ARGS() \
  const u16 *x = (const u16*)ka[0], *Lre = (const u16*)ka[1], *Lim = (const u16*)ka[2], *Bre = (const u16*)ka[3], *Bim = (const u16*)ka[4], *Cre = (const u16*)ka[5], \
            *Cim = (const u16*)ka[6], *logdt = (const u16*)ka[7], *Dskip = (const u16*)kb[0], *w_r = (const u16*)kb[1], *w_gate = (const u16*)kb[2], \
            *w_up = (const u16*)kb[3], *w_down = (const u16*)kb[4], *gate_w = (const u16*)kb[5]; \
  char* ws = (char*)kb[6]; float* out = (float*)kb[7]; \
  const rsrc_t rws = mk(ws, W_END), rx = mk(x, 2048 * 1024 * 2), rz = mk(x, 0)
  STAMP(0);
  int tid = threadIdx.x, bid = blockIdx.x;
  {
    unsigned t_[8], u_[2];
    asm volatile(ASM_EN : [w] "=&s"(t_[0]), [j] "=&s"(t_[1]), [t] "=&s"(t_[2]), [sa] "=&s"(t_[3]), [sc] "=&s"(t_[4]), [m] "=&s"(t_[5]), [r] "=&s"(t_[6]), [c] "=&s"(t_[7]), [v0] "=&v"(u_[0]), [v1] "=&v"(u_[1]) : [kp] "s"(__builtin_amdgcn_kernarg_segment_ptr()), [tid] "v"(threadIdx.x), [bid] "s"(blockIdx.x), [cpf] "n"(CODEPF), [ms0] "n"(M_S0) : "memory", "scc", "m0", "s20", "s21", "s22", "s23", "v255");
    asm volatile("" : "+s"(bid)); asm volatile("" : "+v"(tid));
  }
  const int lane = tid & 63, wid = __builtin_amdgcn_readfirstlane(tid >> 6), xcd = bid & 7, loc = bid >> 3;
  const int c = (xcd >> 1) * 4 + (loc >> 3), j = (xcd & 1) * 8 + (loc & 7);
  const int fr = lane & 15, fq = lane >> 4;

  if (__builtin_expect(wid == 4 || wid == 5, 1)) {
    ARGREQ("");
    const int l = wid - 4;
    __builtin_amdgcn_s_setprio(LPRIO);
    const int l3 = lane >> 3;
    {
      const unsigned one = tid >= 272 ? 1u : 0u;
      asm volatile("ds_write_b128 %0, %1" ::"v"(L_FLG + min(tid - 256, 111) * 16), "v"((u32x4){one, one, one, one}) : "memory");
    }
    int bud = SPIN_MAX * 4;
    unsigned dst = 1 ? l * 49152 : 131072 - l * 114688, ci = M_CONS - 64 + 1 * 16, t_s0, vc = 0;
    unsigned b0 = (c * 128 + l * (j * 64 - c * 128) + l3 * (8 - 7 * l)) * 2048 + ((lane & 7) ^ l3) * 16, wrap = 163840 - l * 32768;
    unsigned so[8];
#pragma unroll
    for (int q = 0; q < 8; ++q) so[q] = (64 * (q & 1) + (q >> 1) + l * (8 * q - 64 * (q & 1) - (q >> 1))) * 2048;
    unsigned sk = (((xcd >> 1) + 2 * (xcd & 1)) & 3) * 512 + 1 * 128, n = 16 - 1, mka = M_LAND + 1 * 8 + l * 4, v0 = b0 + sk;
    asm volatile("" : "+s"(dst), "+v"(b0), "+s"(wrap), "+s"(so[1]), "+s"(so[2]), "+s"(so[3]), "+s"(so[4]), "+s"(so[5]), "+s"(so[6]), "+s"(so[7]),
                 "+s"(sk), "+s"(mka), "+v"(v0));
    asm volatile("s_waitcnt lgkmcnt(0)" : "+s"(ka), "+s"(kb)::"memory");
    ARGS();
    {
      const rsrc_t r0 = mk(l ? Bre : x, (1u << 22) >> l), r1 = mk(l ? Bim : x + 4096, l ? (1u << 21) : (1u << 22) - 8192);
      STAMP(8);
      asm volatile(ASM_LA : [dst] "+&s"(dst), [sk] "+&s"(sk), [ci] "+&s"(ci), [n] "+&s"(n), [mk] "+&s"(mka), [bud] "+&s"(bud), [s0] "=&s"(t_s0), [v0] "+&v"(v0), [vc] "+&v"(vc) : [r0] "s"(r0), [r1] "s"(r1), [rz] "s"(rz), [wrap] "s"(wrap), [b0] "v"(b0), [vw] "v"(M_WR), [so0] "s"(so[0]), [so1] "s"(so[1]), [so2] "s"(so[2]), [so3] "s"(so[3]), [so4] "s"(so[4]), [so5] "s"(so[5]), [so6] "s"(so[6]), [so7] "s"(so[7]) : "memory", "scc", "m0", "v24", "v25", "v26", "v27", "v28", "v29", "v30", "v31");
    }
    STAMP(1);
    {
      const rsrc_t r0 = mk(Cre, 1 << 21), r1 = mk(Cim, 1 << 21);
      const unsigned b0 = (j * 64 + 4 * l3) * 2048 + (((lane & 7) ^ (l3 >> 1)) * 16), b1 = b0 ^ 64, wrap = 147456, cth = M_CONS + (15 - l) * 16;
      unsigned so[8];
#pragma unroll
      for (int q = 0; q < 8; ++q) so[q] = (32 * (q & 1) + (q >> 1)) * 2048;
      unsigned sk = l * 128, hi = (j & 8) * 128, n = 8, mka = M_LAND + (17 + l) * 8, v0 = b0 + sk + hi, v1 = b1 + sk + hi, t_s1;
      const rsrc_t rp = mk(l ? Cim : Cre, 1 << 21);
      const unsigned p0 = b0 + (hi ^ 0x400);
      unsigned vd = l * 8192 + lane * 16;
      asm volatile(ASM_LB : [dst] "+&s"(dst), [sk] "+&s"(sk), [ci] "+&s"(ci), [n] "+&s"(n), [mk] "+&s"(mka), [bud] "+&s"(bud), [s0] "=&s"(t_s0), [v0] "+&v"(v0), [vc] "+&v"(vc), [v1] "+&v"(v1), [hi] "+&s"(hi), [s1] "=&s"(t_s1), [vd] "+&v"(vd) : [r0] "s"(r0), [r1] "s"(r1), [rz] "s"(rz), [wrap] "s"(wrap), [b0] "v"(b0), [vw] "v"(M_WR + 16), [cth] "s"(cth), [b1] "v"(b1), [nend] "s"(3 + l), [rp] "s"(rp), [p0] "v"(p0), [p1] "v"(p0 ^ 64), [vm] "v"(M_CONS + 256), [vl] "v"(M_LAND + 26 * 8 + 4 * l), [so0] "s"(so[0]), [so1] "s"(so[1]), [so2] "s"(so[2]), [so3] "s"(so[3]), [so4] "s"(so[4]), [so5] "s"(so[5]), [so6] "s"(so[6]), [so7] "s"(so[7]) : "memory", "scc", "m0", "v24", "v25", "v26", "v27", "v28", "v29", "v30", "v31", "v32", "v33", "v34", "v35", "v36", "v37", "v38", "v39", "v40", "v41", "v42", "v43", "v44", "v45", "v46", "v47", "v48", "v49", "v50", "v51", "v52", "v53", "v54", "v55", "v56", "v57", "v58", "v59", "v60", "v61", "v62", "v63", "v64", "v65", "v66", "v67", "v68", "v69", "v70", "v71", "v72", "v73", "v74", "v75", "v76", "v77", "v78", "v79", "v80", "v81", "v82", "v83", "v84", "v85", "v86", "v87", "v88", "v89", "v90", "v91", "v92", "v93", "v94", "v95", "v96", "v97", "v98", "v99", "v100", "v101", "v102", "v103", "v104", "v105", "v106", "v107", "v108", "v109", "v110", "v111", "v112", "v113", "v114", "v115", "v116", "v117", "v118", "v119", "v120", "v121", "v122", "v123", "v124", "v125", "v126", "v127", "v128", "v129", "v130", "v131", "v132", "v133", "v134", "v135", "v136", "v137", "v138", "v139", "v140", "v141", "v142", "v143", "v144", "v145", "v146", "v147", "v148", "v149", "v150", "v151", "v152", "v153", "v154", "v155", "v156", "v157", "v158", "v159", "v160", "v161", "v162", "v163", "v164", "v165", "v166", "v167", "v168", "v169", "v170", "v171", "v172", "v173", "v174", "v175", "v176", "v177", "v178", "v179", "v180", "v181", "v182", "v183", "v184", "v185", "v186", "v187", "v188", "v189", "v190", "v191", "v192", "v193", "v194", "v195", "v196", "v197", "v198", "v199", "v200", "v201", "v202", "v203", "v204", "v205", "v206", "v207", "v208", "v209", "v210", "v211", "v212", "v213", "v214", "v215", "v216", "v217", "v218", "v219", "v220", "v221", "v222", "v223", "v224", "v225", "v226", "v227", "v228", "v229", "v230", "v231", "v232", "v233", "v234", "v235", "v236", "v237", "v238", "v239", "v240", "v241", "v242", "v243", "v244", "v245", "v246", "v247", "v248", "v249", "v250", "v251", "v252", "v253", "v254", "v255");
    }
    waitv<0>();
    STAMP(3);
    STAMPS();
    __builtin_amdgcn_endpgm();
  }

  if (__builtin_expect(wid >= 6, 1)) {
    const int xw = wid - 6;
    __builtin_amdgcn_s_setprio(XPRIO0);
    ARGREQ("\n\ts_waitcnt lgkmcnt(0)");
    ARGS();
    const int row0 = c * 128 + j * 8 + xw * 4;
    const int l = xw;
    const bool ffn = !NOFFN && loc < 28;
    const int sg = xcd * 7 + (loc >> 2), kq = loc & 3;
    u32x4 w1[4][8], w2[2][8];
    unsigned tagA, tagB;
    const int sb = HELP ? (3 - (int)((size_t)w_up >> 7)) & 7 : 9, s1 = ffn ? sg : 8 * xcd + sb;
    const bool gu = ffn ? !(xw && (sg & 7) == sb) : HELP && !NOFFN && xw && xcd < 7;
    const int pg = ffn ? WGRP : 0, grp = __builtin_amdgcn_readfirstlane(gu ? pg : 9);
    auto wburst = [&](int p, unsigned gate) __attribute__((always_inline)) {
      const bool on = grp == p;
      const rsrc_t r1 = mk(xw ? w_up : w_gate, D_ * F_ * 2);
      const unsigned o1 = (kq * 256 + 32 * (fr >> 3) + 8 * fq) * (F_ * 2) + (s1 * 64 + 8 * (fr & 7)) * 2;
      if (pg == p) {
        lds_wait0(M_LAND + gate * 8);
        if (on) mark(rz, M_WR + xw * 4);
        __builtin_amdgcn_s_sleep(WSLP);
      }
#pragma unroll
      for (int u = 0; u < 4; ++u) {
        ld8c(w1[u], r1, o1, u * 64 * (F_ * 2), F_ * 2, grp, p);
        if (u == WRES && on) mark(rz, M_WR + 8 + xw * 4);
      }
      if (pg == p)
        __builtin_amdgcn_raw_ptr_buffer_load_lds(mk(xw ? Cim : Cre, 1 << 21), (LAS void*)(smem + L_JUNK + 256 + xw * 256), 4, (j * 64 + lane) * 2048 + ((c + lane) & 15) * 128, 0, 0, 0);
    };
    {
      unsigned ep;
      u32x4 xv[8], wv[2], gv[2];
      unsigned pl[3];
      const rsrc_t rw = mk(w_r, 2048), rg = mk(gate_w, 2048);
      const rsrc_t rp0 = mk(Lre, 2048), rp1 = mk(Lim, 2048), rp2 = mk(logdt, 2048);
      const unsigned zero = 0, l16 = lane * 16, xo = row0 * 2048 + lane * 16, po = (j * 64 + lane) * 2;
      if (XG < -1) { asm volatile("" ::"s"(rg), "s"(rp0), "s"(rp1), "s"(rp2), "s"(rx), "v"(xo), "v"(po)); bar(); }
      asm volatile("s_nop 4\n\tbuffer_load_dword %0, %3, %5, 0 offen sc1\n\tbuffer_load_dwordx4 %1, %4, %6, 0 offen\n\tbuffer_load_dwordx4 %2, %4, %6, 0 offen offset:1024"
                   : "=&v"(ep), "=&v"(wv[0]), "=&v"(wv[1]) : "v"(zero), "v"(l16), "s"(rws), "s"(rw) : "memory");
      asm volatile("s_nop 4\n\tbuffer_load_dwordx4 %3, %9, %10, 0 offen\n\tbuffer_load_dwordx4 %4, %9, %10, 0 offen offset:1024\n\t"
                   "buffer_load_ushort %0, %5, %6, 0 offen\n\tbuffer_load_ushort %1, %5, %7, 0 offen\n\tbuffer_load_ushort %2, %5, %8, 0 offen"
                   : "=&v"(pl[0]), "=&v"(pl[1]), "=&v"(pl[2]), "=&v"(gv[0]), "=&v"(gv[1]) : "v"(po), "s"(rp0), "s"(rp1), "s"(rp2), "v"(l16), "s"(rg) : "memory");
      asm volatile("s_nop 4\n\tbuffer_atomic_swap %0, %1, %2, 0 offen sc1" ::"v"(zero), "v"(tid == 384 && bid < 160 ? W_HF + bid * 128 : 0xf0000000u), "s"(rws) : "memory");
      if (XG >= -1) bar();
      if (XG >= 0) lds_wait0(M_LAND + XG * 8);
      ld8(xv, rx, xo, 0, 1024);
      asm volatile("s_waitcnt vmcnt(0)" : "+v"(ep), "+v"(xv[0]), "+v"(xv[1]), "+v"(xv[2]), "+v"(xv[3]), "+v"(xv[4]), "+v"(xv[5]), "+v"(xv[6]),
                   "+v"(xv[7]), "+v"(wv[0]), "+v"(wv[1]), "+v"(gv[0]), "+v"(gv[1]), "+v"(pl[0]), "+v"(pl[1]), "+v"(pl[2])::"memory");
      tagA = ep + 1u; tagB = ep + 0x40000001u;
      float sr[4];
#pragma unroll
      for (int r = 0; r < 4; ++r) {
        sr[r] = 0.f;
#pragma unroll
        for (int h = 0; h < 2; ++h)
#pragma unroll
          for (int e = 0; e < 4; ++e) { const unsigned xu = xv[2 * r + h][e], wu = wv[h][e]; sr[r] = dot2(xu, wu, sr[r]); }
      }
      {
        const unsigned rb = route_code(f2bf(sum4(sr)));
        const unsigned r01 = (unsigned)__builtin_amdgcn_readlane((int)rb, 0) | ((unsigned)__builtin_amdgcn_readlane((int)rb, 16) << 16);
        const unsigned r23 = (unsigned)__builtin_amdgcn_readlane((int)rb, 32) | ((unsigned)__builtin_amdgcn_readlane((int)rb, 48) << 16);
        st128c(rws, lane == 0 ? W_R2 + row0 * 4 : 0xf0000000u, (u32x4){r01, tagA, r23, tagB});
      }
#pragma unroll
      for (int r = 0; r < 4; ++r) {
        sr[r] = 0.f;
#pragma unroll
        for (int h = 0; h < 2; ++h)
#pragma unroll
          for (int e = 0; e < 4; ++e) {
            const unsigned xu = xv[2 * r + h][e], gu = gv[h][e];
            sr[r] = dot2(xu, gu, sr[r]);
          }
      }
      {
        const float z = rbf(sum4(sr));
        st64c(rws, fr == 0 ? W_RG + (row0 + fq) * 8 : 0xf0000000u, (u32x2){__float_as_uint(rbf(1.f / (1.f + __expf(-z)))), tagB});
      }
      STAMP(8);
      __builtin_amdgcn_s_setprio(XPRIO);
#pragma unroll
      for (int u = 0; u < 4; ++u) asm volatile("" : "=v"(w1[u][0]), "=v"(w1[u][1]), "=v"(w1[u][2]), "=v"(w1[u][3]), "=v"(w1[u][4]), "=v"(w1[u][5]), "=v"(w1[u][6]), "=v"(w1[u][7]));
#pragma unroll
      for (int u = 0; u < 2; ++u) asm volatile("" : "=v"(w2[u][0]), "=v"(w2[u][1]), "=v"(w2[u][2]), "=v"(w2[u][3]), "=v"(w2[u][4]), "=v"(w2[u][5]), "=v"(w2[u][6]), "=v"(w2[u][7]));
      wburst(0, WS0);
      if (xw == 1) {
        const float lre = bf2f(pl[0]), lim = bf2f(pl[1]);
        const float dt = rbf(__expf(bf2f(pl[2])));
        const float er = expf(dt * lre);
        float sn, cs;
        sincosf(dt * lim, &sn, &cs);
        const f32x2 l1 = {mul1(er, cs), mul1(er, sn)};
        const float nr = l1.x - 1.f, ni = l1.y, den = fma1(lre, lre, mul1(lim, lim));
        const f32x2 cf = {fma1(nr, lre, mul1(ni, lim)) / den, fnma1(nr, lim, mul1(ni, lre)) / den};
        const f32x2 l2 = cmul(l1, l1), l4 = cmul(l2, l2), l8 = cmul(l4, l4), l16_ = cmul(l8, l8), l32 = cmul(l16_, l16_), l64 = cmul(l32, l32),
                    l128 = cmul(l64, l64), l256 = cmul(l128, l128), l384 = cmul(l256, l128), l512 = cmul(l256, l256);
        auto put = [&](int e, f32x2 v) { const float re = v.x, im = v.y; asm volatile("ds_write2_b32 %0, %1, %2 offset0:0 offset1:64" ::"v"(L_TAB + e * 512 + lane * 4), "v"(re), "v"(im) : "memory"); };
        put(0, l1); put(1, cf); put(2, l8); put(3, l16_); put(4, l32); put(5, l64); put(6, l512); put(7, (f32x2){1.f, 0.f}); put(8, l128); put(9, l256); put(10, l384);
        lds_wr32(M_LAND + 16 * 8, 0u);
      }
    }
    unsigned m = 0, m2 = 0, m3 = 0, m4 = 0;
    auto gkey = [&](unsigned k) { return (k & 0xffff0000u) | ((k & 28u) << 6) | (k & 3u) | (unsigned)(252 - lane * 4); };
    lds_wait0(M_LAND + (pg == 0 ? TKG0 : TKG) * 8);
    if (xw == 1) {
#pragma unroll
      for (int h = 0; h < 2; ++h) {
        poll32(rws, W_RG + (c * 128 + h * 64 + lane) * 8 + 4, tagB, true);
        __builtin_amdgcn_raw_ptr_buffer_load_lds(rws, (LAS void*)(smem + L_G + h * 256), 4, W_RG + (c * 128 + h * 64 + lane) * 8, 0, 0, 16);
      }
      mark(rz, M_GT);
      STAMP(1);
    } else {
      {
        u32x4 g[8];
#pragma unroll 1
        for (int it = 0; it < SPIN_MAX; ++it) {
          FRESH();
#pragma unroll
          for (int q = 0; q < 8; ++q) g[q] = ld128c(rws, W_R2 + lane * 16 + q * 1024);
          bool ok = true;
#pragma unroll
          for (int q = 0; q < 8; ++q) ok = ok && g[q].y == tagA && g[q].w == tagB;
          if (__all(ok)) break;
          __builtin_amdgcn_s_sleep(16);
        }
        mark(rz, M_LAND + 16 * 8 + 4);
        STAMP(1);
        auto lkey = [&](int i) { const unsigned w = g[i >> 2][i & 2]; return ((i & 1) ? (w & 0xffff0000u) : (w << 16)) | (unsigned)(31 - i); };
        m = lkey(0);
#pragma unroll
        for (int i = 1; i < 32; ++i) {
          const unsigned k = lkey(i);
          m4 = med3u(m3, m4, k); m3 = med3u(m2, m3, k); m2 = med3u(m, m2, k); m = max(m, k);
        }
      }
      m = gkey(m); m2 = gkey(m2); m3 = gkey(m3); m4 = gkey(m4);
    }
    asm volatile("" : "+v"(m), "+v"(m2), "+v"(m3), "+v"(m4));
    wburst(1, WS1);
    if (xw == 0) {
      unsigned mine = 0;
#pragma unroll 1
      for (int s = 0; s < 8; ++s) {
        const unsigned mm = wave_maxu(m);
        mine = (lane >> 3) == s ? mm : mine;
        const bool hit = m == mm;
        m = hit ? m2 : m; m2 = hit ? m3 : m2; m3 = hit ? m4 : m3; m4 = hit ? 0u : m4;
      }
      if (__ballot(m == 0u)) {
        unsigned lim = 0xffffffffu;
#pragma unroll 1
        for (int s = 0; s < 8; ++s) {
          unsigned mm = 0;
#pragma unroll 1
          for (int q = 0; q < 8; ++q) {
            const u32x4 w = ld128c(rws, W_R2 + lane * 16 + q * 1024);
#pragma unroll
            for (int e = 0; e < 4; ++e) {
              const unsigned k = gkey(((e & 1) ? (w[e & 2] & 0xffff0000u) : (w[e & 2] << 16)) | (unsigned)(31 - 4 * q - e));
              mm = max(mm, k < lim || s == 0 ? k : 0u);
            }
          }
          lim = wave_maxu(mm);
          mine = (lane >> 3) == s ? lim : mine;
        }
      }
      const unsigned myidx = 2047u - (mine & 0xffffu);
      {
#pragma unroll
        for (int u = 0; u < 4; ++u)
          __builtin_amdgcn_raw_ptr_buffer_load_lds(rx, (LAS void*)(smem + L_XS + u * 1024), 16, myidx * 2048 + (loc & 3) * 512 + (lane & 7) * 16, u * 128, 0, 0);
      }
      const float myrs = rbf(1.f / (1.f + __expf(-key_val(mine))));
      if ((lane & 7) == 0) asm volatile("ds_write_b64 %0, %1\n\ts_waitcnt lgkmcnt(0)" ::"v"(L_SEL + lane), "v"((u32x2){myidx, __float_as_uint(myrs)}) : "memory");
      mark(rz, M_XS);
    }
    STAMP(2);
    __builtin_amdgcn_s_setprio(FPRIO);
    lds_wait0(M_XS);
    const u32x2 sel = lds_rd64(L_SEL + (lane & 7) * 8);
    const unsigned fa = ((fr & 7) * 8 + 4 * (fr >> 3) + fq) * 16, lp = (fr & 7) * 32 + (fr >> 3) * 16, tk = 4 * (fq & 1) + 2 * (fq >> 1);
    waitv<0>();
    STAMP(10);
    {
      if (0 && ffn) { lds_wait0(M_LAND + (W2G - 2) * 8); mark(rz, M_WR + 16 + xw * 4); }
      if (ffn) lds_wait0(M_LAND + W2G * 8);
      __builtin_amdgcn_s_setprio(3);
      const rsrc_t r2 = mk(w_down, D_ * F_ * 2);
      const unsigned o2 = (sg * 64 + 32 * (fr >> 3) + 8 * fq) * 2048 + (kq * 256 + xw * 128 + 8 * (fr & 7)) * 2;
      const int on = __builtin_amdgcn_readfirstlane(ffn ? 1 : 0);
#pragma unroll
      for (int u = 0; u < 2; ++u) {
        ld8c(w2[u], r2, o2, u * 128, 2048, on, 1);
        if (0 == u + 1 && ffn) mark(rz, M_WR + 24 + xw * 4);
      }
      __builtin_amdgcn_s_setprio(FPRIO);
    }
    f32x4 d[8], o[2];
    if (gu) {
      u32x4 xa[4];
      asm volatile("ds_read_b128 %0, %4\n\tds_read_b128 %1, %4 offset:1024\n\tds_read_b128 %2, %4 offset:2048\n\tds_read_b128 %3, %4 offset:3072\n\ts_waitcnt lgkmcnt(0)"
                   : "=&v"(xa[0]), "=&v"(xa[1]), "=&v"(xa[2]), "=&v"(xa[3]) : "v"(L_XS + fa) : "memory");
#pragma unroll
      for (int q = 0; q < 8; ++q) d[q] = (f32x4){0.f, 0.f, 0.f, 0.f};
#pragma unroll
      for (int u = 0; u < 4; ++u) {
        asm volatile("" : "+v"(w1[u][0]), "+v"(w1[u][1]), "+v"(w1[u][2]), "+v"(w1[u][3]), "+v"(w1[u][4]), "+v"(w1[u][5]), "+v"(w1[u][6]), "+v"(w1[u][7]));
        unit_mma(w1[u], __builtin_bit_cast(bf16x8, xa[u]), d);
        asm volatile("" : "+v"(d[0]), "+v"(d[1]), "+v"(d[2]), "+v"(d[3]), "+v"(d[4]), "+v"(d[5]), "+v"(d[6]), "+v"(d[7]));
      }
      fold(d, o);
      STAMP(11);
      st2x128c(rws, W_GU + ((s1 * 8 + tk) * 8 + kq * 2 + l) * 256 + lp, 2048, o);
      count1(rws, W_GF + s1 * 128);
    }
    STAMP(3);
    if (ffn) {
      {
        if (GUG) lds_wait0(M_LAND + GUG * 8);
        __builtin_amdgcn_s_setprio(GPRIO);
        count_wait(rws, W_GF + sg * 128, 8);
        f32x4 a[8];
#pragma unroll
        for (int q = 0; q < 8; ++q) a[q] = __builtin_bit_cast(f32x4, ld128c(rws, W_GU + (sg * 8 + 4 * l + fq) * 2048 + fr * 16 + q * 256));
        float a2[4];
#pragma unroll
        for (int e = 0; e < 4; ++e) {
          const float g = rbf(add1(add1(a[0][e], a[2][e]), add1(a[4][e], a[6][e]))), u = rbf(add1(add1(a[1][e], a[3][e]), add1(a[5][e], a[7][e])));
          a2[e] = mul1(rbf(g / add1(1.f, __expf(-g))), u);
        }
        asm volatile("ds_write_b64 %0, %1\n\ts_nop 3" ::"v"(L_ACT + (4 * l + fq) * 128 + fr * 8), "v"((u32x2){pk2(a2[0], a2[1]), pk2(a2[2], a2[3])}) : "memory");
        lds_wr32(FL_TK + l * 4, 1u);
        lds_wait_ge(FL_TK + (l ^ 1) * 4, 1u);
      }
      const u32x4 af = lds_rd128(L_ACT + fa);
      STAMP(4);
      waitv<0>();
#pragma unroll
      for (int u = 0; u < 2; ++u)
        asm volatile("" : "+v"(w2[u][0]), "+v"(w2[u][1]), "+v"(w2[u][2]), "+v"(w2[u][3]), "+v"(w2[u][4]), "+v"(w2[u][5]), "+v"(w2[u][6]), "+v"(w2[u][7]));
#pragma unroll
      for (int u = 0; u < 2; ++u) {
#pragma unroll
        for (int q = 0; q < 8; ++q) d[q] = (f32x4){0.f, 0.f, 0.f, 0.f};
        unit_mma(w2[u], __builtin_bit_cast(bf16x8, af), d);
        fold(d, o);
        st2x128c(rws, W_YP + ((tk * 16 + kq * 4 + l * 2 + u) * 56 + sg) * 256 + lp, 16 * 56 * 256, o);
      }
      count1(rws, W_YF + (kq * 2 + l) * 128);
      STAMP(5);
    }
    u64 rows = __ballot(lane < 8 && (sel.x >> 7) == (unsigned)c);
    if (l) rows &= rows - 1;
    if (!NOFFN && rows) {
      lds_wait0(M_CONS + (16 + YG) * 16);
      __builtin_amdgcn_s_setprio(YPRIO);
      count_wait(rws, W_YF + (j >> 1) * 128, 56);
      STAMP(9);
#pragma unroll 1
      for (; rows; rows &= rows - 1, rows &= rows - 1) {
        const int s = __builtin_ctzll(rows);
        const int t = __builtin_amdgcn_readlane(sel.x, s);
        const float rs = __uint_as_float(__builtin_amdgcn_readlane(sel.y, s));
        f32x4 a[14];
#pragma unroll
        for (int k = 0; k < 14; ++k) a[k] = __builtin_bit_cast(f32x4, ld128c(rws, W_YP + (s * 16 + j) * (56 * 256) + lane * 16 + k * 1024));
        const float xf = bf2f(__builtin_amdgcn_raw_buffer_load_b16(rx, t * 2048 + j * 128 + fr * 8 + fq * 2, 0, 0));
        const float omg = rbf(1.f - __uint_as_float(lds_rd32(L_G + (t & 127) * 4)));
        float l0 = a[0][0], l1 = a[0][1], h0 = a[0][2], h1 = a[0][3];
#pragma unroll
        for (int k = 1; k < 14; ++k) { l0 = add1(l0, a[k][0]); l1 = add1(l1, a[k][1]); h0 = add1(h0, a[k][2]); h1 = add1(h1, a[k][3]); }
        asm volatile("s_nop 1" : "+v"(l0), "+v"(l1), "+v"(h0), "+v"(h1));
        auto p = __builtin_amdgcn_permlane16_swap(__float_as_uint(l0), __float_as_uint(l1), false, false);
        auto q = __builtin_amdgcn_permlane16_swap(__float_as_uint(h0), __float_as_uint(h1), false, false);
        const float u = __uint_as_float(p[0]) + __uint_as_float(p[1]), w = __uint_as_float(q[0]) + __uint_as_float(q[1]);
        auto r = __builtin_amdgcn_permlane32_swap(__float_as_uint(u), __float_as_uint(w), false, false);
        const float y = __uint_as_float(r[0]) + __uint_as_float(r[1]);
        lds_wr32(L_BROW + s * 256 + fr * 16 + fq * 4, __float_as_uint(mul1(omg, rbf(xf + rbf(mul1(rbf(y), rs))) - xf)));
      }
    }
    lds_wr32(FL_XD + l * 4, 1u);
    waitv<0>();
    STAMP(6);
    STAMPS();
    __builtin_amdgcn_endpgm();
  }

  __builtin_amdgcn_s_setprio(MPRIO);
  ARGREQ("\n\ts_waitcnt lgkmcnt(0)");
  ARGS();
  if (wid == PT - 1) {
    const unsigned q = loc & 3, g = loc >> 2, h = (xcd & 1) << 20, c0 = (xcd >> 1) * 4 + q;
    auto sel = [](unsigned u, unsigned v, unsigned a, unsigned b) {
      unsigned d; asm("s_cmp_eq_u32 %1, %2\n\ts_cselect_b32 %0, %3, %4" : "=s"(d) : "s"(u), "n"(v), "s"(a), "s"(b) : "scc"); return d; };
    const unsigned arg = sel(g, 7, 0, sel(g, 6, 15, sel(g, 5, 14, sel(g, 4, 5 + (q >> 1), sel(g, 3, sel(q, 3, 14, 10 + q), 10 + g)))));
    const unsigned off = sel(g, 7, min(q << 21, (4u << 20) - 4), sel(g, 6, (c0 << 19) + (h >> 9), sel(g, 5, W_H + (c0 << 20) + 384,
                             sel(g, 4, h + (q & 1) * ((1u << 20) - 4), sel(g, 3, sel(q, 3, W_H - 128, D_ * F_ * 2 - 4), q << 21)))));
    u64 ptr; unsigned junk;
    asm volatile("s_load_dwordx2 %0, %2, %3\n\ts_waitcnt lgkmcnt(0)\n\ts_load_dword %1, %0, %4\n\ts_waitcnt lgkmcnt(0)"
                 : "=&s"(ptr), "=&s"(junk) : "s"(__builtin_amdgcn_kernarg_segment_ptr()), "s"(arg * 8), "s"(off) : "memory");
  }
  unsigned tagA, tagB;
  int bud = SPIN_MAX * 4;
  const int tl0 = 32 * fq + 2 * wid;
  u32x2 xe[2][4], dv;
  {
    const rsrc_t rd = mk(Dskip, 2048);
    const unsigned dofs = (j * 64 + 4 * fr) * 2, xo = (32 * wid + 4 * fq) * 128 + ((fr >> 1) ^ (4 * (fq & 1))) * 16 + (fr & 1) * 8;
    const unsigned ks = (j - 4 * ((xcd >> 1) + 2 * (xcd & 1))) & 15;
    const int x0 = (fq ^ (fr & 7)) * 16, x1 = ((4 + fq) ^ (fr & 7)) * 16;
    const unsigned pa = 16384 + (wid * 16 + fr) * 128, pb = 131072 + fr * 128;
    unsigned pa0 = pa + x0, pa1 = pa + x1, pb0 = pb + x0, pb1 = pb + x1;
    const unsigned tq = L_TAB + (wid * 16 + fq * 4) * 4, tcl = L_TAB + 6 * 512 + (wid * 16 + fr) * 4, tcw = tcl + 512 + fq * 512;
    const unsigned l16 = HOFS(lane), pubo = (fq * 4 + (fr >> 2)) * 16, plo = fr * 16 + (3 - fq) * 1024;
    const unsigned sag = W_AGG + (j * 16 + c) * 1024 + wid * 256, sht = W_HT + (j * 4 + wid) * 128;
    unsigned sh = W_H + (c * 64 + 4 * j + wid) * 8192 * 2, fl = M_LAND, scn = M_CONS - 16 + wid * 4;
    unsigned sl[4];
    u64 em[4];
#pragma unroll
    for (int q = 0; q < 4; ++q) {
      sl[q] = sag - (4 + 4 * q) * 1024;
      const int n = min(max(c - 4 * q, 0), 4);
      em[q] = n == 4 ? ~0ull : (1ull << (16 * n)) - 1ull;
    }
    unsigned t_sk, t_s0, t_s1, t_s2, t_s3, t_sx;
    asm volatile(ASM_MA : [pa0] "+&v"(pa0), [pa1] "+&v"(pa1), [pb0] "+&v"(pb0), [pb1] "+&v"(pb1), [fl] "+&v"(fl), [sk] "=&s"(t_sk), [s0] "=&s"(t_s0), [s1] "=&s"(t_s1), [s2] "=&s"(t_s2), [s3] "=&s"(t_s3), [sx] "=&s"(t_sx), [bud] "+&s"(bud), [scn] "+&s"(scn), [sh] "+&s"(sh), [tgA] "=&s"(tagA), [tgB] "=&s"(tagB), [xe00] "=&v"(xe[0][0]), [xe01] "=&v"(xe[0][1]), [xe02] "=&v"(xe[0][2]), [xe03] "=&v"(xe[0][3]), [xe10] "=&v"(xe[1][0]), [xe11] "=&v"(xe[1][1]), [xe12] "=&v"(xe[1][2]), [xe13] "=&v"(xe[1][3]), [dv] "=&v"(dv) : [tq] "v"(tq), [tcl] "v"(tcl), [tcw] "v"(tcw), [pubo] "v"(pubo), [plo] "v"(plo), [l16] "v"(l16), [xo] "v"(xo), [dofs] "v"(dofs), [wrap] "s"(-98304), [wrap2] "s"(-131072), [srd] "s"(rws), [ks] "s"(ks), [srdd] "s"(rd), [srz] "s"(rz), [sag] "s"(sag), [mcar] "s"(M_CAR), [eh] "s"((1u << c) - 1u), [sht] "s"(sht), [sl0] "s"(sl[0]), [em0] "s"(em[0]), [sl1] "s"(sl[1]), [em1] "s"(em[1]), [sl2] "s"(sl[2]), [em2] "s"(em[2]), [sl3] "s"(sl[3]), [em3] "s"(em[3]) : "memory", "scc", "vcc", "m0", "v64", "v65", "v66", "v67", "v68", "v69", "v70", "v71", "v72", "v73", "v74", "v75", "v76", "v77", "v78", "v79", "v80", "v81", "v82", "v83", "v84", "v85", "v86", "v87", "v88", "v89", "v90", "v91", "v92", "v93", "v94", "v95", "v96", "v97", "v98", "v99", "v100", "v101", "v102", "v103", "v104", "v105", "v106", "v107", "v108", "v109", "v110", "v111", "v112", "v113", "v114", "v115", "v116", "v117", "v118", "v119", "v120", "v121", "v122", "v123", "v124", "v125", "v126", "v127", "v128", "v129", "v130", "v131", "v132", "v133", "v134", "v135", "v136", "v137", "v138", "v139", "v140", "v141", "v142", "v143", "v144", "v145", "v146", "v147", "v148", "v149", "v150", "v151", "v152", "v153", "v154", "v155", "v156", "v157", "v158", "v159", "v160", "v161", "v162", "v163", "v164", "v165", "v166", "v167", "v168", "v169", "v170", "v171", "v172", "v173", "v174", "v175", "v176", "v177", "v178", "v179", "v180", "v181", "v182", "v183", "v184", "v185", "v186", "v187", "v188", "v189", "v190", "v191", "v192", "v193", "v194", "v195", "v196", "v197", "v198", "v199", "v200", "v201", "v202", "v203", "v204", "v205", "v206", "v207", "v208", "v209", "v210", "v211", "v212", "v213", "v214", "v215", "v216", "v217", "v218", "v219", "v220", "v221", "v222", "v223", "v224", "v225", "v226", "v227", "v228", "v229", "v230", "v231", "v232", "v233", "v234", "v235", "v236", "v237", "v238", "v239", "v240", "v241", "v242", "v243", "v244", "v245", "v246", "v247", "v248", "v249", "v250", "v251", "v252", "v253", "v254", "v255");
  }
  f32x2 g2[4], base[4][4];
  unsigned cnt = 1, mine, dp[8];
  {
    unsigned selu, m0_, m1_;
#pragma unroll 1
    for (int it = 0; it < SPIN_MAX * 4; ++it) {
      asm volatile("ds_read_b32 %5, %7\n\tds_read_b32 %6, %7 offset:%10\n\tds_read_b32 %4, %8\n\tds_read_b64 %0, %9\n\tds_read_b64 %1, %9 offset:32\n\t"
                   "ds_read_b64 %2, %9 offset:64\n\tds_read_b64 %3, %9 offset:96\n\ts_waitcnt lgkmcnt(0)"
                   : "=&v"(g2[0]), "=&v"(g2[1]), "=&v"(g2[2]), "=&v"(g2[3]), "=&v"(selu), "=&v"(m0_), "=&v"(m1_)
                   : "v"(M_GT), "v"(L_SEL + (lane & 7) * 8), "v"(L_G + tl0 * 4), "n"(M_XS - M_GT) : "memory");
      if (__builtin_amdgcn_readfirstlane(m0_ | m1_) == 0u) break;
      __builtin_amdgcn_s_sleep(1);
    }
    f32x2 dd[4];
#pragma unroll
    for (int n = 0; n < 4; ++n) { const unsigned w = dv[n >> 1]; const float f = (n & 1) ? bfhi(w) : bf2f(w & 0xffffu); dd[n] = (f32x2){f, f}; }
#pragma unroll
    for (int r = 0; r < 4; ++r) {
      const unsigned ob = pk2(1.f - g2[r].x, 1.f - g2[r].y);
      const f32x2 omg = {bf2f(ob & 0xffffu), bfhi(ob)};
#pragma unroll
      for (int n = 0; n < 4; ++n) {
        const unsigned w0 = xe[0][r][n >> 1], w1 = xe[1][r][n >> 1];
        const f32x2 xf = {(n & 1) ? bfhi(w0) : bf2f(w0 & 0xffffu), (n & 1) ? bfhi(w1) : bf2f(w1 & 0xffffu)};
        base[r][n] = pkmul(xf, pkfma(g2[r], dd[n], omg));
      }
    }
#pragma unroll
    for (int r = 0; r < 4; ++r) asm volatile("" : "+v"(base[r][0]), "+v"(base[r][1]), "+v"(base[r][2]), "+v"(base[r][3]));
    asm volatile("s_nop 4\n\ts_waitcnt vmcnt(0)\n\ts_mov_b64 exec, 1\n\tbuffer_atomic_add %0, off, %1, %2 sc0 sc1\n\ts_mov_b64 exec, -1"
                 : "+v"(cnt) : "s"(rws), "s"(W_HF + (c * 2 + (j >> 3)) * 128) : "memory");
    STAMP(9);
    mine = (unsigned)__ballot(lane < 8 && (selu >> 7) == (unsigned)c && ((selu >> 1) & 3u) == (unsigned)wid);
#pragma unroll
    for (int q = 0; q < 8; ++q) dp[q] = L_ONE - 16;
#pragma unroll 1
    for (unsigned m = mine; m; m &= m - 1) {
      const int s = __builtin_ctz(m), t = __builtin_amdgcn_readlane((int)selu, s) & 127;
#pragma unroll
      for (int q = 0; q < 8; ++q) dp[q] = t == tl0 + 8 * (q >> 1) + (q & 1) ? L_BROW + s * 256 + fr * 16 : dp[q];
    }
  }
  f32x4 ac2[2][4];
  float bs[8][4];
#pragma unroll
  for (int q = 0; q < 8; ++q)
#pragma unroll
    for (int n = 0; n < 4; ++n) bs[q][n] = base[q >> 1][n][q & 1];
  {
#pragma unroll
    for (int m = 0; m < 2; ++m)
#pragma unroll
      for (int n = 0; n < 4; ++n) ac2[m][n] = (f32x4){0.f, 0.f, 0.f, 0.f};
    unsigned pc[4], fl2 = M_LAND + 26 * 8, vo = ((j & 8) * 32768 + wid * 2048) * 2 + HOFS(lane);
    const unsigned hr = M_HR, scn2 = M_CONS + 256 + wid * 4, hb_ = W_H + c * 524288 * 2, f9 = M_LAND + 17 * 8 + min(lane, 17) * 4;
#pragma unroll
    for (int q = 0; q < 4; ++q) pc[q] = fr * 128 + (((2 * q + (fq >> 1)) ^ ((fr >> 1) & 7)) * 16) + 8192 * (fq & 1);
    const unsigned sf = W_HF + (c * 2 + (j >> 3)) * 128, sf1 = sf ^ 128;
    unsigned t_sk, t_s0, t_s1;
    asm volatile(ASM_MB : [c00] "+&v"(ac2[0][0]), [c01] "+&v"(ac2[0][1]), [c02] "+&v"(ac2[0][2]), [c03] "+&v"(ac2[0][3]), [c10] "+&v"(ac2[1][0]), [c11] "+&v"(ac2[1][1]), [c12] "+&v"(ac2[1][2]), [c13] "+&v"(ac2[1][3]), [pc0] "+&v"(pc[0]), [pc1] "+&v"(pc[1]), [pc2] "+&v"(pc[2]), [pc3] "+&v"(pc[3]), [b00] "+&v"(bs[0][0]), [b01] "+&v"(bs[0][1]), [b02] "+&v"(bs[0][2]), [b03] "+&v"(bs[0][3]), [b10] "+&v"(bs[1][0]), [b11] "+&v"(bs[1][1]), [b12] "+&v"(bs[1][2]), [b13] "+&v"(bs[1][3]), [b20] "+&v"(bs[2][0]), [b21] "+&v"(bs[2][1]), [b22] "+&v"(bs[2][2]), [b23] "+&v"(bs[2][3]), [b30] "+&v"(bs[3][0]), [b31] "+&v"(bs[3][1]), [b32] "+&v"(bs[3][2]), [b33] "+&v"(bs[3][3]), [b40] "+&v"(bs[4][0]), [b41] "+&v"(bs[4][1]), [b42] "+&v"(bs[4][2]), [b43] "+&v"(bs[4][3]), [b50] "+&v"(bs[5][0]), [b51] "+&v"(bs[5][1]), [b52] "+&v"(bs[5][2]), [b53] "+&v"(bs[5][3]), [b60] "+&v"(bs[6][0]), [b61] "+&v"(bs[6][1]), [b62] "+&v"(bs[6][2]), [b63] "+&v"(bs[6][3]), [b70] "+&v"(bs[7][0]), [b71] "+&v"(bs[7][1]), [b72] "+&v"(bs[7][2]), [b73] "+&v"(bs[7][3]), [vo] "+&v"(vo), [fl] "+&v"(fl2), [sg] "=&s"(t_sk), [s0] "=&s"(t_s0), [s1] "=&s"(t_s1), [bud] "+&s"(bud) : [d0] "v"(dp[0]), [d1] "v"(dp[1]), [d2] "v"(dp[2]), [d3] "v"(dp[3]), [d4] "v"(dp[4]), [d5] "v"(dp[5]), [d6] "v"(dp[6]), [d7] "v"(dp[7]), [cnt] "v"(cnt), [xd] "v"(FL_XD), [mine] "s"(mine), [srd] "s"(rws), [srz] "s"(rz), [hr] "v"(hr), [f9] "v"(f9), [w] "s"(wid), [scn] "s"(scn2), [sf] "s"(sf), [sf1] "s"(sf1), [k2] "s"(0x4000), [h0] "s"(hb_ + 0), [h1] "s"(hb_ + 16384), [h2] "s"(hb_ + 32768), [h3] "s"(hb_ + 49152), [h4] "s"(hb_ + 65536), [h5] "s"(hb_ + 81920), [h6] "s"(hb_ + 98304), [h7] "s"(hb_ + 114688) : "memory", "scc", "vcc", "m0", "v64", "v65", "v66", "v67", "v68", "v69", "v70", "v71", "v72", "v73", "v74", "v75", "v76", "v77", "v78", "v79", "v80", "v81", "v82", "v83", "v84", "v85", "v86", "v87", "v88", "v89", "v90", "v91", "v92", "v93", "v94", "v95", "v96", "v97", "v98", "v99", "v100", "v101", "v102", "v103", "v104", "v105", "v106", "v107", "v108", "v109", "v110", "v111", "v112", "v113", "v114", "v115", "v116", "v117", "v118", "v119", "v120", "v121", "v122", "v123", "v124", "v125", "v126", "v127", "v128", "v129", "v130", "v131", "v132", "v133", "v134", "v135", "v136", "v137", "v138", "v139", "v140", "v141", "v142", "v143", "v144", "v145", "v146", "v147", "v148", "v149", "v150", "v151", "v152", "v153", "v154", "v155", "v156", "v157", "v158", "v159", "v160", "v161", "v162", "v163", "v164", "v165", "v166", "v167", "v168", "v169", "v170", "v171", "v172", "v173", "v174", "v175", "v176", "v177", "v178", "v179", "v180", "v181", "v182", "v183", "v184", "v185", "v186", "v187", "v188", "v189", "v190", "v191", "v192", "v193", "v194", "v195", "v196");
  }
  STAMP(6);
  asm volatile("" : "+v"(g2[0]), "+v"(g2[1]), "+v"(g2[2]), "+v"(g2[3]));
  int ln2 = lane;
  asm volatile("" : "+v"(ln2));
  const int fr2 = ln2 & 15, tl2 = 32 * (ln2 >> 4) + 2 * wid;
  f32x4 ov[2][4];
#pragma unroll
  for (int e = 0; e < 2; ++e)
#pragma unroll
    for (int r = 0; r < 4; ++r) {
      const float gg = 2.f * g2[r][e];
#pragma unroll
      for (int n = 0; n < 4; ++n) ov[e][r][n] = fma1(gg, ac2[e][n][r], bs[2 * r + e][n]);
    }
  {
    const rsrc_t ro = mk(out, 2048 * 1024 * 4);
    const unsigned oo = (c * 128 + tl2) * 4096 + (j * 64 + 4 * fr2) * 4, oo2 = oo + 4096;
    asm volatile("s_nop 4\n\t"
                 "buffer_store_dwordx4 %0, %8, %10, 0 offen" OST "\n\tbuffer_store_dwordx4 %4, %9, %10, 0 offen" OST "\n\t"
                 "buffer_store_dwordx4 %1, %8, %10, %11 offen" OST "\n\tbuffer_store_dwordx4 %5, %9, %10, %11 offen" OST "\n\t"
                 "buffer_store_dwordx4 %2, %8, %10, %12 offen" OST "\n\tbuffer_store_dwordx4 %6, %9, %10, %12 offen" OST "\n\t"
                 "buffer_store_dwordx4 %3, %8, %10, %13 offen" OST "\n\tbuffer_store_dwordx4 %7, %9, %10, %13 offen" OST "\n\t"
                 "s_nop 7\n\ts_nop 7"
                 ::"v"(ov[0][0]), "v"(ov[0][1]), "v"(ov[0][2]), "v"(ov[0][3]), "v"(ov[1][0]), "v"(ov[1][1]), "v"(ov[1][2]), "v"(ov[1][3]), "v"(oo), "v"(oo2), "s"(ro),
                 "s"(32768), "s"(65536), "s"(98304) : "memory");
  }
  if (c == 15 && j == 0 && wid == 0 && lane == 0) st64c(rws, W_EPOCH, (u32x2){tagA, tagB});
  STAMP(7);
  STAMPS();
#endif
}

extern "C" void kernel_launch(void* const* d_in, const int* in_sizes, int n_in, void* d_out, int out_size, void* d_ws, size_t ws_size,
                              hipStream_t stream) {
  (void)hipFuncSetAttribute((const void*)fused, hipFuncAttributeMaxDynamicSharedMemorySize, LDS_BYTES);
  fused<<<256, 512, LDS_BYTES, stream>>>((const u16*)d_in[0], (const u16*)d_in[1], (const u16*)d_in[2], (const u16*)d_in[3], (const u16*)d_in[4],
                                         (const u16*)d_in[5], (const u16*)d_in[6], (const u16*)d_in[7], (const u16*)d_in[8], (const u16*)d_in[10],
                                         (const u16*)d_in[11], (const u16*)d_in[12], (const u16*)d_in[13], (const u16*)d_in[14], (char*)d_ws + WS_BASE(ws_size),
                                         (float*)d_out);
}
```
